```python
import torch

def _softmax(x, axis=-1):
    return torch.softmax(x, dim=axis)

def _gelu(x):
    return torch.nn.functional.gelu(x, approximate='tanh')

def _silu(x):
    return torch.nn.functional.silu(x)

def _sigmoid(x):
    return torch.sigmoid(x)

def _layernorm(x, axis=-1, eps=1e-05):
    m = x.mean(dim=axis, keepdim=True)
    v = x.var(dim=axis, keepdim=True, unbiased=False)
    return (x - m) / torch.sqrt(v + eps)

def _scatter_add(tgt, idx, src, axis=0):
    return tgt.clone().index_add_(axis, idx, src)

def _segment_sum(src, seg, n):
    out = torch.zeros((n,) + src.shape[1:], dtype=src.dtype)
    return out.index_add_(0, seg, src)

def _pad(x, pw):
    flat = tuple((v for pair in reversed(pw) for v in pair))
    return torch.nn.functional.pad(x, flat)
kernel_name = 'recurrent_scan__memory_aug_recurrent_mem_tr__moe_arch_mod__prompt'

def setup_inputs():

    _w = lambda a: (lambda t: t.to(torch.bfloat16) if t.is_floating_point() else t)(torch.as_tensor(a))

    def _A_setup():
        B, S, Dm = (1, 2048, 640)
        H, Dh = (32, 128)
        M, seg = (8, 32)
        seg = min(seg, S)
        S = S // seg * seg
        return {'x': _w((lambda *s: torch.randn(*s, dtype=torch.bfloat16) * 0.05)(B, S, Dm)), 'mem0': _w((lambda *s: torch.randn(*s, dtype=torch.bfloat16) * 0.2)(B, M, Dm)), 'Wq': _w((lambda *s: torch.randn(*s, dtype=torch.bfloat16) * 1.6)(Dm, H, Dh) * Dm ** (-0.5)), 'Wk': _w((lambda *s: torch.randn(*s, dtype=torch.bfloat16) * 1.6)(Dm, H, Dh) * Dm ** (-0.5)), 'Wv': _w((lambda *s: torch.randn(*s, dtype=torch.bfloat16) * 0.2)(Dm, H, Dh) * Dm ** (-0.5)), 'Wo': _w((lambda *s: torch.randn(*s, dtype=torch.bfloat16) * 0.2)(H, Dh, Dm) * (H * Dh) ** (-0.5)), 'seg_len': seg}
    _A_in = _A_setup()

    def _B_setup():
        B, S, D, F, C = (1, 2048, 640, 1728, 8)
        return {'x': _w((lambda *s: torch.randn(*s, dtype=torch.bfloat16) * 0.05)(B, S, D)), 'w_r': _w((lambda *s: torch.randn(*s, dtype=torch.bfloat16) * 0.2)(D)), 'w_gate': _w((lambda *s: torch.randn(*s, dtype=torch.bfloat16) * 0.1)(D, F)), 'w_up': _w((lambda *s: torch.randn(*s, dtype=torch.bfloat16) * 0.1)(D, F)), 'w_down': _w((lambda *s: torch.randn(*s, dtype=torch.bfloat16) * 0.1)(F, D))}
    _B_in = _B_setup()
    return {'A_x': _A_in['x'], 'A_mem0': _A_in['mem0'], 'A_Wq': _A_in['Wq'], 'A_Wk': _A_in['Wk'], 'A_Wv': _A_in['Wv'], 'A_Wo': _A_in['Wo'], 'A_seg_len': _A_in['seg_len'], 'B_x': _B_in['x'], 'B_w_r': _B_in['w_r'], 'B_w_gate': _B_in['w_gate'], 'B_w_up': _B_in['w_up'], 'B_w_down': _B_in['w_down']}

def reference(A_x, A_mem0, A_Wq, A_Wk, A_Wv, A_Wo, A_seg_len, B_x, B_w_r, B_w_gate, B_w_up, B_w_down):

    def _A_ref(x, mem0, Wq, Wk, Wv, Wo, seg_len):
        B, S, Dm = x.shape
        H, Dh = (Wq.shape[1], Wq.shape[2])
        M = mem0.shape[1]
        scale = Dh ** (-0.5)
        n_seg = S // seg_len
        L = M + seg_len
        idx = torch.arange(L)
        mask = (idx[:, None] < M) | (idx[:, None] >= idx[None, :])
        mem = mem0
        outs = []
        for s in range(n_seg):
            xs = x[:, s * seg_len:(s + 1) * seg_len, :].clone()
            h = torch.concatenate([mem, xs], axis=1)
            q = torch.einsum('btd,dhe->bhte', h, Wq)
            k = torch.einsum('btd,dhe->bhte', h, Wk)
            v = torch.einsum('btd,dhe->bhte', h, Wv)
            scores = torch.einsum('bhqe,bhke->bhqk', q, k) * scale
            scores = torch.where(mask[None, None], scores, -1000000000.0)
            p = _softmax(scores, axis=-1)
            o = torch.einsum('bhqk,bhke->bhqe', p, v)
            o = torch.einsum('bhte,hed->btd', o, Wo)
            mem = o[:, :M, :].clone()
            outs.append(o[:, M:, :])
        return torch.concatenate(outs, axis=1)

    def _B_ref(x, w_r, w_gate, w_up, w_down):
        B, S, D = x.shape
        C = 8
        r = x @ w_r
        idx = torch.argsort(-r, axis=-1)[:, :C]
        idx_e = (lambda x, n, axis: torch.repeat_interleave(x, n, dim=axis))(idx.reshape(B, C, 1), D, axis=2)
        x_sel = (lambda x, i, axis: torch.take_along_dim(x, i, dim=axis))(x, idx_e, axis=1)
        g = _silu(x_sel @ w_gate)
        u = x_sel @ w_up
        y = g * u @ w_down
        r_sel = _sigmoid((lambda x, i, axis: torch.take_along_dim(x, i, dim=axis))(r, idx, axis=1))
        y = y * r_sel.reshape(B, C, 1)
        b_off = (torch.arange(B) * S).reshape(B, 1)
        flat_idx = (idx + b_off).reshape(B * C)
        zero = (x * 0.0).reshape(B * S, D)
        upd = _scatter_add(zero, flat_idx, y.reshape(B * C, D), axis=0)
        return x + upd.reshape(B, S, D)
    x = A_x
    xn = _layernorm(x, axis=-1)
    s = _A_ref(xn, A_mem0, A_Wq, A_Wk, A_Wv, A_Wo, A_seg_len)
    g = _silu(_B_ref(xn, B_w_r, B_w_gate, B_w_up, B_w_down))
    return x + s * g
```

```cpp
#include <hip/hip_runtime.h>
#include <hip/hip_bf16.h>
#include <stdint.h>

constexpr int S_ = 2048, D_ = 640, H_ = 32, DH_ = 128, NSEG = 64, SEGL = 32, FF_ = 1728, TOPC = 8;
constexpr int HD_ = H_ * DH_;
constexpr int NRED = 40;
constexpr int NFILL = 14;

typedef unsigned short u16;
typedef unsigned int u32;
typedef unsigned long long u64;
typedef __attribute__((ext_vector_type(8))) __bf16 bf16x8;
typedef __attribute__((ext_vector_type(16))) __bf16 bf16x16;
typedef __attribute__((ext_vector_type(4))) float f32x4;
typedef __attribute__((ext_vector_type(4))) unsigned int u32x4;
typedef __attribute__((ext_vector_type(8))) unsigned int u32x8;
typedef __attribute__((ext_vector_type(2))) unsigned int u32x2;

__device__ __forceinline__ float bf2f(u16 h) { return __uint_as_float(((u32)h) << 16); }
typedef __bf16 bf16x2_t __attribute__((ext_vector_type(2)));
typedef float f32x2_t __attribute__((ext_vector_type(2)));
__device__ __forceinline__ u16 f2bf(float f) {
    __bf16 h = (__bf16)f;
    return __builtin_bit_cast(u16, h);
}
__device__ __forceinline__ float bfr(float f) { return (float)(__bf16)f; }
__device__ __forceinline__ u32 pack2(float a, float b) {
    f32x2_t f = {a, b};
    bf16x2_t h = __builtin_convertvector(f, bf16x2_t);
    return __builtin_bit_cast(u32, h);
}
__device__ __forceinline__ bf16x8 as_bf(u32x4 v) { return __builtin_bit_cast(bf16x8, v); }
__device__ __forceinline__ f32x4 mfma16(u32x4 a, u32x4 b, f32x4 c) {
    return __builtin_amdgcn_mfma_f32_16x16x32_bf16(as_bf(a), as_bf(b), c, 0, 0, 0);
}
__device__ __forceinline__ f32x4 smf(u32x4 a, u32x4 b0, u32x4 b1, f32x4 c, int idx) {
    u32x8 b = {b0[0], b0[1], b0[2], b0[3], b1[0], b1[1], b1[2], b1[3]};
    return __builtin_amdgcn_smfmac_f32_16x16x64_bf16(as_bf(a), __builtin_bit_cast(bf16x16, b), c, idx, 0, 0);
}
__device__ __forceinline__ int dmap(int p) {
    int q = p & 15;
    return (p & ~15) | (q & 1) | ((q & 2) << 2) | ((q & 4) >> 1) | ((q & 8) >> 1);
}

constexpr size_t al256(size_t x) { return (x + 255) & ~(size_t)255; }
constexpr size_t OFF_XN = 0;
constexpr size_t OFF_XNP = OFF_XN + al256((size_t)S_ * D_ * 2);
constexpr size_t OFF_R = OFF_XNP + al256((size_t)S_ * D_ * 2);
constexpr size_t OFF_PX = OFF_R + al256((size_t)S_ * 4);
constexpr size_t OFF_PXM = OFF_PX + al256((size_t)NSEG * H_ * 2048);
constexpr size_t OFF_HB = OFF_PXM + al256((size_t)NSEG * 8 * 2048);
constexpr size_t OFF_UPD = OFF_HB + al256((size_t)16 * FF_ * 2);
constexpr size_t OFF_GEN = OFF_UPD + al256((size_t)8 * D_ * 2);
constexpr size_t OFF_CLAIM = OFF_GEN + 256;
constexpr size_t OFF_INI = OFF_CLAIM + 16384;
constexpr size_t OFF_ERR = OFF_INI + 2048;
constexpr size_t OFF_FB = OFF_ERR + 256;
constexpr size_t OFF_FP = OFF_FB + 4096;
constexpr size_t OFF_FZ = OFF_FP + 1024;
constexpr size_t OFF_FR = OFF_FZ + 2048;
constexpr size_t OFF_RDY = OFF_FR + 4096;
constexpr size_t OFF_IDX = OFF_RDY + 1024;
constexpr size_t OFF_CNT = OFF_IDX + 256;
constexpr size_t ZERO_BEGIN = OFF_ERR, ZERO_END = OFF_CNT + 1024;
constexpr size_t OFF_WIN = (size_t)12 << 20;
constexpr size_t WS_TOTAL = OFF_WIN + ((size_t)240 << 20);
static_assert(ZERO_END <= OFF_WIN && WS_TOTAL <= ((size_t)256 << 20), "ws");

constexpr u32 SLOT0 = 20u << 20;
constexpr u32 SL_P = 4 * 32768, SL_TS = 2 * 32768 + 1024, SL_REC = 3 * 32768, SL_2ND = 4 * 32768;
constexpr u32 L_M2 = 81920;
constexpr u32 FAR_BX = 1u << 20;
constexpr u32 NEAR_Z = 26u << 20;
#define RING 16
constexpr u32 NEAR_B = NEAR_Z + NSEG * NRED * 8192;
constexpr u32 NEAR_QX = NEAR_B + 32 * NRED * 32768;
constexpr u32 NEAR_KX = NEAR_QX + NSEG * H_ * 8192;
#define QFL 0u
#define KFL 1u
static_assert(2 * ((size_t)NEAR_KX + NSEG * H_ * 8192) + ((size_t)2 << 20) <= WS_TOTAL - OFF_WIN && SLOT0 + (32u << 20) <= 2 * NEAR_Z && NEAR_B % (1u << 20) == 0 && FAR_BX + NSEG * NRED * 32768 <= NEAR_QX, "window");
__device__ __forceinline__ u32 b_at(u32 base, u32 n, u32 sg, u32 ct) { return base + (ct < 32 ? (sg << 20) + (ct << 15) : (n << 20) + (sg << 18) + ((ct - 32) << 15)); }
__device__ __forceinline__ u32 slot_off(u32 wp, int s) {
    u32 r, t;
    asm("s_lshr_b32 %0, %3, 2\n\ts_xor_b32 %0, %0, %2\n\ts_and_b32 %0, %0, 1\n\ts_lshl_b32 %0, %0, 13\n\ts_lshl_b32 %1, %3, 19\n\t"
        "s_add_u32 %0, %0, %1\n\ts_andn2_b32 %1, %2, 15\n\ts_add_u32 %0, %0, %1\n\ts_and_b32 %1, %2, 8\n\ts_lshl_b32 %1, %1, 15\n\ts_add_u32 %0, %0, %1\n\t"
        "s_add_u32 %0, %0, %4" : "=&s"(r), "=&s"(t) : "s"(wp), "s"(s), "n"(SLOT0) : "scc");
    return r;
}
__device__ __forceinline__ u32 slot_v(u32 wp, u32 s) { return (wp & ~15u) + SLOT0 + (s << 19) + (((wp ^ (s >> 2)) & 1u) << 13) + ((wp & 8u) << 15); }
__device__ __forceinline__ u32 far_v(u32 wp, u32 L) { return (wp & ~15u) + 2 * L + ((~(wp ^ (L >> 20)) & 1u) << 13); }
__device__ __forceinline__ u32 tab(u32 t, int s) { return __builtin_amdgcn_readlane(t, s); }
__device__ __forceinline__ u32 copy_off(u32 c, int s) { const u32 v = c ^ (u32)s; return ((v & 1u) << 14) | ((v & 2u) << 11); }
__device__ __forceinline__ u32 done_at(u32 wp, u32 sg, u32 p) { return far_v(wp ^ 1u, (sg * 2 + (p >> 6)) * 8192) + (p & 63u) * 128; }
__device__ __forceinline__ u32 z_at(u32 wp, u32 s, u32 ct) {
    const u32 X = 2 * NEAR_Z + (s * 10 + (ct >> 2)) * 65536;
    return (wp & ~15u) + X + (((wp ^ (X >> 21)) & 1u) << 13) + copy_off(ct ^ (wp >> 1) ^ 1u, (int)(X >> 19));
}
__device__ __forceinline__ u32 far_off(u32 wp, u32 L) {
    u32 r, t;
    asm("s_lshr_b32 %0, %3, 20\n\ts_xnor_b32 %0, %0, %2\n\ts_and_b32 %0, %0, 1\n\ts_lshl_b32 %0, %0, 13\n\ts_lshl_b32 %1, %3, 1\n\t"
        "s_add_u32 %0, %0, %1\n\ts_andn2_b32 %1, %2, 15\n\ts_add_u32 %0, %0, %1" : "=&s"(r), "=&s"(t) : "s"(wp), "s"(L) : "scc");
    return r;
}
constexpr u32 SENT = 0xffbfffbfu;
constexpr u32 MARK = 0xffbeffbeu;
constexpr u32 OOB = 0x80000000u;
__device__ __forceinline__ u32 amap(u32 L) { return ((L >> 13) << 14) | (L & 8191u); }
__device__ __forceinline__ u32 win_off(const char* ws) {
    u64 a = (u64)(uintptr_t)ws;
    u64 w = (a + OFF_WIN + ((u64)2 << 20) - 1) & ~(((u64)2 << 20) - 1);
    return (u32)(w - a);
}

constexpr int AUX_SC1 = 16;
typedef __amdgpu_buffer_rsrc_t rsrc_t;
__device__ __forceinline__ rsrc_t mk_rsrc(const void* p) {
    return __builtin_amdgcn_make_buffer_rsrc((void*)p, 0, 0x7fffffff, 0x00020000);
}
__device__ __forceinline__ u32x4 ldx4(rsrc_t r, u32 off, u32 so = 0) { return __builtin_amdgcn_raw_buffer_load_b128(r, off, so, AUX_SC1); }
__device__ __forceinline__ u32x2 ldx2(rsrc_t r, u32 off, u32 so = 0) { return __builtin_amdgcn_raw_buffer_load_b64(r, off, so, AUX_SC1); }
__device__ __forceinline__ u32 ldx1(rsrc_t r, u32 off, u32 so = 0) { return __builtin_amdgcn_raw_buffer_load_b32(r, off, so, AUX_SC1); }
__device__ __forceinline__ void stx4(rsrc_t r, u32 off, u32x4 v, u32 so = 0) { __builtin_amdgcn_raw_buffer_store_b128(v, r, off + so, 0, AUX_SC1); }
__device__ __forceinline__ void stx2(rsrc_t r, u32 off, u32x2 v, u32 so = 0) { __builtin_amdgcn_raw_buffer_store_b64(v, r, off, so, AUX_SC1); }
__device__ __forceinline__ void stx1(rsrc_t r, u32 off, u32 v, u32 so = 0) { __builtin_amdgcn_raw_buffer_store_b32(v, r, off, so, AUX_SC1); }
#define DRAIN() asm volatile("s_waitcnt vmcnt(0)" ::: "memory")
#define CFENCE() asm volatile("" ::: "memory")
#define LBAR() asm volatile("s_waitcnt lgkmcnt(0)\n\ts_barrier" ::: "memory")

constexpr u32 SPIN_CAP = 1u << 18;
__device__ __forceinline__ int lane_id() {
    int l;
    asm volatile("v_mbcnt_lo_u32_b32 %0, -1, 0\n\tv_mbcnt_hi_u32_b32 %0, -1, %0" : "=v"(l));
    __builtin_assume(l >= 0 && l < 64);
    return l;
}

__device__ __forceinline__ float xmax16(float x) {
    u32 a = __float_as_uint(x);
    auto r = __builtin_amdgcn_permlane16_swap(a, a, false, false);
    return fmaxf(__uint_as_float(r[0]), __uint_as_float(r[1]));
}
__device__ __forceinline__ float xmax32(float x) {
    u32 a = __float_as_uint(x);
    auto r = __builtin_amdgcn_permlane32_swap(a, a, false, false);
    return fmaxf(__uint_as_float(r[0]), __uint_as_float(r[1]));
}
__device__ __forceinline__ float xadd16(float x) {
    u32 a = __float_as_uint(x);
    auto r = __builtin_amdgcn_permlane16_swap(a, a, false, false);
    return __uint_as_float(r[0]) + __uint_as_float(r[1]);
}
__device__ __forceinline__ float xadd32(float x) {
    u32 a = __float_as_uint(x);
    auto r = __builtin_amdgcn_permlane32_swap(a, a, false, false);
    return __uint_as_float(r[0]) + __uint_as_float(r[1]);
}
__device__ __forceinline__ u32 umax3(u32 a, u32 b, u32 c) { return max(max(a, b), c); }

__device__ __forceinline__ void wait_flags(rsrc_t rs, u32 off, bool act, u32 target, bool& dead, u32* errw, u32 code, int lane) {
    u32 spins = 0;
#pragma unroll 1
    while (!dead) {
        CFENCE();
        u32x4 f = ldx4(rs, off);
        bool ok = !act || ((f[0] >= target) & (f[1] >= target) & (f[2] >= target) & (f[3] >= target));
        if (__all(ok)) break;
        __builtin_amdgcn_s_sleep(2);
        if (++spins > SPIN_CAP) { if (lane == 0) atomicOr(errw, code); dead = true; }
        if ((spins & 63) == 0 && ldx1(rs, (u32)OFF_ERR) != 0u) dead = true;
    }
    CFENCE();
}
__device__ __forceinline__ void wait_word(rsrc_t rs, u32 off, bool act, u32 target, bool& dead, u32* errw, u32 code, int lane) {
    u32 spins = 0;
#pragma unroll 1
    while (!dead) {
        CFENCE();
        u32 f = ldx1(rs, off);
        if (__all(!act || f >= target)) break;
        __builtin_amdgcn_s_sleep(4);
        if (++spins > SPIN_CAP) { if (lane == 0) atomicOr(errw, code); dead = true; }
        if ((spins & 63) == 0 && ldx1(rs, (u32)OFF_ERR) != 0u) dead = true;
    }
    CFENCE();
}

__device__ __forceinline__ u32 cal_tag(u64 tag, u32 v) { return ((u32)tag ^ (u32)(tag >> 32)) + v * 0x9E3779B1u; }
__device__ __forceinline__ void wait_done(rsrc_t rs, u32 off, u32 dv, u32& cap, u32* errw) {
    u32 spins = 0;
#pragma unroll 1
    for (; spins < cap; ++spins) {
        CFENCE();
        if (__all(ldx1(rs, off) == dv)) break;
        __builtin_amdgcn_s_sleep(12);
    }
    if (spins >= cap) { atomicOr(errw, 16u); cap = 1; }
    CFENCE();
}
__device__ __forceinline__ u32 done_tag(u64 tag) { return cal_tag(tag, 1u) | 1u; }
__device__ __forceinline__ bool rows_ok(u32x4 f, u64 tag) { return (f[0] == (u32)tag) & (f[1] == (u32)(tag >> 32)) & (f[2] == (u32)tag) & (f[3] == (u32)(tag >> 32)); }
__device__ __forceinline__ bool ini_ok(u32x4 c, u64 tag) { return (c[1] == cal_tag(tag, c[0])) & (c[3] == cal_tag(tag, c[2])); }
__device__ __forceinline__ void wait_rows(rsrc_t rs, u64 tag, int lane) {
#pragma unroll 1
    for (u32 spins = 0; spins < SPIN_CAP; ++spins) {
        CFENCE();
        u32x4 f = ldx4(rs, (u32)(OFF_INI + 1024 + lane * 16));
        if (__all(rows_ok(f, tag))) break;
        __builtin_amdgcn_s_sleep(4);
    }
    CFENCE();
}
__device__ __forceinline__ u32 to_sgpr(u32 x) {
    u32 r;
    asm volatile("s_nop 0\n\tv_readfirstlane_b32 %0, %1\n\ts_nop 4" : "=s"(r) : "v"(x));
    return r;
}
__device__ __forceinline__ u32 wp_of(const char* ws, u32x4 c) {
    u32 v = c[0] + c[2];
    v += __builtin_amdgcn_update_dpp(0u, v, 0xb1, 0xf, 0xf, false);
    v += __builtin_amdgcn_update_dpp(0u, v, 0x4e, 0xf, 0xf, false);
    v += __builtin_amdgcn_update_dpp(0u, v, 0x124, 0xf, 0xf, false);
    v += __builtin_amdgcn_update_dpp(0u, v, 0x128, 0xf, 0xf, false);
    const u32 r = __builtin_amdgcn_readlane(v, 48) + 0x5f5f5f5fu;
    return to_sgpr(win_off(ws) | ((r >> 7) & 1u) | ((r >> 14) & 2u) | ((r >> 21) & 4u) | ((r >> 28) & 8u));
}
__device__ __forceinline__ u32 wait_init(rsrc_t rs, const char* ws, u64 tag, int lane) {
    u32x4 c = {0u, 0u, 0u, 0u};
#pragma unroll 1
    for (u32 spins = 0; spins < SPIN_CAP; ++spins) {
        CFENCE();
        c = ldx4(rs, (u32)(OFF_INI + (lane | 48) * 16));
        if (__all(ini_ok(c, tag))) break;
        __builtin_amdgcn_s_sleep(4);
    }
    return wp_of(ws, c);
}

#define STAMPS() do { } while (0)
#define STAMP(k) do { } while (0)
#define RSTAMP(k) do { } while (0)
#define STAMP2(k, r) do { } while (0)
#define FLUSH() do { } while (0)
#define WSTAMP(id) do { } while (0)
#define WDUMP() do { } while (0)
#define CSTAMP(id) do { } while (0)
constexpr u32 INI_ZERO_CHUNKS = (u32)((ZERO_END - ZERO_BEGIN) / 16);
static_assert(INI_ZERO_CHUNKS <= 32 * 64, "reset");

__device__ __forceinline__ float rsum16(float x) {
    x += __uint_as_float(__builtin_amdgcn_update_dpp(0u, __float_as_uint(x), 0xb1, 0xf, 0xf, false));
    x += __uint_as_float(__builtin_amdgcn_update_dpp(0u, __float_as_uint(x), 0x4e, 0xf, 0xf, false));
    x += __uint_as_float(__builtin_amdgcn_update_dpp(0u, __float_as_uint(x), 0x124, 0xf, 0xf, false));
    x += __uint_as_float(__builtin_amdgcn_update_dpp(0u, __float_as_uint(x), 0x128, 0xf, 0xf, false));
    return x;
}

__device__ __forceinline__ void ln_regs(u32x4 (&xv)[5]) {
    float sum = 0.f;
#pragma unroll
    for (int i = 0; i < 5; ++i)
#pragma unroll
        for (int j = 0; j < 4; ++j) sum += __uint_as_float(xv[i][j] << 16) + __uint_as_float(xv[i][j] & 0xffff0000u);
    const float mean = rsum16(sum) * (1.0f / D_);
    float vs = 0.f;
#pragma unroll
    for (int i = 0; i < 5; ++i)
#pragma unroll
        for (int j = 0; j < 4; ++j) {
            float d0 = __uint_as_float(xv[i][j] << 16) - mean, d1 = __uint_as_float(xv[i][j] & 0xffff0000u) - mean;
            vs += d0 * d0 + d1 * d1;
        }
    const float var = bfr(rsum16(vs) * (1.0f / D_));
    const float m_bf = bfr(mean);
    const float rden = 1.0f / bfr(sqrtf(bfr(var + 1e-05f)));
#pragma unroll
    for (int i = 0; i < 5; ++i)
#pragma unroll
        for (int j = 0; j < 4; ++j)
            xv[i][j] = pack2(bfr(__uint_as_float(xv[i][j] << 16) - m_bf) * rden, bfr(__uint_as_float(xv[i][j] & 0xffff0000u) - m_bf) * rden);
}
__device__ __forceinline__ u32x4 ln_perm(const u32x4 o, const int n) {
    const u32 s0 = (n & 1) ? o[0] : o[2], s1 = (n & 1) ? o[1] : o[3];
    const u32 t0 = __builtin_amdgcn_update_dpp(0u, s0, 0xb1, 0xf, 0xf, false), t1 = __builtin_amdgcn_update_dpp(0u, s1, 0xb1, 0xf, 0xf, false);
    return (n & 1) ? (u32x4){t0, o[2], t1, o[3]} : (u32x4){o[0], t0, o[1], t1};
}
__device__ __forceinline__ u32 img_off(const int row, const int n, const int i) {
    return (u32)((row >> 4) * 20480 + (4 * i + (n >> 2)) * 1024 + (n & 3) * 256 + (row & 15) * 16);
}
__device__ __forceinline__ void ln_load(const int row0, const int lane, const u16* __restrict__ x, const u16* __restrict__ w_r, u32x4 (&xv)[5], u32x4 (&wv)[5]) {
    const int n = lane & 15, row = row0 + (lane >> 4);
#pragma unroll
    for (int i = 0; i < 5; ++i) xv[i] = *(const u32x4*)(x + (size_t)row * D_ + (n + 16 * i) * 8);
#pragma unroll
    for (int i = 0; i < 5; ++i) wv[i] = *(const u32x4*)(w_r + (n + 16 * i) * 8);
}
__device__ __forceinline__ void ln_store(const int row0, const int lane, const u32x4 (&xv)[5], const u32x4 (&wv)[5], rsrc_t rs) {
    const int n = lane & 15, row = row0 + (lane >> 4);
#pragma unroll
    for (int i = 0; i < 5; ++i) stx4(rs, (u32)OFF_XNP + img_off(row, n, i), ln_perm(xv[i], n));
    CFENCE();
    float dot = 0.f;
#pragma unroll
    for (int i = 0; i < 5; ++i) {
        stx4(rs, (u32)OFF_XN + (u32)(row * D_ + (n + 16 * i) * 8) * 2, xv[i]);
#pragma unroll
        for (int j = 0; j < 4; ++j)
            dot += __uint_as_float(xv[i][j] << 16) * __uint_as_float(wv[i][j] << 16) + __uint_as_float(xv[i][j] & 0xffff0000u) * __uint_as_float(wv[i][j] & 0xffff0000u);
    }
    dot = rsum16(dot);
    stx1(rs, n == 0 ? (u32)(OFF_R + row * 4) : OOB, __float_as_uint(bfr(dot)));
}
__device__ __forceinline__ u32x4 mk_srd(const char* ws) {
    const u64 a = (u64)(uintptr_t)ws;
    return (u32x4){(u32)a, (u32)(a >> 32), 0x7fffffffu, 0x00020000u};
}

__device__ __forceinline__ u32 probe(const int pi, const u32 xcc, const u32 hl, char* __restrict__ ws) {
    const u32x4 srd = mk_srd(ws);
    const u32 base = win_off(ws) + SLOT0 + (56u << 19) + (u32)(6 + (pi >> 4)) * 32768u + (u32)(pi & 15) * 128u;
    const int lane = lane_id();
    const u32 zero = 0;
    asm volatile("buffer_atomic_or %0, %1, %2, %3 offen sc1\n\ts_waitcnt vmcnt(0)" :: "v"(zero), "v"(lane < 16 ? (u32)lane * 2048u : OOB), "s"(srd), "s"(base) : "memory");
    const u32 one = lane == 0 ? 0u : OOB;
    auto lat = [&](u32 q) {
        u64 t0, t1, t2;
        asm volatile("s_memtime %0\n\tbuffer_atomic_or %3, %4, %5, %6 offen sc1\n\ts_waitcnt vmcnt(0)\n\ts_memtime %1\n\t"
                     "buffer_atomic_or %3, %4, %5, %6 offen sc1\n\ts_waitcnt vmcnt(0)\n\ts_memtime %2\n\ts_waitcnt lgkmcnt(0)"
                     : "=&s"(t0), "=&s"(t1), "=&s"(t2) : "v"(zero), "v"(one), "s"(srd), "s"(base + q * 2048u) : "memory");
        return min((u32)t1 - (u32)t0, (u32)t2 - (u32)t1);
    };
    u32 q = (u32)pi * 5u & 15u, best = lat(q), votes = 0x01010101u;
    const u32 flip = (xcc >> 2) | ((xcc & 3u) << 1) | (hl << 3);
#pragma unroll 1
    for (u32 r = 0; r < 2; ++r) {
        const u32 i = r * (1u + ((u32)pi >> 2) % 3u + ((u32)pi & 3u)) % 3u + r, bit = (0x1284u >> (4 * i)) & 15u;
        u32 t = lat(q ^ bit);
        if (r) { best = min(best, lat(q)); t = min(t, lat(q ^ bit)); }
        if ((t + 6u < best || best + 6u < t) && !(r && xcc >= 4)) votes += ((((q & bit) != 0) ^ (t < best) ^ ((flip >> i) & 1u)) * 2u - 1u) << (8 * i);
        if (t < best) { best = t; q ^= bit; }
    }
    return votes;
}

typedef __attribute__((ext_vector_type(32))) unsigned int u32x32;
__device__ __forceinline__ u32x4 wfrag(const u32x32 (&W)[5], const int i) {
    return (u32x4){W[i >> 3][(i & 7) * 4], W[i >> 3][(i & 7) * 4 + 1], W[i >> 3][(i & 7) * 4 + 2], W[i >> 3][(i & 7) * 4 + 3]};
}
#define PIN_W(W) asm volatile("" : "+{v[16:47]}"(W[0]), "+{v[48:79]}"(W[1]), "+{v[80:111]}"(W[2]), "+{v[112:143]}"(W[3]), "+{v[144:175]}"(W[4]))
typedef __attribute__((ext_vector_type(4))) short s16x4;
typedef __attribute__((address_space(3))) s16x4* lds_s16x4p;
__device__ __forceinline__ u32x2 trd(u32 ldsaddr) {
    return __builtin_bit_cast(u32x2, __builtin_amdgcn_ds_read_tr16_b64_v4i16((lds_s16x4p)ldsaddr));
}
template <bool NT>
__device__ __forceinline__ void dma16(const void* g, u32 lds_base) {
    u32 sv;
    if (NT)
        asm volatile("s_mov_b32 %0, m0\n\ts_mov_b32 m0, %2\n\ts_nop 0\n\tglobal_load_lds_dwordx4 %1, off nt\n\ts_mov_b32 m0, %0"
                     : "=&s"(sv) : "v"(g), "s"(lds_base) : "memory");
    else
        asm volatile("s_mov_b32 %0, m0\n\ts_mov_b32 m0, %2\n\ts_nop 0\n\tglobal_load_lds_dwordx4 %1, off\n\ts_mov_b32 m0, %0"
                     : "=&s"(sv) : "v"(g), "s"(lds_base) : "memory");
}
__device__ __forceinline__ void dma16_sc1(const void* g, u32 lds_base) {
    u32 sv;
    asm volatile("s_mov_b32 %0, m0\n\ts_mov_b32 m0, %2\n\ts_nop 0\n\tglobal_load_lds_dwordx4 %1, off sc1\n\ts_mov_b32 m0, %0"
                 : "=&s"(sv) : "v"(g), "s"(lds_base) : "memory");
}
template <int ORD, typename F>
__device__ __forceinline__ void load_weights(F&& after, const u16* __restrict__ p0, const u16* __restrict__ p1, const bool wo1, char* smem,
                                             const int wave, const int lane, const int sa, const int ta, const int sb, const int tb,
                                             u32x32 (&W)[5], const bool mix = false) {
    const u32 lbase = (u32)(uintptr_t)(__attribute__((address_space(3))) char*)smem;
    const int strm = wave >> 2;
    const bool wo = wo1 && strm == 1;
    const u16* src = strm ? p1 : p0;
    const u32 sstep = wo ? 128u : 128u * HD_;
    u32 so[8];
#pragma unroll
    for (int i = 0; i < 8; ++i) {
        const int j = (wave & 3) * 8 + i, r = 4 * j + (lane >> 4);
        const int f = (lane >> 4) | (((j >> 1) & 1) << 2);
        const int ch = (lane & 15) ^ (f << 1);
        const int e = (r & ~31) | (((r >> 2) & 1) << 4) | (((r >> 3) & 3) << 2) | (r & 3);
        so[i] = wo ? (u32)(e * D_ + ch * 8) : (u32)(dmap(r) * HD_ + ch * 8);
    }
    auto issue = [&](int st) {
        const u32 dst = __builtin_amdgcn_readfirstlane(lbase + (u32)((st & 1) * 65536 + strm * 32768 + (wave & 3) * 8192));
#pragma unroll
        for (int i = 0; i < 8; ++i) dma16<((ORD >> 20) & 1) != 0>(src + so[i] + (size_t)((ORD >> (4 * st)) & 15) * sstep, dst + i * 1024);
    };
    const int g = lane >> 4, q = (lane >> 2) & 3, p = lane & 3;
    const int f = q | ((g & 1) << 2);
    const u32 ra = lbase + (u32)(sa * 32768 + (8 * g + q) * 256 + (((ta ^ f) << 1) | (p >> 1)) * 16 + (p & 1) * 8);
    const u32 rb = lbase + (u32)(sb * 32768 + (8 * g + q) * 256 + (((tb ^ f) << 1) | (p >> 1)) * 16 + (p & 1) * 8);
    issue(0);
    issue(1);
#pragma unroll
    for (int st = 0; st < 5; ++st) {
        if (st < 4) asm volatile("s_waitcnt vmcnt(8)" ::: "memory"); else asm volatile("s_waitcnt vmcnt(0)" ::: "memory");
        LBAR();
#pragma unroll
        for (int k = 0; k < 4; ++k) {
            const u32 o = (u32)((st & 1) * 65536 + k * 8192);
            u32x2 a0 = trd(ra + o), a1 = trd(ra + o + 1024), b0 = trd(rb + o), b1 = trd(rb + o + 1024);
            const int ia = (((ORD >> (4 * st)) & 15) * 4 + k) * 4, ib = 80 + ia;
            if (mix) {
                const int ic = 2 * ia;
                W[ic >> 5][ic & 31] = a0[0]; W[ic >> 5][(ic & 31) + 2] = a0[1]; W[ic >> 5][(ic & 31) + 4] = a1[0]; W[ic >> 5][(ic & 31) + 6] = a1[1];
                W[ic >> 5][(ic & 31) + 1] = b0[0]; W[ic >> 5][(ic & 31) + 3] = b0[1]; W[ic >> 5][(ic & 31) + 5] = b1[0]; W[ic >> 5][(ic & 31) + 7] = b1[1];
                continue;
            }
            W[ia >> 5][ia & 31] = a0[0]; W[ia >> 5][(ia & 31) + 1] = a0[1]; W[ia >> 5][(ia & 31) + 2] = a1[0]; W[ia >> 5][(ia & 31) + 3] = a1[1];
            W[ib >> 5][ib & 31] = b0[0]; W[ib >> 5][(ib & 31) + 1] = b0[1]; W[ib >> 5][(ib & 31) + 2] = b1[0]; W[ib >> 5][(ib & 31) + 3] = b1[1];
        }
        LBAR();
        after(st + 16);
        if (st + 2 < 5) issue(st + 2);
        after(st);
    }
}

typedef __attribute__((ext_vector_type(16))) unsigned int u32x16;
typedef __attribute__((ext_vector_type(16))) float f32x16;
constexpr bool TWO = 1;
constexpr u32 WORD = 0x54321076;
#define STP
#define STA
__device__ __forceinline__ void s_p0(const u32x32 (&W)[5], u32 va, u32 lz, u32 sidx, u32 qw, u32 kw, u32 fv, u32 vm, u32x4 rs, u32 pfo, u32 pfd, u32 fo, u32 fd STP) {
    u32 dl;
    asm volatile(
        ".p2align 6\n\t"
        "v_mov_b32 v206, %[va]\n\t"
        "s_mov_b32 %[dl], 81920\n\t"
        "v_mov_b64 v[248:249], 0\n\t"
        "v_mov_b64 v[250:251], 0\n\t"
        "v_mov_b64 v[252:253], 0\n\t"
        "v_mov_b64 v[254:255], 0\n\t"
        "v_mov_b32 v205, %[fv]\n\t"
        "s_mov_b32 s92, m0\n\t"
        "s_mov_b32 m0, %[pfd]\n\t"
        "s_add_u32 s93, %[pfo], 0x1000\n\t"
        "buffer_load_dwordx4 %[vm], %[rs], %[pfo] offen offset:0 sc1 lds\n\t"
        "buffer_load_dwordx4 %[vm], %[rs], %[pfo] offen offset:1024 sc1 lds\n\t"
        "buffer_load_dwordx4 %[vm], %[rs], %[pfo] offen offset:2048 sc1 lds\n\t"
        "buffer_load_dwordx4 %[vm], %[rs], %[pfo] offen offset:3072 sc1 lds\n\t"
        "s_add_u32 m0, m0, 0x1000\n\t"
        "s_nop 0\n\t"
        "buffer_load_dwordx4 %[vm], %[rs], s93 offen offset:0 sc1 lds\n\t"
        "buffer_load_dwordx4 %[vm], %[rs], s93 offen offset:1024 sc1 lds\n\t"
        "buffer_load_dwordx4 %[vm], %[rs], s93 offen offset:2048 sc1 lds\n\t"
        "buffer_load_dwordx4 %[vm], %[rs], s93 offen offset:3072 sc1 lds\n\t"
        "s_mov_b32 m0, s92\n\t"
        "s_setprio 3\n\t"
        "Lb1%=:\n\t"
        "s_barrier\n\t"
        "ds_read_b128 v[208:211], v206 offset:0\n\t"
        "ds_read_b128 v[212:215], v206 offset:1024\n\t"
        "ds_read_b128 v[216:219], v206 offset:2048\n\t"
        "ds_read_b128 v[220:223], v206 offset:3072\n\t"
        "ds_read_b128 v[224:227], v206 offset:4096\n\t"
        "ds_read_b128 v[228:231], v206 offset:5120\n\t"
        "s_waitcnt lgkmcnt(5)\n\t"
        "v_smfmac_f32_16x16x64_bf16 v[248:251], v[208:211], v[16:23], %[sidx]\n\t"
        "ds_read_b128 v[232:235], v206 offset:6144\n\t"
        "s_waitcnt lgkmcnt(5)\n\t"
        "v_smfmac_f32_16x16x64_bf16 v[248:251], v[212:215], v[24:31], %[sidx]\n\t"
        "ds_read_b128 v[236:239], v206 offset:7168\n\t"
        "s_waitcnt lgkmcnt(5)\n\t"
        "v_smfmac_f32_16x16x64_bf16 v[248:251], v[216:219], v[32:39], %[sidx]\n\t"
        "ds_read_b128 v[240:243], v206 offset:8192\n\t"
        "s_waitcnt lgkmcnt(5)\n\t"
        "v_smfmac_f32_16x16x64_bf16 v[248:251], v[220:223], v[40:47], %[sidx]\n\t"
        "ds_read_b128 v[244:247], v206 offset:10240\n\t"
        "s_waitcnt lgkmcnt(5)\n\t"
        "v_smfmac_f32_16x16x64_bf16 v[248:251], v[224:227], v[48:55], %[sidx]\n\t"
        "s_waitcnt lgkmcnt(4)\n\t"
        "v_smfmac_f32_16x16x64_bf16 v[248:251], v[228:231], v[56:63], %[sidx]\n\t"
        "s_waitcnt lgkmcnt(3)\n\t"
        "v_smfmac_f32_16x16x64_bf16 v[248:251], v[232:235], v[64:71], %[sidx]\n\t"
        "s_waitcnt lgkmcnt(2)\n\t"
        "v_smfmac_f32_16x16x64_bf16 v[248:251], v[236:239], v[72:79], %[sidx]\n\t"
        "s_waitcnt lgkmcnt(1)\n\t"
        "v_smfmac_f32_16x16x64_bf16 v[248:251], v[240:243], v[80:87], %[sidx]\n\t"
        "s_waitcnt lgkmcnt(0)\n\t"
        "v_smfmac_f32_16x16x64_bf16 v[248:251], v[244:247], v[88:95], %[sidx]\n\t"
        "s_setprio 0\n\t"
        "s_nop 6\n\t"
        "v_cvt_pk_bf16_f32 v200, v248, v249\n\t"
        "v_cvt_pk_bf16_f32 v201, v250, v251\n\t"
        "ds_write2_b32 %[qw], v200, v201 offset1:96\n\t"
        "v_fma_f32 v202, v248, v249, v250\n\t"
        "v_cmp_u_f32 vcc, v202, v251\n\t"
        "s_cbranch_vccnz Lnan%=\n\t"
        "Lok%=:\n\t"
        "s_mov_b32 m0, %[fd]\n\t"
        "s_nop 0\n\t"
        "buffer_load_dword %[fo], %[rs], 0 offen sc1 lds\n\t"
        "s_mov_b32 m0, s92\n\t"
        "s_waitcnt lgkmcnt(0)\n\t"
        "s_barrier\n\t"
        "v_smfmac_f32_16x16x64_bf16 v[252:255], v[208:211], v[96:103], %[sidx]\n\t"
        "v_smfmac_f32_16x16x64_bf16 v[252:255], v[212:215], v[104:111], %[sidx]\n\t"
        "v_smfmac_f32_16x16x64_bf16 v[252:255], v[216:219], v[112:119], %[sidx]\n\t"
        "v_smfmac_f32_16x16x64_bf16 v[252:255], v[220:223], v[120:127], %[sidx]\n\t"
        "v_smfmac_f32_16x16x64_bf16 v[252:255], v[224:227], v[128:135], %[sidx]\n\t"
        "v_smfmac_f32_16x16x64_bf16 v[252:255], v[228:231], v[136:143], %[sidx]\n\t"
        "v_smfmac_f32_16x16x64_bf16 v[252:255], v[232:235], v[144:151], %[sidx]\n\t"
        "v_smfmac_f32_16x16x64_bf16 v[252:255], v[236:239], v[152:159], %[sidx]\n\t"
        "v_smfmac_f32_16x16x64_bf16 v[252:255], v[240:243], v[160:167], %[sidx]\n\t"
        "v_smfmac_f32_16x16x64_bf16 v[252:255], v[244:247], v[168:175], %[sidx]\n\t"
        "s_waitcnt vmcnt(1)\n\t"
        "s_nop 6\n\t"
        "v_add_f32 v252, v252, v254\n\t"
        "v_add_f32 v253, v253, v255\n\t"
        "v_cvt_pk_bf16_f32 v204, v252, v253\n\t"
        "ds_write_b64 %[kw], v[204:205]\n\t"
        "s_barrier\n\t"
        "s_branch Lend%=\n\t"
        "Lnan%=:\n\t"
        "v_mbcnt_lo_u32_b32 v202, -1, 0\n\t"
        "v_mbcnt_hi_u32_b32 v202, -1, v202\n\t"
        "v_lshl_add_u32 v207, v202, 7, %[lz]\n\t"
        "v_and_b32 v202, 31, v202\n\t"
        "v_lshl_add_u32 v207, v202, 2, v207\n\t"
        "ds_read2st64_b32 v[202:203], v207 offset1:32\n\t"
        "s_waitcnt lgkmcnt(0)\n\t"
        "s_mov_b64 exec, 0x00ff00ff\n\t"
        "v_max_u32 v202, v202, v203\n\t"
        "s_mov_b64 exec, -1\n\t"
        "v_cmp_eq_u32 vcc, 0xffbfffbf, v202\n\t"
        "s_cbranch_vccnz Lredo%=\n\t"
        "v_max_u32 v207, v208, v209\n\t"
        "v_max3_u32 v207, v207, v210, v211\n\t"
        "v_max3_u32 v207, v207, v212, v213\n\t"
        "v_max3_u32 v207, v207, v214, v215\n\t"
        "v_max3_u32 v207, v207, v216, v217\n\t"
        "v_max3_u32 v207, v207, v218, v219\n\t"
        "v_max3_u32 v207, v207, v220, v221\n\t"
        "v_max3_u32 v207, v207, v222, v223\n\t"
        "v_max3_u32 v207, v207, v224, v225\n\t"
        "v_max3_u32 v207, v207, v226, v227\n\t"
        "v_max3_u32 v207, v207, v228, v229\n\t"
        "v_max3_u32 v207, v207, v230, v231\n\t"
        "v_max3_u32 v207, v207, v232, v233\n\t"
        "v_max3_u32 v207, v207, v234, v235\n\t"
        "v_max3_u32 v207, v207, v236, v237\n\t"
        "v_max3_u32 v207, v207, v238, v239\n\t"
        "v_max3_u32 v207, v207, v240, v241\n\t"
        "v_max3_u32 v207, v207, v242, v243\n\t"
        "v_max3_u32 v207, v207, v244, v245\n\t"
        "v_max3_u32 v207, v207, v246, v247\n\t"
        "v_cmp_eq_u32 vcc, 0xffbfffbf, v207\n\t"
        "s_cbranch_vccz Lok%=\n\t"
        "Lredo%=:\n\t"
        "s_waitcnt lgkmcnt(0)\n\t"
        "v_add_u32 v206, %[dl], v206\n\t"
        "v_mov_b64 v[248:249], 0\n\t"
        "v_mov_b64 v[250:251], 0\n\t"
        "v_mov_b64 v[252:253], 0\n\t"
        "v_mov_b64 v[254:255], 0\n\t"
        "s_add_u32 %[lz], %[lz], %[dl]\n\t"
        "s_sub_u32 %[dl], 0, %[dl]\n\t"
        "s_setprio 3\n\t"
        "s_barrier\n\t"
        "s_branch Lb1%=\n\t"
        "Lend%=:\n\t"
        : [lz] "+&s"(lz), [dl] "=&s"(dl)
        : [w0] "{v[16:47]}"(W[0]), [w1] "{v[48:79]}"(W[1]), [w2] "{v[80:111]}"(W[2]), [w3] "{v[112:143]}"(W[3]), [w4] "{v[144:175]}"(W[4]), [va] "v"(va), [sidx] "v"(sidx), [qw] "v"(qw), [kw] "v"(kw), [fv] "s"(fv), [vm] "v"(vm), [rs] "s"(rs), [pfo] "s"(pfo), [pfd] "s"(pfd), [fo] "v"(fo), [fd] "s"(fd)
        : "s92", "s93", "v200", "v201", "v202", "v203", "v204", "v205", "v206", "v207", "v208", "v209", "v210", "v211", "v212", "v213", "v214", "v215", "v216", "v217", "v218", "v219", "v220", "v221", "v222", "v223", "v224", "v225", "v226", "v227", "v228", "v229", "v230", "v231", "v232", "v233", "v234", "v235", "v236", "v237", "v238", "v239", "v240", "v241", "v242", "v243", "v244", "v245", "v246", "v247", "v248", "v249", "v250", "v251", "v252", "v253", "v254", "v255", "vcc", "scc", "memory");
}

__device__ __forceinline__ void s_p1(const u32x32 (&W)[5], u32 va, u32 lz, u32 sidx, u32 qw, u32 kw, u32 fv, u32x4 p0, u32 p1, u32 o0, u32 o1, u32x4 rs STP) {
    u32 dl;
    asm volatile(
        ".p2align 6\n\t"
        "v_mov_b32 v206, %[va]\n\t"
        "s_mov_b32 %[dl], 81920\n\t"
        "v_mov_b64 v[248:249], 0\n\t"
        "v_mov_b64 v[250:251], 0\n\t"
        "v_mov_b64 v[252:253], 0\n\t"
        "v_mov_b64 v[254:255], 0\n\t"
        "v_mov_b32 v205, %[fv]\n\t"
        "s_setprio 3\n\t"
        "s_waitcnt vmcnt(0)\n\t"
        "Lb1%=:\n\t"
        "s_barrier\n\t"
        "ds_read_b128 v[208:211], v206 offset:0\n\t"
        "ds_read_b128 v[212:215], v206 offset:1024\n\t"
        "ds_read_b128 v[216:219], v206 offset:2048\n\t"
        "ds_read_b128 v[220:223], v206 offset:3072\n\t"
        "ds_read_b128 v[224:227], v206 offset:4096\n\t"
        "ds_read_b128 v[228:231], v206 offset:5120\n\t"
        "s_waitcnt lgkmcnt(5)\n\t"
        "v_smfmac_f32_16x16x64_bf16 v[248:251], v[208:211], v[16:23], %[sidx]\n\t"
        "ds_read_b128 v[232:235], v206 offset:6144\n\t"
        "s_waitcnt lgkmcnt(5)\n\t"
        "v_smfmac_f32_16x16x64_bf16 v[248:251], v[212:215], v[24:31], %[sidx]\n\t"
        "ds_read_b128 v[236:239], v206 offset:7168\n\t"
        "s_waitcnt lgkmcnt(5)\n\t"
        "v_smfmac_f32_16x16x64_bf16 v[248:251], v[216:219], v[32:39], %[sidx]\n\t"
        "ds_read_b128 v[240:243], v206 offset:8192\n\t"
        "s_waitcnt lgkmcnt(5)\n\t"
        "v_smfmac_f32_16x16x64_bf16 v[248:251], v[220:223], v[40:47], %[sidx]\n\t"
        "ds_read_b128 v[244:247], v206 offset:10240\n\t"
        "s_waitcnt lgkmcnt(5)\n\t"
        "v_smfmac_f32_16x16x64_bf16 v[248:251], v[224:227], v[48:55], %[sidx]\n\t"
        "s_waitcnt lgkmcnt(4)\n\t"
        "v_smfmac_f32_16x16x64_bf16 v[248:251], v[228:231], v[56:63], %[sidx]\n\t"
        "s_waitcnt lgkmcnt(3)\n\t"
        "v_smfmac_f32_16x16x64_bf16 v[248:251], v[232:235], v[64:71], %[sidx]\n\t"
        "s_waitcnt lgkmcnt(2)\n\t"
        "v_smfmac_f32_16x16x64_bf16 v[248:251], v[236:239], v[72:79], %[sidx]\n\t"
        "s_waitcnt lgkmcnt(1)\n\t"
        "v_smfmac_f32_16x16x64_bf16 v[248:251], v[240:243], v[80:87], %[sidx]\n\t"
        "s_waitcnt lgkmcnt(0)\n\t"
        "v_smfmac_f32_16x16x64_bf16 v[248:251], v[244:247], v[88:95], %[sidx]\n\t"
        "s_setprio 0\n\t"
        "s_nop 6\n\t"
        "v_cvt_pk_bf16_f32 v200, v248, v249\n\t"
        "v_cvt_pk_bf16_f32 v201, v250, v251\n\t"
        "ds_write2_b32 %[qw], v200, v201 offset1:96\n\t"
        "v_fma_f32 v202, v248, v249, v250\n\t"
        "v_cmp_u_f32 vcc, v202, v251\n\t"
        "s_cbranch_vccnz Lnan%=\n\t"
        "Lok%=:\n\t"
        "buffer_store_dwordx4 v[192:195], %[o0], %[rs], 0 offen sc1\n\t"
        "buffer_store_dword v196, %[o1], %[rs], 0 offen sc1\n\t"
        "s_waitcnt lgkmcnt(0)\n\t"
        "s_barrier\n\t"
        "s_nop 15\n\t"
        "s_nop 15\n\t"
        "s_nop 15\n\t"
        "s_nop 3\n\t"
        "v_smfmac_f32_16x16x64_bf16 v[252:255], v[208:211], v[96:103], %[sidx]\n\t"
        "v_smfmac_f32_16x16x64_bf16 v[252:255], v[212:215], v[104:111], %[sidx]\n\t"
        "v_smfmac_f32_16x16x64_bf16 v[252:255], v[216:219], v[112:119], %[sidx]\n\t"
        "v_smfmac_f32_16x16x64_bf16 v[252:255], v[220:223], v[120:127], %[sidx]\n\t"
        "v_smfmac_f32_16x16x64_bf16 v[252:255], v[224:227], v[128:135], %[sidx]\n\t"
        "v_smfmac_f32_16x16x64_bf16 v[252:255], v[228:231], v[136:143], %[sidx]\n\t"
        "v_smfmac_f32_16x16x64_bf16 v[252:255], v[232:235], v[144:151], %[sidx]\n\t"
        "v_smfmac_f32_16x16x64_bf16 v[252:255], v[236:239], v[152:159], %[sidx]\n\t"
        "v_smfmac_f32_16x16x64_bf16 v[252:255], v[240:243], v[160:167], %[sidx]\n\t"
        "v_smfmac_f32_16x16x64_bf16 v[252:255], v[244:247], v[168:175], %[sidx]\n\t"
        "s_nop 7\n\t"
        "v_add_f32 v252, v252, v254\n\t"
        "v_add_f32 v253, v253, v255\n\t"
        "v_cvt_pk_bf16_f32 v204, v252, v253\n\t"
        "ds_write_b64 %[kw], v[204:205]\n\t"
        "s_barrier\n\t"
        "s_branch Lend%=\n\t"
        "Lnan%=:\n\t"
        "v_mbcnt_lo_u32_b32 v202, -1, 0\n\t"
        "v_mbcnt_hi_u32_b32 v202, -1, v202\n\t"
        "v_lshl_add_u32 v207, v202, 7, %[lz]\n\t"
        "v_and_b32 v202, 31, v202\n\t"
        "v_lshl_add_u32 v207, v202, 2, v207\n\t"
        "ds_read2st64_b32 v[202:203], v207 offset1:32\n\t"
        "s_waitcnt lgkmcnt(0)\n\t"
        "s_mov_b64 exec, 0x00ff00ff\n\t"
        "v_max_u32 v202, v202, v203\n\t"
        "s_mov_b64 exec, -1\n\t"
        "v_cmp_eq_u32 vcc, 0xffbfffbf, v202\n\t"
        "s_cbranch_vccnz Lredo%=\n\t"
        "v_max_u32 v207, v208, v209\n\t"
        "v_max3_u32 v207, v207, v210, v211\n\t"
        "v_max3_u32 v207, v207, v212, v213\n\t"
        "v_max3_u32 v207, v207, v214, v215\n\t"
        "v_max3_u32 v207, v207, v216, v217\n\t"
        "v_max3_u32 v207, v207, v218, v219\n\t"
        "v_max3_u32 v207, v207, v220, v221\n\t"
        "v_max3_u32 v207, v207, v222, v223\n\t"
        "v_max3_u32 v207, v207, v224, v225\n\t"
        "v_max3_u32 v207, v207, v226, v227\n\t"
        "v_max3_u32 v207, v207, v228, v229\n\t"
        "v_max3_u32 v207, v207, v230, v231\n\t"
        "v_max3_u32 v207, v207, v232, v233\n\t"
        "v_max3_u32 v207, v207, v234, v235\n\t"
        "v_max3_u32 v207, v207, v236, v237\n\t"
        "v_max3_u32 v207, v207, v238, v239\n\t"
        "v_max3_u32 v207, v207, v240, v241\n\t"
        "v_max3_u32 v207, v207, v242, v243\n\t"
        "v_max3_u32 v207, v207, v244, v245\n\t"
        "v_max3_u32 v207, v207, v246, v247\n\t"
        "v_cmp_eq_u32 vcc, 0xffbfffbf, v207\n\t"
        "s_cbranch_vccz Lok%=\n\t"
        "Lredo%=:\n\t"
        "s_waitcnt lgkmcnt(0)\n\t"
        "v_add_u32 v206, %[dl], v206\n\t"
        "v_mov_b64 v[248:249], 0\n\t"
        "v_mov_b64 v[250:251], 0\n\t"
        "v_mov_b64 v[252:253], 0\n\t"
        "v_mov_b64 v[254:255], 0\n\t"
        "s_add_u32 %[lz], %[lz], %[dl]\n\t"
        "s_sub_u32 %[dl], 0, %[dl]\n\t"
        "s_setprio 3\n\t"
        "s_barrier\n\t"
        "s_branch Lb1%=\n\t"
        "Lend%=:\n\t"
        : [lz] "+&s"(lz), [dl] "=&s"(dl)
        : [w0] "{v[16:47]}"(W[0]), [w1] "{v[48:79]}"(W[1]), [w2] "{v[80:111]}"(W[2]), [w3] "{v[112:143]}"(W[3]), [w4] "{v[144:175]}"(W[4]), [va] "v"(va), [sidx] "v"(sidx), [qw] "v"(qw), [kw] "v"(kw), [fv] "s"(fv), [p0] "{v[192:195]}"(p0), [p1] "{v196}"(p1), [o0] "v"(o0), [o1] "v"(o1), [rs] "s"(rs)
        : "v200", "v201", "v202", "v203", "v204", "v205", "v206", "v207", "v208", "v209", "v210", "v211", "v212", "v213", "v214", "v215", "v216", "v217", "v218", "v219", "v220", "v221", "v222", "v223", "v224", "v225", "v226", "v227", "v228", "v229", "v230", "v231", "v232", "v233", "v234", "v235", "v236", "v237", "v238", "v239", "v240", "v241", "v242", "v243", "v244", "v245", "v246", "v247", "v248", "v249", "v250", "v251", "v252", "v253", "v254", "v255", "vcc", "scc", "memory");
}

__device__ __forceinline__ void s_p2(const u32x32 (&W)[5], u32 va, u32 lz, u32 sidx, u32 qw, u32 kw, u32 fv, u32 vm, u32x4 rs, u32 pfo, u32 pfd STP) {
    u32 dl;
    asm volatile(
        ".p2align 6\n\t"
        "v_mov_b32 v206, %[va]\n\t"
        "s_mov_b32 %[dl], 81920\n\t"
        "v_mov_b64 v[248:249], 0\n\t"
        "v_mov_b64 v[250:251], 0\n\t"
        "v_mov_b64 v[252:253], 0\n\t"
        "v_mov_b64 v[254:255], 0\n\t"
        "v_mov_b32 v205, %[fv]\n\t"
        "s_setprio 3\n\t"
        "s_waitcnt vmcnt(0)\n\t"
        "Lb1%=:\n\t"
        "s_barrier\n\t"
        "ds_read_b128 v[208:211], v206 offset:0\n\t"
        "ds_read_b128 v[212:215], v206 offset:1024\n\t"
        "ds_read_b128 v[216:219], v206 offset:2048\n\t"
        "ds_read_b128 v[220:223], v206 offset:3072\n\t"
        "ds_read_b128 v[224:227], v206 offset:4096\n\t"
        "ds_read_b128 v[228:231], v206 offset:5120\n\t"
        "s_waitcnt lgkmcnt(5)\n\t"
        "v_smfmac_f32_16x16x64_bf16 v[248:251], v[208:211], v[16:23], %[sidx]\n\t"
        "ds_read_b128 v[232:235], v206 offset:6144\n\t"
        "s_waitcnt lgkmcnt(5)\n\t"
        "v_smfmac_f32_16x16x64_bf16 v[248:251], v[212:215], v[24:31], %[sidx]\n\t"
        "ds_read_b128 v[236:239], v206 offset:7168\n\t"
        "s_waitcnt lgkmcnt(5)\n\t"
        "v_smfmac_f32_16x16x64_bf16 v[248:251], v[216:219], v[32:39], %[sidx]\n\t"
        "ds_read_b128 v[240:243], v206 offset:8192\n\t"
        "s_waitcnt lgkmcnt(5)\n\t"
        "v_smfmac_f32_16x16x64_bf16 v[248:251], v[220:223], v[40:47], %[sidx]\n\t"
        "ds_read_b128 v[244:247], v206 offset:10240\n\t"
        "s_waitcnt lgkmcnt(5)\n\t"
        "v_smfmac_f32_16x16x64_bf16 v[248:251], v[224:227], v[48:55], %[sidx]\n\t"
        "s_waitcnt lgkmcnt(4)\n\t"
        "v_smfmac_f32_16x16x64_bf16 v[248:251], v[228:231], v[56:63], %[sidx]\n\t"
        "s_waitcnt lgkmcnt(3)\n\t"
        "v_smfmac_f32_16x16x64_bf16 v[248:251], v[232:235], v[64:71], %[sidx]\n\t"
        "s_waitcnt lgkmcnt(2)\n\t"
        "v_smfmac_f32_16x16x64_bf16 v[248:251], v[236:239], v[72:79], %[sidx]\n\t"
        "s_waitcnt lgkmcnt(1)\n\t"
        "v_smfmac_f32_16x16x64_bf16 v[248:251], v[240:243], v[80:87], %[sidx]\n\t"
        "s_waitcnt lgkmcnt(0)\n\t"
        "v_smfmac_f32_16x16x64_bf16 v[248:251], v[244:247], v[88:95], %[sidx]\n\t"
        "s_setprio 0\n\t"
        "s_nop 6\n\t"
        "v_cvt_pk_bf16_f32 v200, v248, v249\n\t"
        "v_cvt_pk_bf16_f32 v201, v250, v251\n\t"
        "ds_write2_b32 %[qw], v200, v201 offset1:96\n\t"
        "v_fma_f32 v202, v248, v249, v250\n\t"
        "v_cmp_u_f32 vcc, v202, v251\n\t"
        "s_cbranch_vccnz Lnan%=\n\t"
        "Lok%=:\n\t"
        "s_mov_b32 s92, m0\n\t"
        "s_mov_b32 m0, %[pfd]\n\t"
        "s_add_u32 s93, %[pfo], 0x1000\n\t"
        "buffer_load_dwordx4 %[vm], %[rs], %[pfo] offen offset:0 sc1 lds\n\t"
        "buffer_load_dwordx4 %[vm], %[rs], %[pfo] offen offset:1024 sc1 lds\n\t"
        "buffer_load_dwordx4 %[vm], %[rs], %[pfo] offen offset:2048 sc1 lds\n\t"
        "buffer_load_dwordx4 %[vm], %[rs], %[pfo] offen offset:3072 sc1 lds\n\t"
        "s_add_u32 m0, m0, 0x1000\n\t"
        "s_nop 0\n\t"
        "buffer_load_dwordx4 %[vm], %[rs], s93 offen offset:0 sc1 lds\n\t"
        "buffer_load_dwordx4 %[vm], %[rs], s93 offen offset:1024 sc1 lds\n\t"
        "buffer_load_dwordx4 %[vm], %[rs], s93 offen offset:2048 sc1 lds\n\t"
        "buffer_load_dwordx4 %[vm], %[rs], s93 offen offset:3072 sc1 lds\n\t"
        "s_mov_b32 m0, s92\n\t"
        "s_waitcnt lgkmcnt(0)\n\t"
        "s_barrier\n\t"
        "v_smfmac_f32_16x16x64_bf16 v[252:255], v[208:211], v[96:103], %[sidx]\n\t"
        "v_smfmac_f32_16x16x64_bf16 v[252:255], v[212:215], v[104:111], %[sidx]\n\t"
        "v_smfmac_f32_16x16x64_bf16 v[252:255], v[216:219], v[112:119], %[sidx]\n\t"
        "v_smfmac_f32_16x16x64_bf16 v[252:255], v[220:223], v[120:127], %[sidx]\n\t"
        "v_smfmac_f32_16x16x64_bf16 v[252:255], v[224:227], v[128:135], %[sidx]\n\t"
        "v_smfmac_f32_16x16x64_bf16 v[252:255], v[228:231], v[136:143], %[sidx]\n\t"
        "v_smfmac_f32_16x16x64_bf16 v[252:255], v[232:235], v[144:151], %[sidx]\n\t"
        "v_smfmac_f32_16x16x64_bf16 v[252:255], v[236:239], v[152:159], %[sidx]\n\t"
        "v_smfmac_f32_16x16x64_bf16 v[252:255], v[240:243], v[160:167], %[sidx]\n\t"
        "v_smfmac_f32_16x16x64_bf16 v[252:255], v[244:247], v[168:175], %[sidx]\n\t"
        "s_nop 7\n\t"
        "v_add_f32 v252, v252, v254\n\t"
        "v_add_f32 v253, v253, v255\n\t"
        "v_cvt_pk_bf16_f32 v204, v252, v253\n\t"
        "ds_write_b64 %[kw], v[204:205]\n\t"
        "s_barrier\n\t"
        "s_branch Lend%=\n\t"
        "Lnan%=:\n\t"
        "v_mbcnt_lo_u32_b32 v202, -1, 0\n\t"
        "v_mbcnt_hi_u32_b32 v202, -1, v202\n\t"
        "v_lshl_add_u32 v207, v202, 7, %[lz]\n\t"
        "v_and_b32 v202, 31, v202\n\t"
        "v_lshl_add_u32 v207, v202, 2, v207\n\t"
        "ds_read2st64_b32 v[202:203], v207 offset1:32\n\t"
        "s_waitcnt lgkmcnt(0)\n\t"
        "s_mov_b64 exec, 0x00ff00ff\n\t"
        "v_max_u32 v202, v202, v203\n\t"
        "s_mov_b64 exec, -1\n\t"
        "v_cmp_eq_u32 vcc, 0xffbfffbf, v202\n\t"
        "s_cbranch_vccnz Lredo%=\n\t"
        "v_max_u32 v207, v208, v209\n\t"
        "v_max3_u32 v207, v207, v210, v211\n\t"
        "v_max3_u32 v207, v207, v212, v213\n\t"
        "v_max3_u32 v207, v207, v214, v215\n\t"
        "v_max3_u32 v207, v207, v216, v217\n\t"
        "v_max3_u32 v207, v207, v218, v219\n\t"
        "v_max3_u32 v207, v207, v220, v221\n\t"
        "v_max3_u32 v207, v207, v222, v223\n\t"
        "v_max3_u32 v207, v207, v224, v225\n\t"
        "v_max3_u32 v207, v207, v226, v227\n\t"
        "v_max3_u32 v207, v207, v228, v229\n\t"
        "v_max3_u32 v207, v207, v230, v231\n\t"
        "v_max3_u32 v207, v207, v232, v233\n\t"
        "v_max3_u32 v207, v207, v234, v235\n\t"
        "v_max3_u32 v207, v207, v236, v237\n\t"
        "v_max3_u32 v207, v207, v238, v239\n\t"
        "v_max3_u32 v207, v207, v240, v241\n\t"
        "v_max3_u32 v207, v207, v242, v243\n\t"
        "v_max3_u32 v207, v207, v244, v245\n\t"
        "v_max3_u32 v207, v207, v246, v247\n\t"
        "v_cmp_eq_u32 vcc, 0xffbfffbf, v207\n\t"
        "s_cbranch_vccz Lok%=\n\t"
        "Lredo%=:\n\t"
        "s_waitcnt lgkmcnt(0)\n\t"
        "v_add_u32 v206, %[dl], v206\n\t"
        "v_mov_b64 v[248:249], 0\n\t"
        "v_mov_b64 v[250:251], 0\n\t"
        "v_mov_b64 v[252:253], 0\n\t"
        "v_mov_b64 v[254:255], 0\n\t"
        "s_add_u32 %[lz], %[lz], %[dl]\n\t"
        "s_sub_u32 %[dl], 0, %[dl]\n\t"
        "s_setprio 3\n\t"
        "s_barrier\n\t"
        "s_branch Lb1%=\n\t"
        "Lend%=:\n\t"
        : [lz] "+&s"(lz), [dl] "=&s"(dl)
        : [w0] "{v[16:47]}"(W[0]), [w1] "{v[48:79]}"(W[1]), [w2] "{v[80:111]}"(W[2]), [w3] "{v[112:143]}"(W[3]), [w4] "{v[144:175]}"(W[4]), [va] "v"(va), [sidx] "v"(sidx), [qw] "v"(qw), [kw] "v"(kw), [fv] "s"(fv), [vm] "v"(vm), [rs] "s"(rs), [pfo] "s"(pfo), [pfd] "s"(pfd)
        : "s92", "s93", "v200", "v201", "v202", "v203", "v204", "v205", "v206", "v207", "v208", "v209", "v210", "v211", "v212", "v213", "v214", "v215", "v216", "v217", "v218", "v219", "v220", "v221", "v222", "v223", "v224", "v225", "v226", "v227", "v228", "v229", "v230", "v231", "v232", "v233", "v234", "v235", "v236", "v237", "v238", "v239", "v240", "v241", "v242", "v243", "v244", "v245", "v246", "v247", "v248", "v249", "v250", "v251", "v252", "v253", "v254", "v255", "vcc", "scc", "memory");
}

__device__ __forceinline__ void s_e(const u32x32 (&W)[5], u32 va, u32 lz, u32 sidx, u32 qw, u32 kw, u32 fv, u32 kxa, u32 qtr, u32 eo, u32 ew, float lim, u32x4 rs, u32 so STP) {
    u32 dl;
    asm volatile(
        ".p2align 6\n\t"
        "v_mov_b32 v206, %[va]\n\t"
        "s_mov_b32 %[dl], 81920\n\t"
        "v_mov_b64 v[248:249], 0\n\t"
        "v_mov_b64 v[250:251], 0\n\t"
        "v_mov_b64 v[252:253], 0\n\t"
        "v_mov_b64 v[254:255], 0\n\t"
        "v_mov_b32 v205, %[fv]\n\t"
        "ds_read_b128 v[176:179], %[kxa] offset:0\n\t"
        "ds_read_b128 v[180:183], %[kxa] offset:1024\n\t"
        "ds_read_b128 v[184:187], %[kxa] offset:2048\n\t"
        "ds_read_b128 v[188:191], %[kxa] offset:3072\n\t"
        "s_add_u32 s92, %[so], 0x1000\n\t"
        "s_add_u32 s93, %[so], 0x4000\n\t"
        "s_add_u32 s94, %[so], 0x5000\n\t"
        "s_setprio 2\n\t"
        "s_waitcnt lgkmcnt(0)\n\t"
        "Lb1%=:\n\t"
        "s_barrier\n\t"
        "s_nop 11\n\t"
        "ds_read_b128 v[208:211], v206 offset:0\n\t"
        "ds_read_b128 v[212:215], v206 offset:1024\n\t"
        "ds_read_b128 v[216:219], v206 offset:2048\n\t"
        "ds_read_b128 v[220:223], v206 offset:3072\n\t"
        "ds_read_b128 v[224:227], v206 offset:4096\n\t"
        "ds_read_b128 v[228:231], v206 offset:5120\n\t"
        "s_waitcnt lgkmcnt(5)\n\t"
        "v_smfmac_f32_16x16x64_bf16 v[248:251], v[208:211], v[16:23], %[sidx]\n\t"
        "ds_read_b128 v[232:235], v206 offset:6144\n\t"
        "s_waitcnt lgkmcnt(5)\n\t"
        "v_smfmac_f32_16x16x64_bf16 v[248:251], v[212:215], v[24:31], %[sidx]\n\t"
        "ds_read_b128 v[236:239], v206 offset:7168\n\t"
        "s_waitcnt lgkmcnt(5)\n\t"
        "v_smfmac_f32_16x16x64_bf16 v[248:251], v[216:219], v[32:39], %[sidx]\n\t"
        "ds_read_b128 v[240:243], v206 offset:8192\n\t"
        "s_waitcnt lgkmcnt(5)\n\t"
        "v_smfmac_f32_16x16x64_bf16 v[248:251], v[220:223], v[40:47], %[sidx]\n\t"
        "ds_read_b128 v[244:247], v206 offset:10240\n\t"
        "s_waitcnt lgkmcnt(5)\n\t"
        "v_smfmac_f32_16x16x64_bf16 v[248:251], v[224:227], v[48:55], %[sidx]\n\t"
        "s_waitcnt lgkmcnt(4)\n\t"
        "v_smfmac_f32_16x16x64_bf16 v[248:251], v[228:231], v[56:63], %[sidx]\n\t"
        "s_waitcnt lgkmcnt(3)\n\t"
        "v_smfmac_f32_16x16x64_bf16 v[248:251], v[232:235], v[64:71], %[sidx]\n\t"
        "s_waitcnt lgkmcnt(2)\n\t"
        "v_smfmac_f32_16x16x64_bf16 v[248:251], v[236:239], v[72:79], %[sidx]\n\t"
        "s_waitcnt lgkmcnt(1)\n\t"
        "v_smfmac_f32_16x16x64_bf16 v[248:251], v[240:243], v[80:87], %[sidx]\n\t"
        "s_waitcnt lgkmcnt(0)\n\t"
        "v_smfmac_f32_16x16x64_bf16 v[248:251], v[244:247], v[88:95], %[sidx]\n\t"
        "v_smfmac_f32_16x16x64_bf16 v[252:255], v[208:211], v[96:103], %[sidx]\n\t"
        "v_smfmac_f32_16x16x64_bf16 v[252:255], v[212:215], v[104:111], %[sidx]\n\t"
        "v_cvt_pk_bf16_f32 v200, v248, v249\n\t"
        "v_cvt_pk_bf16_f32 v201, v250, v251\n\t"
        "ds_write2_b32 %[qw], v200, v201 offset1:96\n\t"
        "v_fma_f32 v202, v248, v249, v250\n\t"
        "v_smfmac_f32_16x16x64_bf16 v[252:255], v[216:219], v[112:119], %[sidx]\n\t"
        "v_cmp_u_f32 vcc, v202, v251\n\t"
        "v_smfmac_f32_16x16x64_bf16 v[252:255], v[220:223], v[120:127], %[sidx]\n\t"
        "s_waitcnt lgkmcnt(0)\n\t"
        "s_cbranch_vccnz Lnan%=\n\t"
        "Lok%=:\n\t"
        "s_barrier\n\t"
        "ds_read_b64_tr_b16 v[192:193], %[qtr] offset:0\n\t"
        "ds_read_b64_tr_b16 v[194:195], %[qtr] offset:768\n\t"
        "v_smfmac_f32_16x16x64_bf16 v[252:255], v[224:227], v[128:135], %[sidx]\n\t"
        "ds_read_b64_tr_b16 v[196:197], %[qtr] offset:1536\n\t"
        "ds_read_b64_tr_b16 v[198:199], %[qtr] offset:2304\n\t"
        "v_smfmac_f32_16x16x64_bf16 v[252:255], v[228:231], v[136:143], %[sidx]\n\t"
        "ds_read_b64_tr_b16 v[208:209], %[qtr] offset:3072\n\t"
        "ds_read_b64_tr_b16 v[210:211], %[qtr] offset:3840\n\t"
        "v_smfmac_f32_16x16x64_bf16 v[252:255], v[232:235], v[144:151], %[sidx]\n\t"
        "ds_read_b64_tr_b16 v[212:213], %[qtr] offset:4608\n\t"
        "ds_read_b64_tr_b16 v[214:215], %[qtr] offset:5376\n\t"
        "v_smfmac_f32_16x16x64_bf16 v[252:255], v[236:239], v[152:159], %[sidx]\n\t"
        "s_waitcnt lgkmcnt(6)\n\t"
        "v_mfma_f32_16x16x32_bf16 v[248:251], v[176:179], v[192:195], 0\n\t"
        "s_waitcnt lgkmcnt(4)\n\t"
        "v_mfma_f32_16x16x32_bf16 v[248:251], v[180:183], v[196:199], v[248:251]\n\t"
        "s_waitcnt lgkmcnt(2)\n\t"
        "v_mfma_f32_16x16x32_bf16 v[248:251], v[184:187], v[208:211], v[248:251]\n\t"
        "s_waitcnt lgkmcnt(0)\n\t"
        "v_mfma_f32_16x16x32_bf16 v[248:251], v[188:191], v[212:215], v[248:251]\n\t"
        "v_smfmac_f32_16x16x64_bf16 v[252:255], v[240:243], v[160:167], %[sidx]\n\t"
        "v_smfmac_f32_16x16x64_bf16 v[252:255], v[244:247], v[168:175], %[sidx]\n\t"
        "v_add_f32_dpp v192, v248, v248 row_ror:8 row_mask:0xf bank_mask:0x3\n\t"
        "v_add_f32_dpp v193, v249, v249 row_ror:8 row_mask:0xf bank_mask:0x3\n\t"
        "v_add_f32_dpp v192, v250, v250 row_ror:8 row_mask:0xf bank_mask:0xc\n\t"
        "v_add_f32_dpp v193, v251, v251 row_ror:8 row_mask:0xf bank_mask:0xc\n\t"
        "v_exp_f32 v194, v192\n\t"
        "v_exp_f32 v195, v193\n\t"
        "v_max_f32_e64 v196, |v192|, |v193|\n\t"
        "v_cvt_pk_bf16_f32 v194, v194, v195\n\t"
        "v_cmpx_le_f32 vcc, v196, %[lim]\n\t"
        "buffer_store_dword v194, %[eo], %[rs], %[so] offen sc1\n\t"
        "buffer_store_dword v194, %[eo], %[rs], s92 offen sc1\n\t"
        "buffer_store_dword v194, %[eo], %[rs], s93 offen sc1\n\t"
        "buffer_store_dword v194, %[eo], %[rs], s94 offen sc1\n\t"
        "s_mov_b64 exec, -1\n\t"
        "v_cndmask_b32 v194, -1, v194, vcc\n\t"
        "ds_write_b32 %[ew], v194\n\t"
        "v_add_f32 v252, v252, v254\n\t"
        "v_add_f32 v253, v253, v255\n\t"
        "v_cvt_pk_bf16_f32 v204, v252, v253\n\t"
        "ds_write_b64 %[kw], v[204:205]\n\t"
        "s_barrier\n\t"
        "s_branch Lend%=\n\t"
        "Lnan%=:\n\t"
        "v_mbcnt_lo_u32_b32 v202, -1, 0\n\t"
        "v_mbcnt_hi_u32_b32 v202, -1, v202\n\t"
        "v_lshl_add_u32 v207, v202, 7, %[lz]\n\t"
        "v_and_b32 v202, 31, v202\n\t"
        "v_lshl_add_u32 v207, v202, 2, v207\n\t"
        "ds_read2st64_b32 v[202:203], v207 offset1:32\n\t"
        "s_waitcnt lgkmcnt(0)\n\t"
        "s_mov_b64 exec, 0x00ff00ff\n\t"
        "v_max_u32 v202, v202, v203\n\t"
        "s_mov_b64 exec, -1\n\t"
        "v_cmp_eq_u32 vcc, 0xffbfffbf, v202\n\t"
        "s_cbranch_vccnz Lredo%=\n\t"
        "v_max_u32 v207, v208, v209\n\t"
        "v_max3_u32 v207, v207, v210, v211\n\t"
        "v_max3_u32 v207, v207, v212, v213\n\t"
        "v_max3_u32 v207, v207, v214, v215\n\t"
        "v_max3_u32 v207, v207, v216, v217\n\t"
        "v_max3_u32 v207, v207, v218, v219\n\t"
        "v_max3_u32 v207, v207, v220, v221\n\t"
        "v_max3_u32 v207, v207, v222, v223\n\t"
        "v_max3_u32 v207, v207, v224, v225\n\t"
        "v_max3_u32 v207, v207, v226, v227\n\t"
        "v_max3_u32 v207, v207, v228, v229\n\t"
        "v_max3_u32 v207, v207, v230, v231\n\t"
        "v_max3_u32 v207, v207, v232, v233\n\t"
        "v_max3_u32 v207, v207, v234, v235\n\t"
        "v_max3_u32 v207, v207, v236, v237\n\t"
        "v_max3_u32 v207, v207, v238, v239\n\t"
        "v_max3_u32 v207, v207, v240, v241\n\t"
        "v_max3_u32 v207, v207, v242, v243\n\t"
        "v_max3_u32 v207, v207, v244, v245\n\t"
        "v_max3_u32 v207, v207, v246, v247\n\t"
        "v_cmp_eq_u32 vcc, 0xffbfffbf, v207\n\t"
        "s_cbranch_vccz Lok%=\n\t"
        "Lredo%=:\n\t"
        "s_waitcnt lgkmcnt(0)\n\t"
        "v_add_u32 v206, %[dl], v206\n\t"
        "v_mov_b64 v[248:249], 0\n\t"
        "v_mov_b64 v[250:251], 0\n\t"
        "v_mov_b64 v[252:253], 0\n\t"
        "v_mov_b64 v[254:255], 0\n\t"
        "s_add_u32 %[lz], %[lz], %[dl]\n\t"
        "s_sub_u32 %[dl], 0, %[dl]\n\t"
        "s_setprio 2\n\t"
        "s_barrier\n\t"
        "s_branch Lb1%=\n\t"
        "Lend%=:\n\t"
        : [lz] "+&s"(lz), [dl] "=&s"(dl)
        : [w0] "{v[16:47]}"(W[0]), [w1] "{v[48:79]}"(W[1]), [w2] "{v[80:111]}"(W[2]), [w3] "{v[112:143]}"(W[3]), [w4] "{v[144:175]}"(W[4]), [va] "v"(va), [sidx] "v"(sidx), [qw] "v"(qw), [kw] "v"(kw), [fv] "s"(fv), [kxa] "v"(kxa), [qtr] "v"(qtr), [eo] "v"(eo), [ew] "v"(ew), [lim] "v"(lim), [rs] "s"(rs), [so] "s"(so)
        : "s92", "s93", "s94", "v176", "v177", "v178", "v179", "v180", "v181", "v182", "v183", "v184", "v185", "v186", "v187", "v188", "v189", "v190", "v191", "v192", "v193", "v194", "v195", "v196", "v197", "v198", "v199", "v200", "v201", "v202", "v203", "v204", "v205", "v206", "v207", "v208", "v209", "v210", "v211", "v212", "v213", "v214", "v215", "v216", "v217", "v218", "v219", "v220", "v221", "v222", "v223", "v224", "v225", "v226", "v227", "v228", "v229", "v230", "v231", "v232", "v233", "v234", "v235", "v236", "v237", "v238", "v239", "v240", "v241", "v242", "v243", "v244", "v245", "v246", "v247", "v248", "v249", "v250", "v251", "v252", "v253", "v254", "v255", "vcc", "scc", "memory");
}

__device__ __forceinline__ void s_f(const u32x32 (&W)[5], u32 va, u32 lz, u32 sidx, u32 qw, u32 kw, u32 fv, u32& bad, u32x4 ones, u32 qtr, u32 er, u32 po, float c1, float ind0, float ind1, u32x4 rs, u32 so, float ce, float big, u64 jm STP) {
    u32 dl;
    asm volatile(
        ".p2align 6\n\t"
        "v_mov_b32 v206, %[va]\n\t"
        "s_mov_b32 %[dl], 81920\n\t"
        "v_mov_b64 v[248:249], 0\n\t"
        "v_mov_b64 v[250:251], 0\n\t"
        "v_mov_b64 v[252:253], 0\n\t"
        "v_mov_b64 v[254:255], 0\n\t"
        "v_mov_b32 v205, %[fv]\n\t"
        "s_add_u32 s92, %[so], 0x1000\n\t"
        "s_add_u32 s93, %[so], 0x4000\n\t"
        "s_add_u32 s94, %[so], 0x5000\n\t"
        "s_mov_b32 %[bad], 0\n\t"
        "s_setprio 2\n\t"
        "Lb1%=:\n\t"
        "s_barrier\n\t"
        "s_nop 11\n\t"
        "ds_read_b128 v[208:211], v206 offset:0\n\t"
        "ds_read_b128 v[212:215], v206 offset:1024\n\t"
        "ds_read_b128 v[216:219], v206 offset:2048\n\t"
        "ds_read_b128 v[220:223], v206 offset:3072\n\t"
        "ds_read_b128 v[224:227], v206 offset:4096\n\t"
        "ds_read_b128 v[228:231], v206 offset:5120\n\t"
        "s_waitcnt lgkmcnt(5)\n\t"
        "v_smfmac_f32_16x16x64_bf16 v[248:251], v[208:211], v[16:23], %[sidx]\n\t"
        "ds_read_b128 v[232:235], v206 offset:6144\n\t"
        "s_waitcnt lgkmcnt(5)\n\t"
        "v_smfmac_f32_16x16x64_bf16 v[248:251], v[212:215], v[24:31], %[sidx]\n\t"
        "ds_read_b128 v[236:239], v206 offset:7168\n\t"
        "s_waitcnt lgkmcnt(5)\n\t"
        "v_smfmac_f32_16x16x64_bf16 v[248:251], v[216:219], v[32:39], %[sidx]\n\t"
        "ds_read_b128 v[240:243], v206 offset:8192\n\t"
        "s_waitcnt lgkmcnt(5)\n\t"
        "v_smfmac_f32_16x16x64_bf16 v[248:251], v[220:223], v[40:47], %[sidx]\n\t"
        "ds_read_b128 v[244:247], v206 offset:10240\n\t"
        "s_waitcnt lgkmcnt(5)\n\t"
        "v_smfmac_f32_16x16x64_bf16 v[248:251], v[224:227], v[48:55], %[sidx]\n\t"
        "s_waitcnt lgkmcnt(4)\n\t"
        "v_smfmac_f32_16x16x64_bf16 v[248:251], v[228:231], v[56:63], %[sidx]\n\t"
        "s_waitcnt lgkmcnt(3)\n\t"
        "v_smfmac_f32_16x16x64_bf16 v[248:251], v[232:235], v[64:71], %[sidx]\n\t"
        "s_waitcnt lgkmcnt(2)\n\t"
        "v_smfmac_f32_16x16x64_bf16 v[248:251], v[236:239], v[72:79], %[sidx]\n\t"
        "s_waitcnt lgkmcnt(1)\n\t"
        "v_smfmac_f32_16x16x64_bf16 v[248:251], v[240:243], v[80:87], %[sidx]\n\t"
        "s_waitcnt lgkmcnt(0)\n\t"
        "v_smfmac_f32_16x16x64_bf16 v[248:251], v[244:247], v[88:95], %[sidx]\n\t"
        "v_smfmac_f32_16x16x64_bf16 v[252:255], v[208:211], v[96:103], %[sidx]\n\t"
        "v_smfmac_f32_16x16x64_bf16 v[252:255], v[212:215], v[104:111], %[sidx]\n\t"
        "v_cvt_pk_bf16_f32 v200, v248, v249\n\t"
        "v_cvt_pk_bf16_f32 v201, v250, v251\n\t"
        "ds_write2_b32 %[qw], v200, v201 offset1:96\n\t"
        "v_fma_f32 v202, v248, v249, v250\n\t"
        "v_smfmac_f32_16x16x64_bf16 v[252:255], v[216:219], v[112:119], %[sidx]\n\t"
        "v_cmp_u_f32 vcc, v202, v251\n\t"
        "v_smfmac_f32_16x16x64_bf16 v[252:255], v[220:223], v[120:127], %[sidx]\n\t"
        "s_waitcnt lgkmcnt(0)\n\t"
        "s_cbranch_vccnz Lnan%=\n\t"
        "Lok%=:\n\t"
        "s_barrier\n\t"
        "v_smfmac_f32_16x16x64_bf16 v[252:255], v[224:227], v[128:135], %[sidx]\n\t"
        "v_smfmac_f32_16x16x64_bf16 v[252:255], v[228:231], v[136:143], %[sidx]\n\t"
        "v_smfmac_f32_16x16x64_bf16 v[252:255], v[232:235], v[144:151], %[sidx]\n\t"
        "v_smfmac_f32_16x16x64_bf16 v[252:255], v[236:239], v[152:159], %[sidx]\n\t"
        "v_smfmac_f32_16x16x64_bf16 v[252:255], v[240:243], v[160:167], %[sidx]\n\t"
        "v_smfmac_f32_16x16x64_bf16 v[252:255], v[244:247], v[168:175], %[sidx]\n\t"
        "s_nop 7\n\t"
        "v_add_f32 v252, v252, v254\n\t"
        "v_add_f32 v253, v253, v255\n\t"
        "v_cvt_pk_bf16_f32 v204, v252, v253\n\t"
        "ds_write_b64 %[kw], v[204:205]\n\t"
        "v_mov_b32 v237, 0\n\t"
        "ds_read_b64_tr_b16 v[192:193], %[qtr] offset:0\n\t"
        "ds_read_b64_tr_b16 v[194:195], %[qtr] offset:768\n\t"
        "ds_read_b64_tr_b16 v[196:197], %[qtr] offset:1536\n\t"
        "ds_read_b64_tr_b16 v[198:199], %[qtr] offset:2304\n\t"
        "ds_read_b64_tr_b16 v[208:209], %[qtr] offset:3072\n\t"
        "ds_read_b64_tr_b16 v[210:211], %[qtr] offset:3840\n\t"
        "ds_read_b64_tr_b16 v[212:213], %[qtr] offset:4608\n\t"
        "ds_read_b64_tr_b16 v[214:215], %[qtr] offset:5376\n\t"
        "s_nop 15\n\t"
        "s_nop 5\n\t"
        "ds_read_b64_tr_b16 v[224:225], %[qtr] offset:7680\n\t"
        "ds_read_b64_tr_b16 v[226:227], %[qtr] offset:8448\n\t"
        "ds_read_b64_tr_b16 v[228:229], %[qtr] offset:10752\n\t"
        "ds_read_b64_tr_b16 v[230:231], %[qtr] offset:11520\n\t"
        "s_waitcnt lgkmcnt(2)\n\t"
        "v_mfma_f32_16x16x32_bf16 v[248:251], v[224:227], v[196:199], 0\n\t"
        "s_waitcnt lgkmcnt(0)\n\t"
        "v_mfma_f32_16x16x32_bf16 v[248:251], v[228:231], v[212:215], v[248:251]\n\t"
        "v_min3_u32 v203, v224, v226, v228\n\t"
        "v_min_u32 v203, v203, v230\n\t"
        "s_setprio 3\n\t"
        "s_barrier\n\t"
        "ds_read_b64_tr_b16 v[224:225], %[qtr] offset:6144\n\t"
        "ds_read_b64_tr_b16 v[226:227], %[qtr] offset:6912\n\t"
        "ds_read_b64_tr_b16 v[228:229], %[qtr] offset:9216\n\t"
        "ds_read_b64_tr_b16 v[230:231], %[qtr] offset:9984\n\t"
        "ds_read_b128 v[240:243], %[er]\n\t"
        "s_waitcnt lgkmcnt(3)\n\t"
        "v_mfma_f32_16x16x32_bf16 v[248:251], v[224:227], v[192:195], v[248:251]\n\t"
        "s_waitcnt lgkmcnt(1)\n\t"
        "v_mfma_f32_16x16x32_bf16 v[248:251], v[228:231], v[208:211], v[248:251]\n\t"
        "v_min3_u32 v203, v224, v226, v203\n\t"
        "s_waitcnt lgkmcnt(0)\n\t"
        "Lgo%=:\n\t"
        "v_mfma_f32_16x16x32_bf16 v[244:247], v[176:179], v[240:243], 0\n\t"
        "v_min3_u32 v203, v228, v230, v203\n\t"
        "v_cmp_ne_u32 s[96:97], %[fv], v203\n\t"
        "s_nop 0\n\t"
        "v_add_f32_dpp v248, v248, v248 row_ror:8 row_mask:0xf bank_mask:0xf\n\t"
        "v_add_f32_dpp v249, v249, v249 row_ror:8 row_mask:0xf bank_mask:0xf\n\t"
        "v_mul_f32 v248, %[ce], v248\n\t"
        "v_mul_f32 v249, %[ce], v249\n\t"
        "v_exp_f32 v250, v248\n\t"
        "v_exp_f32 v251, v249\n\t"
        "s_and_b64 s[96:97], s[96:97], %[jm]\n\t"
        "v_cvt_pk_bf16_f32 v236, v250, v251\n\t"
        "v_fma_f32 v250, v250, %[c1], %[ind0]\n\t"
        "v_fma_f32 v251, v251, %[c1], %[ind1]\n\t"
        "v_mfma_f32_16x16x16_bf16 v[244:247], v[176:177], v[236:237], v[244:247]\n\t"
        "s_cbranch_scc1 Lt%=\n\t"
        "s_nop 6\n\t"
        "v_rcp_f32 v246, v244\n\t"
        "v_cmpx_gt_f32 vcc, %[big], v244\n\t"
        "v_pk_mul_f32 v[250:251], v[250:251], v[246:247] op_sel_hi:[1,0]\n\t"
        "v_cvt_pk_bf16_f32 v250, v250, v251\n\t"
        "buffer_store_dword v250, %[po], %[rs], %[so] offen sc1\n\t"
        "buffer_store_dword v250, %[po], %[rs], s92 offen sc1\n\t"
        "buffer_store_dword v250, %[po], %[rs], s93 offen sc1\n\t"
        "buffer_store_dword v250, %[po], %[rs], s94 offen sc1\n\t"
        "s_cmp_lg_u64 exec, -1\n\t"
        "s_mov_b64 exec, -1\n\t"
        "s_cselect_b32 %[bad], 1, 0\n\t"
        "s_branch Lend%=\n\t"
        "Lt%=:\n\t"
        "s_nop 7\n\t"
        "ds_read_b64_tr_b16 v[216:217], %[qtr] offset:6144\n\t"
        "ds_read_b64_tr_b16 v[218:219], %[qtr] offset:6912\n\t"
        "ds_read_b64_tr_b16 v[220:221], %[qtr] offset:7680\n\t"
        "ds_read_b64_tr_b16 v[222:223], %[qtr] offset:8448\n\t"
        "ds_read_b64_tr_b16 v[224:225], %[qtr] offset:9216\n\t"
        "ds_read_b64_tr_b16 v[226:227], %[qtr] offset:9984\n\t"
        "ds_read_b64_tr_b16 v[228:229], %[qtr] offset:10752\n\t"
        "ds_read_b64_tr_b16 v[230:231], %[qtr] offset:11520\n\t"
        "ds_read_b128 v[240:243], %[er]\n\t"
        "s_waitcnt lgkmcnt(0)\n\t"
        "v_mfma_f32_16x16x32_bf16 v[248:251], v[216:219], v[192:195], 0\n\t"
        "v_mfma_f32_16x16x32_bf16 v[248:251], v[220:223], v[196:199], v[248:251]\n\t"
        "v_mfma_f32_16x16x32_bf16 v[248:251], v[224:227], v[208:211], v[248:251]\n\t"
        "v_mfma_f32_16x16x32_bf16 v[248:251], v[228:231], v[212:215], v[248:251]\n\t"
        "v_min3_u32 v203, v216, v218, v220\n\t"
        "v_min3_u32 v203, v222, v224, v203\n\t"
        "v_min_u32 v203, v226, v203\n\t"
        "s_branch Lgo%=\n\t"
        "Lnan%=:\n\t"
        "v_mbcnt_lo_u32_b32 v202, -1, 0\n\t"
        "v_mbcnt_hi_u32_b32 v202, -1, v202\n\t"
        "v_lshl_add_u32 v207, v202, 7, %[lz]\n\t"
        "v_and_b32 v202, 31, v202\n\t"
        "v_lshl_add_u32 v207, v202, 2, v207\n\t"
        "ds_read2st64_b32 v[202:203], v207 offset1:32\n\t"
        "s_waitcnt lgkmcnt(0)\n\t"
        "s_mov_b64 exec, 0x00ff00ff\n\t"
        "v_max_u32 v202, v202, v203\n\t"
        "s_mov_b64 exec, -1\n\t"
        "v_cmp_eq_u32 vcc, 0xffbfffbf, v202\n\t"
        "s_cbranch_vccnz Lredo%=\n\t"
        "v_max_u32 v207, v208, v209\n\t"
        "v_max3_u32 v207, v207, v210, v211\n\t"
        "v_max3_u32 v207, v207, v212, v213\n\t"
        "v_max3_u32 v207, v207, v214, v215\n\t"
        "v_max3_u32 v207, v207, v216, v217\n\t"
        "v_max3_u32 v207, v207, v218, v219\n\t"
        "v_max3_u32 v207, v207, v220, v221\n\t"
        "v_max3_u32 v207, v207, v222, v223\n\t"
        "v_max3_u32 v207, v207, v224, v225\n\t"
        "v_max3_u32 v207, v207, v226, v227\n\t"
        "v_max3_u32 v207, v207, v228, v229\n\t"
        "v_max3_u32 v207, v207, v230, v231\n\t"
        "v_max3_u32 v207, v207, v232, v233\n\t"
        "v_max3_u32 v207, v207, v234, v235\n\t"
        "v_max3_u32 v207, v207, v236, v237\n\t"
        "v_max3_u32 v207, v207, v238, v239\n\t"
        "v_max3_u32 v207, v207, v240, v241\n\t"
        "v_max3_u32 v207, v207, v242, v243\n\t"
        "v_max3_u32 v207, v207, v244, v245\n\t"
        "v_max3_u32 v207, v207, v246, v247\n\t"
        "v_cmp_eq_u32 vcc, 0xffbfffbf, v207\n\t"
        "s_cbranch_vccz Lok%=\n\t"
        "Lredo%=:\n\t"
        "s_waitcnt lgkmcnt(0)\n\t"
        "v_add_u32 v206, %[dl], v206\n\t"
        "v_mov_b64 v[248:249], 0\n\t"
        "v_mov_b64 v[250:251], 0\n\t"
        "v_mov_b64 v[252:253], 0\n\t"
        "v_mov_b64 v[254:255], 0\n\t"
        "s_add_u32 %[lz], %[lz], %[dl]\n\t"
        "s_sub_u32 %[dl], 0, %[dl]\n\t"
        "s_setprio 2\n\t"
        "s_barrier\n\t"
        "s_branch Lb1%=\n\t"
        "Lend%=:\n\t"
        : [lz] "+&s"(lz), [dl] "=&s"(dl), [bad] "=&s"(bad)
        : [w0] "{v[16:47]}"(W[0]), [w1] "{v[48:79]}"(W[1]), [w2] "{v[80:111]}"(W[2]), [w3] "{v[112:143]}"(W[3]), [w4] "{v[144:175]}"(W[4]), [va] "v"(va), [sidx] "v"(sidx), [qw] "v"(qw), [kw] "v"(kw), [fv] "s"(fv), [ones] "{v[176:179]}"(ones), [qtr] "v"(qtr), [er] "v"(er), [po] "v"(po), [c1] "v"(c1), [ind0] "v"(ind0), [ind1] "v"(ind1), [rs] "s"(rs), [so] "s"(so), [ce] "s"(ce), [big] "s"(big), [jm] "s"(jm)
        : "s92", "s93", "s94", "s95", "s96", "s97", "v192", "v193", "v194", "v195", "v196", "v197", "v198", "v199", "v200", "v201", "v202", "v203", "v204", "v205", "v206", "v207", "v208", "v209", "v210", "v211", "v212", "v213", "v214", "v215", "v216", "v217", "v218", "v219", "v220", "v221", "v222", "v223", "v224", "v225", "v226", "v227", "v228", "v229", "v230", "v231", "v232", "v233", "v234", "v235", "v236", "v237", "v238", "v239", "v240", "v241", "v242", "v243", "v244", "v245", "v246", "v247", "v248", "v249", "v250", "v251", "v252", "v253", "v254", "v255", "vcc", "scc", "memory");
}

__device__ __forceinline__ void s_m(const u32x32 (&W)[5], u32 va, u32 lz, u32 sidx, u32 qw, u32 kw, u32 fv, u32& left, u32& poll, u32& lv, u32& lim, u32& tg, u32& irr, u32 ki, u32 off, u32 st, u32 tol, u32 tol2, u32 qp, u32 qg, u32 esc, u32 rn, u32 rkn, u32 so2, u32& c0, u32& r0, u32& te, u32& tn, u32& gb, u32 lev, u32 own, u32 gp, u32 lk, u32 kr, u32 dw, u32 df, u32 vm, u32 so, u32 lb, u32x4 rs, u32 tso, u32 tsl, u32 sn, u32 tsw, u32 ro, u32 rb, u32 rk STP) {
    u32 dl;
    asm volatile(
        ".p2align 6\n\t"
        "s_mov_b32 s94, m0\n\t"
        "s_add_u32 s92, %[so], 0x8000\n\t"
        "s_add_u32 s93, %[so], 0x10000\n\t"
        "s_mov_b32 m0, %[lb]\n\t"
        "s_setprio 2\n\t"
        "s_mov_b32 %[lv], 0\n\t"
        "s_cmp_lg_u32 %[poll], 1\n\t"
        "s_cbranch_scc1 Lpb%=\n\t"
        "v_lshrrev_b32 v201, 2, %[vm]\n\t"
        "v_add_u32 v201, %[lb], v201\n\t"
        "s_waitcnt vmcnt(0)\n\t"
        "ds_read_b32 v200, v201 offset:29696\n\t"
        "s_cmp_lg_u32 %[own], 0\n\t"
        "s_mov_b64 s[96:97], 0\n\t"
        "s_mov_b32 s98, %[rb]\n\t"
        "s_mov_b32 s99, 0x2000\n\t"
        "s_waitcnt lgkmcnt(0)\n\t"
        "v_cmp_gt_i32 vcc, s96, v200\n\t"
        "s_cbranch_scc1 Ls%=\n\t"
        "s_cbranch_vccnz Ls%=\n\t"
        "Lg%=:\n\t"
        "s_add_u32 s95, %[te], %[gp]\n\t"
        "s_add_u32 s95, s95, %[gb]\n\t"
        "Lda_%=:\n\t"
        "s_memrealtime s[90:91]\n\t"
        "s_waitcnt lgkmcnt(0)\n\t"
        "s_mul_i32 s90, s90, 24\n\t"
        "s_sub_u32 s88, s95, s90\n\t"
        "s_sub_u32 s89, s88, 620\n\t"
        "s_cmp_gt_u32 s89, 0x4000\n\t"
        "s_cbranch_scc1 Lea_%=\n\t"
        "s_sleep 1\n\t"
        "s_branch Lda_%=\n\t"
        "Lea_%=:\n\t"
        "s_memrealtime s[64:65]\n\t"
        "s_nop 0\n\t"
        "s_memrealtime s[66:67]\n\t"
        "s_nop 0\n\t"
        "s_memrealtime s[68:69]\n\t"
        "s_nop 0\n\t"
        "s_memrealtime s[70:71]\n\t"
        "s_nop 0\n\t"
        "s_memrealtime s[72:73]\n\t"
        "s_nop 0\n\t"
        "s_memrealtime s[74:75]\n\t"
        "s_memtime s[90:91]\n\t"
        "s_memrealtime s[76:77]\n\t"
        "s_nop 0\n\t"
        "s_memrealtime s[78:79]\n\t"
        "s_nop 0\n\t"
        "s_memrealtime s[80:81]\n\t"
        "s_nop 0\n\t"
        "s_memrealtime s[82:83]\n\t"
        "s_nop 0\n\t"
        "s_memrealtime s[84:85]\n\t"
        "s_nop 0\n\t"
        "s_memrealtime s[86:87]\n\t"
        "s_nop 0\n\t"
        "s_nop 7\n\t"
        "s_waitcnt lgkmcnt(0)\n\t"
        "s_add_u32 s64, s64, s66\n\t"
        "s_add_u32 s64, s64, s68\n\t"
        "s_add_u32 s64, s64, s70\n\t"
        "s_add_u32 s64, s64, s72\n\t"
        "s_add_u32 s64, s64, s74\n\t"
        "s_add_u32 s64, s64, s76\n\t"
        "s_add_u32 s64, s64, s78\n\t"
        "s_add_u32 s64, s64, s80\n\t"
        "s_add_u32 s64, s64, s82\n\t"
        "s_add_u32 s64, s64, s84\n\t"
        "s_add_u32 s64, s64, s86\n\t"
        "s_lshl_b32 s64, s64, 1\n\t"
        "s_mov_b32 %[c0], s90\n\t"
        "s_mov_b32 %[r0], s64\n\t"
        "s_sub_u32 s88, s95, s64\n\t"
        "s_mul_i32 s88, s88, %[kr]\n\t"
        "s_ashr_i32 s88, s88, 12\n\t"
        "s_sub_u32 s88, s88, 244\n\t"
        "s_nop 1\n\t"
        "s_max_i32 s88, s88, 0\n\t"
        "s_min_i32 s88, s88, 384\n\t"
        "s_andn2_b32 s88, s88, 3\n\t"
        "s_getpc_b64 s[90:91]\n\t"
        "Lga_%=:\n\t"
        "s_sub_u32 s90, s90, s88\n\t"
        "s_subb_u32 s91, s91, 0\n\t"
        "s_add_u32 s90, s90, Lza_%=-Lga_%=\n\t"
        "s_addc_u32 s91, s91, 0\n\t"
        "s_setpc_b64 s[90:91]\n\t"
        "s_nop 0\n\ts_nop 0\n\ts_nop 0\n\ts_nop 0\n\t"
        "s_nop 0\n\ts_nop 0\n\ts_nop 0\n\ts_nop 0\n\t"
        "s_nop 0\n\ts_nop 0\n\ts_nop 0\n\ts_nop 0\n\t"
        "s_nop 0\n\ts_nop 0\n\ts_nop 0\n\ts_nop 0\n\t"
        "s_nop 0\n\ts_nop 0\n\ts_nop 0\n\ts_nop 0\n\t"
        "s_nop 0\n\ts_nop 0\n\ts_nop 0\n\ts_nop 0\n\t"
        "s_nop 0\n\ts_nop 0\n\ts_nop 0\n\ts_nop 0\n\t"
        "s_nop 0\n\ts_nop 0\n\ts_nop 0\n\ts_nop 0\n\t"
        "s_nop 0\n\ts_nop 0\n\ts_nop 0\n\ts_nop 0\n\t"
        "s_nop 0\n\ts_nop 0\n\ts_nop 0\n\ts_nop 0\n\t"
        "s_nop 0\n\ts_nop 0\n\ts_nop 0\n\ts_nop 0\n\t"
        "s_nop 0\n\ts_nop 0\n\ts_nop 0\n\ts_nop 0\n\t"
        "s_nop 0\n\ts_nop 0\n\ts_nop 0\n\ts_nop 0\n\t"
        "s_nop 0\n\ts_nop 0\n\ts_nop 0\n\ts_nop 0\n\t"
        "s_nop 0\n\ts_nop 0\n\ts_nop 0\n\ts_nop 0\n\t"
        "s_nop 0\n\ts_nop 0\n\ts_nop 0\n\ts_nop 0\n\t"
        "s_nop 0\n\ts_nop 0\n\ts_nop 0\n\ts_nop 0\n\t"
        "s_nop 0\n\ts_nop 0\n\ts_nop 0\n\ts_nop 0\n\t"
        "s_nop 0\n\ts_nop 0\n\ts_nop 0\n\ts_nop 0\n\t"
        "s_nop 0\n\ts_nop 0\n\ts_nop 0\n\ts_nop 0\n\t"
        "s_nop 0\n\ts_nop 0\n\ts_nop 0\n\ts_nop 0\n\t"
        "s_nop 0\n\ts_nop 0\n\ts_nop 0\n\ts_nop 0\n\t"
        "s_nop 0\n\ts_nop 0\n\ts_nop 0\n\ts_nop 0\n\t"
        "s_nop 0\n\ts_nop 0\n\ts_nop 0\n\ts_nop 0\n\t"
        "Lza_%=:\n\t"
        "buffer_load_dwordx4 %[vm], %[rs], %[so] offen offset:0 sc1 lds\n\t"
        "buffer_load_dwordx4 %[vm], %[rs], %[so] offen offset:1024 sc1 lds\n\t"
        "buffer_load_dwordx4 %[vm], %[rs], %[so] offen offset:2048 sc1 lds\n\t"
        "buffer_load_dwordx4 %[vm], %[rs], %[so] offen offset:3072 sc1 lds\n\t"
        "s_add_u32 m0, m0, 0x1000\n\t"
        "s_nop 0\n\t"
        "buffer_load_dwordx4 %[vm], %[rs], s92 offen offset:0 sc1 lds\n\t"
        "buffer_load_dwordx4 %[vm], %[rs], s92 offen offset:1024 sc1 lds\n\t"
        "buffer_load_dwordx4 %[vm], %[rs], s92 offen offset:2048 sc1 lds\n\t"
        "buffer_load_dwordx4 %[vm], %[rs], s92 offen offset:3072 sc1 lds\n\t"
        "s_add_u32 m0, m0, 0x1000\n\t"
        "s_nop 0\n\t"
        "buffer_load_dwordx4 %[vm], %[rs], s93 offen offset:0 sc1 lds\n\t"
        "buffer_load_dwordx4 %[vm], %[rs], s93 offen offset:2048 sc1 lds\n\t"
        "s_cmp_lg_u32 s96, 0\n\t"
        "s_cbranch_scc1 Lnn%=\n\t"
        "v_readlane_b32 %[gb], v200, 0\n\t"
        "s_and_b32 %[gb], %[gb], 15\n\t"
        "s_lshl_b32 %[gb], %[gb], 4\n\t"
        "v_subrev_u32 v200, %[tn], v200\n\t"
        "v_bfe_i32 v200, v200, 0, 30\n\t"
        "s_nop 1\n\t"
        "v_max_i32_dpp v200, v200, v200 quad_perm:[1,0,3,2] row_mask:0xf bank_mask:0xf\n\t"
        "s_nop 1\n\t"
        "v_max_i32_dpp v200, v200, v200 quad_perm:[2,3,0,1] row_mask:0xf bank_mask:0xf\n\t"
        "s_nop 1\n\t"
        "v_max_i32_dpp v200, v200, v200 row_ror:4 row_mask:0xf bank_mask:0xf\n\t"
        "s_nop 1\n\t"
        "v_max_i32_dpp v200, v200, v200 row_ror:8 row_mask:0xf bank_mask:0xf\n\t"
        "s_nop 0\n\t"
        "v_readlane_b32 s88, v200, 0\n\t"
        "v_readlane_b32 s89, v200, 16\n\t"
        "s_max_i32 s88, s88, s89\n\t"
        "s_add_u32 %[tn], %[tn], s88\n\t"
        "Lnn%=:\n\t"
        "s_add_u32 s88, %[tn], %[dw]\n\t"
        "s_add_u32 s88, s88, %[gb]\n\t"
        "s_add_u32 s89, %[tn], %[gp]\n\t"
        "s_sub_u32 s88, s88, %[lev]\n\t"
        "s_and_b32 s88, s88, 0x3ffffff0\n\t"
        "s_or_b32 s88, s88, %[lev]\n\t"
        "v_mov_b32 v193, %[sn]\n\t"
        "v_mov_b32 v194, s89\n\t"
        "v_mov_b32 v196, s88\n\t"
        "s_mov_b32 exec_hi, 0\n\t"
        "buffer_store_dwordx4 v[196:199], %[tsw], %[rs], %[tso] offen sc1\n\t"
        "buffer_store_dwordx4 v[192:195], %[tsw], %[rs], %[tsl] offen sc1\n\t"
        "s_mov_b32 exec_hi, -1\n\t"
        "s_mov_b32 %[te], %[tn]\n\t"
        "s_add_u32 %[tn], %[tn], %[df]\n\t"
        "s_add_u32 %[tn], %[tn], %[gb]\n\t"
        "s_mov_b32 %[irr], s96\n\t"
        "s_cmp_lg_u32 s96, 0\n\t"
        "s_cselect_b32 s88, %[esc], 0\n\t"
        "s_add_u32 %[qg], %[qg], s88\n\t"
        "s_add_u32 %[qp], %[qp], s88\n\t"
        "s_add_u32 %[tg], s95, %[st]\n\t"
        "s_sub_u32 %[lim], %[tg], %[r0]\n\t"
        "s_mul_i32 %[lim], %[lim], %[kr]\n\t"
        "s_ashr_i32 %[lim], %[lim], 12\n\t"
        "s_add_u32 %[lim], %[lim], %[c0]\n\t"
        "Lpb%=:\n\t"
        "s_mov_b64 s[96:97], 0xffffffff\n\t"
        "s_mov_b32 s97, 0\n\t"
        "v_mov_b32 v206, %[va]\n\t"
        "s_mov_b32 %[dl], 81920\n\t"
        "v_mov_b64 v[248:249], 0\n\t"
        "v_mov_b64 v[250:251], 0\n\t"
        "v_mov_b64 v[252:253], 0\n\t"
        "v_mov_b64 v[254:255], 0\n\t"
        "v_mov_b32 v205, %[fv]\n\t"
        "v_mbcnt_lo_u32_b32 v202, -1, 0\n\t"
        "v_mbcnt_hi_u32_b32 v202, -1, v202\n\t"
        "v_lshl_add_u32 v187, v202, 7, %[lz]\n\t"
        "v_and_b32 v202, 31, v202\n\t"
        "v_lshl_add_u32 v187, v202, 2, v187\n\t"
        "s_add_u32 s88, %[te], %[qg]\n\t"
        "s_and_b32 s88, s88, 0x3ffffffe\n\t"
        "s_bitset1_b32 s88, 31\n\t"
        "s_cmp_lt_u32 %[lv], 4\n\t"
        "s_cselect_b32 s88, s88, 0xbfffffff\n\t"
        "v_mov_b32 v188, s88\n\t"
        "s_add_u32 s95, %[lim], %[tol]\n\t"
        "s_mov_b32 m0, s94\n\t"
        "s_waitcnt vmcnt(2)\n\t"
        "Lb1%=:\n\t"
        "ds_read2st64_b32 v[202:203], v187 offset1:32\n\t"
        "s_barrier\n\t"
        "s_memtime s[90:91]\n\t"
        "s_waitcnt lgkmcnt(0)\n\t"
        "s_mov_b64 exec, 0x00ff00ff\n\t"
        "v_max_u32 v202, v202, v203\n\t"
        "s_mov_b64 exec, -1\n\t"
        "v_cmp_eq_u32 vcc, 0xffbfffbf, v202\n\t"
        "s_sub_u32 s88, s95, s90\n\t"
        "s_cmp_lt_i32 s88, 0\n\t"
        "ds_read_b128 v[208:211], v206 offset:0\n\t"
        "ds_read_b128 v[212:215], v206 offset:1024\n\t"
        "s_cbranch_vccnz Lbad%=\n\t"
        "s_cbranch_scc1 Llate%=\n\t"
        "Lfr%=:\n\t"
        "ds_read_b128 v[216:219], v206 offset:2048\n\t"
        "ds_read_b128 v[220:223], v206 offset:3072\n\t"
        "ds_read_b128 v[224:227], v206 offset:4096\n\t"
        "ds_read_b128 v[228:231], v206 offset:5120\n\t"
        "s_waitcnt lgkmcnt(5)\n\t"
        "v_smfmac_f32_16x16x64_bf16 v[248:251], v[208:211], v[16:23], %[sidx]\n\t"
        "ds_read_b128 v[232:235], v206 offset:6144\n\t"
        "s_waitcnt lgkmcnt(5)\n\t"
        "v_smfmac_f32_16x16x64_bf16 v[248:251], v[212:215], v[24:31], %[sidx]\n\t"
        "ds_read_b128 v[236:239], v206 offset:7168\n\t"
        "s_waitcnt lgkmcnt(5)\n\t"
        "v_smfmac_f32_16x16x64_bf16 v[248:251], v[216:219], v[32:39], %[sidx]\n\t"
        "ds_read_b128 v[240:243], v206 offset:8192\n\t"
        "s_waitcnt lgkmcnt(5)\n\t"
        "v_smfmac_f32_16x16x64_bf16 v[248:251], v[220:223], v[40:47], %[sidx]\n\t"
        "ds_read_b128 v[244:247], v206 offset:10240\n\t"
        "s_waitcnt lgkmcnt(5)\n\t"
        "v_smfmac_f32_16x16x64_bf16 v[248:251], v[224:227], v[48:55], %[sidx]\n\t"
        "s_waitcnt lgkmcnt(4)\n\t"
        "v_smfmac_f32_16x16x64_bf16 v[248:251], v[228:231], v[56:63], %[sidx]\n\t"
        "s_waitcnt lgkmcnt(3)\n\t"
        "v_smfmac_f32_16x16x64_bf16 v[248:251], v[232:235], v[64:71], %[sidx]\n\t"
        "s_waitcnt lgkmcnt(2)\n\t"
        "v_smfmac_f32_16x16x64_bf16 v[248:251], v[236:239], v[72:79], %[sidx]\n\t"
        "s_waitcnt lgkmcnt(1)\n\t"
        "v_smfmac_f32_16x16x64_bf16 v[248:251], v[240:243], v[80:87], %[sidx]\n\t"
        "s_waitcnt lgkmcnt(0)\n\t"
        "v_smfmac_f32_16x16x64_bf16 v[248:251], v[244:247], v[88:95], %[sidx]\n\t"
        "v_smfmac_f32_16x16x64_bf16 v[252:255], v[208:211], v[96:103], %[sidx]\n\t"
        "v_smfmac_f32_16x16x64_bf16 v[252:255], v[212:215], v[104:111], %[sidx]\n\t"
        "v_cvt_pk_bf16_f32 v200, v248, v249\n\t"
        "v_cvt_pk_bf16_f32 v201, v250, v251\n\t"
        "ds_write2_b32 %[qw], v200, v201 offset1:96\n\t"
        "v_fma_f32 v202, v248, v249, v250\n\t"
        "v_smfmac_f32_16x16x64_bf16 v[252:255], v[216:219], v[112:119], %[sidx]\n\t"
        "v_cmp_u_f32 vcc, v202, v251\n\t"
        "v_smfmac_f32_16x16x64_bf16 v[252:255], v[220:223], v[120:127], %[sidx]\n\t"
        "s_waitcnt lgkmcnt(0)\n\t"
        "s_cbranch_vccnz Lnan%=\n\t"
        "Lok%=:\n\t"
        "s_barrier\n\t"
        "v_smfmac_f32_16x16x64_bf16 v[252:255], v[224:227], v[128:135], %[sidx]\n\t"
        "v_smfmac_f32_16x16x64_bf16 v[252:255], v[228:231], v[136:143], %[sidx]\n\t"
        "v_smfmac_f32_16x16x64_bf16 v[252:255], v[232:235], v[144:151], %[sidx]\n\t"
        "v_smfmac_f32_16x16x64_bf16 v[252:255], v[236:239], v[152:159], %[sidx]\n\t"
        "v_smfmac_f32_16x16x64_bf16 v[252:255], v[240:243], v[160:167], %[sidx]\n\t"
        "v_smfmac_f32_16x16x64_bf16 v[252:255], v[244:247], v[168:175], %[sidx]\n\t"
        "s_add_u32 m0, %[lb], 29696\n\t"
        "s_cmp_lg_u32 %[lv], 0\n\t"
        "buffer_load_dword %[ro], %[rs], %[rn] offen sc1 lds\n\t"
        "s_nop 4\n\t"
        "v_add_f32 v252, v252, v254\n\t"
        "v_add_f32 v253, v253, v255\n\t"
        "v_cvt_pk_bf16_f32 v204, v252, v253\n\t"
        "ds_write_b64 %[kw], v[204:205]\n\t"
        "s_barrier\n\t"
        "s_cbranch_scc1 Lmore%=\n\t"
        "s_mov_b32 m0, s94\n\t"
        "s_branch Lend%=\n\t"
        "Lbad%=:\n\t"
        "s_cmp_gt_u32 %[lv], 4\n\t"
        "s_cselect_b32 s88, 0x80000000, %[tso]\n\t"
        "s_mov_b64 exec, s[96:97]\n\t"
        "buffer_store_dwordx4 v[188:191], %[tsw], %[rs], s88 offen sc1\n\t"
        "s_mov_b64 exec, -1\n\t"
        "s_mov_b32 s92, %[so]\n\t"
        "s_mov_b32 %[so], %[so2]\n\t"
        "s_mov_b32 %[so2], s92\n\t"
        "s_add_u32 s92, %[so], 0x8000\n\t"
        "s_add_u32 s93, %[so], 0x10000\n\t"
        "s_add_u32 m0, %[lz], %[dl]\n\t"
        "s_sub_i32 %[left], %[left], 1\n\t"
        "s_cmp_lt_i32 %[left], 0\n\t"
        "s_cbranch_scc1 Lgu%=\n\t"
        "buffer_load_dwordx4 %[vm], %[rs], %[so] offen offset:0 sc1 lds\n\t"
        "buffer_load_dwordx4 %[vm], %[rs], %[so] offen offset:1024 sc1 lds\n\t"
        "buffer_load_dwordx4 %[vm], %[rs], %[so] offen offset:2048 sc1 lds\n\t"
        "buffer_load_dwordx4 %[vm], %[rs], %[so] offen offset:3072 sc1 lds\n\t"
        "s_add_u32 m0, m0, 0x1000\n\t"
        "s_nop 0\n\t"
        "buffer_load_dwordx4 %[vm], %[rs], s92 offen offset:0 sc1 lds\n\t"
        "buffer_load_dwordx4 %[vm], %[rs], s92 offen offset:1024 sc1 lds\n\t"
        "buffer_load_dwordx4 %[vm], %[rs], s92 offen offset:2048 sc1 lds\n\t"
        "buffer_load_dwordx4 %[vm], %[rs], s92 offen offset:3072 sc1 lds\n\t"
        "s_add_u32 m0, m0, 0x1000\n\t"
        "s_nop 0\n\t"
        "buffer_load_dwordx4 %[vm], %[rs], s93 offen offset:0 sc1 lds\n\t"
        "buffer_load_dwordx4 %[vm], %[rs], s93 offen offset:2048 sc1 lds\n\t"
        "Lgv%=:\n\t"
        "s_mov_b32 m0, s94\n\t"
        "s_add_u32 %[lim], %[lim], %[qp]\n\t"
        "s_add_u32 %[lv], %[lv], 1\n\t"
        "s_cmp_gt_u32 %[lv], 4\n\t"
        "s_cselect_b32 s88, 0, %[qg]\n\t"
        "s_cselect_b32 %[lim], %[c0], %[lim]\n\t"
        "s_add_u32 %[te], %[te], s88\n\t"
        "s_add_u32 %[tn], %[tn], s88\n\t"
        "s_cmp_eq_u32 %[lv], 1\n\t"
        "s_cselect_b32 s88, 700, 0\n\t"
        "s_add_u32 %[qg], %[qg], s88\n\t"
        "s_add_u32 %[qp], %[qp], s88\n\t"
        "s_mov_b32 %[tol], %[tol2]\n\t"
        "s_min_u32 s89, %[lv], 4\n\t"
        "s_lshl_b32 s89, s89, 3\n\t"
        "s_add_u32 s89, s89, 24\n\t"
        "s_lshl_b64 s[96:97], 0xff, s89\n\t"
        "s_add_u32 s88, %[te], %[qg]\n\t"
        "s_and_b32 s88, s88, 0x3ffffffe\n\t"
        "s_bitset1_b32 s88, 31\n\t"
        "s_cmp_lt_u32 %[lv], 4\n\t"
        "s_cselect_b32 s88, s88, 0xbfffffff\n\t"
        "v_mov_b32 v188, s88\n\t"
        "s_add_u32 s95, %[lim], %[tol]\n\t"
        "s_branch Lredo%=\n\t"
        "Llate%=:\n\t"
        "s_cmp_gt_u32 %[lv], 4\n\t"
        "s_cselect_b32 s91, 1536, 0\n\t"
        "s_cmp_eq_u32 %[sn], 0\n\t"
        "s_cselect_b32 s91, 1536, s91\n\t"
        "s_sub_u32 s88, s90, %[c0]\n\t"
        "s_min_u32 s88, s88, 0x3ffff\n\t"
        "s_mul_i32 s88, s88, %[ki]\n\t"
        "s_lshr_b32 s88, s88, 12\n\t"
        "s_add_u32 s88, s88, %[r0]\n\t"
        "s_add_u32 s88, s88, %[off]\n\t"
        "s_add_u32 s88, s88, s91\n\t"
        "s_sub_u32 s88, s88, %[te]\n\t"
        "s_max_i32 s88, s88, 500\n\t"
        "s_add_u32 s88, s88, %[te]\n\t"
        "s_and_b32 s89, s88, 0x3ffffffd\n\t"
        "s_or_b32 s89, s89, 0x80000001\n\t"
        "v_mov_b32 v188, s89\n\t"
        "s_mov_b64 exec, s[96:97]\n\t"
        "buffer_store_dwordx4 v[188:191], %[tsw], %[rs], %[tso] offen sc1\n\t"
        "s_mov_b64 exec, -1\n\t"
        "s_mov_b32 %[poll], 4\n\t"
        "s_sub_u32 s89, s88, %[te]\n\t"
        "s_max_i32 s89, s89, 0\n\t"
        "s_add_u32 %[te], %[te], s89\n\t"
        "s_add_u32 %[tn], %[tn], s89\n\t"
        "s_branch Lfr%=\n\t"
        "Lmore%=:\n\t"
        "s_add_u32 m0, m0, 0x100\n\t"
        "s_add_u32 s88, %[rkn], 0\n\t"
        "buffer_load_dword %[ro], %[rs], s88 offen sc1 lds\n\t"
        "s_cmp_eq_u32 %[lv], 1\n\t"
        "s_cbranch_scc1 Lmo%=\n\t"
        "s_add_u32 m0, m0, 0x100\n\t"
        "s_add_u32 s88, %[rkn], 4096\n\t"
        "buffer_load_dword %[ro], %[rs], s88 offen sc1 lds\n\t"
        "s_cmp_eq_u32 %[lv], 2\n\t"
        "s_cbranch_scc1 Lmo%=\n\t"
        "s_add_u32 m0, m0, 0x100\n\t"
        "s_add_u32 s88, %[rkn], 16384\n\t"
        "buffer_load_dword %[ro], %[rs], s88 offen sc1 lds\n\t"
        "s_cmp_eq_u32 %[lv], 3\n\t"
        "s_cbranch_scc1 Lmo%=\n\t"
        "s_add_u32 m0, m0, 0x100\n\t"
        "s_add_u32 s88, %[rkn], 20480\n\t"
        "buffer_load_dword %[ro], %[rs], s88 offen sc1 lds\n\t"
        "Lmo%=:\n\t"
        "s_mov_b32 m0, s94\n\t"
        "s_branch Lend%=\n\t"
        "Lnan%=:\n\t"
        "v_max_u32 v207, v208, v209\n\t"
        "v_max3_u32 v207, v207, v210, v211\n\t"
        "v_max3_u32 v207, v207, v212, v213\n\t"
        "v_max3_u32 v207, v207, v214, v215\n\t"
        "v_max3_u32 v207, v207, v216, v217\n\t"
        "v_max3_u32 v207, v207, v218, v219\n\t"
        "v_max3_u32 v207, v207, v220, v221\n\t"
        "v_max3_u32 v207, v207, v222, v223\n\t"
        "v_max3_u32 v207, v207, v224, v225\n\t"
        "v_max3_u32 v207, v207, v226, v227\n\t"
        "v_max3_u32 v207, v207, v228, v229\n\t"
        "v_max3_u32 v207, v207, v230, v231\n\t"
        "v_max3_u32 v207, v207, v232, v233\n\t"
        "v_max3_u32 v207, v207, v234, v235\n\t"
        "v_max3_u32 v207, v207, v236, v237\n\t"
        "v_max3_u32 v207, v207, v238, v239\n\t"
        "v_max3_u32 v207, v207, v240, v241\n\t"
        "v_max3_u32 v207, v207, v242, v243\n\t"
        "v_max3_u32 v207, v207, v244, v245\n\t"
        "v_max3_u32 v207, v207, v246, v247\n\t"
        "v_cmp_eq_u32 vcc, 0xffbfffbf, v207\n\t"
        "s_cbranch_vccz Lok%=\n\t"
        "s_branch Lbad%=\n\t"
        "Lredo%=:\n\t"
        "s_waitcnt lgkmcnt(0)\n\t"
        "v_add_u32 v206, %[dl], v206\n\t"
        "v_add_u32 v187, %[dl], v187\n\t"
        "v_mov_b64 v[248:249], 0\n\t"
        "v_mov_b64 v[250:251], 0\n\t"
        "v_mov_b64 v[252:253], 0\n\t"
        "v_mov_b64 v[254:255], 0\n\t"
        "s_add_u32 %[lz], %[lz], %[dl]\n\t"
        "s_sub_u32 %[dl], 0, %[dl]\n\t"
        "s_setprio 2\n\t"
        "s_barrier\n\t"
        "s_waitcnt vmcnt(0)\n\t"
        "s_branch Lb1%=\n\t"
        "Lgu%=:\n\t"
        "v_add_u32 v207, m0, %[vm]\n\t"
        "v_mov_b64 v[196:197], 0\n\t"
        "v_mov_b64 v[198:199], 0\n\t"
        "ds_write_b128 v207, v[196:199] offset:0\n\t"
        "ds_write_b128 v207, v[196:199] offset:1024\n\t"
        "ds_write_b128 v207, v[196:199] offset:2048\n\t"
        "ds_write_b128 v207, v[196:199] offset:3072\n\t"
        "ds_write_b128 v207, v[196:199] offset:4096\n\t"
        "ds_write_b128 v207, v[196:199] offset:5120\n\t"
        "ds_write_b128 v207, v[196:199] offset:6144\n\t"
        "ds_write_b128 v207, v[196:199] offset:7168\n\t"
        "ds_write_b128 v207, v[196:199] offset:8192\n\t"
        "ds_write_b128 v207, v[196:199] offset:9216\n\t"
        "ds_write_b128 v207, v[196:199] offset:10240\n\t"
        "s_branch Lgv%=\n\t"
        "Ls%=:\n\t"
        "s_mov_b32 s90, 0xbfffffff\n\t"
        "s_cmp_eq_u32 s97, 0\n\t"
        "s_cselect_b32 s90, 0xffbfffbf, s90\n\t"
        "s_mov_b32 s96, 0xc0000000\n\t"
        "s_waitcnt vmcnt(0) lgkmcnt(0)\n\t"
        "v_cmp_eq_u32 s[88:89], s90, v200\n\t"
        "v_cmp_gt_i32 vcc, s96, v200\n\t"
        "v_and_b32 v202, 0xc0000001, v200\n\t"
        "v_subrev_u32 v200, %[te], v200\n\t"
        "s_sub_u32 s99, s99, 1\n\t"
        "s_cselect_b32 s88, 0, s88\n\t"
        "s_or_b32 s89, s88, vcc_lo\n\t"
        "s_cbranch_scc0 Lu%=\n\t"
        "v_bfe_i32 v200, v200, 0, 30\n\t"
        "s_cmp_lg_u32 s88, 0\n\t"
        "v_cndmask_b32 v200, 0, v200, vcc\n\t"
        "s_cbranch_scc1 Lq%=\n\t"
        "v_cmp_eq_u32 vcc, 0x80000000, v202\n\t"
        "s_and_b32 s98, s97, 1\n\t"
        "v_max_i32_dpp v200, v200, v200 quad_perm:[1,0,3,2] row_mask:0xf bank_mask:0xf\n\t"
        "s_lshl_b32 s98, s98, 12\n\t"
        "s_lshr_b32 s88, s97, 1\n\t"
        "v_max_i32_dpp v200, v200, v200 quad_perm:[2,3,0,1] row_mask:0xf bank_mask:0xf\n\t"
        "s_lshl_b32 s88, s88, 14\n\t"
        "s_add_u32 s98, s98, s88\n\t"
        "v_max_i32_dpp v200, v200, v200 row_ror:4 row_mask:0xf bank_mask:0xf\n\t"
        "s_add_u32 s98, s98, %[rk]\n\t"
        "s_add_u32 s97, s97, 1\n\t"
        "v_max_i32_dpp v200, v200, v200 row_ror:8 row_mask:0xf bank_mask:0xf\n\t"
        "s_nop 0\n\t"
        "v_readlane_b32 s88, v200, 0\n\t"
        "v_readlane_b32 s89, v200, 16\n\t"
        "s_max_i32 s88, s88, s89\n\t"
        "s_max_i32 s88, s88, 0\n\t"
        "s_add_u32 %[te], %[te], s88\n\t"
        "s_add_u32 %[tn], %[tn], s88\n\t"
        "s_cbranch_vccz Lu%=\n\t"
        "s_cmp_gt_u32 s97, 4\n\t"
        "s_cbranch_scc1 Lu%=\n\t"
        "v_add_u32 v201, 0x100, v201\n\t"
        "s_cmp_le_u32 s97, %[own]\n\t"
        "ds_read_b32 v200, v201 offset:29696\n\t"
        "s_cbranch_scc1 Ls%=\n\t"
        "s_sub_u32 s90, %[te], %[lk]\n\t"
        "Lt%=:\n\t"
        "s_memrealtime s[88:89]\n\t"
        "s_waitcnt lgkmcnt(0)\n\t"
        "s_mul_i32 s88, s88, 24\n\t"
        "s_sub_u32 s88, s90, s88\n\t"
        "s_cmp_lt_u32 s88, 0x4000\n\t"
        "s_cbranch_scc1 Lt%=\n\t"
        "s_add_u32 s89, %[rk], 20480\n\t"
        "s_cmp_lt_i32 s88, -3000\n\t"
        "s_cselect_b32 s97, 4, s97\n\t"
        "s_cselect_b32 s98, s89, s98\n\t"
        "v_mov_b32 v200, 0\n\t"
        "buffer_load_dword v200, %[ro], %[rs], s98 offen sc1\n\t"
        "s_branch Ls%=\n\t"
        "Lq%=:\n\t"
        "s_sleep 6\n\t"
        "buffer_load_dword v200, %[ro], %[rs], s98 offen sc1\n\t"
        "s_branch Ls%=\n\t"
        "Lu%=:\n\t"
        "v_mov_b32 v200, 0\n\t"
        "s_branch Lg%=\n\t"
        "Lend%=:\n\t"
        : [poll] "+&s"(poll), [lim] "+&s"(lim), [tg] "+&s"(tg), [tol] "+&s"(tol), [qg] "+&s"(qg), [qp] "+&s"(qp), [lv] "=&s"(lv), [so] "+&s"(so), [c0] "+&s"(c0), [r0] "+&s"(r0), [te] "+&s"(te), [tn] "+&s"(tn), [gb] "+&s"(gb), [irr] "+&s"(irr), [lz] "+&s"(lz), [dl] "=&s"(dl), [so2] "+&s"(so2), [left] "+&s"(left)
        : [ki] "s"(ki), [st] "s"(st), [tol2] "s"(tol2), [off] "s"(off), [rn] "s"(rn), [rkn] "s"(rkn), [esc] "s"(esc), [vm] "v"(vm), [lb] "s"(lb), [rs] "s"(rs), [own] "s"(own), [gp] "s"(gp), [kr] "s"(kr), [dw] "s"(dw), [df] "s"(df), [tso] "s"(tso), [tsl] "s"(tsl), [sn] "s"(sn), [tsw] "v"(tsw), [ro] "v"(ro), [rb] "s"(rb), [rk] "s"(rk), [lk] "s"(lk), [lev] "s"(lev), [w0] "{v[16:47]}"(W[0]), [w1] "{v[48:79]}"(W[1]), [w2] "{v[80:111]}"(W[2]), [w3] "{v[112:143]}"(W[3]), [w4] "{v[144:175]}"(W[4]), [va] "v"(va), [sidx] "v"(sidx), [qw] "v"(qw), [kw] "v"(kw), [fv] "s"(fv)
        : "s64", "s65", "s66", "s67", "s68", "s69", "s70", "s71", "s72", "s73", "s74", "s75", "s76", "s77", "s78", "s79", "s80", "s81", "s82", "s83", "s84", "s85", "s86", "s87", "s88", "s89", "s90", "s91", "s92", "s93", "s94", "s95", "s96", "s97", "s98", "s99", "v187", "v188", "v189", "v190", "v191", "v192", "v193", "v194", "v195", "v196", "v197", "v198", "v199", "v200", "v201", "v202", "v203", "v204", "v205", "v206", "v207", "v208", "v209", "v210", "v211", "v212", "v213", "v214", "v215", "v216", "v217", "v218", "v219", "v220", "v221", "v222", "v223", "v224", "v225", "v226", "v227", "v228", "v229", "v230", "v231", "v232", "v233", "v234", "v235", "v236", "v237", "v238", "v239", "v240", "v241", "v242", "v243", "v244", "v245", "v246", "v247", "v248", "v249", "v250", "v251", "v252", "v253", "v254", "v255", "vcc", "scc", "memory");
}

__device__ __forceinline__ void s_g(u32& c0, u32& r0, u32& te, u32& tn, u32& gb, u32 lev, u32 own, u32 gp, u32 lk, u32 kr, u32 dw, u32 df, u32 vm, u32 so, u32 lb, u32x4 rs, u32 tso, u32 tsl, u32 sn, u32 tsw, u32 ro, u32 rb, u32 rk STP) {
    asm volatile(
        ".p2align 6\n\t"
        "s_mov_b32 s94, m0\n\t"
        "s_add_u32 s92, %[so], 0x8000\n\t"
        "s_add_u32 s93, %[so], 0x10000\n\t"
        "s_mov_b32 m0, %[lb]\n\t"
        "v_lshrrev_b32 v201, 2, %[vm]\n\t"
        "v_add_u32 v201, %[lb], v201\n\t"
        "s_waitcnt vmcnt(0)\n\t"
        "ds_read_b32 v200, v201 offset:29696\n\t"
        "s_cmp_lg_u32 %[own], 0\n\t"
        "s_mov_b64 s[96:97], 0\n\t"
        "s_mov_b32 s98, %[rb]\n\t"
        "s_mov_b32 s99, 0x2000\n\t"
        "s_waitcnt lgkmcnt(0)\n\t"
        "v_cmp_gt_i32 vcc, s96, v200\n\t"
        "s_cbranch_scc1 Ls%=\n\t"
        "s_cbranch_vccnz Ls%=\n\t"
        "Lg%=:\n\t"
        "s_add_u32 s95, %[te], %[gp]\n\t"
        "s_add_u32 s95, s95, %[gb]\n\t"
        "Lda_%=:\n\t"
        "s_memrealtime s[90:91]\n\t"
        "s_waitcnt lgkmcnt(0)\n\t"
        "s_mul_i32 s90, s90, 24\n\t"
        "s_sub_u32 s88, s95, s90\n\t"
        "s_sub_u32 s89, s88, 620\n\t"
        "s_cmp_gt_u32 s89, 0x4000\n\t"
        "s_cbranch_scc1 Lea_%=\n\t"
        "s_sleep 1\n\t"
        "s_branch Lda_%=\n\t"
        "Lea_%=:\n\t"
        "s_memrealtime s[64:65]\n\t"
        "s_nop 0\n\t"
        "s_memrealtime s[66:67]\n\t"
        "s_nop 0\n\t"
        "s_memrealtime s[68:69]\n\t"
        "s_nop 0\n\t"
        "s_memrealtime s[70:71]\n\t"
        "s_nop 0\n\t"
        "s_memrealtime s[72:73]\n\t"
        "s_nop 0\n\t"
        "s_memrealtime s[74:75]\n\t"
        "s_memtime s[90:91]\n\t"
        "s_memrealtime s[76:77]\n\t"
        "s_nop 0\n\t"
        "s_memrealtime s[78:79]\n\t"
        "s_nop 0\n\t"
        "s_memrealtime s[80:81]\n\t"
        "s_nop 0\n\t"
        "s_memrealtime s[82:83]\n\t"
        "s_nop 0\n\t"
        "s_memrealtime s[84:85]\n\t"
        "s_nop 0\n\t"
        "s_memrealtime s[86:87]\n\t"
        "s_nop 0\n\t"
        "s_nop 7\n\t"
        "s_waitcnt lgkmcnt(0)\n\t"
        "s_add_u32 s64, s64, s66\n\t"
        "s_add_u32 s64, s64, s68\n\t"
        "s_add_u32 s64, s64, s70\n\t"
        "s_add_u32 s64, s64, s72\n\t"
        "s_add_u32 s64, s64, s74\n\t"
        "s_add_u32 s64, s64, s76\n\t"
        "s_add_u32 s64, s64, s78\n\t"
        "s_add_u32 s64, s64, s80\n\t"
        "s_add_u32 s64, s64, s82\n\t"
        "s_add_u32 s64, s64, s84\n\t"
        "s_add_u32 s64, s64, s86\n\t"
        "s_lshl_b32 s64, s64, 1\n\t"
        "s_mov_b32 %[c0], s90\n\t"
        "s_mov_b32 %[r0], s64\n\t"
        "s_sub_u32 s88, s95, s64\n\t"
        "s_mul_i32 s88, s88, %[kr]\n\t"
        "s_ashr_i32 s88, s88, 12\n\t"
        "s_sub_u32 s88, s88, 244\n\t"
        "s_nop 1\n\t"
        "s_max_i32 s88, s88, 0\n\t"
        "s_min_i32 s88, s88, 384\n\t"
        "s_andn2_b32 s88, s88, 3\n\t"
        "s_getpc_b64 s[90:91]\n\t"
        "Lga_%=:\n\t"
        "s_sub_u32 s90, s90, s88\n\t"
        "s_subb_u32 s91, s91, 0\n\t"
        "s_add_u32 s90, s90, Lza_%=-Lga_%=\n\t"
        "s_addc_u32 s91, s91, 0\n\t"
        "s_setpc_b64 s[90:91]\n\t"
        "s_nop 0\n\ts_nop 0\n\ts_nop 0\n\ts_nop 0\n\t"
        "s_nop 0\n\ts_nop 0\n\ts_nop 0\n\ts_nop 0\n\t"
        "s_nop 0\n\ts_nop 0\n\ts_nop 0\n\ts_nop 0\n\t"
        "s_nop 0\n\ts_nop 0\n\ts_nop 0\n\ts_nop 0\n\t"
        "s_nop 0\n\ts_nop 0\n\ts_nop 0\n\ts_nop 0\n\t"
        "s_nop 0\n\ts_nop 0\n\ts_nop 0\n\ts_nop 0\n\t"
        "s_nop 0\n\ts_nop 0\n\ts_nop 0\n\ts_nop 0\n\t"
        "s_nop 0\n\ts_nop 0\n\ts_nop 0\n\ts_nop 0\n\t"
        "s_nop 0\n\ts_nop 0\n\ts_nop 0\n\ts_nop 0\n\t"
        "s_nop 0\n\ts_nop 0\n\ts_nop 0\n\ts_nop 0\n\t"
        "s_nop 0\n\ts_nop 0\n\ts_nop 0\n\ts_nop 0\n\t"
        "s_nop 0\n\ts_nop 0\n\ts_nop 0\n\ts_nop 0\n\t"
        "s_nop 0\n\ts_nop 0\n\ts_nop 0\n\ts_nop 0\n\t"
        "s_nop 0\n\ts_nop 0\n\ts_nop 0\n\ts_nop 0\n\t"
        "s_nop 0\n\ts_nop 0\n\ts_nop 0\n\ts_nop 0\n\t"
        "s_nop 0\n\ts_nop 0\n\ts_nop 0\n\ts_nop 0\n\t"
        "s_nop 0\n\ts_nop 0\n\ts_nop 0\n\ts_nop 0\n\t"
        "s_nop 0\n\ts_nop 0\n\ts_nop 0\n\ts_nop 0\n\t"
        "s_nop 0\n\ts_nop 0\n\ts_nop 0\n\ts_nop 0\n\t"
        "s_nop 0\n\ts_nop 0\n\ts_nop 0\n\ts_nop 0\n\t"
        "s_nop 0\n\ts_nop 0\n\ts_nop 0\n\ts_nop 0\n\t"
        "s_nop 0\n\ts_nop 0\n\ts_nop 0\n\ts_nop 0\n\t"
        "s_nop 0\n\ts_nop 0\n\ts_nop 0\n\ts_nop 0\n\t"
        "s_nop 0\n\ts_nop 0\n\ts_nop 0\n\ts_nop 0\n\t"
        "Lza_%=:\n\t"
        "buffer_load_dwordx4 %[vm], %[rs], %[so] offen offset:0 sc1 lds\n\t"
        "buffer_load_dwordx4 %[vm], %[rs], %[so] offen offset:1024 sc1 lds\n\t"
        "buffer_load_dwordx4 %[vm], %[rs], %[so] offen offset:2048 sc1 lds\n\t"
        "buffer_load_dwordx4 %[vm], %[rs], %[so] offen offset:3072 sc1 lds\n\t"
        "s_add_u32 m0, m0, 0x1000\n\t"
        "s_nop 0\n\t"
        "buffer_load_dwordx4 %[vm], %[rs], s92 offen offset:0 sc1 lds\n\t"
        "buffer_load_dwordx4 %[vm], %[rs], s92 offen offset:1024 sc1 lds\n\t"
        "buffer_load_dwordx4 %[vm], %[rs], s92 offen offset:2048 sc1 lds\n\t"
        "buffer_load_dwordx4 %[vm], %[rs], s92 offen offset:3072 sc1 lds\n\t"
        "s_add_u32 m0, m0, 0x1000\n\t"
        "s_nop 0\n\t"
        "buffer_load_dwordx4 %[vm], %[rs], s93 offen offset:0 sc1 lds\n\t"
        "buffer_load_dwordx4 %[vm], %[rs], s93 offen offset:2048 sc1 lds\n\t"
        "s_cmp_lg_u32 s96, 0\n\t"
        "s_cbranch_scc1 Lnn%=\n\t"
        "v_readlane_b32 %[gb], v200, 0\n\t"
        "s_and_b32 %[gb], %[gb], 15\n\t"
        "s_lshl_b32 %[gb], %[gb], 4\n\t"
        "v_subrev_u32 v200, %[tn], v200\n\t"
        "v_bfe_i32 v200, v200, 0, 30\n\t"
        "s_nop 1\n\t"
        "v_max_i32_dpp v200, v200, v200 quad_perm:[1,0,3,2] row_mask:0xf bank_mask:0xf\n\t"
        "s_nop 1\n\t"
        "v_max_i32_dpp v200, v200, v200 quad_perm:[2,3,0,1] row_mask:0xf bank_mask:0xf\n\t"
        "s_nop 1\n\t"
        "v_max_i32_dpp v200, v200, v200 row_ror:4 row_mask:0xf bank_mask:0xf\n\t"
        "s_nop 1\n\t"
        "v_max_i32_dpp v200, v200, v200 row_ror:8 row_mask:0xf bank_mask:0xf\n\t"
        "s_nop 0\n\t"
        "v_readlane_b32 s88, v200, 0\n\t"
        "v_readlane_b32 s89, v200, 16\n\t"
        "s_max_i32 s88, s88, s89\n\t"
        "s_add_u32 %[tn], %[tn], s88\n\t"
        "Lnn%=:\n\t"
        "s_add_u32 s88, %[tn], %[dw]\n\t"
        "s_add_u32 s88, s88, %[gb]\n\t"
        "s_add_u32 s89, %[tn], %[gp]\n\t"
        "s_sub_u32 s88, s88, %[lev]\n\t"
        "s_and_b32 s88, s88, 0x3ffffff0\n\t"
        "s_or_b32 s88, s88, %[lev]\n\t"
        "v_mov_b32 v193, %[sn]\n\t"
        "v_mov_b32 v194, s89\n\t"
        "v_mov_b32 v196, s88\n\t"
        "s_mov_b32 exec_hi, 0\n\t"
        "buffer_store_dwordx4 v[196:199], %[tsw], %[rs], %[tso] offen sc1\n\t"
        "buffer_store_dwordx4 v[192:195], %[tsw], %[rs], %[tsl] offen sc1\n\t"
        "s_mov_b32 exec_hi, -1\n\t"
        "s_mov_b32 %[te], %[tn]\n\t"
        "s_add_u32 %[tn], %[tn], %[df]\n\t"
        "s_add_u32 %[tn], %[tn], %[gb]\n\t"
        "s_mov_b32 m0, s94\n\t"
        "s_branch Lend%=\n\t"
        "Ls%=:\n\t"
        "s_mov_b32 s90, 0xbfffffff\n\t"
        "s_cmp_eq_u32 s97, 0\n\t"
        "s_cselect_b32 s90, 0xffbfffbf, s90\n\t"
        "s_mov_b32 s96, 0xc0000000\n\t"
        "s_waitcnt vmcnt(0) lgkmcnt(0)\n\t"
        "v_cmp_eq_u32 s[88:89], s90, v200\n\t"
        "v_cmp_gt_i32 vcc, s96, v200\n\t"
        "v_and_b32 v202, 0xc0000001, v200\n\t"
        "v_subrev_u32 v200, %[te], v200\n\t"
        "s_sub_u32 s99, s99, 1\n\t"
        "s_cselect_b32 s88, 0, s88\n\t"
        "s_or_b32 s89, s88, vcc_lo\n\t"
        "s_cbranch_scc0 Lu%=\n\t"
        "v_bfe_i32 v200, v200, 0, 30\n\t"
        "s_cmp_lg_u32 s88, 0\n\t"
        "v_cndmask_b32 v200, 0, v200, vcc\n\t"
        "s_cbranch_scc1 Lq%=\n\t"
        "v_cmp_eq_u32 vcc, 0x80000000, v202\n\t"
        "s_and_b32 s98, s97, 1\n\t"
        "v_max_i32_dpp v200, v200, v200 quad_perm:[1,0,3,2] row_mask:0xf bank_mask:0xf\n\t"
        "s_lshl_b32 s98, s98, 12\n\t"
        "s_lshr_b32 s88, s97, 1\n\t"
        "v_max_i32_dpp v200, v200, v200 quad_perm:[2,3,0,1] row_mask:0xf bank_mask:0xf\n\t"
        "s_lshl_b32 s88, s88, 14\n\t"
        "s_add_u32 s98, s98, s88\n\t"
        "v_max_i32_dpp v200, v200, v200 row_ror:4 row_mask:0xf bank_mask:0xf\n\t"
        "s_add_u32 s98, s98, %[rk]\n\t"
        "s_add_u32 s97, s97, 1\n\t"
        "v_max_i32_dpp v200, v200, v200 row_ror:8 row_mask:0xf bank_mask:0xf\n\t"
        "s_nop 0\n\t"
        "v_readlane_b32 s88, v200, 0\n\t"
        "v_readlane_b32 s89, v200, 16\n\t"
        "s_max_i32 s88, s88, s89\n\t"
        "s_max_i32 s88, s88, 0\n\t"
        "s_add_u32 %[te], %[te], s88\n\t"
        "s_add_u32 %[tn], %[tn], s88\n\t"
        "s_cbranch_vccz Lu%=\n\t"
        "s_cmp_gt_u32 s97, 4\n\t"
        "s_cbranch_scc1 Lu%=\n\t"
        "v_add_u32 v201, 0x100, v201\n\t"
        "s_cmp_le_u32 s97, %[own]\n\t"
        "ds_read_b32 v200, v201 offset:29696\n\t"
        "s_cbranch_scc1 Ls%=\n\t"
        "s_sub_u32 s90, %[te], %[lk]\n\t"
        "Lt%=:\n\t"
        "s_memrealtime s[88:89]\n\t"
        "s_waitcnt lgkmcnt(0)\n\t"
        "s_mul_i32 s88, s88, 24\n\t"
        "s_sub_u32 s88, s90, s88\n\t"
        "s_cmp_lt_u32 s88, 0x4000\n\t"
        "s_cbranch_scc1 Lt%=\n\t"
        "s_add_u32 s89, %[rk], 20480\n\t"
        "s_cmp_lt_i32 s88, -3000\n\t"
        "s_cselect_b32 s97, 4, s97\n\t"
        "s_cselect_b32 s98, s89, s98\n\t"
        "v_mov_b32 v200, 0\n\t"
        "buffer_load_dword v200, %[ro], %[rs], s98 offen sc1\n\t"
        "s_branch Ls%=\n\t"
        "Lq%=:\n\t"
        "s_sleep 6\n\t"
        "buffer_load_dword v200, %[ro], %[rs], s98 offen sc1\n\t"
        "s_branch Ls%=\n\t"
        "Lu%=:\n\t"
        "v_mov_b32 v200, 0\n\t"
        "s_branch Lg%=\n\t"
        "Lend%=:\n\t"
        : [so] "+&s"(so), [c0] "+&s"(c0), [r0] "+&s"(r0), [te] "+&s"(te), [tn] "+&s"(tn), [gb] "+&s"(gb)
        : [vm] "v"(vm), [lb] "s"(lb), [rs] "s"(rs), [own] "s"(own), [gp] "s"(gp), [kr] "s"(kr), [dw] "s"(dw), [df] "s"(df), [tso] "s"(tso), [tsl] "s"(tsl), [sn] "s"(sn), [tsw] "v"(tsw), [ro] "v"(ro), [rb] "s"(rb), [rk] "s"(rk), [lk] "s"(lk), [lev] "s"(lev)
        : "s64", "s65", "s66", "s67", "s68", "s69", "s70", "s71", "s72", "s73", "s74", "s75", "s76", "s77", "s78", "s79", "s80", "s81", "s82", "s83", "s84", "s85", "s86", "s87", "s88", "s89", "s90", "s91", "s92", "s93", "s94", "s95", "s96", "s97", "s98", "s99", "v192", "v193", "v194", "v195", "v196", "v197", "v198", "v199", "v200", "v201", "v202", "vcc", "scc", "memory");
}

__device__ __forceinline__ void s_l(const u32x32 (&W)[5], u32 va, u32 lz, u32 sidx, u32 qw, u32 kw, u32 fv, u32& redo) {
    u32 dl;
    asm volatile(
        ".p2align 6\n\t"
        "v_mov_b32 v206, %[va]\n\t"
        "s_mov_b32 %[dl], 81920\n\t"
        "v_mov_b64 v[248:249], 0\n\t"
        "v_mov_b64 v[250:251], 0\n\t"
        "v_mov_b64 v[252:253], 0\n\t"
        "v_mov_b64 v[254:255], 0\n\t"
        "v_mov_b32 v205, %[fv]\n\t"
        "s_waitcnt vmcnt(0)\n\t"
        "s_barrier\n\t"
        "ds_read_b128 v[208:211], v206 offset:0\n\t"
        "ds_read_b128 v[212:215], v206 offset:1024\n\t"
        "ds_read_b128 v[216:219], v206 offset:2048\n\t"
        "ds_read_b128 v[220:223], v206 offset:3072\n\t"
        "ds_read_b128 v[224:227], v206 offset:4096\n\t"
        "ds_read_b128 v[228:231], v206 offset:5120\n\t"
        "ds_read_b128 v[232:235], v206 offset:6144\n\t"
        "ds_read_b128 v[236:239], v206 offset:7168\n\t"
        "ds_read_b128 v[240:243], v206 offset:8192\n\t"
        "ds_read_b128 v[244:247], v206 offset:10240\n\t"
        "s_waitcnt lgkmcnt(9)\n\t"
        "v_smfmac_f32_16x16x64_bf16 v[252:255], v[208:211], v[96:103], %[sidx]\n\t"
        "s_waitcnt lgkmcnt(8)\n\t"
        "v_smfmac_f32_16x16x64_bf16 v[252:255], v[212:215], v[104:111], %[sidx]\n\t"
        "s_waitcnt lgkmcnt(7)\n\t"
        "v_smfmac_f32_16x16x64_bf16 v[252:255], v[216:219], v[112:119], %[sidx]\n\t"
        "s_waitcnt lgkmcnt(6)\n\t"
        "v_smfmac_f32_16x16x64_bf16 v[252:255], v[220:223], v[120:127], %[sidx]\n\t"
        "s_waitcnt lgkmcnt(5)\n\t"
        "v_smfmac_f32_16x16x64_bf16 v[252:255], v[224:227], v[128:135], %[sidx]\n\t"
        "s_waitcnt lgkmcnt(4)\n\t"
        "v_smfmac_f32_16x16x64_bf16 v[252:255], v[228:231], v[136:143], %[sidx]\n\t"
        "s_waitcnt lgkmcnt(3)\n\t"
        "v_smfmac_f32_16x16x64_bf16 v[252:255], v[232:235], v[144:151], %[sidx]\n\t"
        "s_waitcnt lgkmcnt(2)\n\t"
        "v_smfmac_f32_16x16x64_bf16 v[252:255], v[236:239], v[152:159], %[sidx]\n\t"
        "s_waitcnt lgkmcnt(1)\n\t"
        "v_smfmac_f32_16x16x64_bf16 v[252:255], v[240:243], v[160:167], %[sidx]\n\t"
        "s_waitcnt lgkmcnt(0)\n\t"
        "v_smfmac_f32_16x16x64_bf16 v[252:255], v[244:247], v[168:175], %[sidx]\n\t"
        "s_nop 7\n\t"
        "v_add_f32 v252, v252, v254\n\t"
        "v_add_f32 v253, v253, v255\n\t"
        "v_cvt_pk_bf16_f32 v204, v252, v253\n\t"
        "ds_write_b64 %[kw], v[204:205]\n\t"
        "v_cmp_u_f32 vcc, v252, v253\n\t"
        "s_cbranch_vccnz Lnan%=\n\t"
        "Lok%=:\n\t"
        "s_waitcnt lgkmcnt(0)\n\t"
        "s_barrier\n\t"
        "s_branch Lend%=\n\t"
        "Lnan%=:\n\t"
        "v_max_u32 v207, v208, v209\n\t"
        "v_max3_u32 v207, v207, v210, v211\n\t"
        "v_max3_u32 v207, v207, v212, v213\n\t"
        "v_max3_u32 v207, v207, v214, v215\n\t"
        "v_max3_u32 v207, v207, v216, v217\n\t"
        "v_max3_u32 v207, v207, v218, v219\n\t"
        "v_max3_u32 v207, v207, v220, v221\n\t"
        "v_max3_u32 v207, v207, v222, v223\n\t"
        "v_max3_u32 v207, v207, v224, v225\n\t"
        "v_max3_u32 v207, v207, v226, v227\n\t"
        "v_max3_u32 v207, v207, v228, v229\n\t"
        "v_max3_u32 v207, v207, v230, v231\n\t"
        "v_max3_u32 v207, v207, v232, v233\n\t"
        "v_max3_u32 v207, v207, v234, v235\n\t"
        "v_max3_u32 v207, v207, v236, v237\n\t"
        "v_max3_u32 v207, v207, v238, v239\n\t"
        "v_max3_u32 v207, v207, v240, v241\n\t"
        "v_max3_u32 v207, v207, v242, v243\n\t"
        "v_max3_u32 v207, v207, v244, v245\n\t"
        "v_max3_u32 v207, v207, v246, v247\n\t"
        "v_cmp_eq_u32 vcc, 0xffbfffbf, v207\n\t"
        "s_cbranch_vccz Lok%=\n\t"
        "Lredo%=:\n\t"
        "s_waitcnt lgkmcnt(0)\n\t"
        "s_mov_b32 %[redo], 1\n\t"
        "s_barrier\n\t"
        "s_branch Lend%=\n\t"
        "Lend%=:\n\t"
        : [lz] "+&s"(lz), [dl] "=&s"(dl), [redo] "+&s"(redo)
        : [w0] "{v[16:47]}"(W[0]), [w1] "{v[48:79]}"(W[1]), [w2] "{v[80:111]}"(W[2]), [w3] "{v[112:143]}"(W[3]), [w4] "{v[144:175]}"(W[4]), [va] "v"(va), [sidx] "v"(sidx), [qw] "v"(qw), [kw] "v"(kw), [fv] "s"(fv)
        : "v200", "v201", "v202", "v203", "v204", "v205", "v206", "v207", "v208", "v209", "v210", "v211", "v212", "v213", "v214", "v215", "v216", "v217", "v218", "v219", "v220", "v221", "v222", "v223", "v224", "v225", "v226", "v227", "v228", "v229", "v230", "v231", "v232", "v233", "v234", "v235", "v236", "v237", "v238", "v239", "v240", "v241", "v242", "v243", "v244", "v245", "v246", "v247", "v248", "v249", "v250", "v251", "v252", "v253", "v254", "v255", "vcc", "scc", "memory");
}

__device__ __forceinline__ void gather(u32 tg, u32 kr, u32 vm, u32 so, u32 lb, u32x4 rs) {
    asm volatile(
        ".p2align 6\n\t"
        "s_mov_b32 s94, m0\n\t"
        "s_add_u32 s92, %[so], 0x8000\n\t"
        "s_add_u32 s93, %[so], 0x10000\n\t"
        "s_mov_b32 m0, %[lb]\n\t"
        "Lda_%=:\n\t"
        "s_memrealtime s[90:91]\n\t"
        "s_waitcnt lgkmcnt(0)\n\t"
        "s_mul_i32 s90, s90, 24\n\t"
        "s_sub_u32 s88, %[tg], s90\n\t"
        "s_sub_u32 s89, s88, 620\n\t"
        "s_cmp_gt_u32 s89, 0x4000\n\t"
        "s_cbranch_scc1 Lea_%=\n\t"
        "s_sleep 1\n\t"
        "s_branch Lda_%=\n\t"
        "Lea_%=:\n\t"
        "s_cmp_lt_i32 s88, 120\n\t"
        "s_cbranch_scc1 Lza_%=\n\t"
        "s_memrealtime s[64:65]\n\t"
        "s_nop 0\n\t"
        "s_memrealtime s[66:67]\n\t"
        "s_nop 0\n\t"
        "s_memrealtime s[68:69]\n\t"
        "s_nop 0\n\t"
        "s_memrealtime s[70:71]\n\t"
        "s_nop 0\n\t"
        "s_memrealtime s[72:73]\n\t"
        "s_nop 0\n\t"
        "s_memrealtime s[74:75]\n\t"
        "s_memtime s[90:91]\n\t"
        "s_memrealtime s[76:77]\n\t"
        "s_nop 0\n\t"
        "s_memrealtime s[78:79]\n\t"
        "s_nop 0\n\t"
        "s_memrealtime s[80:81]\n\t"
        "s_nop 0\n\t"
        "s_memrealtime s[82:83]\n\t"
        "s_nop 0\n\t"
        "s_memrealtime s[84:85]\n\t"
        "s_nop 0\n\t"
        "s_memrealtime s[86:87]\n\t"
        "s_nop 0\n\t"
        "s_nop 7\n\t"
        "s_waitcnt lgkmcnt(0)\n\t"
        "s_add_u32 s64, s64, s66\n\t"
        "s_add_u32 s64, s64, s68\n\t"
        "s_add_u32 s64, s64, s70\n\t"
        "s_add_u32 s64, s64, s72\n\t"
        "s_add_u32 s64, s64, s74\n\t"
        "s_add_u32 s64, s64, s76\n\t"
        "s_add_u32 s64, s64, s78\n\t"
        "s_add_u32 s64, s64, s80\n\t"
        "s_add_u32 s64, s64, s82\n\t"
        "s_add_u32 s64, s64, s84\n\t"
        "s_add_u32 s64, s64, s86\n\t"
        "s_lshl_b32 s64, s64, 1\n\t"
        "s_nop 1\n\t"
        "s_sub_u32 s88, %[tg], s64\n\t"
        "s_mul_i32 s88, s88, %[kr]\n\t"
        "s_ashr_i32 s88, s88, 12\n\t"
        "s_sub_u32 s88, s88, 244\n\t"
        "s_nop 1\n\t"
        "s_max_i32 s88, s88, 0\n\t"
        "s_min_i32 s88, s88, 384\n\t"
        "s_andn2_b32 s88, s88, 3\n\t"
        "s_getpc_b64 s[90:91]\n\t"
        "Lga_%=:\n\t"
        "s_sub_u32 s90, s90, s88\n\t"
        "s_subb_u32 s91, s91, 0\n\t"
        "s_add_u32 s90, s90, Lza_%=-Lga_%=\n\t"
        "s_addc_u32 s91, s91, 0\n\t"
        "s_setpc_b64 s[90:91]\n\t"
        "s_nop 0\n\ts_nop 0\n\ts_nop 0\n\ts_nop 0\n\t"
        "s_nop 0\n\ts_nop 0\n\ts_nop 0\n\ts_nop 0\n\t"
        "s_nop 0\n\ts_nop 0\n\ts_nop 0\n\ts_nop 0\n\t"
        "s_nop 0\n\ts_nop 0\n\ts_nop 0\n\ts_nop 0\n\t"
        "s_nop 0\n\ts_nop 0\n\ts_nop 0\n\ts_nop 0\n\t"
        "s_nop 0\n\ts_nop 0\n\ts_nop 0\n\ts_nop 0\n\t"
        "s_nop 0\n\ts_nop 0\n\ts_nop 0\n\ts_nop 0\n\t"
        "s_nop 0\n\ts_nop 0\n\ts_nop 0\n\ts_nop 0\n\t"
        "s_nop 0\n\ts_nop 0\n\ts_nop 0\n\ts_nop 0\n\t"
        "s_nop 0\n\ts_nop 0\n\ts_nop 0\n\ts_nop 0\n\t"
        "s_nop 0\n\ts_nop 0\n\ts_nop 0\n\ts_nop 0\n\t"
        "s_nop 0\n\ts_nop 0\n\ts_nop 0\n\ts_nop 0\n\t"
        "s_nop 0\n\ts_nop 0\n\ts_nop 0\n\ts_nop 0\n\t"
        "s_nop 0\n\ts_nop 0\n\ts_nop 0\n\ts_nop 0\n\t"
        "s_nop 0\n\ts_nop 0\n\ts_nop 0\n\ts_nop 0\n\t"
        "s_nop 0\n\ts_nop 0\n\ts_nop 0\n\ts_nop 0\n\t"
        "s_nop 0\n\ts_nop 0\n\ts_nop 0\n\ts_nop 0\n\t"
        "s_nop 0\n\ts_nop 0\n\ts_nop 0\n\ts_nop 0\n\t"
        "s_nop 0\n\ts_nop 0\n\ts_nop 0\n\ts_nop 0\n\t"
        "s_nop 0\n\ts_nop 0\n\ts_nop 0\n\ts_nop 0\n\t"
        "s_nop 0\n\ts_nop 0\n\ts_nop 0\n\ts_nop 0\n\t"
        "s_nop 0\n\ts_nop 0\n\ts_nop 0\n\ts_nop 0\n\t"
        "s_nop 0\n\ts_nop 0\n\ts_nop 0\n\ts_nop 0\n\t"
        "s_nop 0\n\ts_nop 0\n\ts_nop 0\n\ts_nop 0\n\t"
        "Lza_%=:\n\t"
        "buffer_load_dwordx4 %[vm], %[rs], %[so] offen offset:0 sc1 lds\n\t"
        "buffer_load_dwordx4 %[vm], %[rs], %[so] offen offset:1024 sc1 lds\n\t"
        "buffer_load_dwordx4 %[vm], %[rs], %[so] offen offset:2048 sc1 lds\n\t"
        "buffer_load_dwordx4 %[vm], %[rs], %[so] offen offset:3072 sc1 lds\n\t"
        "s_add_u32 m0, m0, 0x1000\n\t"
        "s_nop 0\n\t"
        "buffer_load_dwordx4 %[vm], %[rs], s92 offen offset:0 sc1 lds\n\t"
        "buffer_load_dwordx4 %[vm], %[rs], s92 offen offset:1024 sc1 lds\n\t"
        "buffer_load_dwordx4 %[vm], %[rs], s92 offen offset:2048 sc1 lds\n\t"
        "buffer_load_dwordx4 %[vm], %[rs], s92 offen offset:3072 sc1 lds\n\t"
        "s_add_u32 m0, m0, 0x1000\n\t"
        "s_nop 0\n\t"
        "buffer_load_dwordx4 %[vm], %[rs], s93 offen offset:0 sc1 lds\n\t"
        "buffer_load_dwordx4 %[vm], %[rs], s93 offen offset:2048 sc1 lds\n\t"
        "s_mov_b32 m0, s94\n\t"
        :
        : [rs] "s"(rs), [tg] "s"(tg), [kr] "s"(kr), [vm] "v"(vm), [lb] "s"(lb), [so] "s"(so)
        : "s64", "s65", "s66", "s67", "s68", "s69", "s70", "s71", "s72", "s73", "s74", "s75", "s76", "s77", "s78", "s79", "s80", "s81", "s82", "s83", "s84", "s85", "s86", "s87", "s88", "s89", "s90", "s91", "s92", "s93", "s94", "vcc", "scc", "memory");
}

__device__ __forceinline__ void calib(u32& c0, u32& r0) {
    asm volatile(
        "s_memrealtime s[64:65]\n\t"
        "s_nop 0\n\t"
        "s_memrealtime s[66:67]\n\t"
        "s_nop 0\n\t"
        "s_memrealtime s[68:69]\n\t"
        "s_nop 0\n\t"
        "s_memrealtime s[70:71]\n\t"
        "s_nop 0\n\t"
        "s_memrealtime s[72:73]\n\t"
        "s_nop 0\n\t"
        "s_memrealtime s[74:75]\n\t"
        "s_memtime s[90:91]\n\t"
        "s_memrealtime s[76:77]\n\t"
        "s_nop 0\n\t"
        "s_memrealtime s[78:79]\n\t"
        "s_nop 0\n\t"
        "s_memrealtime s[80:81]\n\t"
        "s_nop 0\n\t"
        "s_memrealtime s[82:83]\n\t"
        "s_nop 0\n\t"
        "s_memrealtime s[84:85]\n\t"
        "s_nop 0\n\t"
        "s_memrealtime s[86:87]\n\t"
        "s_nop 0\n\t"
        "s_nop 7\n\t"
        "s_waitcnt lgkmcnt(0)\n\t"
        "s_add_u32 s64, s64, s66\n\t"
        "s_add_u32 s64, s64, s68\n\t"
        "s_add_u32 s64, s64, s70\n\t"
        "s_add_u32 s64, s64, s72\n\t"
        "s_add_u32 s64, s64, s74\n\t"
        "s_add_u32 s64, s64, s76\n\t"
        "s_add_u32 s64, s64, s78\n\t"
        "s_add_u32 s64, s64, s80\n\t"
        "s_add_u32 s64, s64, s82\n\t"
        "s_add_u32 s64, s64, s84\n\t"
        "s_add_u32 s64, s64, s86\n\t"
        "s_lshl_b32 s64, s64, 1\n\t"
        "s_mov_b32 %[c0], s90\n\t"
        "s_mov_b32 %[r0], s64\n\t"
        : [c0] "=&s"(c0), [r0] "=&s"(r0)
        :
        : "s64", "s65", "s66", "s67", "s68", "s69", "s70", "s71", "s72", "s73", "s74", "s75", "s76", "s77", "s78", "s79", "s80", "s81", "s82", "s83", "s84", "s85", "s86", "s87", "s88", "s89", "s90", "s91", "vcc", "scc", "memory");
}

__device__ __forceinline__ void px_step(u32x4& p0, u32& p1, u32 ka, u32 qa, u32 ma, u32 ktr, float ce) {
    asm volatile(
        ".p2align 6\n\t"
        "ds_read_b128 v[240:243], %[ma] offset:0\n\t"
        "ds_read_b128 v[244:247], %[ma] offset:1024\n\t"
        "ds_read_b128 v[208:211], %[qa] offset:0\n\t"
        "ds_read_b128 v[224:227], %[ka] offset:0\n\t"
        "ds_read_b128 v[212:215], %[qa] offset:1024\n\t"
        "ds_read_b128 v[228:231], %[ka] offset:1024\n\t"
        "ds_read_b128 v[216:219], %[qa] offset:2048\n\t"
        "ds_read_b128 v[232:235], %[ka] offset:2048\n\t"
        "ds_read_b128 v[220:223], %[qa] offset:3072\n\t"
        "ds_read_b128 v[236:239], %[ka] offset:3072\n\t"
        "s_waitcnt lgkmcnt(6)\n\t"
        "v_mfma_f32_16x16x32_bf16 v[240:243], v[224:227], v[208:211], v[240:243]\n\t"
        "ds_read_b128 v[224:227], %[ka] offset:4096\n\t"
        "s_waitcnt lgkmcnt(5)\n\t"
        "v_mfma_f32_16x16x32_bf16 v[240:243], v[228:231], v[212:215], v[240:243]\n\t"
        "ds_read_b128 v[228:231], %[ka] offset:5120\n\t"
        "s_waitcnt lgkmcnt(4)\n\t"
        "v_mfma_f32_16x16x32_bf16 v[240:243], v[232:235], v[216:219], v[240:243]\n\t"
        "ds_read_b128 v[232:235], %[ka] offset:6144\n\t"
        "s_waitcnt lgkmcnt(3)\n\t"
        "v_mfma_f32_16x16x32_bf16 v[240:243], v[236:239], v[220:223], v[240:243]\n\t"
        "ds_read_b128 v[236:239], %[ka] offset:7168\n\t"
        "s_waitcnt lgkmcnt(3)\n\t"
        "v_mfma_f32_16x16x32_bf16 v[244:247], v[224:227], v[208:211], v[244:247]\n\t"
        "ds_read_b64_tr_b16 v[224:225], %[ktr] offset:0\n\t"
        "ds_read_b64_tr_b16 v[226:227], %[ktr] offset:768\n\t"
        "s_waitcnt lgkmcnt(4)\n\t"
        "v_mfma_f32_16x16x32_bf16 v[244:247], v[228:231], v[212:215], v[244:247]\n\t"
        "ds_read_b64_tr_b16 v[228:229], %[ktr] offset:1536\n\t"
        "ds_read_b64_tr_b16 v[230:231], %[ktr] offset:2304\n\t"
        "s_waitcnt lgkmcnt(5)\n\t"
        "v_mfma_f32_16x16x32_bf16 v[244:247], v[232:235], v[216:219], v[244:247]\n\t"
        "ds_read_b64_tr_b16 v[232:233], %[ktr] offset:3072\n\t"
        "ds_read_b64_tr_b16 v[234:235], %[ktr] offset:3840\n\t"
        "s_waitcnt lgkmcnt(6)\n\t"
        "v_mfma_f32_16x16x32_bf16 v[244:247], v[236:239], v[220:223], v[244:247]\n\t"
        "ds_read_b64_tr_b16 v[236:237], %[ktr] offset:4608\n\t"
        "ds_read_b64_tr_b16 v[238:239], %[ktr] offset:5376\n\t"
        "s_waitcnt lgkmcnt(6)\n\t"
        "v_mfma_f32_16x16x32_bf16 v[248:251], v[224:227], v[208:211], 0\n\t"
        "s_waitcnt lgkmcnt(4)\n\t"
        "v_mfma_f32_16x16x32_bf16 v[248:251], v[228:231], v[212:215], v[248:251]\n\t"
        "s_waitcnt lgkmcnt(2)\n\t"
        "v_mfma_f32_16x16x32_bf16 v[248:251], v[232:235], v[216:219], v[248:251]\n\t"
        "s_waitcnt lgkmcnt(0)\n\t"
        "v_mfma_f32_16x16x32_bf16 v[248:251], v[236:239], v[220:223], v[248:251]\n\t"
        "v_max3_f32 v253, v240, v241, v242\n\t"
        "v_max3_f32 v253, v253, v243, v244\n\t"
        "v_max3_f32 v253, v253, v245, v246\n\t"
        "v_max_f32 v253, v253, v247\n\t"
        "s_nop 3\n\t"
        "v_mul_f32 v248, %[ce], v248\n\t"
        "v_mul_f32 v249, %[ce], v249\n\t"
        "v_max3_f32 v253, v253, v248, v249\n\t"
        "v_mov_b32 v254, v253\n\t"
        "s_nop 1\n\t"
        "v_permlane16_swap_b32 v253, v254\n\t"
        "v_max_f32 v253, v253, v254\n\t"
        "v_mov_b32 v254, v253\n\t"
        "s_nop 1\n\t"
        "v_permlane32_swap_b32 v253, v254\n\t"
        "v_max_f32 v253, v253, v254\n\t"
        "v_sub_f32 v240, v240, v253\n\t"
        "v_sub_f32 v241, v241, v253\n\t"
        "v_sub_f32 v242, v242, v253\n\t"
        "v_sub_f32 v243, v243, v253\n\t"
        "v_sub_f32 v244, v244, v253\n\t"
        "v_sub_f32 v245, v245, v253\n\t"
        "v_sub_f32 v246, v246, v253\n\t"
        "v_sub_f32 v247, v247, v253\n\t"
        "v_sub_f32 v248, v248, v253\n\t"
        "v_sub_f32 v249, v249, v253\n\t"
        "v_exp_f32 v240, v240\n\t"
        "v_exp_f32 v241, v241\n\t"
        "v_exp_f32 v242, v242\n\t"
        "v_exp_f32 v243, v243\n\t"
        "v_exp_f32 v244, v244\n\t"
        "v_exp_f32 v245, v245\n\t"
        "v_exp_f32 v246, v246\n\t"
        "v_exp_f32 v247, v247\n\t"
        "v_exp_f32 v248, v248\n\t"
        "v_exp_f32 v249, v249\n\t"
        "v_pk_add_f32 v[252:253], v[240:241], v[242:243]\n\t"
        "v_pk_add_f32 v[254:255], v[244:245], v[246:247]\n\t"
        "v_pk_add_f32 v[252:253], v[252:253], v[248:249]\n\t"
        "v_pk_add_f32 v[252:253], v[252:253], v[254:255]\n\t"
        "v_add_f32 v252, v252, v253\n\t"
        "v_mov_b32 v253, v252\n\t"
        "s_nop 1\n\t"
        "v_permlane16_swap_b32 v252, v253\n\t"
        "v_add_f32 v252, v252, v253\n\t"
        "v_mov_b32 v253, v252\n\t"
        "s_nop 1\n\t"
        "v_permlane32_swap_b32 v252, v253\n\t"
        "v_add_f32 v252, v252, v253\n\t"
        "v_rcp_f32 v252, v252\n\t"
        "s_nop 0\n\t"
        "v_pk_mul_f32 v[240:241], v[240:241], v[252:253] op_sel_hi:[1,0]\n\t"
        "v_pk_mul_f32 v[242:243], v[242:243], v[252:253] op_sel_hi:[1,0]\n\t"
        "v_pk_mul_f32 v[244:245], v[244:245], v[252:253] op_sel_hi:[1,0]\n\t"
        "v_pk_mul_f32 v[246:247], v[246:247], v[252:253] op_sel_hi:[1,0]\n\t"
        "v_pk_mul_f32 v[248:249], v[248:249], v[252:253] op_sel_hi:[1,0]\n\t"
        "v_cvt_pk_bf16_f32 v192, v240, v241\n\t"
        "v_cvt_pk_bf16_f32 v193, v242, v243\n\t"
        "v_cvt_pk_bf16_f32 v194, v244, v245\n\t"
        "v_cvt_pk_bf16_f32 v195, v246, v247\n\t"
        "v_cvt_pk_bf16_f32 v196, v248, v249\n\t"
        : [p0] "=&{v[192:195]}"(p0), [p1] "=&{v196}"(p1)
        : [ka] "v"(ka), [qa] "v"(qa), [ma] "v"(ma), [ktr] "v"(ktr), [ce] "s"(ce)
        : "v200", "v201", "v202", "v203", "v204", "v205", "v206", "v207", "v208", "v209", "v210", "v211", "v212", "v213", "v214", "v215", "v216", "v217", "v218", "v219", "v220", "v221", "v222", "v223", "v224", "v225", "v226", "v227", "v228", "v229", "v230", "v231", "v232", "v233", "v234", "v235", "v236", "v237", "v238", "v239", "v240", "v241", "v242", "v243", "v244", "v245", "v246", "v247", "v248", "v249", "v250", "v251", "v252", "v253", "v254", "v255", "vcc", "scc", "memory");
}

__device__ __forceinline__ void v_a(const u32x32 (&W)[5], u32 va, u32 lz, u32 sidx, u32 vw STP) {
    u32 dl;
    asm volatile(
        ".p2align 6\n\t"
        "v_mov_b32 v206, %[va]\n\t"
        "s_mov_b32 %[dl], 81920\n\t"
        "v_mov_b64 v[248:249], 0\n\t"
        "v_mov_b64 v[250:251], 0\n\t"
        "v_mov_b64 v[252:253], 0\n\t"
        "v_mov_b64 v[254:255], 0\n\t"
        "s_setprio 3\n\t"
        "Lb1%=:\n\t"
        "s_barrier\n\t"
        "ds_read_b128 v[208:211], v206 offset:0\n\t"
        "ds_read_b128 v[212:215], v206 offset:1024\n\t"
        "ds_read_b128 v[216:219], v206 offset:2048\n\t"
        "ds_read_b128 v[220:223], v206 offset:3072\n\t"
        "ds_read_b128 v[224:227], v206 offset:4096\n\t"
        "ds_read_b128 v[228:231], v206 offset:5120\n\t"
        "s_waitcnt lgkmcnt(5)\n\t"
        "v_smfmac_f32_16x16x64_bf16 v[248:251], v[208:211], v[16:23], %[sidx]\n\t"
        "ds_read_b128 v[232:235], v206 offset:6144\n\t"
        "s_waitcnt lgkmcnt(5)\n\t"
        "v_smfmac_f32_16x16x64_bf16 v[248:251], v[212:215], v[24:31], %[sidx]\n\t"
        "ds_read_b128 v[236:239], v206 offset:7168\n\t"
        "s_waitcnt lgkmcnt(5)\n\t"
        "v_smfmac_f32_16x16x64_bf16 v[248:251], v[216:219], v[32:39], %[sidx]\n\t"
        "ds_read_b128 v[240:243], v206 offset:8192\n\t"
        "s_waitcnt lgkmcnt(5)\n\t"
        "v_smfmac_f32_16x16x64_bf16 v[248:251], v[220:223], v[40:47], %[sidx]\n\t"
        "ds_read_b128 v[244:247], v206 offset:10240\n\t"
        "s_waitcnt lgkmcnt(5)\n\t"
        "v_smfmac_f32_16x16x64_bf16 v[248:251], v[224:227], v[48:55], %[sidx]\n\t"
        "s_waitcnt lgkmcnt(4)\n\t"
        "v_smfmac_f32_16x16x64_bf16 v[248:251], v[228:231], v[56:63], %[sidx]\n\t"
        "s_waitcnt lgkmcnt(3)\n\t"
        "v_smfmac_f32_16x16x64_bf16 v[248:251], v[232:235], v[64:71], %[sidx]\n\t"
        "s_waitcnt lgkmcnt(2)\n\t"
        "v_smfmac_f32_16x16x64_bf16 v[248:251], v[236:239], v[72:79], %[sidx]\n\t"
        "s_waitcnt lgkmcnt(1)\n\t"
        "v_smfmac_f32_16x16x64_bf16 v[248:251], v[240:243], v[80:87], %[sidx]\n\t"
        "s_waitcnt lgkmcnt(0)\n\t"
        "v_smfmac_f32_16x16x64_bf16 v[248:251], v[244:247], v[88:95], %[sidx]\n\t"
        "v_smfmac_f32_16x16x64_bf16 v[252:255], v[208:211], v[96:103], %[sidx]\n\t"
        "v_smfmac_f32_16x16x64_bf16 v[252:255], v[212:215], v[104:111], %[sidx]\n\t"
        "v_add_f32 v248, v248, v250\n\t"
        "v_smfmac_f32_16x16x64_bf16 v[252:255], v[216:219], v[112:119], %[sidx]\n\t"
        "v_add_f32 v249, v249, v251\n\t"
        "v_smfmac_f32_16x16x64_bf16 v[252:255], v[220:223], v[120:127], %[sidx]\n\t"
        "v_cvt_pk_bf16_f32 v204, v248, v249\n\t"
        "ds_write_b32 %[vw], v204\n\t"
        "v_smfmac_f32_16x16x64_bf16 v[252:255], v[224:227], v[128:135], %[sidx]\n\t"
        "v_cmp_u_f32 vcc, v248, v249\n\t"
        "v_smfmac_f32_16x16x64_bf16 v[252:255], v[228:231], v[136:143], %[sidx]\n\t"
        "v_smfmac_f32_16x16x64_bf16 v[252:255], v[232:235], v[144:151], %[sidx]\n\t"
        "v_smfmac_f32_16x16x64_bf16 v[252:255], v[236:239], v[152:159], %[sidx]\n\t"
        "s_waitcnt lgkmcnt(0)\n\t"
        "s_barrier\n\t"
        "v_smfmac_f32_16x16x64_bf16 v[252:255], v[240:243], v[160:167], %[sidx]\n\t"
        "v_smfmac_f32_16x16x64_bf16 v[252:255], v[244:247], v[168:175], %[sidx]\n\t"
        "s_cbranch_vccnz Lnan%=\n\t"
        "Lok%=:\n\t"
        "s_nop 6\n\t"
        "v_add_f32 v252, v252, v254\n\t"
        "v_add_f32 v253, v253, v255\n\t"
        "v_cvt_pk_bf16_f32 v205, v252, v253\n\t"
        "ds_write_b32 %[vw], v205 offset:1024\n\t"
        "s_waitcnt lgkmcnt(0)\n\t"
        "s_barrier\n\t"
        "s_setprio 0\n\t"
        "s_sleep 6\n\t"
        "s_branch Lend%=\n\t"
        "Lnan%=:\n\t"
        "v_mbcnt_lo_u32_b32 v202, -1, 0\n\t"
        "v_mbcnt_hi_u32_b32 v202, -1, v202\n\t"
        "v_lshl_add_u32 v207, v202, 7, %[lz]\n\t"
        "v_and_b32 v202, 31, v202\n\t"
        "v_lshl_add_u32 v207, v202, 2, v207\n\t"
        "ds_read2st64_b32 v[202:203], v207 offset1:32\n\t"
        "s_waitcnt lgkmcnt(0)\n\t"
        "s_mov_b64 exec, 0x00ff00ff\n\t"
        "v_max_u32 v202, v202, v203\n\t"
        "s_mov_b64 exec, -1\n\t"
        "v_cmp_eq_u32 vcc, 0xffbfffbf, v202\n\t"
        "s_cbranch_vccnz Lredo%=\n\t"
        "v_max_u32 v207, v208, v209\n\t"
        "v_max3_u32 v207, v207, v210, v211\n\t"
        "v_max3_u32 v207, v207, v212, v213\n\t"
        "v_max3_u32 v207, v207, v214, v215\n\t"
        "v_max3_u32 v207, v207, v216, v217\n\t"
        "v_max3_u32 v207, v207, v218, v219\n\t"
        "v_max3_u32 v207, v207, v220, v221\n\t"
        "v_max3_u32 v207, v207, v222, v223\n\t"
        "v_max3_u32 v207, v207, v224, v225\n\t"
        "v_max3_u32 v207, v207, v226, v227\n\t"
        "v_max3_u32 v207, v207, v228, v229\n\t"
        "v_max3_u32 v207, v207, v230, v231\n\t"
        "v_max3_u32 v207, v207, v232, v233\n\t"
        "v_max3_u32 v207, v207, v234, v235\n\t"
        "v_max3_u32 v207, v207, v236, v237\n\t"
        "v_max3_u32 v207, v207, v238, v239\n\t"
        "v_max3_u32 v207, v207, v240, v241\n\t"
        "v_max3_u32 v207, v207, v242, v243\n\t"
        "v_max3_u32 v207, v207, v244, v245\n\t"
        "v_max3_u32 v207, v207, v246, v247\n\t"
        "v_cmp_eq_u32 vcc, 0xffbfffbf, v207\n\t"
        "s_cbranch_vccz Lok%=\n\t"
        "Lredo%=:\n\t"
        "s_waitcnt lgkmcnt(0)\n\t"
        "v_add_u32 v206, %[dl], v206\n\t"
        "v_mov_b64 v[248:249], 0\n\t"
        "v_mov_b64 v[250:251], 0\n\t"
        "v_mov_b64 v[252:253], 0\n\t"
        "v_mov_b64 v[254:255], 0\n\t"
        "s_add_u32 %[lz], %[lz], %[dl]\n\t"
        "s_sub_u32 %[dl], 0, %[dl]\n\t"
        "s_setprio 3\n\t"
        "s_barrier\n\t"
        "s_branch Lb1%=\n\t"
        "Lend%=:\n\t"
        : [lz] "+&s"(lz), [dl] "=&s"(dl)
        : [w0] "{v[16:47]}"(W[0]), [w1] "{v[48:79]}"(W[1]), [w2] "{v[80:111]}"(W[2]), [w3] "{v[112:143]}"(W[3]), [w4] "{v[144:175]}"(W[4]), [va] "v"(va), [sidx] "v"(sidx), [vw] "v"(vw)
        : "v202", "v203", "v204", "v205", "v206", "v207", "v208", "v209", "v210", "v211", "v212", "v213", "v214", "v215", "v216", "v217", "v218", "v219", "v220", "v221", "v222", "v223", "v224", "v225", "v226", "v227", "v228", "v229", "v230", "v231", "v232", "v233", "v234", "v235", "v236", "v237", "v238", "v239", "v240", "v241", "v242", "v243", "v244", "v245", "v246", "v247", "v248", "v249", "v250", "v251", "v252", "v253", "v254", "v255", "vcc", "scc", "memory");
}

__device__ __forceinline__ void v_b0(const u32x32 (&W)[5], u32 va, u32 lz, u32 si, u32 vr, u32 z1, u32 z2, u32 z3, u32x4 rs STP) {
    u32 dl;
    asm volatile(
        ".p2align 6\n\t"
        "s_setprio 2\n\t"
        "v_mov_b32 v206, %[va]\n\t"
        "s_mov_b32 %[dl], 81920\n\t"
        "v_mov_b64 v[176:177], 0\n\t"
        "v_mov_b64 v[178:179], 0\n\t"
        "v_mov_b64 v[180:181], 0\n\t"
        "v_mov_b64 v[182:183], 0\n\t"
        "v_mov_b64 v[184:185], 0\n\t"
        "v_mov_b64 v[186:187], 0\n\t"
        "v_mov_b64 v[188:189], 0\n\t"
        "v_mov_b64 v[190:191], 0\n\t"
        "v_mov_b64 v[192:193], 0\n\t"
        "v_mov_b64 v[194:195], 0\n\t"
        "Lb1%=:\n\t"
        "s_barrier\n\t"
        "s_barrier\n\t"
        "ds_read_b64_tr_b16 v[248:249], %[vr] offset:0\n\t"
        "ds_read_b64_tr_b16 v[250:251], %[vr] offset:256\n\t"
        "ds_read_b64_tr_b16 v[252:253], %[vr] offset:512\n\t"
        "ds_read_b64_tr_b16 v[254:255], %[vr] offset:768\n\t"
        "s_waitcnt lgkmcnt(0)\n\t"
        "v_lshlrev_b32 v204, 16, v248\n\t"
        "v_cmp_u_f32 vcc, v204, v204\n\t"
        "s_cbranch_vccnz Lnb%=\n\t"
        "Lok%=:\n\t"
        "v_smfmac_f32_16x16x64_bf16 v[176:179], v[248:251], v[16:23], %[si]\n\t"
        "v_smfmac_f32_16x16x64_bf16 v[180:183], v[248:251], v[48:55], %[si]\n\t"
        "v_smfmac_f32_16x16x64_bf16 v[184:187], v[248:251], v[80:87], %[si]\n\t"
        "v_smfmac_f32_16x16x64_bf16 v[188:191], v[248:251], v[112:119], %[si]\n\t"
        "v_smfmac_f32_16x16x64_bf16 v[192:195], v[248:251], v[144:151], %[si]\n\t"
        "v_smfmac_f32_16x16x64_bf16 v[176:179], v[252:255], v[24:31], %[si]\n\t"
        "v_smfmac_f32_16x16x64_bf16 v[180:183], v[252:255], v[56:63], %[si]\n\t"
        "v_smfmac_f32_16x16x64_bf16 v[184:187], v[252:255], v[88:95], %[si]\n\t"
        "s_barrier\n\t"
        "ds_read_b64_tr_b16 v[208:209], %[vr] offset:1024\n\t"
        "ds_read_b64_tr_b16 v[210:211], %[vr] offset:1280\n\t"
        "ds_read_b64_tr_b16 v[212:213], %[vr] offset:1536\n\t"
        "ds_read_b64_tr_b16 v[214:215], %[vr] offset:1792\n\t"
        "v_smfmac_f32_16x16x64_bf16 v[188:191], v[252:255], v[120:127], %[si]\n\t"
        "v_smfmac_f32_16x16x64_bf16 v[192:195], v[252:255], v[152:159], %[si]\n\t"
        "s_waitcnt lgkmcnt(2)\n\t"
        "v_smfmac_f32_16x16x64_bf16 v[176:179], v[208:211], v[32:39], %[si]\n\t"
        "s_waitcnt lgkmcnt(0)\n\t"
        "v_smfmac_f32_16x16x64_bf16 v[176:179], v[212:215], v[40:47], %[si]\n\t"
        "v_smfmac_f32_16x16x64_bf16 v[180:183], v[208:211], v[64:71], %[si]\n\t"
        "v_smfmac_f32_16x16x64_bf16 v[180:183], v[212:215], v[72:79], %[si]\n\t"
        "v_cvt_pk_bf16_f32 v216, v176, v177\n\t"
        "v_smfmac_f32_16x16x64_bf16 v[184:187], v[208:211], v[96:103], %[si]\n\t"
        "v_cvt_pk_bf16_f32 v217, v178, v179\n\t"
        "v_smfmac_f32_16x16x64_bf16 v[184:187], v[212:215], v[104:111], %[si]\n\t"
        "v_cvt_pk_bf16_f32 v218, v180, v181\n\t"
        "v_smfmac_f32_16x16x64_bf16 v[188:191], v[208:211], v[128:135], %[si]\n\t"
        "v_cvt_pk_bf16_f32 v219, v182, v183\n\t"
        "v_smfmac_f32_16x16x64_bf16 v[188:191], v[212:215], v[136:143], %[si]\n\t"
        "v_permlane16_swap_b32 v216, v218\n\t"
        "v_smfmac_f32_16x16x64_bf16 v[192:195], v[208:211], v[160:167], %[si]\n\t"
        "v_permlane16_swap_b32 v217, v219\n\t"
        "buffer_store_dwordx4 v[216:219], %[z1], %[rs], 0 offen sc1\n\t"
        "v_smfmac_f32_16x16x64_bf16 v[192:195], v[212:215], v[168:175], %[si]\n\t"
        "v_cvt_pk_bf16_f32 v220, v184, v185\n\t"
        "v_cvt_pk_bf16_f32 v222, v188, v189\n\t"
        "v_cvt_pk_bf16_f32 v221, v186, v187\n\t"
        "v_cvt_pk_bf16_f32 v223, v190, v191\n\t"
        "v_permlane16_swap_b32 v220, v222\n\t"
        "s_nop 0\n\t"
        "v_permlane16_swap_b32 v221, v223\n\t"
        "buffer_store_dwordx4 v[220:223], %[z2], %[rs], 0 offen sc1\n\t"
        "v_cvt_pk_bf16_f32 v224, v192, v193\n\t"
        "v_cvt_pk_bf16_f32 v225, v194, v195\n\t"
        "buffer_store_dwordx2 v[224:225], %[z3], %[rs], 0 offen sc1\n\t"
        "s_branch Lend%=\n\t"
        "Lnb%=:\n\t"
        "ds_read_b128 v[208:211], v206 offset:0\n\t"
        "ds_read_b128 v[212:215], v206 offset:1024\n\t"
        "ds_read_b128 v[216:219], v206 offset:2048\n\t"
        "ds_read_b128 v[220:223], v206 offset:3072\n\t"
        "ds_read_b128 v[224:227], v206 offset:4096\n\t"
        "ds_read_b128 v[228:231], v206 offset:5120\n\t"
        "ds_read_b128 v[232:235], v206 offset:6144\n\t"
        "ds_read_b128 v[236:239], v206 offset:7168\n\t"
        "ds_read_b128 v[240:243], v206 offset:8192\n\t"
        "ds_read_b128 v[244:247], v206 offset:10240\n\t"
        "s_waitcnt lgkmcnt(0)\n\t"
        "Lnan%=:\n\t"
        "v_mbcnt_lo_u32_b32 v202, -1, 0\n\t"
        "v_mbcnt_hi_u32_b32 v202, -1, v202\n\t"
        "v_lshl_add_u32 v207, v202, 7, %[lz]\n\t"
        "v_and_b32 v202, 31, v202\n\t"
        "v_lshl_add_u32 v207, v202, 2, v207\n\t"
        "ds_read2st64_b32 v[202:203], v207 offset1:32\n\t"
        "s_waitcnt lgkmcnt(0)\n\t"
        "s_mov_b64 exec, 0x00ff00ff\n\t"
        "v_max_u32 v202, v202, v203\n\t"
        "s_mov_b64 exec, -1\n\t"
        "v_cmp_eq_u32 vcc, 0xffbfffbf, v202\n\t"
        "s_cbranch_vccnz Lredo%=\n\t"
        "v_max_u32 v207, v208, v209\n\t"
        "v_max3_u32 v207, v207, v210, v211\n\t"
        "v_max3_u32 v207, v207, v212, v213\n\t"
        "v_max3_u32 v207, v207, v214, v215\n\t"
        "v_max3_u32 v207, v207, v216, v217\n\t"
        "v_max3_u32 v207, v207, v218, v219\n\t"
        "v_max3_u32 v207, v207, v220, v221\n\t"
        "v_max3_u32 v207, v207, v222, v223\n\t"
        "v_max3_u32 v207, v207, v224, v225\n\t"
        "v_max3_u32 v207, v207, v226, v227\n\t"
        "v_max3_u32 v207, v207, v228, v229\n\t"
        "v_max3_u32 v207, v207, v230, v231\n\t"
        "v_max3_u32 v207, v207, v232, v233\n\t"
        "v_max3_u32 v207, v207, v234, v235\n\t"
        "v_max3_u32 v207, v207, v236, v237\n\t"
        "v_max3_u32 v207, v207, v238, v239\n\t"
        "v_max3_u32 v207, v207, v240, v241\n\t"
        "v_max3_u32 v207, v207, v242, v243\n\t"
        "v_max3_u32 v207, v207, v244, v245\n\t"
        "v_max3_u32 v207, v207, v246, v247\n\t"
        "v_cmp_eq_u32 vcc, 0xffbfffbf, v207\n\t"
        "s_cbranch_vccz Lok%=\n\t"
        "Lredo%=:\n\t"
        "s_waitcnt lgkmcnt(0)\n\t"
        "v_add_u32 v206, %[dl], v206\n\t"
        "v_mov_b64 v[248:249], 0\n\t"
        "v_mov_b64 v[250:251], 0\n\t"
        "v_mov_b64 v[252:253], 0\n\t"
        "v_mov_b64 v[254:255], 0\n\t"
        "s_add_u32 %[lz], %[lz], %[dl]\n\t"
        "s_sub_u32 %[dl], 0, %[dl]\n\t"
        "s_setprio 2\n\t"
        "s_barrier\n\t"
        "s_branch Lb1%=\n\t"
        "Lend%=:\n\t"
        : [lz] "+&s"(lz), [dl] "=&s"(dl)
        : [w0] "{v[16:47]}"(W[0]), [w1] "{v[48:79]}"(W[1]), [w2] "{v[80:111]}"(W[2]), [w3] "{v[112:143]}"(W[3]), [w4] "{v[144:175]}"(W[4]), [va] "v"(va), [si] "v"(si), [vr] "v"(vr), [z1] "v"(z1), [z2] "v"(z2), [z3] "v"(z3), [rs] "s"(rs)
        : "v176", "v177", "v178", "v179", "v180", "v181", "v182", "v183", "v184", "v185", "v186", "v187", "v188", "v189", "v190", "v191", "v192", "v193", "v194", "v195", "v202", "v203", "v204", "v205", "v206", "v207", "v208", "v209", "v210", "v211", "v212", "v213", "v214", "v215", "v216", "v217", "v218", "v219", "v220", "v221", "v222", "v223", "v224", "v225", "v226", "v227", "v228", "v229", "v230", "v231", "v232", "v233", "v234", "v235", "v236", "v237", "v238", "v239", "v240", "v241", "v242", "v243", "v244", "v245", "v246", "v247", "v248", "v249", "v250", "v251", "v252", "v253", "v254", "v255", "vcc", "scc", "memory");
}

__device__ __forceinline__ void v_b1(const u32x32 (&W)[5], u32 va, u32 lz, u32 si, u32 vr, u32 z1, u32 z2, u32 z3, u32x4 rs, u32& left, u32& c0, u32& r0, u32& te, u32& tn, u32& gb, u32 poll, u32 gp, u32 lk, u32 kr, u32 df, u32 vm, u32 so, u32 so2, u32 lb, u32 ro, u32 rn, u32 rb, u32 rk STP) {
    u32 dl;
    asm volatile(
        ".p2align 6\n\t"
        "s_setprio 2\n\t"
        "s_mov_b32 s94, m0\n\t"
        "s_add_u32 s92, %[so], 0x8000\n\t"
        "s_add_u32 s93, %[so], 0x10000\n\t"
        "s_mov_b32 m0, %[lb]\n\t"
        "s_cmp_eq_u32 %[poll], 0\n\t"
        "s_cbranch_scc1 Lpb%=\n\t"
        "v_lshrrev_b32 v201, 2, %[vm]\n\t"
        "v_add_u32 v201, %[lb], v201\n\t"
        "s_waitcnt vmcnt(0)\n\t"
        "ds_read_b32 v200, v201 offset:29696\n\t"
        "s_mov_b64 s[96:97], 0\n\t"
        "s_mov_b32 s98, %[rb]\n\t"
        "s_mov_b32 s99, 0x2000\n\t"
        "s_waitcnt lgkmcnt(0)\n\t"
        "v_cmp_gt_i32 vcc, s96, v200\n\t"
        "s_cbranch_vccnz Ls%=\n\t"
        "Lg%=:\n\t"
        "s_add_u32 s95, %[te], %[gp]\n\t"
        "s_add_u32 s95, s95, %[gb]\n\t"
        "Lda_%=:\n\t"
        "s_memrealtime s[90:91]\n\t"
        "s_waitcnt lgkmcnt(0)\n\t"
        "s_mul_i32 s90, s90, 24\n\t"
        "s_sub_u32 s88, s95, s90\n\t"
        "s_sub_u32 s89, s88, 620\n\t"
        "s_cmp_gt_u32 s89, 0x4000\n\t"
        "s_cbranch_scc1 Lea_%=\n\t"
        "s_sleep 1\n\t"
        "s_branch Lda_%=\n\t"
        "Lea_%=:\n\t"
        "s_memrealtime s[64:65]\n\t"
        "s_nop 0\n\t"
        "s_memrealtime s[66:67]\n\t"
        "s_nop 0\n\t"
        "s_memrealtime s[68:69]\n\t"
        "s_nop 0\n\t"
        "s_memrealtime s[70:71]\n\t"
        "s_nop 0\n\t"
        "s_memrealtime s[72:73]\n\t"
        "s_nop 0\n\t"
        "s_memrealtime s[74:75]\n\t"
        "s_memtime s[90:91]\n\t"
        "s_memrealtime s[76:77]\n\t"
        "s_nop 0\n\t"
        "s_memrealtime s[78:79]\n\t"
        "s_nop 0\n\t"
        "s_memrealtime s[80:81]\n\t"
        "s_nop 0\n\t"
        "s_memrealtime s[82:83]\n\t"
        "s_nop 0\n\t"
        "s_memrealtime s[84:85]\n\t"
        "s_nop 0\n\t"
        "s_memrealtime s[86:87]\n\t"
        "s_nop 0\n\t"
        "s_nop 7\n\t"
        "s_waitcnt lgkmcnt(0)\n\t"
        "s_add_u32 s64, s64, s66\n\t"
        "s_add_u32 s64, s64, s68\n\t"
        "s_add_u32 s64, s64, s70\n\t"
        "s_add_u32 s64, s64, s72\n\t"
        "s_add_u32 s64, s64, s74\n\t"
        "s_add_u32 s64, s64, s76\n\t"
        "s_add_u32 s64, s64, s78\n\t"
        "s_add_u32 s64, s64, s80\n\t"
        "s_add_u32 s64, s64, s82\n\t"
        "s_add_u32 s64, s64, s84\n\t"
        "s_add_u32 s64, s64, s86\n\t"
        "s_lshl_b32 s64, s64, 1\n\t"
        "s_mov_b32 %[c0], s90\n\t"
        "s_mov_b32 %[r0], s64\n\t"
        "s_sub_u32 s88, s95, s64\n\t"
        "s_mul_i32 s88, s88, %[kr]\n\t"
        "s_ashr_i32 s88, s88, 12\n\t"
        "s_sub_u32 s88, s88, 244\n\t"
        "s_nop 1\n\t"
        "s_max_i32 s88, s88, 0\n\t"
        "s_min_i32 s88, s88, 384\n\t"
        "s_andn2_b32 s88, s88, 3\n\t"
        "s_getpc_b64 s[90:91]\n\t"
        "Lga_%=:\n\t"
        "s_sub_u32 s90, s90, s88\n\t"
        "s_subb_u32 s91, s91, 0\n\t"
        "s_add_u32 s90, s90, Lza_%=-Lga_%=\n\t"
        "s_addc_u32 s91, s91, 0\n\t"
        "s_setpc_b64 s[90:91]\n\t"
        "s_nop 0\n\ts_nop 0\n\ts_nop 0\n\ts_nop 0\n\t"
        "s_nop 0\n\ts_nop 0\n\ts_nop 0\n\ts_nop 0\n\t"
        "s_nop 0\n\ts_nop 0\n\ts_nop 0\n\ts_nop 0\n\t"
        "s_nop 0\n\ts_nop 0\n\ts_nop 0\n\ts_nop 0\n\t"
        "s_nop 0\n\ts_nop 0\n\ts_nop 0\n\ts_nop 0\n\t"
        "s_nop 0\n\ts_nop 0\n\ts_nop 0\n\ts_nop 0\n\t"
        "s_nop 0\n\ts_nop 0\n\ts_nop 0\n\ts_nop 0\n\t"
        "s_nop 0\n\ts_nop 0\n\ts_nop 0\n\ts_nop 0\n\t"
        "s_nop 0\n\ts_nop 0\n\ts_nop 0\n\ts_nop 0\n\t"
        "s_nop 0\n\ts_nop 0\n\ts_nop 0\n\ts_nop 0\n\t"
        "s_nop 0\n\ts_nop 0\n\ts_nop 0\n\ts_nop 0\n\t"
        "s_nop 0\n\ts_nop 0\n\ts_nop 0\n\ts_nop 0\n\t"
        "s_nop 0\n\ts_nop 0\n\ts_nop 0\n\ts_nop 0\n\t"
        "s_nop 0\n\ts_nop 0\n\ts_nop 0\n\ts_nop 0\n\t"
        "s_nop 0\n\ts_nop 0\n\ts_nop 0\n\ts_nop 0\n\t"
        "s_nop 0\n\ts_nop 0\n\ts_nop 0\n\ts_nop 0\n\t"
        "s_nop 0\n\ts_nop 0\n\ts_nop 0\n\ts_nop 0\n\t"
        "s_nop 0\n\ts_nop 0\n\ts_nop 0\n\ts_nop 0\n\t"
        "s_nop 0\n\ts_nop 0\n\ts_nop 0\n\ts_nop 0\n\t"
        "s_nop 0\n\ts_nop 0\n\ts_nop 0\n\ts_nop 0\n\t"
        "s_nop 0\n\ts_nop 0\n\ts_nop 0\n\ts_nop 0\n\t"
        "s_nop 0\n\ts_nop 0\n\ts_nop 0\n\ts_nop 0\n\t"
        "s_nop 0\n\ts_nop 0\n\ts_nop 0\n\ts_nop 0\n\t"
        "s_nop 0\n\ts_nop 0\n\ts_nop 0\n\ts_nop 0\n\t"
        "Lza_%=:\n\t"
        "buffer_load_dwordx4 %[vm], %[rs], %[so] offen offset:0 sc1 lds\n\t"
        "buffer_load_dwordx4 %[vm], %[rs], %[so] offen offset:1024 sc1 lds\n\t"
        "buffer_load_dwordx4 %[vm], %[rs], %[so] offen offset:2048 sc1 lds\n\t"
        "buffer_load_dwordx4 %[vm], %[rs], %[so] offen offset:3072 sc1 lds\n\t"
        "s_add_u32 m0, m0, 0x1000\n\t"
        "s_nop 0\n\t"
        "buffer_load_dwordx4 %[vm], %[rs], s92 offen offset:0 sc1 lds\n\t"
        "buffer_load_dwordx4 %[vm], %[rs], s92 offen offset:1024 sc1 lds\n\t"
        "buffer_load_dwordx4 %[vm], %[rs], s92 offen offset:2048 sc1 lds\n\t"
        "buffer_load_dwordx4 %[vm], %[rs], s92 offen offset:3072 sc1 lds\n\t"
        "s_add_u32 m0, m0, 0x1000\n\t"
        "s_nop 0\n\t"
        "buffer_load_dwordx4 %[vm], %[rs], s93 offen offset:0 sc1 lds\n\t"
        "buffer_load_dwordx4 %[vm], %[rs], s93 offen offset:2048 sc1 lds\n\t"
        "s_cmp_lg_u32 s96, 0\n\t"
        "s_cbranch_scc1 Lnn%=\n\t"
        "v_readlane_b32 %[gb], v200, 0\n\t"
        "s_and_b32 %[gb], %[gb], 15\n\t"
        "s_lshl_b32 %[gb], %[gb], 4\n\t"
        "v_subrev_u32 v200, %[tn], v200\n\t"
        "v_bfe_i32 v200, v200, 0, 30\n\t"
        "s_nop 1\n\t"
        "v_max_i32_dpp v200, v200, v200 quad_perm:[1,0,3,2] row_mask:0xf bank_mask:0xf\n\t"
        "s_nop 1\n\t"
        "v_max_i32_dpp v200, v200, v200 quad_perm:[2,3,0,1] row_mask:0xf bank_mask:0xf\n\t"
        "s_nop 1\n\t"
        "v_max_i32_dpp v200, v200, v200 row_ror:4 row_mask:0xf bank_mask:0xf\n\t"
        "s_nop 1\n\t"
        "v_max_i32_dpp v200, v200, v200 row_ror:8 row_mask:0xf bank_mask:0xf\n\t"
        "s_nop 0\n\t"
        "v_readlane_b32 s88, v200, 0\n\t"
        "v_readlane_b32 s89, v200, 16\n\t"
        "s_max_i32 s88, s88, s89\n\t"
        "s_add_u32 %[tn], %[tn], s88\n\t"
        "Lnn%=:\n\t"
        "s_mov_b32 %[te], %[tn]\n\t"
        "s_add_u32 %[tn], %[tn], %[df]\n\t"
        "s_add_u32 %[tn], %[tn], %[gb]\n\t"
        "Lpb%=:\n\t"
        "v_mbcnt_lo_u32_b32 v202, -1, 0\n\t"
        "v_mbcnt_hi_u32_b32 v202, -1, v202\n\t"
        "v_lshl_add_u32 v205, v202, 7, %[lz]\n\t"
        "v_and_b32 v202, 31, v202\n\t"
        "v_lshl_add_u32 v205, v202, 2, v205\n\t"
        "v_mov_b32 v206, %[va]\n\t"
        "s_mov_b32 %[dl], 81920\n\t"
        "v_mov_b64 v[176:177], 0\n\t"
        "v_mov_b64 v[178:179], 0\n\t"
        "v_mov_b64 v[180:181], 0\n\t"
        "v_mov_b64 v[182:183], 0\n\t"
        "v_mov_b64 v[184:185], 0\n\t"
        "v_mov_b64 v[186:187], 0\n\t"
        "v_mov_b64 v[188:189], 0\n\t"
        "v_mov_b64 v[190:191], 0\n\t"
        "v_mov_b64 v[192:193], 0\n\t"
        "v_mov_b64 v[194:195], 0\n\t"
        "s_mov_b32 m0, s94\n\t"
        "s_waitcnt vmcnt(0)\n\t"
        "Lb1%=:\n\t"
        "ds_read2st64_b32 v[202:203], v205 offset1:32\n\t"
        "s_barrier\n\t"
        "s_waitcnt lgkmcnt(0)\n\t"
        "s_mov_b64 exec, 0x00ff00ff\n\t"
        "v_max_u32 v202, v202, v203\n\t"
        "s_mov_b64 exec, -1\n\t"
        "v_cmp_eq_u32 vcc, 0xffbfffbf, v202\n\t"
        "s_cbranch_vccnz Lbad%=\n\t"
        "s_barrier\n\t"
        "ds_read_b64_tr_b16 v[248:249], %[vr] offset:0\n\t"
        "ds_read_b64_tr_b16 v[250:251], %[vr] offset:256\n\t"
        "ds_read_b64_tr_b16 v[252:253], %[vr] offset:512\n\t"
        "ds_read_b64_tr_b16 v[254:255], %[vr] offset:768\n\t"
        "s_waitcnt lgkmcnt(0)\n\t"
        "v_lshlrev_b32 v204, 16, v248\n\t"
        "v_cmp_u_f32 vcc, v204, v204\n\t"
        "s_cbranch_vccnz Lnb%=\n\t"
        "Lok%=:\n\t"
        "v_smfmac_f32_16x16x64_bf16 v[176:179], v[248:251], v[16:23], %[si]\n\t"
        "v_smfmac_f32_16x16x64_bf16 v[180:183], v[248:251], v[48:55], %[si]\n\t"
        "v_smfmac_f32_16x16x64_bf16 v[184:187], v[248:251], v[80:87], %[si]\n\t"
        "v_smfmac_f32_16x16x64_bf16 v[188:191], v[248:251], v[112:119], %[si]\n\t"
        "v_smfmac_f32_16x16x64_bf16 v[192:195], v[248:251], v[144:151], %[si]\n\t"
        "v_smfmac_f32_16x16x64_bf16 v[176:179], v[252:255], v[24:31], %[si]\n\t"
        "v_smfmac_f32_16x16x64_bf16 v[180:183], v[252:255], v[56:63], %[si]\n\t"
        "v_smfmac_f32_16x16x64_bf16 v[184:187], v[252:255], v[88:95], %[si]\n\t"
        "s_barrier\n\t"
        "ds_read_b64_tr_b16 v[208:209], %[vr] offset:1024\n\t"
        "ds_read_b64_tr_b16 v[210:211], %[vr] offset:1280\n\t"
        "ds_read_b64_tr_b16 v[212:213], %[vr] offset:1536\n\t"
        "ds_read_b64_tr_b16 v[214:215], %[vr] offset:1792\n\t"
        "v_smfmac_f32_16x16x64_bf16 v[188:191], v[252:255], v[120:127], %[si]\n\t"
        "v_smfmac_f32_16x16x64_bf16 v[192:195], v[252:255], v[152:159], %[si]\n\t"
        "s_waitcnt lgkmcnt(2)\n\t"
        "v_smfmac_f32_16x16x64_bf16 v[176:179], v[208:211], v[32:39], %[si]\n\t"
        "s_waitcnt lgkmcnt(0)\n\t"
        "v_smfmac_f32_16x16x64_bf16 v[176:179], v[212:215], v[40:47], %[si]\n\t"
        "v_smfmac_f32_16x16x64_bf16 v[180:183], v[208:211], v[64:71], %[si]\n\t"
        "v_smfmac_f32_16x16x64_bf16 v[180:183], v[212:215], v[72:79], %[si]\n\t"
        "v_cvt_pk_bf16_f32 v216, v176, v177\n\t"
        "v_smfmac_f32_16x16x64_bf16 v[184:187], v[208:211], v[96:103], %[si]\n\t"
        "v_cvt_pk_bf16_f32 v217, v178, v179\n\t"
        "v_smfmac_f32_16x16x64_bf16 v[184:187], v[212:215], v[104:111], %[si]\n\t"
        "v_cvt_pk_bf16_f32 v218, v180, v181\n\t"
        "v_smfmac_f32_16x16x64_bf16 v[188:191], v[208:211], v[128:135], %[si]\n\t"
        "v_cvt_pk_bf16_f32 v219, v182, v183\n\t"
        "v_smfmac_f32_16x16x64_bf16 v[188:191], v[212:215], v[136:143], %[si]\n\t"
        "v_permlane16_swap_b32 v216, v218\n\t"
        "v_smfmac_f32_16x16x64_bf16 v[192:195], v[208:211], v[160:167], %[si]\n\t"
        "v_permlane16_swap_b32 v217, v219\n\t"
        "buffer_store_dwordx4 v[216:219], %[z1], %[rs], 0 offen sc1\n\t"
        "v_smfmac_f32_16x16x64_bf16 v[192:195], v[212:215], v[168:175], %[si]\n\t"
        "v_cvt_pk_bf16_f32 v220, v184, v185\n\t"
        "v_cvt_pk_bf16_f32 v222, v188, v189\n\t"
        "v_cvt_pk_bf16_f32 v221, v186, v187\n\t"
        "v_cvt_pk_bf16_f32 v223, v190, v191\n\t"
        "v_permlane16_swap_b32 v220, v222\n\t"
        "s_nop 0\n\t"
        "v_permlane16_swap_b32 v221, v223\n\t"
        "buffer_store_dwordx4 v[220:223], %[z2], %[rs], 0 offen sc1\n\t"
        "v_cvt_pk_bf16_f32 v224, v192, v193\n\t"
        "v_cvt_pk_bf16_f32 v225, v194, v195\n\t"
        "buffer_store_dwordx2 v[224:225], %[z3], %[rs], 0 offen sc1\n\t"
        "s_add_u32 m0, %[lb], 29696\n\t"
        "s_nop 0\n\t"
        "buffer_load_dword %[ro], %[rs], %[rn] offen sc1 lds\n\t"
        "s_mov_b32 m0, s94\n\t"
        "s_branch Lend%=\n\t"
        "Lnb%=:\n\t"
        "ds_read_b128 v[208:211], v206 offset:0\n\t"
        "ds_read_b128 v[212:215], v206 offset:1024\n\t"
        "ds_read_b128 v[216:219], v206 offset:2048\n\t"
        "ds_read_b128 v[220:223], v206 offset:3072\n\t"
        "ds_read_b128 v[224:227], v206 offset:4096\n\t"
        "ds_read_b128 v[228:231], v206 offset:5120\n\t"
        "ds_read_b128 v[232:235], v206 offset:6144\n\t"
        "ds_read_b128 v[236:239], v206 offset:7168\n\t"
        "ds_read_b128 v[240:243], v206 offset:8192\n\t"
        "ds_read_b128 v[244:247], v206 offset:10240\n\t"
        "s_waitcnt lgkmcnt(0)\n\t"
        "Lnan%=:\n\t"
        "v_max_u32 v207, v208, v209\n\t"
        "v_max3_u32 v207, v207, v210, v211\n\t"
        "v_max3_u32 v207, v207, v212, v213\n\t"
        "v_max3_u32 v207, v207, v214, v215\n\t"
        "v_max3_u32 v207, v207, v216, v217\n\t"
        "v_max3_u32 v207, v207, v218, v219\n\t"
        "v_max3_u32 v207, v207, v220, v221\n\t"
        "v_max3_u32 v207, v207, v222, v223\n\t"
        "v_max3_u32 v207, v207, v224, v225\n\t"
        "v_max3_u32 v207, v207, v226, v227\n\t"
        "v_max3_u32 v207, v207, v228, v229\n\t"
        "v_max3_u32 v207, v207, v230, v231\n\t"
        "v_max3_u32 v207, v207, v232, v233\n\t"
        "v_max3_u32 v207, v207, v234, v235\n\t"
        "v_max3_u32 v207, v207, v236, v237\n\t"
        "v_max3_u32 v207, v207, v238, v239\n\t"
        "v_max3_u32 v207, v207, v240, v241\n\t"
        "v_max3_u32 v207, v207, v242, v243\n\t"
        "v_max3_u32 v207, v207, v244, v245\n\t"
        "v_max3_u32 v207, v207, v246, v247\n\t"
        "v_cmp_eq_u32 vcc, 0xffbfffbf, v207\n\t"
        "s_cbranch_vccz Lok%=\n\t"
        "s_branch Lby%=\n\t"
        "Lredo%=:\n\t"
        "s_waitcnt lgkmcnt(0)\n\t"
        "v_add_u32 v206, %[dl], v206\n\t"
        "v_add_u32 v205, %[dl], v205\n\t"
        "v_mov_b64 v[248:249], 0\n\t"
        "v_mov_b64 v[250:251], 0\n\t"
        "v_mov_b64 v[252:253], 0\n\t"
        "v_mov_b64 v[254:255], 0\n\t"
        "s_add_u32 %[lz], %[lz], %[dl]\n\t"
        "s_sub_u32 %[dl], 0, %[dl]\n\t"
        "s_setprio 2\n\t"
        "s_barrier\n\t"
        "s_waitcnt vmcnt(0)\n\t"
        "s_branch Lb1%=\n\t"
        "Lbad%=:\n\t"
        "s_mov_b32 s95, 0\n\t"
        "Lbz%=:\n\t"
        "s_mov_b32 s92, %[so]\n\t"
        "s_mov_b32 %[so], %[so2]\n\t"
        "s_mov_b32 %[so2], s92\n\t"
        "s_add_u32 s92, %[so], 0x8000\n\t"
        "s_add_u32 s93, %[so], 0x10000\n\t"
        "s_add_u32 m0, %[lz], %[dl]\n\t"
        "s_sub_i32 %[left], %[left], 1\n\t"
        "s_cmp_lt_i32 %[left], 0\n\t"
        "s_cbranch_scc1 Lgu%=\n\t"
        "buffer_load_dwordx4 %[vm], %[rs], %[so] offen offset:0 sc1 lds\n\t"
        "buffer_load_dwordx4 %[vm], %[rs], %[so] offen offset:1024 sc1 lds\n\t"
        "buffer_load_dwordx4 %[vm], %[rs], %[so] offen offset:2048 sc1 lds\n\t"
        "buffer_load_dwordx4 %[vm], %[rs], %[so] offen offset:3072 sc1 lds\n\t"
        "s_add_u32 m0, m0, 0x1000\n\t"
        "s_nop 0\n\t"
        "buffer_load_dwordx4 %[vm], %[rs], s92 offen offset:0 sc1 lds\n\t"
        "buffer_load_dwordx4 %[vm], %[rs], s92 offen offset:1024 sc1 lds\n\t"
        "buffer_load_dwordx4 %[vm], %[rs], s92 offen offset:2048 sc1 lds\n\t"
        "buffer_load_dwordx4 %[vm], %[rs], s92 offen offset:3072 sc1 lds\n\t"
        "s_add_u32 m0, m0, 0x1000\n\t"
        "s_nop 0\n\t"
        "buffer_load_dwordx4 %[vm], %[rs], s93 offen offset:0 sc1 lds\n\t"
        "buffer_load_dwordx4 %[vm], %[rs], s93 offen offset:2048 sc1 lds\n\t"
        "Lgv%=:\n\t"
        "s_mov_b32 m0, s94\n\t"
        "s_cmp_lg_u32 s95, 0\n\t"
        "s_cbranch_scc1 Lredo%=\n\t"
        "s_barrier\n\t"
        "s_branch Lredo%=\n\t"
        "Lby%=:\n\t"
        "s_mov_b32 s95, 1\n\t"
        "s_branch Lbz%=\n\t"
        "Lgu%=:\n\t"
        "v_add_u32 v207, m0, %[vm]\n\t"
        "v_mov_b64 v[196:197], 0\n\t"
        "v_mov_b64 v[198:199], 0\n\t"
        "ds_write_b128 v207, v[196:199] offset:0\n\t"
        "ds_write_b128 v207, v[196:199] offset:1024\n\t"
        "ds_write_b128 v207, v[196:199] offset:2048\n\t"
        "ds_write_b128 v207, v[196:199] offset:3072\n\t"
        "ds_write_b128 v207, v[196:199] offset:4096\n\t"
        "ds_write_b128 v207, v[196:199] offset:5120\n\t"
        "ds_write_b128 v207, v[196:199] offset:6144\n\t"
        "ds_write_b128 v207, v[196:199] offset:7168\n\t"
        "ds_write_b128 v207, v[196:199] offset:8192\n\t"
        "ds_write_b128 v207, v[196:199] offset:9216\n\t"
        "ds_write_b128 v207, v[196:199] offset:10240\n\t"
        "s_branch Lgv%=\n\t"
        "Ls%=:\n\t"
        "s_mov_b32 s90, 0xbfffffff\n\t"
        "s_cmp_eq_u32 s97, 0\n\t"
        "s_cselect_b32 s90, 0xffbfffbf, s90\n\t"
        "s_mov_b32 s96, 0xc0000000\n\t"
        "s_waitcnt vmcnt(0) lgkmcnt(0)\n\t"
        "v_cmp_eq_u32 s[88:89], s90, v200\n\t"
        "v_cmp_gt_i32 vcc, s96, v200\n\t"
        "v_and_b32 v202, 0xc0000001, v200\n\t"
        "v_subrev_u32 v200, %[te], v200\n\t"
        "s_sub_u32 s99, s99, 1\n\t"
        "s_cselect_b32 s88, 0, s88\n\t"
        "s_or_b32 s89, s88, vcc_lo\n\t"
        "s_cbranch_scc0 Lu%=\n\t"
        "v_bfe_i32 v200, v200, 0, 30\n\t"
        "s_cmp_lg_u32 s88, 0\n\t"
        "v_cndmask_b32 v200, 0, v200, vcc\n\t"
        "s_cbranch_scc1 Lq%=\n\t"
        "v_cmp_eq_u32 vcc, 0x80000000, v202\n\t"
        "s_and_b32 s98, s97, 1\n\t"
        "v_max_i32_dpp v200, v200, v200 quad_perm:[1,0,3,2] row_mask:0xf bank_mask:0xf\n\t"
        "s_lshl_b32 s98, s98, 12\n\t"
        "s_lshr_b32 s88, s97, 1\n\t"
        "v_max_i32_dpp v200, v200, v200 quad_perm:[2,3,0,1] row_mask:0xf bank_mask:0xf\n\t"
        "s_lshl_b32 s88, s88, 14\n\t"
        "s_add_u32 s98, s98, s88\n\t"
        "v_max_i32_dpp v200, v200, v200 row_ror:4 row_mask:0xf bank_mask:0xf\n\t"
        "s_add_u32 s98, s98, %[rk]\n\t"
        "s_add_u32 s97, s97, 1\n\t"
        "v_max_i32_dpp v200, v200, v200 row_ror:8 row_mask:0xf bank_mask:0xf\n\t"
        "s_nop 0\n\t"
        "v_readlane_b32 s88, v200, 0\n\t"
        "v_readlane_b32 s89, v200, 16\n\t"
        "s_max_i32 s88, s88, s89\n\t"
        "s_max_i32 s88, s88, 0\n\t"
        "s_add_u32 %[te], %[te], s88\n\t"
        "s_add_u32 %[tn], %[tn], s88\n\t"
        "s_cbranch_vccz Lu%=\n\t"
        "s_cmp_gt_u32 s97, 4\n\t"
        "s_cbranch_scc1 Lu%=\n\t"
        "s_sub_u32 s90, %[te], %[lk]\n\t"
        "Lt%=:\n\t"
        "s_memrealtime s[88:89]\n\t"
        "s_waitcnt lgkmcnt(0)\n\t"
        "s_mul_i32 s88, s88, 24\n\t"
        "s_sub_u32 s88, s90, s88\n\t"
        "s_cmp_lt_u32 s88, 0x4000\n\t"
        "s_cbranch_scc1 Lt%=\n\t"
        "s_add_u32 s89, %[rk], 20480\n\t"
        "s_cmp_lt_i32 s88, -3000\n\t"
        "s_cselect_b32 s97, 4, s97\n\t"
        "s_cselect_b32 s98, s89, s98\n\t"
        "v_mov_b32 v200, 0\n\t"
        "buffer_load_dword v200, %[ro], %[rs], s98 offen sc1\n\t"
        "s_branch Ls%=\n\t"
        "Lq%=:\n\t"
        "s_sleep 6\n\t"
        "buffer_load_dword v200, %[ro], %[rs], s98 offen sc1\n\t"
        "s_branch Ls%=\n\t"
        "Lu%=:\n\t"
        "v_mov_b32 v200, 0\n\t"
        "s_branch Lg%=\n\t"
        "Lend%=:\n\t"
        : [c0] "+&s"(c0), [r0] "+&s"(r0), [so] "+&s"(so), [te] "+&s"(te), [tn] "+&s"(tn), [gb] "+&s"(gb), [lz] "+&s"(lz), [dl] "=&s"(dl), [so2] "+&s"(so2), [left] "+&s"(left)
        : [w0] "{v[16:47]}"(W[0]), [w1] "{v[48:79]}"(W[1]), [w2] "{v[80:111]}"(W[2]), [w3] "{v[112:143]}"(W[3]), [w4] "{v[144:175]}"(W[4]), [va] "v"(va), [si] "v"(si), [vr] "v"(vr), [z1] "v"(z1), [z2] "v"(z2), [z3] "v"(z3), [rs] "s"(rs), [poll] "s"(poll), [gp] "s"(gp), [kr] "s"(kr), [df] "s"(df), [rn] "s"(rn), [vm] "v"(vm), [lb] "s"(lb), [ro] "v"(ro), [rb] "s"(rb), [rk] "s"(rk), [lk] "s"(lk)
        : "s64", "s65", "s66", "s67", "s68", "s69", "s70", "s71", "s72", "s73", "s74", "s75", "s76", "s77", "s78", "s79", "s80", "s81", "s82", "s83", "s84", "s85", "s86", "s87", "s88", "s89", "s90", "s91", "s92", "s93", "s94", "s95", "s96", "s97", "s98", "s99", "v176", "v177", "v178", "v179", "v180", "v181", "v182", "v183", "v184", "v185", "v186", "v187", "v188", "v189", "v190", "v191", "v192", "v193", "v194", "v195", "v196", "v197", "v198", "v199", "v200", "v201", "v202", "v203", "v204", "v205", "v206", "v207", "v208", "v209", "v210", "v211", "v212", "v213", "v214", "v215", "v216", "v217", "v218", "v219", "v220", "v221", "v222", "v223", "v224", "v225", "v226", "v227", "v228", "v229", "v230", "v231", "v232", "v233", "v234", "v235", "v236", "v237", "v238", "v239", "v240", "v241", "v242", "v243", "v244", "v245", "v246", "v247", "v248", "v249", "v250", "v251", "v252", "v253", "v254", "v255", "vcc", "scc", "memory");
}

__device__ __forceinline__ void r_u(u32 bo, u32 eo, u32 si, u32 tw, u32x4 rs, u32 so, u32 bs, u32 fv, u32 uw, u32 lag STP) {
    asm volatile(
        ".p2align 6\n\t"
        "s_add_u32 s93, %[bs], 0x1000\n\t"
        "s_mov_b32 s94, 0x1000\n\t"
        "s_barrier\n\t"
        "buffer_load_dwordx4 v[16:19], %[bo], %[rs], %[bs] offen offset:0 sc1\n\t"
        "buffer_load_dwordx4 v[20:23], %[bo], %[rs], %[bs] offen offset:1024 sc1\n\t"
        "buffer_load_dwordx4 v[24:27], %[bo], %[rs], %[bs] offen offset:2048 sc1\n\t"
        "buffer_load_dwordx4 v[28:31], %[bo], %[rs], %[bs] offen offset:3072 sc1\n\t"
        "buffer_load_dwordx4 v[32:35], %[bo], %[rs], s93 offen offset:0 sc1\n\t"
        "buffer_load_dwordx4 v[36:39], %[bo], %[rs], s93 offen offset:1024 sc1\n\t"
        "buffer_load_dwordx4 v[40:43], %[bo], %[rs], s93 offen offset:2048 sc1\n\t"
        "buffer_load_dwordx4 v[44:47], %[bo], %[rs], s93 offen offset:3072 sc1\n\t"
        "v_mov_b64 v[64:65], 0\n\t"
        "v_mov_b64 v[66:67], 0\n\t"
        "v_mov_b64 v[68:69], 0\n\t"
        "v_mov_b64 v[70:71], 0\n\t"
        "v_mov_b64 v[72:73], 0\n\t"
        "v_mov_b64 v[74:75], 0\n\t"
        "v_mov_b64 v[76:77], 0\n\t"
        "v_mov_b64 v[78:79], 0\n\t"
        "s_mov_b32 s88, %[lag]\n\t"
        "s_setprio 3\n\t"
        "s_barrier\n\t"
        "Ll%=:\n\t"
        "s_sub_u32 s88, s88, 1\n\t"
        "s_cbranch_scc1 Lm%=\n\t"
        "s_nop 7\n\t"
        "s_branch Ll%=\n\t"
        "Lm%=:\n\t"
        "buffer_load_dwordx4 v[48:51], %[eo], %[rs], %[so] offen offset:0 sc1\n\t"
        "buffer_load_dwordx4 v[52:55], %[eo], %[rs], %[so] offen offset:1024 sc1\n\t"
        "buffer_load_dwordx4 v[56:59], %[eo], %[rs], %[so] offen offset:2048 sc1\n\t"
        "buffer_load_dwordx4 v[60:63], %[eo], %[rs], %[so] offen offset:3072 sc1\n\t"
        "s_waitcnt vmcnt(4)\n\t"
        "v_swap_b32 v17, v20\n\t"
        "v_swap_b32 v19, v22\n\t"
        "v_swap_b32 v25, v28\n\t"
        "v_swap_b32 v27, v30\n\t"
        "v_swap_b32 v33, v36\n\t"
        "v_swap_b32 v35, v38\n\t"
        "v_swap_b32 v41, v44\n\t"
        "v_swap_b32 v43, v46\n\t"
        "s_nop 0\n\t"
        "Le%=:\n\t"
        "s_waitcnt vmcnt(3)\n\t"
        "v_smfmac_f32_16x16x64_bf16 v[64:67], v[48:51], v[16:23], %[si]\n\t"
        "s_waitcnt vmcnt(2)\n\t"
        "v_smfmac_f32_16x16x64_bf16 v[68:71], v[52:55], v[24:31], %[si]\n\t"
        "s_waitcnt vmcnt(1)\n\t"
        "v_smfmac_f32_16x16x64_bf16 v[72:75], v[56:59], v[32:39], %[si]\n\t"
        "v_max3_u32 v244, v48, v52, v56\n\t"
        "s_waitcnt vmcnt(0)\n\t"
        "v_smfmac_f32_16x16x64_bf16 v[76:79], v[60:63], v[40:47], %[si]\n\t"
        "v_cvt_pk_bf16_f32 v80, v64, v65\n\t"
        "v_cvt_pk_bf16_f32 v81, v66, v67\n\t"
        "ds_write_b64 %[uw], v[80:81]\n\t"
        "v_cvt_pk_bf16_f32 v82, v68, v69\n\t"
        "v_cvt_pk_bf16_f32 v83, v70, v71\n\t"
        "ds_write_b64 %[uw], v[82:83] offset:256\n\t"
        "v_max_u32 v244, v244, v60\n\t"
        "v_cmp_eq_u32 vcc, 0xffbfffbf, v244\n\t"
        "v_cvt_pk_bf16_f32 v84, v72, v73\n\t"
        "v_cvt_pk_bf16_f32 v85, v74, v75\n\t"
        "v_cvt_pk_bf16_f32 v86, v76, v77\n\t"
        "v_cvt_pk_bf16_f32 v87, v78, v79\n\t"
        "ds_write_b64 %[uw], v[84:85] offset:1024\n\t"
        "ds_write_b64 %[uw], v[86:87] offset:1280\n\t"
        "s_cbranch_vccnz Lf%=\n\t"
        "Lh%=:\n\t"
        "s_waitcnt lgkmcnt(0)\n\t"
        "s_barrier\n\t"
        "s_setprio 0\n\t"
        "s_sleep 4\n\t"
        "s_branch Lend%=\n\t"
        "Lf%=:\n\t"
        "s_sub_u32 s94, s94, 1\n\t"
        "s_cbranch_scc1 Lh%=\n\t"
        "Lwa%=:\n\t"
        "ds_read_b64 v[244:245], %[tw] offset:0\n\t"
        "s_memrealtime s[90:91]\n\t"
        "s_waitcnt lgkmcnt(0)\n\t"
        "v_readfirstlane_b32 s88, v244\n\t"
        "v_readfirstlane_b32 s89, v245\n\t"
        "s_mul_i32 s90, s90, 24\n\t"
        "s_cmp_lg_u32 s89, %[fv]\n\t"
        "s_cbranch_scc1 Lwa%=\n\t"
        "s_add_u32 s88, s88, -96\n\t"
        "s_sub_u32 s88, s88, s90\n\t"
        "s_cmp_lt_u32 s88, 0x4000\n\t"
        "s_cbranch_scc1 Lwa%=\n\t"
        "buffer_load_dwordx4 v[48:51], %[eo], %[rs], %[so] offen offset:0 sc1\n\t"
        "buffer_load_dwordx4 v[52:55], %[eo], %[rs], %[so] offen offset:1024 sc1\n\t"
        "buffer_load_dwordx4 v[56:59], %[eo], %[rs], %[so] offen offset:2048 sc1\n\t"
        "buffer_load_dwordx4 v[60:63], %[eo], %[rs], %[so] offen offset:3072 sc1\n\t"
        "v_mov_b64 v[64:65], 0\n\t"
        "v_mov_b64 v[66:67], 0\n\t"
        "v_mov_b64 v[68:69], 0\n\t"
        "v_mov_b64 v[70:71], 0\n\t"
        "v_mov_b64 v[72:73], 0\n\t"
        "v_mov_b64 v[74:75], 0\n\t"
        "v_mov_b64 v[76:77], 0\n\t"
        "v_mov_b64 v[78:79], 0\n\t"
        "s_branch Le%=\n\t"
        "Lend%=:\n\t"
        :
        : [bo] "v"(bo), [eo] "v"(eo), [si] "v"(si), [tw] "v"(tw), [rs] "s"(rs), [so] "s"(so), [bs] "s"(bs), [fv] "s"(fv), [uw] "v"(uw), [lag] "s"(lag)
        : "s88", "s89", "s90", "s91", "s92", "s93", "s94", "v16", "v17", "v18", "v19", "v20", "v21", "v22", "v23", "v24", "v25", "v26", "v27", "v28", "v29", "v30", "v31", "v32", "v33", "v34", "v35", "v36", "v37", "v38", "v39", "v40", "v41", "v42", "v43", "v44", "v45", "v46", "v47", "v48", "v49", "v50", "v51", "v52", "v53", "v54", "v55", "v56", "v57", "v58", "v59", "v60", "v61", "v62", "v63", "v64", "v65", "v66", "v67", "v68", "v69", "v70", "v71", "v72", "v73", "v74", "v75", "v76", "v77", "v78", "v79", "v80", "v81", "v82", "v83", "v84", "v85", "v86", "v87", "v244", "v245", "v246", "vcc", "scc", "memory");
}

__device__ __forceinline__ void r_f(u32 bo, u32 eo, u32 si, u32 tw, u32x4 rs, u32 so, u32 bs, u32 fv, u32& bad, u32x8 za, u32 po, u32 mo, u32 sf, u32 ur, u32 zs, u32 ps, u32 sm, u32 d0, u32 d1 STP) {
    asm volatile(
        ".p2align 6\n\t"
        "s_add_u32 s93, %[bs], 0x1000\n\t"
        "s_mov_b32 s94, 0x1000\n\t"
        "s_barrier\n\t"
        "buffer_load_dwordx4 v[16:19], %[bo], %[rs], %[bs] offen offset:0 sc1\n\t"
        "buffer_load_dwordx4 v[20:23], %[bo], %[rs], %[bs] offen offset:1024 sc1\n\t"
        "buffer_load_dwordx4 v[24:27], %[bo], %[rs], %[bs] offen offset:2048 sc1\n\t"
        "buffer_load_dwordx4 v[28:31], %[bo], %[rs], %[bs] offen offset:3072 sc1\n\t"
        "buffer_load_dwordx4 v[32:35], %[bo], %[rs], s93 offen offset:0 sc1\n\t"
        "buffer_load_dwordx4 v[36:39], %[bo], %[rs], s93 offen offset:1024 sc1\n\t"
        "buffer_load_dwordx4 v[40:43], %[bo], %[rs], s93 offen offset:2048 sc1\n\t"
        "buffer_load_dwordx4 v[44:47], %[bo], %[rs], s93 offen offset:3072 sc1\n\t"
        "v_mov_b64 v[64:65], 0\n\t"
        "v_mov_b64 v[66:67], 0\n\t"
        "v_mov_b64 v[68:69], 0\n\t"
        "v_mov_b64 v[70:71], 0\n\t"
        "v_mov_b64 v[72:73], 0\n\t"
        "v_mov_b64 v[74:75], 0\n\t"
        "v_mov_b64 v[76:77], 0\n\t"
        "v_mov_b64 v[78:79], 0\n\t"
        "s_mov_b32 %[bad], 0\n\t"
        "s_add_u32 s65, %[ps], 0x8000\n\t"
        "s_add_u32 s64, %[sm], 4096\n\t"
        "s_add_u32 s66, %[sm], 16384\n\t"
        "s_add_u32 s67, %[sm], 20480\n\t"
        "s_add_u32 s68, %[sm], 131072\n\t"
        "s_add_u32 s69, %[sm], 135168\n\t"
        "s_add_u32 s70, %[sm], 147456\n\t"
        "s_add_u32 s71, %[sm], 151552\n\t"
        "s_mov_b32 s72, 0\n\t"
        "v_mov_b64 v[180:181], 0\n\t"
        "v_mov_b64 v[182:183], 0\n\t"
        "s_setprio 3\n\t"
        "s_barrier\n\t"
        "s_getpc_b64 s[90:91]\n\t"
        "Lga_%=:\n\t"
        "s_sub_u32 s90, s90, %[d0]\n\t"
        "s_subb_u32 s91, s91, 0\n\t"
        "s_add_u32 s90, s90, Lza_%=-Lga_%=\n\t"
        "s_addc_u32 s91, s91, 0\n\t"
        "s_setpc_b64 s[90:91]\n\t"
        "s_nop 0\n\ts_nop 0\n\ts_nop 0\n\ts_nop 0\n\t"
        "s_nop 0\n\ts_nop 0\n\ts_nop 0\n\ts_nop 0\n\t"
        "s_nop 0\n\ts_nop 0\n\ts_nop 0\n\ts_nop 0\n\t"
        "s_nop 0\n\ts_nop 0\n\ts_nop 0\n\ts_nop 0\n\t"
        "s_nop 0\n\ts_nop 0\n\ts_nop 0\n\ts_nop 0\n\t"
        "s_nop 0\n\ts_nop 0\n\ts_nop 0\n\ts_nop 0\n\t"
        "s_nop 0\n\ts_nop 0\n\ts_nop 0\n\ts_nop 0\n\t"
        "s_nop 0\n\ts_nop 0\n\ts_nop 0\n\ts_nop 0\n\t"
        "s_nop 0\n\ts_nop 0\n\ts_nop 0\n\ts_nop 0\n\t"
        "s_nop 0\n\ts_nop 0\n\ts_nop 0\n\ts_nop 0\n\t"
        "s_nop 0\n\ts_nop 0\n\ts_nop 0\n\ts_nop 0\n\t"
        "s_nop 0\n\ts_nop 0\n\ts_nop 0\n\ts_nop 0\n\t"
        "s_nop 0\n\ts_nop 0\n\ts_nop 0\n\ts_nop 0\n\t"
        "s_nop 0\n\ts_nop 0\n\ts_nop 0\n\ts_nop 0\n\t"
        "s_nop 0\n\ts_nop 0\n\ts_nop 0\n\ts_nop 0\n\t"
        "s_nop 0\n\ts_nop 0\n\ts_nop 0\n\ts_nop 0\n\t"
        "Lza_%=:\n\t"
        "Lst%=:\n\t"
        "buffer_load_dwordx4 v[48:51], %[eo], %[rs], %[so] offen offset:0 sc1\n\t"
        "buffer_load_dwordx4 v[52:55], %[eo], %[rs], %[so] offen offset:1024 sc1\n\t"
        "buffer_load_dwordx4 v[56:59], %[eo], %[rs], %[so] offen offset:2048 sc1\n\t"
        "buffer_load_dwordx4 v[60:63], %[eo], %[rs], %[so] offen offset:3072 sc1\n\t"
        "buffer_load_dwordx4 v[88:91], v198, %[rs], %[zs] offen sc1\n\t"
        "buffer_load_dwordx4 v[92:95], v199, %[rs], %[zs] offen sc1\n\t"
        "buffer_load_dwordx4 v[96:99], v192, %[rs], %[zs] offen sc1\n\t"
        "buffer_load_dwordx4 v[100:103], v193, %[rs], %[zs] offen sc1\n\t"
        "buffer_load_dwordx4 v[104:107], v194, %[rs], %[zs] offen sc1\n\t"
        "buffer_load_dwordx4 v[108:111], v195, %[rs], %[zs] offen sc1\n\t"
        "buffer_load_dwordx4 v[112:115], v196, %[rs], %[zs] offen sc1\n\t"
        "buffer_load_dwordx4 v[116:119], v197, %[rs], %[zs] offen sc1\n\t"
        "s_getpc_b64 s[90:91]\n\t"
        "Lgb_%=:\n\t"
        "s_sub_u32 s90, s90, %[d1]\n\t"
        "s_subb_u32 s91, s91, 0\n\t"
        "s_add_u32 s90, s90, Lzb_%=-Lgb_%=\n\t"
        "s_addc_u32 s91, s91, 0\n\t"
        "s_setpc_b64 s[90:91]\n\t"
        "s_nop 0\n\ts_nop 0\n\ts_nop 0\n\ts_nop 0\n\t"
        "s_nop 0\n\ts_nop 0\n\ts_nop 0\n\ts_nop 0\n\t"
        "s_nop 0\n\ts_nop 0\n\ts_nop 0\n\ts_nop 0\n\t"
        "s_nop 0\n\ts_nop 0\n\ts_nop 0\n\ts_nop 0\n\t"
        "s_nop 0\n\ts_nop 0\n\ts_nop 0\n\ts_nop 0\n\t"
        "s_nop 0\n\ts_nop 0\n\ts_nop 0\n\ts_nop 0\n\t"
        "s_nop 0\n\ts_nop 0\n\ts_nop 0\n\ts_nop 0\n\t"
        "s_nop 0\n\ts_nop 0\n\ts_nop 0\n\ts_nop 0\n\t"
        "s_nop 0\n\ts_nop 0\n\ts_nop 0\n\ts_nop 0\n\t"
        "s_nop 0\n\ts_nop 0\n\ts_nop 0\n\ts_nop 0\n\t"
        "s_nop 0\n\ts_nop 0\n\ts_nop 0\n\ts_nop 0\n\t"
        "s_nop 0\n\ts_nop 0\n\ts_nop 0\n\ts_nop 0\n\t"
        "s_nop 0\n\ts_nop 0\n\ts_nop 0\n\ts_nop 0\n\t"
        "s_nop 0\n\ts_nop 0\n\ts_nop 0\n\ts_nop 0\n\t"
        "s_nop 0\n\ts_nop 0\n\ts_nop 0\n\ts_nop 0\n\t"
        "s_nop 0\n\ts_nop 0\n\ts_nop 0\n\ts_nop 0\n\t"
        "Lzb_%=:\n\t"
        "buffer_load_dwordx4 v[120:123], %[po], %[rs], s65 offen offset:1024 sc1\n\t"
        "buffer_load_dwordx4 v[124:127], %[po], %[rs], s65 offen offset:1536 sc1\n\t"
        "buffer_load_dwordx4 v[128:131], %[po], %[rs], %[ps] offen offset:0 sc1\n\t"
        "buffer_load_dwordx4 v[132:135], %[po], %[rs], %[ps] offen offset:512 sc1\n\t"
        "buffer_load_dwordx4 v[136:139], %[po], %[rs], %[ps] offen offset:1024 sc1\n\t"
        "buffer_load_dwordx4 v[140:143], %[po], %[rs], %[ps] offen offset:1536 sc1\n\t"
        "buffer_load_dwordx4 v[144:147], %[po], %[rs], s65 offen offset:0 sc1\n\t"
        "buffer_load_dwordx4 v[148:151], %[po], %[rs], s65 offen offset:512 sc1\n\t"
        "s_cmp_lg_u32 s94, 0x1000\n\t"
        "s_cbranch_scc1 Le%=\n\t"
        "ds_read_b32 v244, %[tw] offset:8\n\t"
        "s_waitcnt vmcnt(20)\n\t"
        "v_swap_b32 v17, v20\n\t"
        "v_swap_b32 v19, v22\n\t"
        "v_swap_b32 v25, v28\n\t"
        "v_swap_b32 v27, v30\n\t"
        "v_swap_b32 v33, v36\n\t"
        "v_swap_b32 v35, v38\n\t"
        "v_swap_b32 v41, v44\n\t"
        "v_swap_b32 v43, v46\n\t"
        "s_nop 0\n\t"
        "s_waitcnt lgkmcnt(0)\n\t"
        "v_cmp_ne_u32 vcc, %[fv], v244\n\t"
        "s_cbranch_vccnz Lmv%=\n\t"
        "Le%=:\n\t"
        "s_waitcnt vmcnt(19)\n\t"
        "v_smfmac_f32_16x16x64_bf16 v[64:67], v[48:51], v[16:23], %[si]\n\t"
        "s_waitcnt vmcnt(18)\n\t"
        "v_smfmac_f32_16x16x64_bf16 v[68:71], v[52:55], v[24:31], %[si]\n\t"
        "s_waitcnt vmcnt(17)\n\t"
        "v_smfmac_f32_16x16x64_bf16 v[72:75], v[56:59], v[32:39], %[si]\n\t"
        "v_max3_u32 v244, v48, v52, v56\n\t"
        "s_waitcnt vmcnt(16)\n\t"
        "v_smfmac_f32_16x16x64_bf16 v[76:79], v[60:63], v[40:47], %[si]\n\t"
        "v_cvt_pk_bf16_f32 v80, v64, v65\n\t"
        "v_cvt_pk_bf16_f32 v81, v66, v67\n\t"
        "v_cvt_pk_bf16_f32 v82, v68, v69\n\t"
        "v_cvt_pk_bf16_f32 v83, v70, v71\n\t"
        "v_max_u32 v244, v244, v60\n\t"
        "v_cmp_eq_u32 vcc, 0xffbfffbf, v244\n\t"
        "v_permlane16_swap_b32 v80, v82\n\t"
        "v_permlane16_swap_b32 v81, v83\n\t"
        "v_cvt_pk_bf16_f32 v84, v72, v73\n\t"
        "v_cvt_pk_bf16_f32 v85, v74, v75\n\t"
        "v_cvt_pk_bf16_f32 v86, v76, v77\n\t"
        "v_cvt_pk_bf16_f32 v87, v78, v79\n\t"
        "s_cbranch_vccnz Lf%=\n\t"
        "Lh%=:\n\t"
        "s_barrier\n\t"
        "ds_read_b128 v[156:159], %[ur] offset:0\n\t"
        "ds_read_b128 v[160:163], %[ur] offset:1024\n\t"
        "ds_read_b128 v[164:167], %[ur] offset:2048\n\t"
        "ds_read_b128 v[168:171], %[ur] offset:3072\n\t"
        "ds_read_b128 v[172:175], %[ur] offset:4096\n\t"
        "ds_read_b128 v[176:179], %[ur] offset:5120\n\t"
        "v_permlane16_swap_b32 v84, v86\n\t"
        "v_permlane16_swap_b32 v85, v87\n\t"
        "Lgo%=:\n\t"
        "s_waitcnt vmcnt(7)\n\t"
        "v_mov_b64 v[152:153], v[120:121]\n\t"
        "s_waitcnt vmcnt(6)\n\t"
        "v_smfmac_f32_16x16x64_bf16 v[180:183], v[122:125], v[80:87], %[sf]\n\t"
        "s_waitcnt vmcnt(5)\n\t"
        "v_smfmac_f32_16x16x64_bf16 v[180:183], v[126:129], v[88:95], %[sf]\n\t"
        "s_waitcnt vmcnt(4) lgkmcnt(4)\n\t"
        "v_smfmac_f32_16x16x64_bf16 v[180:183], v[130:133], v[156:163], %[sf]\n\t"
        "s_waitcnt vmcnt(3)\n\t"
        "v_smfmac_f32_16x16x64_bf16 v[180:183], v[134:137], v[96:103], %[sf]\n\t"
        "s_waitcnt vmcnt(2) lgkmcnt(2)\n\t"
        "v_smfmac_f32_16x16x64_bf16 v[180:183], v[138:141], v[164:171], %[sf]\n\t"
        "s_waitcnt vmcnt(1)\n\t"
        "v_smfmac_f32_16x16x64_bf16 v[180:183], v[142:145], v[104:111], %[sf]\n\t"
        "s_waitcnt vmcnt(0) lgkmcnt(0)\n\t"
        "v_smfmac_f32_16x16x64_bf16 v[180:183], v[146:149], v[172:179], %[sf]\n\t"
        "s_waitcnt vmcnt(0)\n\t"
        "v_smfmac_f32_16x16x64_bf16 v[180:183], v[150:153], v[112:119], %[sf]\n\t"
        "s_nop 7\n\t"
        "v_add_f32 v184, v180, v182\n\t"
        "v_add_f32 v185, v181, v183\n\t"
        "v_add_f32 v186, v180, v182\n\t"
        "s_nop 0\n\t"
        "v_mov_b32_dpp v186, v185 row_ror:8 row_mask:0xf bank_mask:0xc\n\t"
        "v_mov_b32_dpp v185, v184 row_ror:8 row_mask:0xf bank_mask:0x3\n\t"
        "v_cvt_pk_bf16_f32 v187, v186, v185\n\t"
        "v_cmpx_o_f32 vcc, v186, v185\n\t"
        "buffer_store_dword v187, %[mo], %[rs], %[sm] offen sc1\n\t"
        "buffer_store_dword v187, %[mo], %[rs], s64 offen sc1\n\t"
        "buffer_store_dword v187, %[mo], %[rs], s66 offen sc1\n\t"
        "buffer_store_dword v187, %[mo], %[rs], s67 offen sc1\n\t"
        "buffer_store_dword v187, %[mo], %[rs], s68 offen sc1\n\t"
        "buffer_store_dword v187, %[mo], %[rs], s69 offen sc1\n\t"
        "buffer_store_dword v187, %[mo], %[rs], s70 offen sc1\n\t"
        "buffer_store_dword v187, %[mo], %[rs], s71 offen sc1\n\t"
        "s_cmp_eq_u64 exec, -1\n\t"
        "s_mov_b64 exec, -1\n\t"
        "s_cbranch_scc1 Lend%=\n\t"
        "v_max_u32 v244, v88, v120\n\t"
        "v_max3_u32 v244, v244, v92, v124\n\t"
        "v_max3_u32 v244, v244, v96, v128\n\t"
        "v_max3_u32 v244, v244, v100, v132\n\t"
        "v_max3_u32 v244, v244, v104, v136\n\t"
        "v_max3_u32 v244, v244, v108, v140\n\t"
        "v_max3_u32 v244, v244, v112, v144\n\t"
        "v_max3_u32 v244, v244, v116, v148\n\t"
        "v_cmp_eq_u32 vcc, 0xffbfffbf, v244\n\t"
        "s_mov_b32 %[bad], 3\n\t"
        "s_sub_i32 s94, s94, 1\n\t"
        "s_cmp_lt_i32 s94, 0\n\t"
        "s_cbranch_scc1 Lend%=\n\t"
        "s_cbranch_vccz Lend%=\n\t"
        "s_mov_b32 %[bad], 1\n\t"
        "buffer_load_dwordx4 v[88:91], v198, %[rs], %[zs] offen sc1\n\t"
        "buffer_load_dwordx4 v[92:95], v199, %[rs], %[zs] offen sc1\n\t"
        "buffer_load_dwordx4 v[96:99], v192, %[rs], %[zs] offen sc1\n\t"
        "buffer_load_dwordx4 v[100:103], v193, %[rs], %[zs] offen sc1\n\t"
        "buffer_load_dwordx4 v[104:107], v194, %[rs], %[zs] offen sc1\n\t"
        "buffer_load_dwordx4 v[108:111], v195, %[rs], %[zs] offen sc1\n\t"
        "buffer_load_dwordx4 v[112:115], v196, %[rs], %[zs] offen sc1\n\t"
        "buffer_load_dwordx4 v[116:119], v197, %[rs], %[zs] offen sc1\n\t"
        "buffer_load_dwordx4 v[120:123], %[po], %[rs], s65 offen offset:1024 sc1\n\t"
        "buffer_load_dwordx4 v[124:127], %[po], %[rs], s65 offen offset:1536 sc1\n\t"
        "buffer_load_dwordx4 v[128:131], %[po], %[rs], %[ps] offen offset:0 sc1\n\t"
        "buffer_load_dwordx4 v[132:135], %[po], %[rs], %[ps] offen offset:512 sc1\n\t"
        "buffer_load_dwordx4 v[136:139], %[po], %[rs], %[ps] offen offset:1024 sc1\n\t"
        "buffer_load_dwordx4 v[140:143], %[po], %[rs], %[ps] offen offset:1536 sc1\n\t"
        "buffer_load_dwordx4 v[144:147], %[po], %[rs], s65 offen offset:0 sc1\n\t"
        "buffer_load_dwordx4 v[148:151], %[po], %[rs], s65 offen offset:512 sc1\n\t"
        "v_mov_b64 v[180:181], 0\n\t"
        "v_mov_b64 v[182:183], 0\n\t"
        "s_branch Lgo%=\n\t"
        "Lmv%=:\n\t"
        "ds_read_b96 v[244:246], %[tw]\n\t"
        "s_waitcnt lgkmcnt(0)\n\t"
        "v_readfirstlane_b32 s88, v245\n\t"
        "v_readfirstlane_b32 s89, v246\n\t"
        "s_cmp_lg_u32 s88, %[fv]\n\t"
        "s_cbranch_scc1 Lmv%=\n\t"
        "s_cmp_eq_u32 s89, %[fv]\n\t"
        "s_cbranch_scc1 Le%=\n\t"
        "Lf%=:\n\t"
        "s_sub_u32 s94, s94, 1\n\t"
        "s_cbranch_scc1 Lh%=\n\t"
        "Lwa%=:\n\t"
        "ds_read_b64 v[244:245], %[tw] offset:0\n\t"
        "s_memrealtime s[90:91]\n\t"
        "s_waitcnt lgkmcnt(0)\n\t"
        "v_readfirstlane_b32 s88, v244\n\t"
        "v_readfirstlane_b32 s89, v245\n\t"
        "s_mul_i32 s90, s90, 24\n\t"
        "s_cmp_lg_u32 s89, %[fv]\n\t"
        "s_cbranch_scc1 Lwa%=\n\t"
        "s_add_u32 s88, s88, 32\n\t"
        "s_sub_u32 s88, s88, s90\n\t"
        "s_cmp_lt_u32 s88, 0x4000\n\t"
        "s_cbranch_scc1 Lwa%=\n\t"
        "v_mov_b64 v[64:65], 0\n\t"
        "v_mov_b64 v[66:67], 0\n\t"
        "v_mov_b64 v[68:69], 0\n\t"
        "v_mov_b64 v[70:71], 0\n\t"
        "v_mov_b64 v[72:73], 0\n\t"
        "v_mov_b64 v[74:75], 0\n\t"
        "v_mov_b64 v[76:77], 0\n\t"
        "v_mov_b64 v[78:79], 0\n\t"
        "s_branch Lst%=\n\t"
        "Lend%=:\n\t"
        : [bad] "=&s"(bad)
        : [bo] "v"(bo), [eo] "v"(eo), [si] "v"(si), [tw] "v"(tw), [rs] "s"(rs), [so] "s"(so), [bs] "s"(bs), [fv] "s"(fv), [po] "v"(po), [mo] "v"(mo), [sf] "v"(sf), [ur] "v"(ur), [za] "{v[192:199]}"(za), [zs] "s"(zs), [ps] "s"(ps), [sm] "s"(sm), [d0] "s"(d0), [d1] "s"(d1)
        : "s64", "s65", "s66", "s67", "s68", "s69", "s70", "s71", "s72", "s88", "s89", "s90", "s91", "s92", "s93", "s94", "v16", "v17", "v18", "v19", "v20", "v21", "v22", "v23", "v24", "v25", "v26", "v27", "v28", "v29", "v30", "v31", "v32", "v33", "v34", "v35", "v36", "v37", "v38", "v39", "v40", "v41", "v42", "v43", "v44", "v45", "v46", "v47", "v48", "v49", "v50", "v51", "v52", "v53", "v54", "v55", "v56", "v57", "v58", "v59", "v60", "v61", "v62", "v63", "v64", "v65", "v66", "v67", "v68", "v69", "v70", "v71", "v72", "v73", "v74", "v75", "v76", "v77", "v78", "v79", "v80", "v81", "v82", "v83", "v84", "v85", "v86", "v87", "v88", "v89", "v90", "v91", "v92", "v93", "v94", "v95", "v96", "v97", "v98", "v99", "v100", "v101", "v102", "v103", "v104", "v105", "v106", "v107", "v108", "v109", "v110", "v111", "v112", "v113", "v114", "v115", "v116", "v117", "v118", "v119", "v120", "v121", "v122", "v123", "v124", "v125", "v126", "v127", "v128", "v129", "v130", "v131", "v132", "v133", "v134", "v135", "v136", "v137", "v138", "v139", "v140", "v141", "v142", "v143", "v144", "v145", "v146", "v147", "v148", "v149", "v150", "v151", "v152", "v153", "v154", "v155", "v156", "v157", "v158", "v159", "v160", "v161", "v162", "v163", "v164", "v165", "v166", "v167", "v168", "v169", "v170", "v171", "v172", "v173", "v174", "v175", "v176", "v177", "v178", "v179", "v180", "v181", "v182", "v183", "v184", "v185", "v186", "v187", "v188", "v189", "v190", "v191", "v244", "v245", "v246", "vcc", "scc", "memory");
}

__device__ __forceinline__ void r_t(u32& c0, u32& r0, u32& te, u32& tn, u32& gb, u32& nl, u32 tw, u32x4 rs, u32 fv, u32 kr, u32 tb, u32 lk0, u32 lk, u32 ro, u32 rb, u32 rk STP) {
    asm volatile(
        ".p2align 6\n\t"
        "s_mov_b64 s[96:97], 0\n\t"
        "s_mov_b32 s98, %[rb]\n\t"
        "s_mov_b32 s99, 0x2000\n\t"
        "v_mov_b32 v200, 0\n\t"
        "s_mov_b32 s92, 0\n\t"
        "s_mov_b32 s94, %[fv]\n\t"
        "s_sub_u32 s95, %[te], %[lk0]\n\t"
        "s_setprio 3\n\t"
        "Lua_%=:\n\t"
        "s_memrealtime s[90:91]\n\t"
        "s_waitcnt lgkmcnt(0)\n\t"
        "s_mul_i32 s90, s90, 24\n\t"
        "s_sub_u32 s88, %[tb], s90\n\t"
        "s_cmp_lt_u32 s88, 0x4000\n\t"
        "s_cbranch_scc1 Lua_%=\n\t"
        "s_barrier\n\t"
        "s_sub_u32 s93, s95, 520\n\t"
        "Lub_%=:\n\t"
        "s_memrealtime s[90:91]\n\t"
        "s_waitcnt lgkmcnt(0)\n\t"
        "s_mul_i32 s90, s90, 24\n\t"
        "s_sub_u32 s88, s93, s90\n\t"
        "s_cmp_lt_u32 s88, 0x4000\n\t"
        "s_cbranch_scc1 Lub_%=\n\t"
        "s_mov_b32 s63, s88\n\t"
        "s_memrealtime s[64:65]\n\t"
        "s_nop 0\n\t"
        "s_memrealtime s[66:67]\n\t"
        "s_nop 0\n\t"
        "s_memrealtime s[68:69]\n\t"
        "s_nop 0\n\t"
        "s_memrealtime s[70:71]\n\t"
        "s_nop 0\n\t"
        "s_memrealtime s[72:73]\n\t"
        "s_nop 0\n\t"
        "s_memrealtime s[74:75]\n\t"
        "s_memtime s[90:91]\n\t"
        "s_memrealtime s[76:77]\n\t"
        "s_nop 0\n\t"
        "s_memrealtime s[78:79]\n\t"
        "s_nop 0\n\t"
        "s_memrealtime s[80:81]\n\t"
        "s_nop 0\n\t"
        "s_memrealtime s[82:83]\n\t"
        "s_nop 0\n\t"
        "s_memrealtime s[84:85]\n\t"
        "s_nop 0\n\t"
        "s_memrealtime s[86:87]\n\t"
        "s_nop 0\n\t"
        "s_nop 7\n\t"
        "s_waitcnt lgkmcnt(0)\n\t"
        "s_add_u32 s64, s64, s66\n\t"
        "s_add_u32 s64, s64, s68\n\t"
        "s_add_u32 s64, s64, s70\n\t"
        "s_add_u32 s64, s64, s72\n\t"
        "s_add_u32 s64, s64, s74\n\t"
        "s_add_u32 s64, s64, s76\n\t"
        "s_add_u32 s64, s64, s78\n\t"
        "s_add_u32 s64, s64, s80\n\t"
        "s_add_u32 s64, s64, s82\n\t"
        "s_add_u32 s64, s64, s84\n\t"
        "s_add_u32 s64, s64, s86\n\t"
        "s_lshl_b32 s64, s64, 1\n\t"
        "s_mov_b32 %[c0], s90\n\t"
        "s_mov_b32 %[r0], s64\n\t"
        "s_sub_u32 s93, s95, %[r0]\n\t"
        "s_mul_i32 s93, s93, %[kr]\n\t"
        "s_ashr_i32 s93, s93, 12\n\t"
        "s_add_u32 s93, s93, %[c0]\n\t"
        "Ldl_%=:\n\t"
        "s_memtime s[90:91]\n\t"
        "s_waitcnt lgkmcnt(0)\n\t"
        "s_sub_u32 s88, s93, s90\n\t"
        "s_sub_u32 s89, s88, 336\n\t"
        "s_cmp_gt_u32 s89, 0x4000\n\t"
        "s_cbranch_scc1 Lel_%=\n\t"
        "s_sleep 1\n\t"
        "s_branch Ldl_%=\n\t"
        "Lel_%=:\n\t"
        "s_sub_u32 s88, s88, 112\n\t"
        "s_max_i32 s88, s88, 0\n\t"
        "s_min_i32 s88, s88, 224\n\t"
        "s_andn2_b32 s88, s88, 3\n\t"
        "s_getpc_b64 s[90:91]\n\t"
        "Lgl_%=:\n\t"
        "s_sub_u32 s90, s90, s88\n\t"
        "s_subb_u32 s91, s91, 0\n\t"
        "s_add_u32 s90, s90, Lzl_%=-Lgl_%=\n\t"
        "s_addc_u32 s91, s91, 0\n\t"
        "s_setpc_b64 s[90:91]\n\t"
        "s_nop 0\n\ts_nop 0\n\ts_nop 0\n\ts_nop 0\n\t"
        "s_nop 0\n\ts_nop 0\n\ts_nop 0\n\ts_nop 0\n\t"
        "s_nop 0\n\ts_nop 0\n\ts_nop 0\n\ts_nop 0\n\t"
        "s_nop 0\n\ts_nop 0\n\ts_nop 0\n\ts_nop 0\n\t"
        "s_nop 0\n\ts_nop 0\n\ts_nop 0\n\ts_nop 0\n\t"
        "s_nop 0\n\ts_nop 0\n\ts_nop 0\n\ts_nop 0\n\t"
        "s_nop 0\n\ts_nop 0\n\ts_nop 0\n\ts_nop 0\n\t"
        "s_nop 0\n\ts_nop 0\n\ts_nop 0\n\ts_nop 0\n\t"
        "s_nop 0\n\ts_nop 0\n\ts_nop 0\n\ts_nop 0\n\t"
        "s_nop 0\n\ts_nop 0\n\ts_nop 0\n\ts_nop 0\n\t"
        "s_nop 0\n\ts_nop 0\n\ts_nop 0\n\ts_nop 0\n\t"
        "s_nop 0\n\ts_nop 0\n\ts_nop 0\n\ts_nop 0\n\t"
        "s_nop 0\n\ts_nop 0\n\ts_nop 0\n\ts_nop 0\n\t"
        "s_nop 0\n\ts_nop 0\n\ts_nop 0\n\ts_nop 0\n\t"
        "Lzl_%=:\n\t"
        "buffer_load_dword v200, %[ro], %[rs], %[rb] offen sc1\n\t"
        "s_cmp_lt_i32 s63, -700\n\t"
        "s_cbranch_scc0 Ly%=\n\t"
        "s_waitcnt vmcnt(0)\n\t"
        "Ly%=:\n\t"
        "s_sub_u32 s93, %[te], %[r0]\n\t"
        "s_mul_i32 s93, s93, %[kr]\n\t"
        "s_ashr_i32 s93, s93, 12\n\t"
        "s_add_u32 s93, s93, %[c0]\n\t"
        "Lde_%=:\n\t"
        "s_memtime s[90:91]\n\t"
        "v_cmp_gt_i32 vcc, s96, v200\n\t"
        "s_waitcnt lgkmcnt(0)\n\t"
        "s_cbranch_vccnz Ls%=\n\t"
        "s_sub_u32 s88, s93, s90\n\t"
        "s_sub_u32 s89, s88, 396\n\t"
        "s_cmp_gt_u32 s89, 0x4000\n\t"
        "s_cbranch_scc1 Lee_%=\n\t"
        "s_sleep 1\n\t"
        "s_branch Lde_%=\n\t"
        "Lee_%=:\n\t"
        "s_sub_u32 s88, s88, 172\n\t"
        "s_max_i32 s88, s88, 0\n\t"
        "s_min_i32 s88, s88, 224\n\t"
        "s_andn2_b32 s88, s88, 3\n\t"
        "s_getpc_b64 s[90:91]\n\t"
        "Lge_%=:\n\t"
        "s_sub_u32 s90, s90, s88\n\t"
        "s_subb_u32 s91, s91, 0\n\t"
        "s_add_u32 s90, s90, Lze_%=-Lge_%=\n\t"
        "s_addc_u32 s91, s91, 0\n\t"
        "s_setpc_b64 s[90:91]\n\t"
        "s_nop 0\n\ts_nop 0\n\ts_nop 0\n\ts_nop 0\n\t"
        "s_nop 0\n\ts_nop 0\n\ts_nop 0\n\ts_nop 0\n\t"
        "s_nop 0\n\ts_nop 0\n\ts_nop 0\n\ts_nop 0\n\t"
        "s_nop 0\n\ts_nop 0\n\ts_nop 0\n\ts_nop 0\n\t"
        "s_nop 0\n\ts_nop 0\n\ts_nop 0\n\ts_nop 0\n\t"
        "s_nop 0\n\ts_nop 0\n\ts_nop 0\n\ts_nop 0\n\t"
        "s_nop 0\n\ts_nop 0\n\ts_nop 0\n\ts_nop 0\n\t"
        "s_nop 0\n\ts_nop 0\n\ts_nop 0\n\ts_nop 0\n\t"
        "s_nop 0\n\ts_nop 0\n\ts_nop 0\n\ts_nop 0\n\t"
        "s_nop 0\n\ts_nop 0\n\ts_nop 0\n\ts_nop 0\n\t"
        "s_nop 0\n\ts_nop 0\n\ts_nop 0\n\ts_nop 0\n\t"
        "s_nop 0\n\ts_nop 0\n\ts_nop 0\n\ts_nop 0\n\t"
        "s_nop 0\n\ts_nop 0\n\ts_nop 0\n\ts_nop 0\n\t"
        "s_nop 0\n\ts_nop 0\n\ts_nop 0\n\ts_nop 0\n\t"
        "Lze_%=:\n\t"
        "v_cmp_gt_i32 vcc, s96, v200\n\t"
        "s_cbranch_vccnz Ls%=\n\t"
        "s_barrier\n\t"
        "s_mov_b32 s92, 1\n\t"
        "s_waitcnt vmcnt(0)\n\t"
        "v_cmp_gt_i32 vcc, s96, v200\n\t"
        "v_mov_b32 v197, %[fv]\n\t"
        "s_cbranch_vccnz Ls%=\n\t"
        "Lw%=:\n\t"
        "s_sub_u32 %[nl], 0x2000, s99\n\t"
        "v_mov_b32 v196, %[te]\n\t"
        "v_mov_b32 v198, s94\n\t"
        "ds_write_b96 %[tw], v[196:198]\n\t"
        "s_cmp_lg_u32 s96, 0\n\t"
        "s_cbranch_scc1 Lnn%=\n\t"
        "v_readlane_b32 %[gb], v200, 0\n\t"
        "s_and_b32 %[gb], %[gb], 15\n\t"
        "s_lshl_b32 %[gb], %[gb], 4\n\t"
        "v_subrev_u32 v200, %[tn], v200\n\t"
        "v_bfe_i32 v200, v200, 0, 30\n\t"
        "s_nop 1\n\t"
        "v_max_i32_dpp v200, v200, v200 quad_perm:[1,0,3,2] row_mask:0xf bank_mask:0xf\n\t"
        "s_nop 1\n\t"
        "v_max_i32_dpp v200, v200, v200 quad_perm:[2,3,0,1] row_mask:0xf bank_mask:0xf\n\t"
        "s_nop 1\n\t"
        "v_max_i32_dpp v200, v200, v200 row_ror:4 row_mask:0xf bank_mask:0xf\n\t"
        "s_nop 1\n\t"
        "v_max_i32_dpp v200, v200, v200 row_ror:8 row_mask:0xf bank_mask:0xf\n\t"
        "s_nop 0\n\t"
        "v_readlane_b32 s88, v200, 0\n\t"
        "v_readlane_b32 s89, v200, 16\n\t"
        "s_max_i32 s88, s88, s89\n\t"
        "s_add_u32 %[tn], %[tn], s88\n\t"
        "Lnn%=:\n\t"
        "s_waitcnt lgkmcnt(0)\n\t"
        "s_barrier\n\t"
        "s_branch Lend%=\n\t"
        "Ls%=:\n\t"
        "s_mov_b32 s90, 0xbfffffff\n\t"
        "s_cmp_eq_u32 s97, 0\n\t"
        "s_cselect_b32 s90, 0xffbfffbf, s90\n\t"
        "s_mov_b32 s96, 0xc0000000\n\t"
        "s_waitcnt vmcnt(0) lgkmcnt(0)\n\t"
        "v_cmp_eq_u32 s[88:89], s90, v200\n\t"
        "v_cmp_gt_i32 vcc, s96, v200\n\t"
        "v_and_b32 v202, 0xc0000001, v200\n\t"
        "v_subrev_u32 v200, %[te], v200\n\t"
        "s_sub_u32 s99, s99, 1\n\t"
        "s_cselect_b32 s88, 0, s88\n\t"
        "s_or_b32 s89, s88, vcc_lo\n\t"
        "s_cbranch_scc0 Lu%=\n\t"
        "v_bfe_i32 v200, v200, 0, 30\n\t"
        "s_cmp_lg_u32 s88, 0\n\t"
        "v_cndmask_b32 v200, 0, v200, vcc\n\t"
        "s_cbranch_scc1 Lq%=\n\t"
        "v_cmp_eq_u32 vcc, 0x80000000, v202\n\t"
        "s_and_b32 s98, s97, 1\n\t"
        "v_max_i32_dpp v200, v200, v200 quad_perm:[1,0,3,2] row_mask:0xf bank_mask:0xf\n\t"
        "s_lshl_b32 s98, s98, 12\n\t"
        "s_lshr_b32 s88, s97, 1\n\t"
        "v_max_i32_dpp v200, v200, v200 quad_perm:[2,3,0,1] row_mask:0xf bank_mask:0xf\n\t"
        "s_lshl_b32 s88, s88, 14\n\t"
        "s_add_u32 s98, s98, s88\n\t"
        "v_max_i32_dpp v200, v200, v200 row_ror:4 row_mask:0xf bank_mask:0xf\n\t"
        "s_add_u32 s98, s98, %[rk]\n\t"
        "s_add_u32 s97, s97, 1\n\t"
        "v_max_i32_dpp v200, v200, v200 row_ror:8 row_mask:0xf bank_mask:0xf\n\t"
        "s_nop 0\n\t"
        "v_readlane_b32 s88, v200, 0\n\t"
        "v_readlane_b32 s89, v200, 16\n\t"
        "s_max_i32 s88, s88, s89\n\t"
        "s_max_i32 s88, s88, 0\n\t"
        "s_add_u32 %[te], %[te], s88\n\t"
        "s_add_u32 %[tn], %[tn], s88\n\t"
        "s_cbranch_vccz Lu%=\n\t"
        "s_cmp_gt_u32 s97, 4\n\t"
        "s_cbranch_scc1 Lu%=\n\t"
        "s_sub_u32 s90, %[te], %[lk]\n\t"
        "Lt%=:\n\t"
        "s_memrealtime s[88:89]\n\t"
        "s_waitcnt lgkmcnt(0)\n\t"
        "s_mul_i32 s88, s88, 24\n\t"
        "s_sub_u32 s88, s90, s88\n\t"
        "s_cmp_lt_u32 s88, 0x4000\n\t"
        "s_cbranch_scc1 Lt%=\n\t"
        "s_add_u32 s89, %[rk], 20480\n\t"
        "s_cmp_lt_i32 s88, -3000\n\t"
        "s_cselect_b32 s97, 4, s97\n\t"
        "s_cselect_b32 s98, s89, s98\n\t"
        "v_mov_b32 v200, 0\n\t"
        "buffer_load_dword v200, %[ro], %[rs], s98 offen sc1\n\t"
        "s_add_u32 s88, s88, %[lk]\n\t"
        "s_cmp_lt_i32 s88, 0\n\t"
        "s_cbranch_scc1 Ls%=\n\t"
        "s_cmp_eq_u32 s92, 0\n\t"
        "s_cbranch_scc1 La%=\n\t"
        "s_branch Ls%=\n\t"
        "Lq%=:\n\t"
        "s_sleep 6\n\t"
        "buffer_load_dword v200, %[ro], %[rs], s98 offen sc1\n\t"
        "s_branch Ls%=\n\t"
        "Lu%=:\n\t"
        "v_mov_b32 v200, 0\n\t"
        "s_cmp_lg_u32 s92, 0\n\t"
        "s_cselect_b32 s94, 0, s94\n\t"
        "s_cbranch_scc1 Lw%=\n\t"
        "La%=:\n\t"
        "s_sub_u32 s88, %[te], %[r0]\n\t"
        "s_cmp_lt_i32 s88, 4000\n\t"
        "s_cbranch_scc1 Ly%=\n\t"
        "s_memrealtime s[64:65]\n\t"
        "s_nop 0\n\t"
        "s_memrealtime s[66:67]\n\t"
        "s_nop 0\n\t"
        "s_memrealtime s[68:69]\n\t"
        "s_nop 0\n\t"
        "s_memrealtime s[70:71]\n\t"
        "s_nop 0\n\t"
        "s_memrealtime s[72:73]\n\t"
        "s_nop 0\n\t"
        "s_memrealtime s[74:75]\n\t"
        "s_memtime s[90:91]\n\t"
        "s_memrealtime s[76:77]\n\t"
        "s_nop 0\n\t"
        "s_memrealtime s[78:79]\n\t"
        "s_nop 0\n\t"
        "s_memrealtime s[80:81]\n\t"
        "s_nop 0\n\t"
        "s_memrealtime s[82:83]\n\t"
        "s_nop 0\n\t"
        "s_memrealtime s[84:85]\n\t"
        "s_nop 0\n\t"
        "s_memrealtime s[86:87]\n\t"
        "s_nop 0\n\t"
        "s_nop 7\n\t"
        "s_waitcnt lgkmcnt(0)\n\t"
        "s_add_u32 s64, s64, s66\n\t"
        "s_add_u32 s64, s64, s68\n\t"
        "s_add_u32 s64, s64, s70\n\t"
        "s_add_u32 s64, s64, s72\n\t"
        "s_add_u32 s64, s64, s74\n\t"
        "s_add_u32 s64, s64, s76\n\t"
        "s_add_u32 s64, s64, s78\n\t"
        "s_add_u32 s64, s64, s80\n\t"
        "s_add_u32 s64, s64, s82\n\t"
        "s_add_u32 s64, s64, s84\n\t"
        "s_add_u32 s64, s64, s86\n\t"
        "s_lshl_b32 s64, s64, 1\n\t"
        "s_mov_b32 %[c0], s90\n\t"
        "s_mov_b32 %[r0], s64\n\t"
        "s_branch Ly%=\n\t"
        "Lend%=:\n\t"
        : [c0] "=&s"(c0), [r0] "=&s"(r0), [nl] "=&s"(nl), [te] "+&s"(te), [tn] "+&s"(tn), [gb] "+&s"(gb)
        : [tw] "v"(tw), [rs] "s"(rs), [fv] "s"(fv), [kr] "s"(kr), [tb] "s"(tb), [lk0] "s"(lk0), [ro] "v"(ro), [rb] "s"(rb), [rk] "s"(rk), [lk] "s"(lk)
        : "s63", "s64", "s65", "s66", "s67", "s68", "s69", "s70", "s71", "s72", "s73", "s74", "s75", "s76", "s77", "s78", "s79", "s80", "s81", "s82", "s83", "s84", "s85", "s86", "s87", "s88", "s89", "s90", "s91", "s92", "s93", "s94", "s95", "s96", "s97", "s98", "s99", "v196", "v197", "v198", "v199", "v200", "v201", "v202", "vcc", "scc", "memory");
}

__device__ __forceinline__ void w_q(const u32x32 (&W)[5], u32x4 rs, u32 now, u32 xa, u32 vm, u32 xs, u32 xd, u32 o1, u32 ob) {
    asm volatile(
        ".p2align 6\n\t"
        "s_setprio 2\n\t"
        "s_barrier\n\t"
        "ds_read_b128 v[176:179], %[xa] offset:0\n\t"
        "ds_read_b128 v[180:183], %[xa] offset:20480\n\t"
        "ds_read_b128 v[184:187], %[xa] offset:1024\n\t"
        "ds_read_b128 v[188:191], %[xa] offset:21504\n\t"
        "ds_read_b128 v[192:195], %[xa] offset:2048\n\t"
        "ds_read_b128 v[196:199], %[xa] offset:22528\n\t"
        "ds_read_b128 v[200:203], %[xa] offset:3072\n\t"
        "ds_read_b128 v[204:207], %[xa] offset:23552\n\t"
        "s_waitcnt lgkmcnt(7)\n\t"
        "v_mfma_f32_16x16x32_bf16 v[208:211], v[16:19], v[176:179], 0\n\t"
        "v_mfma_f32_16x16x32_bf16 v[212:215], v[96:99], v[176:179], 0\n\t"
        "ds_read_b128 v[176:179], %[xa] offset:4096\n\t"
        "s_waitcnt lgkmcnt(7)\n\t"
        "v_mfma_f32_16x16x32_bf16 v[216:219], v[16:19], v[180:183], 0\n\t"
        "v_mfma_f32_16x16x32_bf16 v[220:223], v[96:99], v[180:183], 0\n\t"
        "ds_read_b128 v[180:183], %[xa] offset:24576\n\t"
        "s_waitcnt lgkmcnt(7)\n\t"
        "v_mfma_f32_16x16x32_bf16 v[208:211], v[20:23], v[184:187], v[208:211]\n\t"
        "v_mfma_f32_16x16x32_bf16 v[212:215], v[100:103], v[184:187], v[212:215]\n\t"
        "ds_read_b128 v[184:187], %[xa] offset:5120\n\t"
        "s_waitcnt lgkmcnt(7)\n\t"
        "v_mfma_f32_16x16x32_bf16 v[216:219], v[20:23], v[188:191], v[216:219]\n\t"
        "v_mfma_f32_16x16x32_bf16 v[220:223], v[100:103], v[188:191], v[220:223]\n\t"
        "ds_read_b128 v[188:191], %[xa] offset:25600\n\t"
        "s_waitcnt lgkmcnt(7)\n\t"
        "v_mfma_f32_16x16x32_bf16 v[208:211], v[24:27], v[192:195], v[208:211]\n\t"
        "v_mfma_f32_16x16x32_bf16 v[212:215], v[104:107], v[192:195], v[212:215]\n\t"
        "ds_read_b128 v[192:195], %[xa] offset:6144\n\t"
        "s_waitcnt lgkmcnt(7)\n\t"
        "v_mfma_f32_16x16x32_bf16 v[216:219], v[24:27], v[196:199], v[216:219]\n\t"
        "v_mfma_f32_16x16x32_bf16 v[220:223], v[104:107], v[196:199], v[220:223]\n\t"
        "ds_read_b128 v[196:199], %[xa] offset:26624\n\t"
        "s_waitcnt lgkmcnt(7)\n\t"
        "v_mfma_f32_16x16x32_bf16 v[208:211], v[28:31], v[200:203], v[208:211]\n\t"
        "v_mfma_f32_16x16x32_bf16 v[212:215], v[108:111], v[200:203], v[212:215]\n\t"
        "ds_read_b128 v[200:203], %[xa] offset:7168\n\t"
        "s_waitcnt lgkmcnt(7)\n\t"
        "v_mfma_f32_16x16x32_bf16 v[216:219], v[28:31], v[204:207], v[216:219]\n\t"
        "v_mfma_f32_16x16x32_bf16 v[220:223], v[108:111], v[204:207], v[220:223]\n\t"
        "ds_read_b128 v[204:207], %[xa] offset:27648\n\t"
        "s_waitcnt lgkmcnt(7)\n\t"
        "v_mfma_f32_16x16x32_bf16 v[208:211], v[32:35], v[176:179], v[208:211]\n\t"
        "v_mfma_f32_16x16x32_bf16 v[212:215], v[112:115], v[176:179], v[212:215]\n\t"
        "ds_read_b128 v[176:179], %[xa] offset:8192\n\t"
        "s_waitcnt lgkmcnt(7)\n\t"
        "v_mfma_f32_16x16x32_bf16 v[216:219], v[32:35], v[180:183], v[216:219]\n\t"
        "v_mfma_f32_16x16x32_bf16 v[220:223], v[112:115], v[180:183], v[220:223]\n\t"
        "ds_read_b128 v[180:183], %[xa] offset:28672\n\t"
        "s_waitcnt lgkmcnt(7)\n\t"
        "v_mfma_f32_16x16x32_bf16 v[208:211], v[36:39], v[184:187], v[208:211]\n\t"
        "v_mfma_f32_16x16x32_bf16 v[212:215], v[116:119], v[184:187], v[212:215]\n\t"
        "ds_read_b128 v[184:187], %[xa] offset:9216\n\t"
        "s_waitcnt lgkmcnt(7)\n\t"
        "v_mfma_f32_16x16x32_bf16 v[216:219], v[36:39], v[188:191], v[216:219]\n\t"
        "v_mfma_f32_16x16x32_bf16 v[220:223], v[116:119], v[188:191], v[220:223]\n\t"
        "ds_read_b128 v[188:191], %[xa] offset:29696\n\t"
        "s_waitcnt lgkmcnt(7)\n\t"
        "v_mfma_f32_16x16x32_bf16 v[208:211], v[40:43], v[192:195], v[208:211]\n\t"
        "v_mfma_f32_16x16x32_bf16 v[212:215], v[120:123], v[192:195], v[212:215]\n\t"
        "ds_read_b128 v[192:195], %[xa] offset:10240\n\t"
        "s_waitcnt lgkmcnt(7)\n\t"
        "v_mfma_f32_16x16x32_bf16 v[216:219], v[40:43], v[196:199], v[216:219]\n\t"
        "v_mfma_f32_16x16x32_bf16 v[220:223], v[120:123], v[196:199], v[220:223]\n\t"
        "ds_read_b128 v[196:199], %[xa] offset:30720\n\t"
        "s_waitcnt lgkmcnt(7)\n\t"
        "v_mfma_f32_16x16x32_bf16 v[208:211], v[44:47], v[200:203], v[208:211]\n\t"
        "v_mfma_f32_16x16x32_bf16 v[212:215], v[124:127], v[200:203], v[212:215]\n\t"
        "ds_read_b128 v[200:203], %[xa] offset:11264\n\t"
        "s_waitcnt lgkmcnt(7)\n\t"
        "v_mfma_f32_16x16x32_bf16 v[216:219], v[44:47], v[204:207], v[216:219]\n\t"
        "v_mfma_f32_16x16x32_bf16 v[220:223], v[124:127], v[204:207], v[220:223]\n\t"
        "ds_read_b128 v[204:207], %[xa] offset:31744\n\t"
        "s_waitcnt lgkmcnt(7)\n\t"
        "v_mfma_f32_16x16x32_bf16 v[208:211], v[48:51], v[176:179], v[208:211]\n\t"
        "v_mfma_f32_16x16x32_bf16 v[212:215], v[128:131], v[176:179], v[212:215]\n\t"
        "ds_read_b128 v[176:179], %[xa] offset:12288\n\t"
        "s_waitcnt lgkmcnt(7)\n\t"
        "v_mfma_f32_16x16x32_bf16 v[216:219], v[48:51], v[180:183], v[216:219]\n\t"
        "v_mfma_f32_16x16x32_bf16 v[220:223], v[128:131], v[180:183], v[220:223]\n\t"
        "ds_read_b128 v[180:183], %[xa] offset:32768\n\t"
        "s_waitcnt lgkmcnt(7)\n\t"
        "v_mfma_f32_16x16x32_bf16 v[208:211], v[52:55], v[184:187], v[208:211]\n\t"
        "v_mfma_f32_16x16x32_bf16 v[212:215], v[132:135], v[184:187], v[212:215]\n\t"
        "ds_read_b128 v[184:187], %[xa] offset:13312\n\t"
        "s_waitcnt lgkmcnt(7)\n\t"
        "v_mfma_f32_16x16x32_bf16 v[216:219], v[52:55], v[188:191], v[216:219]\n\t"
        "v_mfma_f32_16x16x32_bf16 v[220:223], v[132:135], v[188:191], v[220:223]\n\t"
        "ds_read_b128 v[188:191], %[xa] offset:33792\n\t"
        "s_waitcnt lgkmcnt(7)\n\t"
        "v_mfma_f32_16x16x32_bf16 v[208:211], v[56:59], v[192:195], v[208:211]\n\t"
        "v_mfma_f32_16x16x32_bf16 v[212:215], v[136:139], v[192:195], v[212:215]\n\t"
        "ds_read_b128 v[192:195], %[xa] offset:14336\n\t"
        "s_waitcnt lgkmcnt(7)\n\t"
        "v_mfma_f32_16x16x32_bf16 v[216:219], v[56:59], v[196:199], v[216:219]\n\t"
        "v_mfma_f32_16x16x32_bf16 v[220:223], v[136:139], v[196:199], v[220:223]\n\t"
        "ds_read_b128 v[196:199], %[xa] offset:34816\n\t"
        "s_waitcnt lgkmcnt(7)\n\t"
        "v_mfma_f32_16x16x32_bf16 v[208:211], v[60:63], v[200:203], v[208:211]\n\t"
        "v_mfma_f32_16x16x32_bf16 v[212:215], v[140:143], v[200:203], v[212:215]\n\t"
        "ds_read_b128 v[200:203], %[xa] offset:15360\n\t"
        "s_waitcnt lgkmcnt(7)\n\t"
        "v_mfma_f32_16x16x32_bf16 v[216:219], v[60:63], v[204:207], v[216:219]\n\t"
        "v_mfma_f32_16x16x32_bf16 v[220:223], v[140:143], v[204:207], v[220:223]\n\t"
        "ds_read_b128 v[204:207], %[xa] offset:35840\n\t"
        "s_waitcnt lgkmcnt(7)\n\t"
        "v_mfma_f32_16x16x32_bf16 v[208:211], v[64:67], v[176:179], v[208:211]\n\t"
        "v_mfma_f32_16x16x32_bf16 v[212:215], v[144:147], v[176:179], v[212:215]\n\t"
        "ds_read_b128 v[176:179], %[xa] offset:16384\n\t"
        "s_waitcnt lgkmcnt(7)\n\t"
        "v_mfma_f32_16x16x32_bf16 v[216:219], v[64:67], v[180:183], v[216:219]\n\t"
        "v_mfma_f32_16x16x32_bf16 v[220:223], v[144:147], v[180:183], v[220:223]\n\t"
        "ds_read_b128 v[180:183], %[xa] offset:36864\n\t"
        "s_waitcnt lgkmcnt(7)\n\t"
        "v_mfma_f32_16x16x32_bf16 v[208:211], v[68:71], v[184:187], v[208:211]\n\t"
        "v_mfma_f32_16x16x32_bf16 v[212:215], v[148:151], v[184:187], v[212:215]\n\t"
        "ds_read_b128 v[184:187], %[xa] offset:17408\n\t"
        "s_waitcnt lgkmcnt(7)\n\t"
        "v_mfma_f32_16x16x32_bf16 v[216:219], v[68:71], v[188:191], v[216:219]\n\t"
        "v_mfma_f32_16x16x32_bf16 v[220:223], v[148:151], v[188:191], v[220:223]\n\t"
        "ds_read_b128 v[188:191], %[xa] offset:37888\n\t"
        "s_waitcnt lgkmcnt(7)\n\t"
        "v_mfma_f32_16x16x32_bf16 v[208:211], v[72:75], v[192:195], v[208:211]\n\t"
        "v_mfma_f32_16x16x32_bf16 v[212:215], v[152:155], v[192:195], v[212:215]\n\t"
        "ds_read_b128 v[192:195], %[xa] offset:18432\n\t"
        "s_waitcnt lgkmcnt(7)\n\t"
        "v_mfma_f32_16x16x32_bf16 v[216:219], v[72:75], v[196:199], v[216:219]\n\t"
        "v_mfma_f32_16x16x32_bf16 v[220:223], v[152:155], v[196:199], v[220:223]\n\t"
        "ds_read_b128 v[196:199], %[xa] offset:38912\n\t"
        "s_waitcnt lgkmcnt(7)\n\t"
        "v_mfma_f32_16x16x32_bf16 v[208:211], v[76:79], v[200:203], v[208:211]\n\t"
        "v_mfma_f32_16x16x32_bf16 v[212:215], v[156:159], v[200:203], v[212:215]\n\t"
        "ds_read_b128 v[200:203], %[xa] offset:19456\n\t"
        "s_waitcnt lgkmcnt(7)\n\t"
        "v_mfma_f32_16x16x32_bf16 v[216:219], v[76:79], v[204:207], v[216:219]\n\t"
        "v_mfma_f32_16x16x32_bf16 v[220:223], v[156:159], v[204:207], v[220:223]\n\t"
        "ds_read_b128 v[204:207], %[xa] offset:39936\n\t"
        "s_waitcnt lgkmcnt(7)\n\t"
        "v_mfma_f32_16x16x32_bf16 v[208:211], v[80:83], v[176:179], v[208:211]\n\t"
        "v_mfma_f32_16x16x32_bf16 v[212:215], v[160:163], v[176:179], v[212:215]\n\t"
        "s_waitcnt lgkmcnt(6)\n\t"
        "v_mfma_f32_16x16x32_bf16 v[216:219], v[80:83], v[180:183], v[216:219]\n\t"
        "v_mfma_f32_16x16x32_bf16 v[220:223], v[160:163], v[180:183], v[220:223]\n\t"
        "s_waitcnt lgkmcnt(5)\n\t"
        "v_mfma_f32_16x16x32_bf16 v[208:211], v[84:87], v[184:187], v[208:211]\n\t"
        "v_mfma_f32_16x16x32_bf16 v[212:215], v[164:167], v[184:187], v[212:215]\n\t"
        "s_waitcnt lgkmcnt(4)\n\t"
        "v_mfma_f32_16x16x32_bf16 v[216:219], v[84:87], v[188:191], v[216:219]\n\t"
        "v_mfma_f32_16x16x32_bf16 v[220:223], v[164:167], v[188:191], v[220:223]\n\t"
        "s_waitcnt lgkmcnt(3)\n\t"
        "v_mfma_f32_16x16x32_bf16 v[208:211], v[88:91], v[192:195], v[208:211]\n\t"
        "v_mfma_f32_16x16x32_bf16 v[212:215], v[168:171], v[192:195], v[212:215]\n\t"
        "s_waitcnt lgkmcnt(2)\n\t"
        "v_mfma_f32_16x16x32_bf16 v[216:219], v[88:91], v[196:199], v[216:219]\n\t"
        "v_mfma_f32_16x16x32_bf16 v[220:223], v[168:171], v[196:199], v[220:223]\n\t"
        "s_waitcnt lgkmcnt(1)\n\t"
        "v_mfma_f32_16x16x32_bf16 v[208:211], v[92:95], v[200:203], v[208:211]\n\t"
        "v_mfma_f32_16x16x32_bf16 v[212:215], v[172:175], v[200:203], v[212:215]\n\t"
        "s_waitcnt lgkmcnt(0)\n\t"
        "v_mfma_f32_16x16x32_bf16 v[216:219], v[92:95], v[204:207], v[216:219]\n\t"
        "v_mfma_f32_16x16x32_bf16 v[220:223], v[172:175], v[204:207], v[220:223]\n\t"
        "s_setprio 0\n\t"
        "s_nop 6\n\t"
        "v_cvt_pk_bf16_f32 v224, v208, v209\n\t"
        "v_cvt_pk_bf16_f32 v225, v210, v211\n\t"
        "v_cvt_pk_bf16_f32 v226, v212, v213\n\t"
        "v_cvt_pk_bf16_f32 v227, v214, v215\n\t"
        "v_cvt_pk_bf16_f32 v228, v216, v217\n\t"
        "v_cvt_pk_bf16_f32 v229, v218, v219\n\t"
        "v_cvt_pk_bf16_f32 v230, v220, v221\n\t"
        "v_cvt_pk_bf16_f32 v231, v222, v223\n\t"
        "s_add_u32 s93, %[ob], 0x1000\n\t"
        "buffer_store_dwordx4 v[224:227], %[o1], %[rs], %[ob] offen sc1\n\t"
        "buffer_store_dwordx4 v[228:231], %[o1], %[rs], s93 offen sc1\n\t"
        "s_mov_b32 s92, m0\n\t"
        "s_add_u32 m0, %[xd], 0\n\t"
        "s_add_u32 s93, %[xs], 0\n\t"
        "buffer_load_dwordx4 %[vm], %[rs], s93 offen offset:0 sc1 lds\n\t"
        "buffer_load_dwordx4 %[vm], %[rs], s93 offen offset:1024 sc1 lds\n\t"
        "buffer_load_dwordx4 %[vm], %[rs], s93 offen offset:2048 sc1 lds\n\t"
        "buffer_load_dwordx4 %[vm], %[rs], s93 offen offset:3072 sc1 lds\n\t"
        "s_add_u32 m0, %[xd], 4096\n\t"
        "s_add_u32 s93, %[xs], 4096\n\t"
        "buffer_load_dwordx4 %[vm], %[rs], s93 offen offset:0 sc1 lds\n\t"
        "s_mov_b32 m0, s92\n\t"
        "s_cmp_eq_u32 %[now], 0\n\t"
        "s_cbranch_scc1 Ln%=\n\t"
        "s_waitcnt vmcnt(0)\n\t"
        "Ln%=:\n\t"
        "s_waitcnt vmcnt(7)\n\t"
        :
        : [w0] "{v[16:47]}"(W[0]), [w1] "{v[48:79]}"(W[1]), [w2] "{v[80:111]}"(W[2]), [w3] "{v[112:143]}"(W[3]), [w4] "{v[144:175]}"(W[4]), [rs] "s"(rs), [xa] "v"(xa), [o1] "v"(o1), [ob] "s"(ob), [vm] "v"(vm), [xs] "s"(xs), [xd] "s"(xd), [now] "s"(now)
        : "s92", "s93", "v176", "v177", "v178", "v179", "v180", "v181", "v182", "v183", "v184", "v185", "v186", "v187", "v188", "v189", "v190", "v191", "v192", "v193", "v194", "v195", "v196", "v197", "v198", "v199", "v200", "v201", "v202", "v203", "v204", "v205", "v206", "v207", "v208", "v209", "v210", "v211", "v212", "v213", "v214", "v215", "v216", "v217", "v218", "v219", "v220", "v221", "v222", "v223", "v224", "v225", "v226", "v227", "v228", "v229", "v230", "v231", "vcc", "scc", "memory");
}

__device__ __forceinline__ void w_k(const u32x32 (&W)[5], u32x4 rs, u32 now, u32 xa, u32 vm, u32 xs, u32 xd, u32 on, u32 o1, u32 ob, float ks, f32x16& acc) {
    asm volatile(
        ".p2align 6\n\t"
        "s_nop 7\n\t"
        "v_mul_f32 v208, %[ks], v208\n\t"
        "v_mul_f32 v209, %[ks], v209\n\t"
        "v_mul_f32 v210, %[ks], v210\n\t"
        "v_mul_f32 v211, %[ks], v211\n\t"
        "v_mul_f32 v212, %[ks], v212\n\t"
        "v_mul_f32 v213, %[ks], v213\n\t"
        "v_mul_f32 v214, %[ks], v214\n\t"
        "v_mul_f32 v215, %[ks], v215\n\t"
        "v_mul_f32 v216, %[ks], v216\n\t"
        "v_mul_f32 v217, %[ks], v217\n\t"
        "v_mul_f32 v218, %[ks], v218\n\t"
        "v_mul_f32 v219, %[ks], v219\n\t"
        "v_mul_f32 v220, %[ks], v220\n\t"
        "v_mul_f32 v221, %[ks], v221\n\t"
        "v_mul_f32 v222, %[ks], v222\n\t"
        "v_mul_f32 v223, %[ks], v223\n\t"
        "v_cvt_pk_bf16_f32 v224, v208, v209\n\t"
        "v_cvt_pk_bf16_f32 v225, v210, v211\n\t"
        "v_cvt_pk_bf16_f32 v226, v212, v213\n\t"
        "v_cvt_pk_bf16_f32 v227, v214, v215\n\t"
        "v_cvt_pk_bf16_f32 v228, v216, v217\n\t"
        "v_cvt_pk_bf16_f32 v229, v218, v219\n\t"
        "v_cvt_pk_bf16_f32 v230, v220, v221\n\t"
        "v_cvt_pk_bf16_f32 v231, v222, v223\n\t"
        "s_add_u32 s93, %[ob], 0x1000\n\t"
        "buffer_store_dwordx4 v[224:227], %[o1], %[rs], %[ob] offen sc1\n\t"
        "buffer_store_dwordx4 v[228:231], %[o1], %[rs], s93 offen sc1\n\t"
        "s_mov_b32 s92, m0\n\t"
        "s_add_u32 m0, %[xd], 0\n\t"
        "s_add_u32 s93, %[xs], 0\n\t"
        "buffer_load_dwordx4 %[vm], %[rs], s93 offen offset:0 sc1 lds\n\t"
        "buffer_load_dwordx4 %[vm], %[rs], s93 offen offset:1024 sc1 lds\n\t"
        "buffer_load_dwordx4 %[vm], %[rs], s93 offen offset:2048 sc1 lds\n\t"
        "buffer_load_dwordx4 %[vm], %[rs], s93 offen offset:3072 sc1 lds\n\t"
        "s_add_u32 m0, %[xd], 4096\n\t"
        "s_add_u32 s93, %[xs], 4096\n\t"
        "buffer_load_dwordx4 %[vm], %[rs], s93 offen offset:0 sc1 lds\n\t"
        "s_mov_b32 m0, s92\n\t"
        "ds_read_b128 v[176:179], %[xa] offset:0\n\t"
        "ds_read_b128 v[180:183], %[xa] offset:20480\n\t"
        "ds_read_b128 v[184:187], %[xa] offset:1024\n\t"
        "ds_read_b128 v[188:191], %[xa] offset:21504\n\t"
        "ds_read_b128 v[192:195], %[xa] offset:2048\n\t"
        "ds_read_b128 v[196:199], %[xa] offset:22528\n\t"
        "ds_read_b128 v[200:203], %[xa] offset:3072\n\t"
        "ds_read_b128 v[204:207], %[xa] offset:23552\n\t"
        "s_waitcnt lgkmcnt(7)\n\t"
        "v_mfma_f32_16x16x32_bf16 v[208:211], v[16:19], v[176:179], 0\n\t"
        "v_mfma_f32_16x16x32_bf16 v[212:215], v[96:99], v[176:179], 0\n\t"
        "ds_read_b128 v[176:179], %[xa] offset:4096\n\t"
        "s_waitcnt lgkmcnt(7)\n\t"
        "v_mfma_f32_16x16x32_bf16 v[216:219], v[16:19], v[180:183], 0\n\t"
        "v_mfma_f32_16x16x32_bf16 v[220:223], v[96:99], v[180:183], 0\n\t"
        "ds_read_b128 v[180:183], %[xa] offset:24576\n\t"
        "s_waitcnt lgkmcnt(7)\n\t"
        "v_mfma_f32_16x16x32_bf16 v[208:211], v[20:23], v[184:187], v[208:211]\n\t"
        "v_mfma_f32_16x16x32_bf16 v[212:215], v[100:103], v[184:187], v[212:215]\n\t"
        "ds_read_b128 v[184:187], %[xa] offset:5120\n\t"
        "s_waitcnt lgkmcnt(7)\n\t"
        "v_mfma_f32_16x16x32_bf16 v[216:219], v[20:23], v[188:191], v[216:219]\n\t"
        "v_mfma_f32_16x16x32_bf16 v[220:223], v[100:103], v[188:191], v[220:223]\n\t"
        "ds_read_b128 v[188:191], %[xa] offset:25600\n\t"
        "s_waitcnt lgkmcnt(7)\n\t"
        "v_mfma_f32_16x16x32_bf16 v[208:211], v[24:27], v[192:195], v[208:211]\n\t"
        "v_mfma_f32_16x16x32_bf16 v[212:215], v[104:107], v[192:195], v[212:215]\n\t"
        "ds_read_b128 v[192:195], %[xa] offset:6144\n\t"
        "s_waitcnt lgkmcnt(7)\n\t"
        "v_mfma_f32_16x16x32_bf16 v[216:219], v[24:27], v[196:199], v[216:219]\n\t"
        "v_mfma_f32_16x16x32_bf16 v[220:223], v[104:107], v[196:199], v[220:223]\n\t"
        "ds_read_b128 v[196:199], %[xa] offset:26624\n\t"
        "s_waitcnt lgkmcnt(7)\n\t"
        "v_mfma_f32_16x16x32_bf16 v[208:211], v[28:31], v[200:203], v[208:211]\n\t"
        "v_mfma_f32_16x16x32_bf16 v[212:215], v[108:111], v[200:203], v[212:215]\n\t"
        "ds_read_b128 v[200:203], %[xa] offset:7168\n\t"
        "s_waitcnt lgkmcnt(7)\n\t"
        "v_mfma_f32_16x16x32_bf16 v[216:219], v[28:31], v[204:207], v[216:219]\n\t"
        "v_mfma_f32_16x16x32_bf16 v[220:223], v[108:111], v[204:207], v[220:223]\n\t"
        "ds_read_b128 v[204:207], %[xa] offset:27648\n\t"
        "s_waitcnt lgkmcnt(7)\n\t"
        "v_mfma_f32_16x16x32_bf16 v[208:211], v[32:35], v[176:179], v[208:211]\n\t"
        "v_mfma_f32_16x16x32_bf16 v[212:215], v[112:115], v[176:179], v[212:215]\n\t"
        "ds_read_b128 v[176:179], %[xa] offset:8192\n\t"
        "s_waitcnt lgkmcnt(7)\n\t"
        "v_mfma_f32_16x16x32_bf16 v[216:219], v[32:35], v[180:183], v[216:219]\n\t"
        "v_mfma_f32_16x16x32_bf16 v[220:223], v[112:115], v[180:183], v[220:223]\n\t"
        "ds_read_b128 v[180:183], %[xa] offset:28672\n\t"
        "s_waitcnt lgkmcnt(7)\n\t"
        "v_mfma_f32_16x16x32_bf16 v[208:211], v[36:39], v[184:187], v[208:211]\n\t"
        "v_mfma_f32_16x16x32_bf16 v[212:215], v[116:119], v[184:187], v[212:215]\n\t"
        "ds_read_b128 v[184:187], %[xa] offset:9216\n\t"
        "s_waitcnt lgkmcnt(7)\n\t"
        "v_mfma_f32_16x16x32_bf16 v[216:219], v[36:39], v[188:191], v[216:219]\n\t"
        "v_mfma_f32_16x16x32_bf16 v[220:223], v[116:119], v[188:191], v[220:223]\n\t"
        "ds_read_b128 v[188:191], %[xa] offset:29696\n\t"
        "s_waitcnt lgkmcnt(7)\n\t"
        "v_mfma_f32_16x16x32_bf16 v[208:211], v[40:43], v[192:195], v[208:211]\n\t"
        "v_mfma_f32_16x16x32_bf16 v[212:215], v[120:123], v[192:195], v[212:215]\n\t"
        "ds_read_b128 v[192:195], %[xa] offset:10240\n\t"
        "s_waitcnt lgkmcnt(7)\n\t"
        "v_mfma_f32_16x16x32_bf16 v[216:219], v[40:43], v[196:199], v[216:219]\n\t"
        "v_mfma_f32_16x16x32_bf16 v[220:223], v[120:123], v[196:199], v[220:223]\n\t"
        "ds_read_b128 v[196:199], %[xa] offset:30720\n\t"
        "s_waitcnt lgkmcnt(7)\n\t"
        "v_mfma_f32_16x16x32_bf16 v[208:211], v[44:47], v[200:203], v[208:211]\n\t"
        "v_mfma_f32_16x16x32_bf16 v[212:215], v[124:127], v[200:203], v[212:215]\n\t"
        "ds_read_b128 v[200:203], %[xa] offset:11264\n\t"
        "s_waitcnt lgkmcnt(7)\n\t"
        "v_mfma_f32_16x16x32_bf16 v[216:219], v[44:47], v[204:207], v[216:219]\n\t"
        "v_mfma_f32_16x16x32_bf16 v[220:223], v[124:127], v[204:207], v[220:223]\n\t"
        "ds_read_b128 v[204:207], %[xa] offset:31744\n\t"
        "s_waitcnt lgkmcnt(7)\n\t"
        "v_mfma_f32_16x16x32_bf16 v[208:211], v[48:51], v[176:179], v[208:211]\n\t"
        "v_mfma_f32_16x16x32_bf16 v[212:215], v[128:131], v[176:179], v[212:215]\n\t"
        "ds_read_b128 v[176:179], %[xa] offset:12288\n\t"
        "s_waitcnt lgkmcnt(7)\n\t"
        "v_mfma_f32_16x16x32_bf16 v[216:219], v[48:51], v[180:183], v[216:219]\n\t"
        "v_mfma_f32_16x16x32_bf16 v[220:223], v[128:131], v[180:183], v[220:223]\n\t"
        "ds_read_b128 v[180:183], %[xa] offset:32768\n\t"
        "s_waitcnt lgkmcnt(7)\n\t"
        "v_mfma_f32_16x16x32_bf16 v[208:211], v[52:55], v[184:187], v[208:211]\n\t"
        "v_mfma_f32_16x16x32_bf16 v[212:215], v[132:135], v[184:187], v[212:215]\n\t"
        "ds_read_b128 v[184:187], %[xa] offset:13312\n\t"
        "s_waitcnt lgkmcnt(7)\n\t"
        "v_mfma_f32_16x16x32_bf16 v[216:219], v[52:55], v[188:191], v[216:219]\n\t"
        "v_mfma_f32_16x16x32_bf16 v[220:223], v[132:135], v[188:191], v[220:223]\n\t"
        "ds_read_b128 v[188:191], %[xa] offset:33792\n\t"
        "s_waitcnt lgkmcnt(7)\n\t"
        "v_mfma_f32_16x16x32_bf16 v[208:211], v[56:59], v[192:195], v[208:211]\n\t"
        "v_mfma_f32_16x16x32_bf16 v[212:215], v[136:139], v[192:195], v[212:215]\n\t"
        "ds_read_b128 v[192:195], %[xa] offset:14336\n\t"
        "s_waitcnt lgkmcnt(7)\n\t"
        "v_mfma_f32_16x16x32_bf16 v[216:219], v[56:59], v[196:199], v[216:219]\n\t"
        "v_mfma_f32_16x16x32_bf16 v[220:223], v[136:139], v[196:199], v[220:223]\n\t"
        "ds_read_b128 v[196:199], %[xa] offset:34816\n\t"
        "s_waitcnt lgkmcnt(7)\n\t"
        "v_mfma_f32_16x16x32_bf16 v[208:211], v[60:63], v[200:203], v[208:211]\n\t"
        "v_mfma_f32_16x16x32_bf16 v[212:215], v[140:143], v[200:203], v[212:215]\n\t"
        "ds_read_b128 v[200:203], %[xa] offset:15360\n\t"
        "s_waitcnt lgkmcnt(7)\n\t"
        "v_mfma_f32_16x16x32_bf16 v[216:219], v[60:63], v[204:207], v[216:219]\n\t"
        "v_mfma_f32_16x16x32_bf16 v[220:223], v[140:143], v[204:207], v[220:223]\n\t"
        "ds_read_b128 v[204:207], %[xa] offset:35840\n\t"
        "s_waitcnt lgkmcnt(7)\n\t"
        "v_mfma_f32_16x16x32_bf16 v[208:211], v[64:67], v[176:179], v[208:211]\n\t"
        "v_mfma_f32_16x16x32_bf16 v[212:215], v[144:147], v[176:179], v[212:215]\n\t"
        "ds_read_b128 v[176:179], %[xa] offset:16384\n\t"
        "s_waitcnt lgkmcnt(7)\n\t"
        "v_mfma_f32_16x16x32_bf16 v[216:219], v[64:67], v[180:183], v[216:219]\n\t"
        "v_mfma_f32_16x16x32_bf16 v[220:223], v[144:147], v[180:183], v[220:223]\n\t"
        "ds_read_b128 v[180:183], %[xa] offset:36864\n\t"
        "s_waitcnt lgkmcnt(7)\n\t"
        "v_mfma_f32_16x16x32_bf16 v[208:211], v[68:71], v[184:187], v[208:211]\n\t"
        "v_mfma_f32_16x16x32_bf16 v[212:215], v[148:151], v[184:187], v[212:215]\n\t"
        "ds_read_b128 v[184:187], %[xa] offset:17408\n\t"
        "s_waitcnt lgkmcnt(7)\n\t"
        "v_mfma_f32_16x16x32_bf16 v[216:219], v[68:71], v[188:191], v[216:219]\n\t"
        "v_mfma_f32_16x16x32_bf16 v[220:223], v[148:151], v[188:191], v[220:223]\n\t"
        "ds_read_b128 v[188:191], %[xa] offset:37888\n\t"
        "s_waitcnt lgkmcnt(7)\n\t"
        "v_mfma_f32_16x16x32_bf16 v[208:211], v[72:75], v[192:195], v[208:211]\n\t"
        "v_mfma_f32_16x16x32_bf16 v[212:215], v[152:155], v[192:195], v[212:215]\n\t"
        "ds_read_b128 v[192:195], %[xa] offset:18432\n\t"
        "s_waitcnt lgkmcnt(7)\n\t"
        "v_mfma_f32_16x16x32_bf16 v[216:219], v[72:75], v[196:199], v[216:219]\n\t"
        "v_mfma_f32_16x16x32_bf16 v[220:223], v[152:155], v[196:199], v[220:223]\n\t"
        "ds_read_b128 v[196:199], %[xa] offset:38912\n\t"
        "s_waitcnt lgkmcnt(7)\n\t"
        "v_mfma_f32_16x16x32_bf16 v[208:211], v[76:79], v[200:203], v[208:211]\n\t"
        "v_mfma_f32_16x16x32_bf16 v[212:215], v[156:159], v[200:203], v[212:215]\n\t"
        "ds_read_b128 v[200:203], %[xa] offset:19456\n\t"
        "s_waitcnt lgkmcnt(7)\n\t"
        "v_mfma_f32_16x16x32_bf16 v[216:219], v[76:79], v[204:207], v[216:219]\n\t"
        "v_mfma_f32_16x16x32_bf16 v[220:223], v[156:159], v[204:207], v[220:223]\n\t"
        "ds_read_b128 v[204:207], %[xa] offset:39936\n\t"
        "s_waitcnt lgkmcnt(7)\n\t"
        "v_mfma_f32_16x16x32_bf16 v[208:211], v[80:83], v[176:179], v[208:211]\n\t"
        "v_mfma_f32_16x16x32_bf16 v[212:215], v[160:163], v[176:179], v[212:215]\n\t"
        "s_waitcnt lgkmcnt(6)\n\t"
        "v_mfma_f32_16x16x32_bf16 v[216:219], v[80:83], v[180:183], v[216:219]\n\t"
        "v_mfma_f32_16x16x32_bf16 v[220:223], v[160:163], v[180:183], v[220:223]\n\t"
        "s_waitcnt lgkmcnt(5)\n\t"
        "v_mfma_f32_16x16x32_bf16 v[208:211], v[84:87], v[184:187], v[208:211]\n\t"
        "v_mfma_f32_16x16x32_bf16 v[212:215], v[164:167], v[184:187], v[212:215]\n\t"
        "s_waitcnt lgkmcnt(4)\n\t"
        "v_mfma_f32_16x16x32_bf16 v[216:219], v[84:87], v[188:191], v[216:219]\n\t"
        "v_mfma_f32_16x16x32_bf16 v[220:223], v[164:167], v[188:191], v[220:223]\n\t"
        "s_waitcnt lgkmcnt(3)\n\t"
        "v_mfma_f32_16x16x32_bf16 v[208:211], v[88:91], v[192:195], v[208:211]\n\t"
        "v_mfma_f32_16x16x32_bf16 v[212:215], v[168:171], v[192:195], v[212:215]\n\t"
        "s_waitcnt lgkmcnt(2)\n\t"
        "v_mfma_f32_16x16x32_bf16 v[216:219], v[88:91], v[196:199], v[216:219]\n\t"
        "v_mfma_f32_16x16x32_bf16 v[220:223], v[168:171], v[196:199], v[220:223]\n\t"
        "s_waitcnt lgkmcnt(1)\n\t"
        "v_mfma_f32_16x16x32_bf16 v[208:211], v[92:95], v[200:203], v[208:211]\n\t"
        "v_mfma_f32_16x16x32_bf16 v[212:215], v[172:175], v[200:203], v[212:215]\n\t"
        "s_waitcnt lgkmcnt(0)\n\t"
        "v_mfma_f32_16x16x32_bf16 v[216:219], v[92:95], v[204:207], v[216:219]\n\t"
        "v_mfma_f32_16x16x32_bf16 v[220:223], v[172:175], v[204:207], v[220:223]\n\t"
        "s_cmp_eq_u32 %[now], 0\n\t"
        "s_cbranch_scc1 Ln%=\n\t"
        "s_nop 5\n\t"
        "v_mul_f32 v208, %[ks], v208\n\t"
        "v_mul_f32 v209, %[ks], v209\n\t"
        "v_mul_f32 v210, %[ks], v210\n\t"
        "v_mul_f32 v211, %[ks], v211\n\t"
        "v_mul_f32 v212, %[ks], v212\n\t"
        "v_mul_f32 v213, %[ks], v213\n\t"
        "v_mul_f32 v214, %[ks], v214\n\t"
        "v_mul_f32 v215, %[ks], v215\n\t"
        "v_mul_f32 v216, %[ks], v216\n\t"
        "v_mul_f32 v217, %[ks], v217\n\t"
        "v_mul_f32 v218, %[ks], v218\n\t"
        "v_mul_f32 v219, %[ks], v219\n\t"
        "v_mul_f32 v220, %[ks], v220\n\t"
        "v_mul_f32 v221, %[ks], v221\n\t"
        "v_mul_f32 v222, %[ks], v222\n\t"
        "v_mul_f32 v223, %[ks], v223\n\t"
        "v_cvt_pk_bf16_f32 v224, v208, v209\n\t"
        "v_cvt_pk_bf16_f32 v225, v210, v211\n\t"
        "v_cvt_pk_bf16_f32 v226, v212, v213\n\t"
        "v_cvt_pk_bf16_f32 v227, v214, v215\n\t"
        "v_cvt_pk_bf16_f32 v228, v216, v217\n\t"
        "v_cvt_pk_bf16_f32 v229, v218, v219\n\t"
        "v_cvt_pk_bf16_f32 v230, v220, v221\n\t"
        "v_cvt_pk_bf16_f32 v231, v222, v223\n\t"
        "s_add_u32 s93, %[on], 0x1000\n\t"
        "buffer_store_dwordx4 v[224:227], %[o1], %[rs], %[on] offen sc1\n\t"
        "buffer_store_dwordx4 v[228:231], %[o1], %[rs], s93 offen sc1\n\t"
        "s_waitcnt vmcnt(0)\n\t"
        "Ln%=:\n\t"
        "s_waitcnt vmcnt(5)\n\t"
        "s_barrier\n\t"
        : [acc] "+{v[208:223]}"(acc)
        : [rs] "s"(rs), [ks] "s"(ks), [o1] "v"(o1), [ob] "s"(ob), [w0] "{v[16:47]}"(W[0]), [w1] "{v[48:79]}"(W[1]), [w2] "{v[80:111]}"(W[2]), [w3] "{v[112:143]}"(W[3]), [w4] "{v[144:175]}"(W[4]), [vm] "v"(vm), [xs] "s"(xs), [xd] "s"(xd), [xa] "v"(xa), [now] "s"(now), [on] "s"(on)
        : "s92", "s93", "v176", "v177", "v178", "v179", "v180", "v181", "v182", "v183", "v184", "v185", "v186", "v187", "v188", "v189", "v190", "v191", "v192", "v193", "v194", "v195", "v196", "v197", "v198", "v199", "v200", "v201", "v202", "v203", "v204", "v205", "v206", "v207", "v224", "v225", "v226", "v227", "v228", "v229", "v230", "v231", "vcc", "scc", "memory");
}

__device__ __forceinline__ void w_kl(u32x4 rs, u32 o1, u32 ob, float ks, f32x16& acc) {
    asm volatile(
        "s_nop 7\n\t"
        "v_mul_f32 v208, %[ks], v208\n\t"
        "v_mul_f32 v209, %[ks], v209\n\t"
        "v_mul_f32 v210, %[ks], v210\n\t"
        "v_mul_f32 v211, %[ks], v211\n\t"
        "v_mul_f32 v212, %[ks], v212\n\t"
        "v_mul_f32 v213, %[ks], v213\n\t"
        "v_mul_f32 v214, %[ks], v214\n\t"
        "v_mul_f32 v215, %[ks], v215\n\t"
        "v_mul_f32 v216, %[ks], v216\n\t"
        "v_mul_f32 v217, %[ks], v217\n\t"
        "v_mul_f32 v218, %[ks], v218\n\t"
        "v_mul_f32 v219, %[ks], v219\n\t"
        "v_mul_f32 v220, %[ks], v220\n\t"
        "v_mul_f32 v221, %[ks], v221\n\t"
        "v_mul_f32 v222, %[ks], v222\n\t"
        "v_mul_f32 v223, %[ks], v223\n\t"
        "v_cvt_pk_bf16_f32 v224, v208, v209\n\t"
        "v_cvt_pk_bf16_f32 v225, v210, v211\n\t"
        "v_cvt_pk_bf16_f32 v226, v212, v213\n\t"
        "v_cvt_pk_bf16_f32 v227, v214, v215\n\t"
        "v_cvt_pk_bf16_f32 v228, v216, v217\n\t"
        "v_cvt_pk_bf16_f32 v229, v218, v219\n\t"
        "v_cvt_pk_bf16_f32 v230, v220, v221\n\t"
        "v_cvt_pk_bf16_f32 v231, v222, v223\n\t"
        "s_add_u32 s93, %[ob], 0x1000\n\t"
        "buffer_store_dwordx4 v[224:227], %[o1], %[rs], %[ob] offen sc1\n\t"
        "buffer_store_dwordx4 v[228:231], %[o1], %[rs], s93 offen sc1\n\t"
        "s_nop 1\n\t"
        : [acc] "+{v[208:223]}"(acc)
        : [rs] "s"(rs), [ks] "s"(ks), [o1] "v"(o1), [ob] "s"(ob)
        : "s93", "v224", "v225", "v226", "v227", "v228", "v229", "v230", "v231", "vcc", "scc", "memory");
}

__device__ __forceinline__ void w_v(const u32x32 (&W)[5], u32x4 rs, u32 xa, u32 vm, u32 xs, u32 xd, u32 vw STP) {
    asm volatile(
        ".p2align 6\n\t"
        "s_mov_b32 s92, m0\n\t"
        "s_add_u32 m0, %[xd], 0\n\t"
        "s_add_u32 s93, %[xs], 0\n\t"
        "buffer_load_dwordx4 %[vm], %[rs], s93 offen offset:0 sc1 lds\n\t"
        "buffer_load_dwordx4 %[vm], %[rs], s93 offen offset:1024 sc1 lds\n\t"
        "buffer_load_dwordx4 %[vm], %[rs], s93 offen offset:2048 sc1 lds\n\t"
        "buffer_load_dwordx4 %[vm], %[rs], s93 offen offset:3072 sc1 lds\n\t"
        "s_add_u32 m0, %[xd], 4096\n\t"
        "s_add_u32 s93, %[xs], 4096\n\t"
        "buffer_load_dwordx4 %[vm], %[rs], s93 offen offset:0 sc1 lds\n\t"
        "buffer_load_dwordx4 %[vm], %[rs], s93 offen offset:1024 sc1 lds\n\t"
        "buffer_load_dwordx4 %[vm], %[rs], s93 offen offset:2048 sc1 lds\n\t"
        "buffer_load_dwordx4 %[vm], %[rs], s93 offen offset:3072 sc1 lds\n\t"
        "s_add_u32 m0, %[xd], 8192\n\t"
        "s_add_u32 s93, %[xs], 8192\n\t"
        "buffer_load_dwordx4 %[vm], %[rs], s93 offen offset:0 sc1 lds\n\t"
        "buffer_load_dwordx4 %[vm], %[rs], s93 offen offset:1024 sc1 lds\n\t"
        "s_mov_b32 m0, s92\n\t"
        "ds_read_b128 v[176:179], %[xa] offset:0\n\t"
        "ds_read_b128 v[180:183], %[xa] offset:20480\n\t"
        "ds_read_b128 v[184:187], %[xa] offset:1024\n\t"
        "ds_read_b128 v[188:191], %[xa] offset:21504\n\t"
        "ds_read_b128 v[192:195], %[xa] offset:2048\n\t"
        "ds_read_b128 v[196:199], %[xa] offset:22528\n\t"
        "ds_read_b128 v[200:203], %[xa] offset:3072\n\t"
        "ds_read_b128 v[204:207], %[xa] offset:23552\n\t"
        "s_waitcnt lgkmcnt(7)\n\t"
        "v_mfma_f32_16x16x32_bf16 v[208:211], v[16:19], v[176:179], 0\n\t"
        "v_mfma_f32_16x16x32_bf16 v[212:215], v[96:99], v[176:179], 0\n\t"
        "ds_read_b128 v[176:179], %[xa] offset:4096\n\t"
        "s_waitcnt lgkmcnt(7)\n\t"
        "v_mfma_f32_16x16x32_bf16 v[216:219], v[16:19], v[180:183], 0\n\t"
        "v_mfma_f32_16x16x32_bf16 v[220:223], v[96:99], v[180:183], 0\n\t"
        "ds_read_b128 v[180:183], %[xa] offset:24576\n\t"
        "s_waitcnt lgkmcnt(7)\n\t"
        "v_mfma_f32_16x16x32_bf16 v[208:211], v[20:23], v[184:187], v[208:211]\n\t"
        "v_mfma_f32_16x16x32_bf16 v[212:215], v[100:103], v[184:187], v[212:215]\n\t"
        "ds_read_b128 v[184:187], %[xa] offset:5120\n\t"
        "s_waitcnt lgkmcnt(7)\n\t"
        "v_mfma_f32_16x16x32_bf16 v[216:219], v[20:23], v[188:191], v[216:219]\n\t"
        "v_mfma_f32_16x16x32_bf16 v[220:223], v[100:103], v[188:191], v[220:223]\n\t"
        "ds_read_b128 v[188:191], %[xa] offset:25600\n\t"
        "s_waitcnt lgkmcnt(7)\n\t"
        "v_mfma_f32_16x16x32_bf16 v[208:211], v[24:27], v[192:195], v[208:211]\n\t"
        "v_mfma_f32_16x16x32_bf16 v[212:215], v[104:107], v[192:195], v[212:215]\n\t"
        "ds_read_b128 v[192:195], %[xa] offset:6144\n\t"
        "s_waitcnt lgkmcnt(7)\n\t"
        "v_mfma_f32_16x16x32_bf16 v[216:219], v[24:27], v[196:199], v[216:219]\n\t"
        "v_mfma_f32_16x16x32_bf16 v[220:223], v[104:107], v[196:199], v[220:223]\n\t"
        "ds_read_b128 v[196:199], %[xa] offset:26624\n\t"
        "s_waitcnt lgkmcnt(7)\n\t"
        "v_mfma_f32_16x16x32_bf16 v[208:211], v[28:31], v[200:203], v[208:211]\n\t"
        "v_mfma_f32_16x16x32_bf16 v[212:215], v[108:111], v[200:203], v[212:215]\n\t"
        "ds_read_b128 v[200:203], %[xa] offset:7168\n\t"
        "s_waitcnt lgkmcnt(7)\n\t"
        "v_mfma_f32_16x16x32_bf16 v[216:219], v[28:31], v[204:207], v[216:219]\n\t"
        "v_mfma_f32_16x16x32_bf16 v[220:223], v[108:111], v[204:207], v[220:223]\n\t"
        "ds_read_b128 v[204:207], %[xa] offset:27648\n\t"
        "s_waitcnt lgkmcnt(7)\n\t"
        "v_mfma_f32_16x16x32_bf16 v[208:211], v[32:35], v[176:179], v[208:211]\n\t"
        "v_mfma_f32_16x16x32_bf16 v[212:215], v[112:115], v[176:179], v[212:215]\n\t"
        "ds_read_b128 v[176:179], %[xa] offset:8192\n\t"
        "s_waitcnt lgkmcnt(7)\n\t"
        "v_mfma_f32_16x16x32_bf16 v[216:219], v[32:35], v[180:183], v[216:219]\n\t"
        "v_mfma_f32_16x16x32_bf16 v[220:223], v[112:115], v[180:183], v[220:223]\n\t"
        "ds_read_b128 v[180:183], %[xa] offset:28672\n\t"
        "s_waitcnt lgkmcnt(7)\n\t"
        "v_mfma_f32_16x16x32_bf16 v[208:211], v[36:39], v[184:187], v[208:211]\n\t"
        "v_mfma_f32_16x16x32_bf16 v[212:215], v[116:119], v[184:187], v[212:215]\n\t"
        "ds_read_b128 v[184:187], %[xa] offset:9216\n\t"
        "s_waitcnt lgkmcnt(7)\n\t"
        "v_mfma_f32_16x16x32_bf16 v[216:219], v[36:39], v[188:191], v[216:219]\n\t"
        "v_mfma_f32_16x16x32_bf16 v[220:223], v[116:119], v[188:191], v[220:223]\n\t"
        "ds_read_b128 v[188:191], %[xa] offset:29696\n\t"
        "s_waitcnt lgkmcnt(7)\n\t"
        "v_mfma_f32_16x16x32_bf16 v[208:211], v[40:43], v[192:195], v[208:211]\n\t"
        "v_mfma_f32_16x16x32_bf16 v[212:215], v[120:123], v[192:195], v[212:215]\n\t"
        "ds_read_b128 v[192:195], %[xa] offset:10240\n\t"
        "s_waitcnt lgkmcnt(7)\n\t"
        "v_mfma_f32_16x16x32_bf16 v[216:219], v[40:43], v[196:199], v[216:219]\n\t"
        "v_mfma_f32_16x16x32_bf16 v[220:223], v[120:123], v[196:199], v[220:223]\n\t"
        "ds_read_b128 v[196:199], %[xa] offset:30720\n\t"
        "s_waitcnt lgkmcnt(7)\n\t"
        "v_mfma_f32_16x16x32_bf16 v[208:211], v[44:47], v[200:203], v[208:211]\n\t"
        "v_mfma_f32_16x16x32_bf16 v[212:215], v[124:127], v[200:203], v[212:215]\n\t"
        "ds_read_b128 v[200:203], %[xa] offset:11264\n\t"
        "s_waitcnt lgkmcnt(7)\n\t"
        "v_mfma_f32_16x16x32_bf16 v[216:219], v[44:47], v[204:207], v[216:219]\n\t"
        "v_mfma_f32_16x16x32_bf16 v[220:223], v[124:127], v[204:207], v[220:223]\n\t"
        "ds_read_b128 v[204:207], %[xa] offset:31744\n\t"
        "s_waitcnt lgkmcnt(7)\n\t"
        "v_mfma_f32_16x16x32_bf16 v[208:211], v[48:51], v[176:179], v[208:211]\n\t"
        "v_mfma_f32_16x16x32_bf16 v[212:215], v[128:131], v[176:179], v[212:215]\n\t"
        "ds_read_b128 v[176:179], %[xa] offset:12288\n\t"
        "s_waitcnt lgkmcnt(7)\n\t"
        "v_mfma_f32_16x16x32_bf16 v[216:219], v[48:51], v[180:183], v[216:219]\n\t"
        "v_mfma_f32_16x16x32_bf16 v[220:223], v[128:131], v[180:183], v[220:223]\n\t"
        "ds_read_b128 v[180:183], %[xa] offset:32768\n\t"
        "s_waitcnt lgkmcnt(7)\n\t"
        "v_mfma_f32_16x16x32_bf16 v[208:211], v[52:55], v[184:187], v[208:211]\n\t"
        "v_mfma_f32_16x16x32_bf16 v[212:215], v[132:135], v[184:187], v[212:215]\n\t"
        "ds_read_b128 v[184:187], %[xa] offset:13312\n\t"
        "s_waitcnt lgkmcnt(7)\n\t"
        "v_mfma_f32_16x16x32_bf16 v[216:219], v[52:55], v[188:191], v[216:219]\n\t"
        "v_mfma_f32_16x16x32_bf16 v[220:223], v[132:135], v[188:191], v[220:223]\n\t"
        "ds_read_b128 v[188:191], %[xa] offset:33792\n\t"
        "s_waitcnt lgkmcnt(7)\n\t"
        "v_mfma_f32_16x16x32_bf16 v[208:211], v[56:59], v[192:195], v[208:211]\n\t"
        "v_mfma_f32_16x16x32_bf16 v[212:215], v[136:139], v[192:195], v[212:215]\n\t"
        "ds_read_b128 v[192:195], %[xa] offset:14336\n\t"
        "s_waitcnt lgkmcnt(7)\n\t"
        "v_mfma_f32_16x16x32_bf16 v[216:219], v[56:59], v[196:199], v[216:219]\n\t"
        "v_mfma_f32_16x16x32_bf16 v[220:223], v[136:139], v[196:199], v[220:223]\n\t"
        "ds_read_b128 v[196:199], %[xa] offset:34816\n\t"
        "s_waitcnt lgkmcnt(7)\n\t"
        "v_mfma_f32_16x16x32_bf16 v[208:211], v[60:63], v[200:203], v[208:211]\n\t"
        "v_mfma_f32_16x16x32_bf16 v[212:215], v[140:143], v[200:203], v[212:215]\n\t"
        "ds_read_b128 v[200:203], %[xa] offset:15360\n\t"
        "s_waitcnt lgkmcnt(7)\n\t"
        "v_mfma_f32_16x16x32_bf16 v[216:219], v[60:63], v[204:207], v[216:219]\n\t"
        "v_mfma_f32_16x16x32_bf16 v[220:223], v[140:143], v[204:207], v[220:223]\n\t"
        "ds_read_b128 v[204:207], %[xa] offset:35840\n\t"
        "s_waitcnt lgkmcnt(7)\n\t"
        "v_mfma_f32_16x16x32_bf16 v[208:211], v[64:67], v[176:179], v[208:211]\n\t"
        "v_mfma_f32_16x16x32_bf16 v[212:215], v[144:147], v[176:179], v[212:215]\n\t"
        "ds_read_b128 v[176:179], %[xa] offset:16384\n\t"
        "s_waitcnt lgkmcnt(7)\n\t"
        "v_mfma_f32_16x16x32_bf16 v[216:219], v[64:67], v[180:183], v[216:219]\n\t"
        "v_mfma_f32_16x16x32_bf16 v[220:223], v[144:147], v[180:183], v[220:223]\n\t"
        "ds_read_b128 v[180:183], %[xa] offset:36864\n\t"
        "s_waitcnt lgkmcnt(7)\n\t"
        "v_mfma_f32_16x16x32_bf16 v[208:211], v[68:71], v[184:187], v[208:211]\n\t"
        "v_mfma_f32_16x16x32_bf16 v[212:215], v[148:151], v[184:187], v[212:215]\n\t"
        "ds_read_b128 v[184:187], %[xa] offset:17408\n\t"
        "s_waitcnt lgkmcnt(7)\n\t"
        "v_mfma_f32_16x16x32_bf16 v[216:219], v[68:71], v[188:191], v[216:219]\n\t"
        "v_mfma_f32_16x16x32_bf16 v[220:223], v[148:151], v[188:191], v[220:223]\n\t"
        "ds_read_b128 v[188:191], %[xa] offset:37888\n\t"
        "s_waitcnt lgkmcnt(7)\n\t"
        "v_mfma_f32_16x16x32_bf16 v[208:211], v[72:75], v[192:195], v[208:211]\n\t"
        "v_mfma_f32_16x16x32_bf16 v[212:215], v[152:155], v[192:195], v[212:215]\n\t"
        "ds_read_b128 v[192:195], %[xa] offset:18432\n\t"
        "s_waitcnt lgkmcnt(7)\n\t"
        "v_mfma_f32_16x16x32_bf16 v[216:219], v[72:75], v[196:199], v[216:219]\n\t"
        "v_mfma_f32_16x16x32_bf16 v[220:223], v[152:155], v[196:199], v[220:223]\n\t"
        "ds_read_b128 v[196:199], %[xa] offset:38912\n\t"
        "s_waitcnt lgkmcnt(7)\n\t"
        "v_mfma_f32_16x16x32_bf16 v[208:211], v[76:79], v[200:203], v[208:211]\n\t"
        "v_mfma_f32_16x16x32_bf16 v[212:215], v[156:159], v[200:203], v[212:215]\n\t"
        "ds_read_b128 v[200:203], %[xa] offset:19456\n\t"
        "s_waitcnt lgkmcnt(7)\n\t"
        "v_mfma_f32_16x16x32_bf16 v[216:219], v[76:79], v[204:207], v[216:219]\n\t"
        "v_mfma_f32_16x16x32_bf16 v[220:223], v[156:159], v[204:207], v[220:223]\n\t"
        "ds_read_b128 v[204:207], %[xa] offset:39936\n\t"
        "s_waitcnt lgkmcnt(7)\n\t"
        "v_mfma_f32_16x16x32_bf16 v[208:211], v[80:83], v[176:179], v[208:211]\n\t"
        "v_mfma_f32_16x16x32_bf16 v[212:215], v[160:163], v[176:179], v[212:215]\n\t"
        "s_waitcnt lgkmcnt(6)\n\t"
        "v_mfma_f32_16x16x32_bf16 v[216:219], v[80:83], v[180:183], v[216:219]\n\t"
        "v_mfma_f32_16x16x32_bf16 v[220:223], v[160:163], v[180:183], v[220:223]\n\t"
        "s_waitcnt lgkmcnt(5)\n\t"
        "v_mfma_f32_16x16x32_bf16 v[208:211], v[84:87], v[184:187], v[208:211]\n\t"
        "v_mfma_f32_16x16x32_bf16 v[212:215], v[164:167], v[184:187], v[212:215]\n\t"
        "s_waitcnt lgkmcnt(4)\n\t"
        "v_mfma_f32_16x16x32_bf16 v[216:219], v[84:87], v[188:191], v[216:219]\n\t"
        "v_mfma_f32_16x16x32_bf16 v[220:223], v[164:167], v[188:191], v[220:223]\n\t"
        "s_waitcnt lgkmcnt(3)\n\t"
        "v_mfma_f32_16x16x32_bf16 v[208:211], v[88:91], v[192:195], v[208:211]\n\t"
        "v_mfma_f32_16x16x32_bf16 v[212:215], v[168:171], v[192:195], v[212:215]\n\t"
        "s_waitcnt lgkmcnt(2)\n\t"
        "v_mfma_f32_16x16x32_bf16 v[216:219], v[88:91], v[196:199], v[216:219]\n\t"
        "v_mfma_f32_16x16x32_bf16 v[220:223], v[168:171], v[196:199], v[220:223]\n\t"
        "s_waitcnt lgkmcnt(1)\n\t"
        "v_mfma_f32_16x16x32_bf16 v[208:211], v[92:95], v[200:203], v[208:211]\n\t"
        "v_mfma_f32_16x16x32_bf16 v[212:215], v[172:175], v[200:203], v[212:215]\n\t"
        "s_waitcnt lgkmcnt(0)\n\t"
        "v_mfma_f32_16x16x32_bf16 v[216:219], v[92:95], v[204:207], v[216:219]\n\t"
        "v_mfma_f32_16x16x32_bf16 v[220:223], v[172:175], v[204:207], v[220:223]\n\t"
        "s_nop 7\n\t"
        "v_cvt_pk_bf16_f32 v224, v208, v209\n\t"
        "v_cvt_pk_bf16_f32 v225, v210, v211\n\t"
        "v_cvt_pk_bf16_f32 v226, v212, v213\n\t"
        "v_cvt_pk_bf16_f32 v227, v214, v215\n\t"
        "v_cvt_pk_bf16_f32 v228, v216, v217\n\t"
        "v_cvt_pk_bf16_f32 v229, v218, v219\n\t"
        "v_cvt_pk_bf16_f32 v230, v220, v221\n\t"
        "v_cvt_pk_bf16_f32 v231, v222, v223\n\t"
        "ds_write_b128 %[vw], v[224:227]\n\t"
        "ds_write_b128 %[vw], v[228:231] offset:4096\n\t"
        "s_waitcnt vmcnt(10) lgkmcnt(0)\n\t"
        "s_barrier\n\t"
        :
        : [w0] "{v[16:47]}"(W[0]), [w1] "{v[48:79]}"(W[1]), [w2] "{v[80:111]}"(W[2]), [w3] "{v[112:143]}"(W[3]), [w4] "{v[144:175]}"(W[4]), [rs] "s"(rs), [vw] "v"(vw), [vm] "v"(vm), [xs] "s"(xs), [xd] "s"(xd), [xa] "v"(xa)
        : "s92", "s93", "v176", "v177", "v178", "v179", "v180", "v181", "v182", "v183", "v184", "v185", "v186", "v187", "v188", "v189", "v190", "v191", "v192", "v193", "v194", "v195", "v196", "v197", "v198", "v199", "v200", "v201", "v202", "v203", "v204", "v205", "v206", "v207", "v208", "v209", "v210", "v211", "v212", "v213", "v214", "v215", "v216", "v217", "v218", "v219", "v220", "v221", "v222", "v223", "v224", "v225", "v226", "v227", "v228", "v229", "v230", "v231", "vcc", "scc", "memory");
}

__device__ __forceinline__ void w_b(const u32x32 (&W)[5], u32x4 rs, u32 now, u32 vr, u32 n1, u32 n2, u32 f1, u32 f2, u32 o1, u32 n0, u32 f0, f32x16& acc STP) {
    asm volatile(
        ".p2align 6\n\t"
        "s_setprio 2\n\t"
        "s_barrier\n\t"
        "ds_read_b128 v[176:179], %[vr] offset:0\n\t"
        "ds_read_b128 v[192:195], %[vr] offset:4096\n\t"
        "ds_read_b128 v[180:183], %[vr] offset:1024\n\t"
        "ds_read_b128 v[196:199], %[vr] offset:5120\n\t"
        "ds_read_b128 v[184:187], %[vr] offset:2048\n\t"
        "ds_read_b128 v[200:203], %[vr] offset:6144\n\t"
        "ds_read_b128 v[188:191], %[vr] offset:3072\n\t"
        "ds_read_b128 v[204:207], %[vr] offset:7168\n\t"
        "s_nop 3\n\t"
        "v_cvt_pk_bf16_f32 v228, v216, v217\n\t"
        "v_cvt_pk_bf16_f32 v229, v218, v219\n\t"
        "v_cvt_pk_bf16_f32 v230, v220, v221\n\t"
        "v_cvt_pk_bf16_f32 v231, v222, v223\n\t"
        "s_waitcnt lgkmcnt(7)\n\t"
        "v_mfma_f32_16x16x32_bf16 v[208:211], v[176:179], v[16:19], 0\n\t"
        "s_waitcnt lgkmcnt(6)\n\t"
        "v_mfma_f32_16x16x32_bf16 v[212:215], v[192:195], v[16:19], 0\n\t"
        "s_waitcnt lgkmcnt(5)\n\t"
        "v_mfma_f32_16x16x32_bf16 v[208:211], v[180:183], v[20:23], v[208:211]\n\t"
        "s_waitcnt lgkmcnt(4)\n\t"
        "v_mfma_f32_16x16x32_bf16 v[212:215], v[196:199], v[20:23], v[212:215]\n\t"
        "s_waitcnt lgkmcnt(3)\n\t"
        "v_mfma_f32_16x16x32_bf16 v[208:211], v[184:187], v[24:27], v[208:211]\n\t"
        "s_add_u32 s92, %[n0], 65536\n\t"
        "buffer_store_dwordx4 v[228:231], %[o1], %[rs], s92 offen sc1\n\t"
        "s_waitcnt lgkmcnt(2)\n\t"
        "v_mfma_f32_16x16x32_bf16 v[212:215], v[200:203], v[24:27], v[212:215]\n\t"
        "s_waitcnt lgkmcnt(1)\n\t"
        "v_mfma_f32_16x16x32_bf16 v[208:211], v[188:191], v[28:31], v[208:211]\n\t"
        "s_waitcnt lgkmcnt(0)\n\t"
        "v_mfma_f32_16x16x32_bf16 v[212:215], v[204:207], v[28:31], v[212:215]\n\t"
        "s_add_u32 s92, %[f0], 65536\n\t"
        "buffer_store_dwordx4 v[228:231], %[o1], %[rs], s92 offen sc1\n\t"
        "v_mfma_f32_16x16x32_bf16 v[216:219], v[176:179], v[96:99], 0\n\t"
        "v_mfma_f32_16x16x32_bf16 v[220:223], v[192:195], v[96:99], 0\n\t"
        "v_mfma_f32_16x16x32_bf16 v[216:219], v[180:183], v[100:103], v[216:219]\n\t"
        "v_cvt_pk_bf16_f32 v224, v208, v209\n\t"
        "v_cvt_pk_bf16_f32 v225, v210, v211\n\t"
        "v_mfma_f32_16x16x32_bf16 v[220:223], v[196:199], v[100:103], v[220:223]\n\t"
        "v_cvt_pk_bf16_f32 v226, v212, v213\n\t"
        "v_cvt_pk_bf16_f32 v227, v214, v215\n\t"
        "v_mfma_f32_16x16x32_bf16 v[216:219], v[184:187], v[104:107], v[216:219]\n\t"
        "s_add_u32 s92, %[n1], 0\n\t"
        "buffer_store_dwordx4 v[224:227], %[o1], %[rs], s92 offen sc1\n\t"
        "v_mfma_f32_16x16x32_bf16 v[220:223], v[200:203], v[104:107], v[220:223]\n\t"
        "v_mfma_f32_16x16x32_bf16 v[216:219], v[188:191], v[108:111], v[216:219]\n\t"
        "v_mfma_f32_16x16x32_bf16 v[220:223], v[204:207], v[108:111], v[220:223]\n\t"
        "s_add_u32 s92, %[f1], 0\n\t"
        "buffer_store_dwordx4 v[224:227], %[o1], %[rs], s92 offen sc1\n\t"
        "v_mfma_f32_16x16x32_bf16 v[208:211], v[176:179], v[32:35], 0\n\t"
        "v_mfma_f32_16x16x32_bf16 v[212:215], v[192:195], v[32:35], 0\n\t"
        "v_mfma_f32_16x16x32_bf16 v[208:211], v[180:183], v[36:39], v[208:211]\n\t"
        "v_cvt_pk_bf16_f32 v228, v216, v217\n\t"
        "v_cvt_pk_bf16_f32 v229, v218, v219\n\t"
        "v_mfma_f32_16x16x32_bf16 v[212:215], v[196:199], v[36:39], v[212:215]\n\t"
        "v_cvt_pk_bf16_f32 v230, v220, v221\n\t"
        "v_cvt_pk_bf16_f32 v231, v222, v223\n\t"
        "v_mfma_f32_16x16x32_bf16 v[208:211], v[184:187], v[40:43], v[208:211]\n\t"
        "s_add_u32 s92, %[n1], 65536\n\t"
        "buffer_store_dwordx4 v[228:231], %[o1], %[rs], s92 offen sc1\n\t"
        "v_mfma_f32_16x16x32_bf16 v[212:215], v[200:203], v[40:43], v[212:215]\n\t"
        "v_mfma_f32_16x16x32_bf16 v[208:211], v[188:191], v[44:47], v[208:211]\n\t"
        "v_mfma_f32_16x16x32_bf16 v[212:215], v[204:207], v[44:47], v[212:215]\n\t"
        "s_add_u32 s92, %[f1], 65536\n\t"
        "buffer_store_dwordx4 v[228:231], %[o1], %[rs], s92 offen sc1\n\t"
        "v_mfma_f32_16x16x32_bf16 v[216:219], v[176:179], v[112:115], 0\n\t"
        "v_mfma_f32_16x16x32_bf16 v[220:223], v[192:195], v[112:115], 0\n\t"
        "v_mfma_f32_16x16x32_bf16 v[216:219], v[180:183], v[116:119], v[216:219]\n\t"
        "v_cvt_pk_bf16_f32 v232, v208, v209\n\t"
        "v_cvt_pk_bf16_f32 v233, v210, v211\n\t"
        "v_mfma_f32_16x16x32_bf16 v[220:223], v[196:199], v[116:119], v[220:223]\n\t"
        "v_cvt_pk_bf16_f32 v234, v212, v213\n\t"
        "v_cvt_pk_bf16_f32 v235, v214, v215\n\t"
        "v_mfma_f32_16x16x32_bf16 v[216:219], v[184:187], v[120:123], v[216:219]\n\t"
        "s_add_u32 s92, %[n1], 524288\n\t"
        "buffer_store_dwordx4 v[232:235], %[o1], %[rs], s92 offen sc1\n\t"
        "v_mfma_f32_16x16x32_bf16 v[220:223], v[200:203], v[120:123], v[220:223]\n\t"
        "v_mfma_f32_16x16x32_bf16 v[216:219], v[188:191], v[124:127], v[216:219]\n\t"
        "v_mfma_f32_16x16x32_bf16 v[220:223], v[204:207], v[124:127], v[220:223]\n\t"
        "s_add_u32 s92, %[f1], 524288\n\t"
        "buffer_store_dwordx4 v[232:235], %[o1], %[rs], s92 offen sc1\n\t"
        "v_mfma_f32_16x16x32_bf16 v[208:211], v[176:179], v[48:51], 0\n\t"
        "v_mfma_f32_16x16x32_bf16 v[212:215], v[192:195], v[48:51], 0\n\t"
        "v_mfma_f32_16x16x32_bf16 v[208:211], v[180:183], v[52:55], v[208:211]\n\t"
        "v_cvt_pk_bf16_f32 v236, v216, v217\n\t"
        "v_cvt_pk_bf16_f32 v237, v218, v219\n\t"
        "v_mfma_f32_16x16x32_bf16 v[212:215], v[196:199], v[52:55], v[212:215]\n\t"
        "v_cvt_pk_bf16_f32 v238, v220, v221\n\t"
        "v_cvt_pk_bf16_f32 v239, v222, v223\n\t"
        "v_mfma_f32_16x16x32_bf16 v[208:211], v[184:187], v[56:59], v[208:211]\n\t"
        "s_add_u32 s92, %[n1], 589824\n\t"
        "buffer_store_dwordx4 v[236:239], %[o1], %[rs], s92 offen sc1\n\t"
        "v_mfma_f32_16x16x32_bf16 v[212:215], v[200:203], v[56:59], v[212:215]\n\t"
        "v_mfma_f32_16x16x32_bf16 v[208:211], v[188:191], v[60:63], v[208:211]\n\t"
        "v_mfma_f32_16x16x32_bf16 v[212:215], v[204:207], v[60:63], v[212:215]\n\t"
        "s_add_u32 s92, %[f1], 589824\n\t"
        "buffer_store_dwordx4 v[236:239], %[o1], %[rs], s92 offen sc1\n\t"
        "v_mfma_f32_16x16x32_bf16 v[216:219], v[176:179], v[128:131], 0\n\t"
        "v_mfma_f32_16x16x32_bf16 v[220:223], v[192:195], v[128:131], 0\n\t"
        "v_mfma_f32_16x16x32_bf16 v[216:219], v[180:183], v[132:135], v[216:219]\n\t"
        "v_cvt_pk_bf16_f32 v224, v208, v209\n\t"
        "v_cvt_pk_bf16_f32 v225, v210, v211\n\t"
        "v_mfma_f32_16x16x32_bf16 v[220:223], v[196:199], v[132:135], v[220:223]\n\t"
        "v_cvt_pk_bf16_f32 v226, v212, v213\n\t"
        "v_cvt_pk_bf16_f32 v227, v214, v215\n\t"
        "v_mfma_f32_16x16x32_bf16 v[216:219], v[184:187], v[136:139], v[216:219]\n\t"
        "s_add_u32 s92, %[n1], 1048576\n\t"
        "buffer_store_dwordx4 v[224:227], %[o1], %[rs], s92 offen sc1\n\t"
        "v_mfma_f32_16x16x32_bf16 v[220:223], v[200:203], v[136:139], v[220:223]\n\t"
        "v_mfma_f32_16x16x32_bf16 v[216:219], v[188:191], v[140:143], v[216:219]\n\t"
        "v_mfma_f32_16x16x32_bf16 v[220:223], v[204:207], v[140:143], v[220:223]\n\t"
        "s_add_u32 s92, %[f1], 1048576\n\t"
        "buffer_store_dwordx4 v[224:227], %[o1], %[rs], s92 offen sc1\n\t"
        "v_mfma_f32_16x16x32_bf16 v[208:211], v[176:179], v[64:67], 0\n\t"
        "v_mfma_f32_16x16x32_bf16 v[212:215], v[192:195], v[64:67], 0\n\t"
        "v_mfma_f32_16x16x32_bf16 v[208:211], v[180:183], v[68:71], v[208:211]\n\t"
        "v_cvt_pk_bf16_f32 v228, v216, v217\n\t"
        "v_cvt_pk_bf16_f32 v229, v218, v219\n\t"
        "v_mfma_f32_16x16x32_bf16 v[212:215], v[196:199], v[68:71], v[212:215]\n\t"
        "v_cvt_pk_bf16_f32 v230, v220, v221\n\t"
        "v_cvt_pk_bf16_f32 v231, v222, v223\n\t"
        "v_mfma_f32_16x16x32_bf16 v[208:211], v[184:187], v[72:75], v[208:211]\n\t"
        "s_add_u32 s92, %[n1], 1114112\n\t"
        "buffer_store_dwordx4 v[228:231], %[o1], %[rs], s92 offen sc1\n\t"
        "v_mfma_f32_16x16x32_bf16 v[212:215], v[200:203], v[72:75], v[212:215]\n\t"
        "v_mfma_f32_16x16x32_bf16 v[208:211], v[188:191], v[76:79], v[208:211]\n\t"
        "v_mfma_f32_16x16x32_bf16 v[212:215], v[204:207], v[76:79], v[212:215]\n\t"
        "s_add_u32 s92, %[f1], 1114112\n\t"
        "buffer_store_dwordx4 v[228:231], %[o1], %[rs], s92 offen sc1\n\t"
        "v_mfma_f32_16x16x32_bf16 v[216:219], v[176:179], v[144:147], 0\n\t"
        "v_mfma_f32_16x16x32_bf16 v[220:223], v[192:195], v[144:147], 0\n\t"
        "v_mfma_f32_16x16x32_bf16 v[216:219], v[180:183], v[148:151], v[216:219]\n\t"
        "v_cvt_pk_bf16_f32 v232, v208, v209\n\t"
        "v_cvt_pk_bf16_f32 v233, v210, v211\n\t"
        "v_mfma_f32_16x16x32_bf16 v[220:223], v[196:199], v[148:151], v[220:223]\n\t"
        "v_cvt_pk_bf16_f32 v234, v212, v213\n\t"
        "v_cvt_pk_bf16_f32 v235, v214, v215\n\t"
        "v_mfma_f32_16x16x32_bf16 v[216:219], v[184:187], v[152:155], v[216:219]\n\t"
        "s_add_u32 s92, %[n1], 1572864\n\t"
        "buffer_store_dwordx4 v[232:235], %[o1], %[rs], s92 offen sc1\n\t"
        "v_mfma_f32_16x16x32_bf16 v[220:223], v[200:203], v[152:155], v[220:223]\n\t"
        "v_mfma_f32_16x16x32_bf16 v[216:219], v[188:191], v[156:159], v[216:219]\n\t"
        "v_mfma_f32_16x16x32_bf16 v[220:223], v[204:207], v[156:159], v[220:223]\n\t"
        "s_add_u32 s92, %[f1], 1572864\n\t"
        "buffer_store_dwordx4 v[232:235], %[o1], %[rs], s92 offen sc1\n\t"
        "v_mfma_f32_16x16x32_bf16 v[208:211], v[176:179], v[80:83], 0\n\t"
        "v_mfma_f32_16x16x32_bf16 v[212:215], v[192:195], v[80:83], 0\n\t"
        "v_mfma_f32_16x16x32_bf16 v[208:211], v[180:183], v[84:87], v[208:211]\n\t"
        "v_cvt_pk_bf16_f32 v236, v216, v217\n\t"
        "v_cvt_pk_bf16_f32 v237, v218, v219\n\t"
        "v_mfma_f32_16x16x32_bf16 v[212:215], v[196:199], v[84:87], v[212:215]\n\t"
        "v_cvt_pk_bf16_f32 v238, v220, v221\n\t"
        "v_cvt_pk_bf16_f32 v239, v222, v223\n\t"
        "v_mfma_f32_16x16x32_bf16 v[208:211], v[184:187], v[88:91], v[208:211]\n\t"
        "s_add_u32 s92, %[n1], 1638400\n\t"
        "buffer_store_dwordx4 v[236:239], %[o1], %[rs], s92 offen sc1\n\t"
        "v_mfma_f32_16x16x32_bf16 v[212:215], v[200:203], v[88:91], v[212:215]\n\t"
        "v_mfma_f32_16x16x32_bf16 v[208:211], v[188:191], v[92:95], v[208:211]\n\t"
        "v_mfma_f32_16x16x32_bf16 v[212:215], v[204:207], v[92:95], v[212:215]\n\t"
        "s_add_u32 s92, %[f1], 1638400\n\t"
        "buffer_store_dwordx4 v[236:239], %[o1], %[rs], s92 offen sc1\n\t"
        "v_mfma_f32_16x16x32_bf16 v[216:219], v[176:179], v[160:163], 0\n\t"
        "v_mfma_f32_16x16x32_bf16 v[220:223], v[192:195], v[160:163], 0\n\t"
        "v_mfma_f32_16x16x32_bf16 v[216:219], v[180:183], v[164:167], v[216:219]\n\t"
        "v_cvt_pk_bf16_f32 v224, v208, v209\n\t"
        "v_cvt_pk_bf16_f32 v225, v210, v211\n\t"
        "v_mfma_f32_16x16x32_bf16 v[220:223], v[196:199], v[164:167], v[220:223]\n\t"
        "v_cvt_pk_bf16_f32 v226, v212, v213\n\t"
        "v_cvt_pk_bf16_f32 v227, v214, v215\n\t"
        "v_mfma_f32_16x16x32_bf16 v[216:219], v[184:187], v[168:171], v[216:219]\n\t"
        "s_add_u32 s92, %[n2], 0\n\t"
        "buffer_store_dwordx4 v[224:227], %[o1], %[rs], s92 offen sc1\n\t"
        "v_mfma_f32_16x16x32_bf16 v[220:223], v[200:203], v[168:171], v[220:223]\n\t"
        "v_mfma_f32_16x16x32_bf16 v[216:219], v[188:191], v[172:175], v[216:219]\n\t"
        "v_mfma_f32_16x16x32_bf16 v[220:223], v[204:207], v[172:175], v[220:223]\n\t"
        "s_add_u32 s92, %[f2], 0\n\t"
        "buffer_store_dwordx4 v[224:227], %[o1], %[rs], s92 offen sc1\n\t"
        "s_setprio 0\n\t"
        "s_cmp_eq_u32 %[now], 0\n\t"
        "s_cbranch_scc1 Ln%=\n\t"
        "s_nop 4\n\t"
        "v_cvt_pk_bf16_f32 v228, v216, v217\n\t"
        "v_cvt_pk_bf16_f32 v229, v218, v219\n\t"
        "v_cvt_pk_bf16_f32 v230, v220, v221\n\t"
        "v_cvt_pk_bf16_f32 v231, v222, v223\n\t"
        "s_add_u32 s92, %[n2], 65536\n\t"
        "buffer_store_dwordx4 v[228:231], %[o1], %[rs], s92 offen sc1\n\t"
        "s_add_u32 s92, %[f2], 65536\n\t"
        "buffer_store_dwordx4 v[228:231], %[o1], %[rs], s92 offen sc1\n\t"
        "s_waitcnt vmcnt(0)\n\t"
        "Ln%=:\n\t"
        "s_waitcnt vmcnt(18)\n\t"
        : [acc] "+{v[208:223]}"(acc)
        : [rs] "s"(rs), [n0] "s"(n0), [f0] "s"(f0), [o1] "v"(o1), [w0] "{v[16:47]}"(W[0]), [w1] "{v[48:79]}"(W[1]), [w2] "{v[80:111]}"(W[2]), [w3] "{v[112:143]}"(W[3]), [w4] "{v[144:175]}"(W[4]), [vr] "v"(vr), [n1] "s"(n1), [n2] "s"(n2), [f1] "s"(f1), [f2] "s"(f2), [now] "s"(now)
        : "s92", "v176", "v177", "v178", "v179", "v180", "v181", "v182", "v183", "v184", "v185", "v186", "v187", "v188", "v189", "v190", "v191", "v192", "v193", "v194", "v195", "v196", "v197", "v198", "v199", "v200", "v201", "v202", "v203", "v204", "v205", "v206", "v207", "v224", "v225", "v226", "v227", "v228", "v229", "v230", "v231", "v232", "v233", "v234", "v235", "v236", "v237", "v238", "v239", "vcc", "scc", "memory");
}

__device__ __forceinline__ void w_bl(u32x4 rs, u32 o1, u32 n0, u32 f0, f32x16& acc) {
    asm volatile(
        "s_setprio 2\n\t"
        "s_barrier\n\t"
        "s_nop 3\n\t"
        "v_cvt_pk_bf16_f32 v228, v216, v217\n\t"
        "v_cvt_pk_bf16_f32 v229, v218, v219\n\t"
        "v_cvt_pk_bf16_f32 v230, v220, v221\n\t"
        "v_cvt_pk_bf16_f32 v231, v222, v223\n\t"
        "s_add_u32 s92, %[n0], 65536\n\t"
        "buffer_store_dwordx4 v[228:231], %[o1], %[rs], s92 offen sc1\n\t"
        "s_add_u32 s92, %[f0], 65536\n\t"
        "buffer_store_dwordx4 v[228:231], %[o1], %[rs], s92 offen sc1\n\t"
        "s_nop 1\n\t"
        : [acc] "+{v[208:223]}"(acc)
        : [rs] "s"(rs), [n0] "s"(n0), [f0] "s"(f0), [o1] "v"(o1)
        : "s92", "v224", "v225", "v226", "v227", "v228", "v229", "v230", "v231", "v232", "v233", "v234", "v235", "v236", "v237", "v238", "v239", "vcc", "scc", "memory");
}

#define TP0 138
#define TP1 8
#define TP2 177
#define TP3 4
#define TP4 40
#define TP5 24
#define TP6 19
#define TP7 6
#define TP8 16
#define TP9 106
#define TP10 6
#define TP11 78
#define TP12 66
#define TP13 170
#define TP14 24
#define TP15 30
#define INJ 0u
#define PER_DFLT (TV(2) + TV(11) + (TV(0) * 4400u >> 12) + TV(10))
#define DBG 0
#define EARLY_N 50
#define BUMP_N 2
#define BUMP_WIN 0x7fcu
#define BUMP_D 8
#define CLK_G 90
#define BUMP_S0 4
#define BUMP_MAX 8
#define GB0 96u
#define TV(idx) ((u32)(TP##idx) * 8u)
#define TLOAD() do { } while (0)

__device__ __forceinline__ void nap(u32 n) {
    u32 hi = n >> 6, lo = (n >> 4) & 3u;
    asm volatile("s_cmp_eq_u32 %0, 0\n\ts_cbranch_scc1 Ln%=\nLm%=:\n\ts_sleep 1\n\ts_sub_u32 %0, %0, 1\n\ts_cmp_lg_u32 %0, 0\n\ts_cbranch_scc1 Lm%=\nLn%=:\n\t"
                 "s_cmp_eq_u32 %1, 0\n\ts_cbranch_scc1 Lp%=\nLo%=:\n\ts_sub_u32 %1, %1, 1\n\ts_cmp_lg_u32 %1, 0\n\ts_cbranch_scc1 Lo%=\nLp%=:"
                 : "+s"(hi), "+s"(lo) :: "scc", "memory");
}
typedef __attribute__((address_space(3))) u32* lds_u32p;
typedef __attribute__((address_space(3))) volatile u32* lds_vu32p;
typedef __attribute__((address_space(3))) u16* lds_u16p;
typedef __attribute__((address_space(3))) u32x2* lds_u32x2p;
typedef __attribute__((address_space(3))) u32x4* lds_u32x4p;
__device__ __forceinline__ u32 rfl(u32 x) { return __builtin_amdgcn_readfirstlane(x); }
__device__ __forceinline__ bool has_sent(u32x4 v) { return (v[0] == SENT) | (v[1] == SENT) | (v[2] == SENT) | (v[3] == SENT); }
__device__ __forceinline__ u32 now32() { return (u32)__builtin_amdgcn_s_memtime(); }
__device__ __forceinline__ u32 step_tag(int s) { return (u32)(s + 1) * 0x10001u; }

__device__ __forceinline__ void mem0_image(const u16* __restrict__ mem0, char* smem, const int tid) {
    for (int c = tid; c < 640; c += 512) {
        u32x4 v = *(const u32x4*)(mem0 + (c & 7) * D_ + (c >> 3) * 8);
#pragma unroll
        for (int i = 0; i < 4; ++i) v[i] = v[i] == SENT ? ~0u : v[i];
        ((u32x4*)smem)[c + (c >= 576 ? 64 : 0)] = v;
    }
}
__device__ __forceinline__ u32 once_more(u32& left, u32 lbase) {
    if (__builtin_expect((int)--left >= 0, 1)) return 2u;
    const int lane = lane_id();
    for (int k = 0; k < 11; ++k) *(lds_u32x4p)(lbase + (u32)(k * 1024 + lane * 16)) = (u32x4){0u, 0u, 0u, 0u};
    asm volatile("s_waitcnt lgkmcnt(0)" ::: "memory");
    return 0u;
}
#define COUNT(i) do { } while (0)
#define COUNT2() do { } while (0)
#define COUNT3() do { } while (0)
#define COUNT4() do { } while (0)
#define COUNT0() do { } while (0)
#define COUNT1(i) do { } while (0)
__device__ __forceinline__ void track_rate(u32 c0, u32 r0, u32& pc, u32& pr, u32& kr) {
    const u32 dr = r0 - pr;
    const int er = (int)(c0 - pc - (dr * kr >> 12)) >> 1;
    u32 k = (u32)max((int)kr + er, 2560);
    asm("" : "+s"(k));
    if (dr < 8192u) kr = min(k, 4352u);
    pc = c0; pr = r0;
}

__device__ __forceinline__ u32 opaque(u32 x) { asm volatile("" : "+s"(x)); return x; }
#define LANES() const int lane = lane_id(), n = lane & 15, g = lane >> 4; (void)n; (void)g
constexpr u32 L_Q = 12288, L_K = 18432, L_E = 24576, L_DR = 25088, L_MSK = 25600, L_KQ = 32768;
constexpr float LIM_X = 32.f, LIM_D = 1.0995116e12f;
__device__ __forceinline__ u32 kq_buf(int sg) { return L_KQ + (u32)(sg % 3) * 16384u; }

__device__ __forceinline__ u32 p_at(int h, int row, int d, int g) {
    return SL_P + (u32)((h >> 4) * 32768 + (h & 4) * 512 + (h & 8) * 128 + (h & 3) * 256 + ((row >> 1) * 4 + (row & 1) + (g & 1) * 2) * 16 + ((d ^ (h >> 1)) & 1) * 8 + (g >> 1) * 4);
}
__device__ __forceinline__ void s_slow(const int h, const int s, rsrc_t rs, u32 so, u32 lbase) {
    const int lane = lane_id(), row = lane & 7, j = lane >> 3;
    const float ce = 0.08838834764831845f * 1.4426950408889634f;
    float sc[5] = {0.f, 0.f, 0.f, 0.f, 0.f};
    const u32 kx = lbase + kq_buf(s) + (u32)((j & 1) * 4096 + (j >> 1) * 64);
#pragma unroll 1
    for (int e = 0; e < DH_; ++e) {
        const u32 qa = lbase + L_Q + (u32)((e >> 4) * 768 + (row >> 2) * 192 + (e & 15) * 8 + (row & 3) * 2);
        const float q = bf2f(*(lds_u16p)qa) + bf2f(*(lds_u16p)(qa + 384));
        const u32 ka = kx + (u32)((e >> 5) * 1024 + ((e >> 2) & 3) * 256 + ((e >> 4) & 1) * 8 + (e & 3) * 2);
#pragma unroll
        for (int i = 0; i < 4; ++i) sc[i] += q * bf2f(*(lds_u16p)(ka + i * 16));
        sc[4] += q * bf2f(*(lds_u16p)(lbase + L_K + (u32)((e >> 4) * 768 + (j >> 1) * 192 + (e & 15) * 8 + (j & 1) * 2)));
    }
    sc[4] *= ce;
    const u32x2 ev = *(lds_u32x2p)(lbase + L_E + (u32)((j >> 1) * 128 + row * 16 + (j & 1) * 8));
    float se = (bf2f((u16)ev[0]) + bf2f((u16)(ev[0] >> 16))) + (bf2f((u16)ev[1]) + bf2f((u16)(ev[1] >> 16)));
    float mx = fmaxf(fmaxf(fmaxf(sc[0], sc[1]), fmaxf(sc[2], sc[3])), sc[4]);
#pragma unroll
    for (int o = 8; o < 64; o <<= 1) { se += __shfl_xor(se, o); mx = fmaxf(mx, __shfl_xor(mx, o)); }
    const bool rew = ev[0] == ~0u || ev[1] == ~0u;
    u64 rw = __ballot(rew);
    rw |= rw >> 32; rw |= rw >> 16; rw |= rw >> 8;
    const bool again = (rw >> row) & 1u;
    float al = exp2f(-mx);
    if (again) {
        const u32x2 en = {pack2(exp2f(sc[0] - mx), exp2f(sc[1] - mx)), pack2(exp2f(sc[2] - mx), exp2f(sc[3] - mx))};
        se = (bf2f((u16)en[0]) + bf2f((u16)(en[0] >> 16))) + (bf2f((u16)en[1]) + bf2f((u16)(en[1] >> 16)));
        al = 1.f;
    }
    float sn = se;
#pragma unroll
    for (int o = 8; o < 64; o <<= 1) sn += __shfl_xor(sn, o);
    if (again) se = sn;
    const float em = exp2f(sc[4] - mx);
    float sm = em;
#pragma unroll
    for (int o = 8; o < 64; o <<= 1) sm += __shfl_xor(sm, o);
    const float rd = 1.f / (sm + al * se);
    if (rw & 255u) {
#pragma unroll 1
        for (u32 spins = 0; spins < SPIN_CAP && !__all(*(lds_vu32p)(lbase + L_DR + (u32)(lane & 1) * 4) == step_tag(s)); ++spins) __builtin_amdgcn_s_sleep(1);
        const u32x2 en = {pack2(exp2f(sc[0] - mx), exp2f(sc[1] - mx)), pack2(exp2f(sc[2] - mx), exp2f(sc[3] - mx))};
        const u32 eo = again ? (u32)((h >> 3) * 32768 + ((h >> 1) & 3) * 1024 + (j >> 2) * 256 + (h & 1) * 128 + row * 16 + (j & 3) * 4) : OOB;
#pragma unroll
        for (int c = 0; c < 8; ++c) stx1(rs, eo, en[c >> 2], so + (c & 1) * 4096 + (c & 2) * 8192 + (c >> 2) * 512);
    }
    DRAIN();
    const u32 pm = pack2(em * rd, __shfl_xor(em * rd, 8));
    const int g = j >> 1;
    u32 dg = pack2(row == 2 * g ? al * rd : 0.f, row == 2 * g + 1 ? al * rd : 0.f);
    if (again && (row >> 1) == (g ^ 1)) dg = MARK;
    const u32 po = p_at(h, row, j & 1, g);
    const bool done = ldx1(rs, po, so) != SENT;
#pragma unroll
    for (int c = 0; c < 4; ++c) stx1(rs, done ? OOB : po, (j & 1) ? dg : pm, so + (c & 1) * 4096 + (c >> 1) * 16384);
}

__device__ __forceinline__ void s_role(const int wave, const int h, const int xg, const u64 tag, const u16* __restrict__ mem0, const u32x32 (&W)[5],
                                       char* __restrict__ ws, char* smem) {
    const int lane = lane_id(), tid = wave * 64 + lane, n = lane & 15, g = lane >> 4;
    u32* errw = (u32*)(ws + OFF_ERR);
    const rsrc_t rs = mk_rsrc(ws);
    const u32x4 srd = mk_srd(ws);
    const u32 lbase = (u32)(uintptr_t)(__attribute__((address_space(3))) char*)smem;
    TLOAD();
    int wslot = 0; const int wid = 128 + h;
    WSTAMP(2);
    const u32 wp = wait_init(rs, ws, tag, lane);
    mem0_image(mem0, smem, tid);
    if (tid < 448) *(u32x4*)(smem + L_K + tid * 16) = (u32x4){0u, 0u, 0u, 0u};
    const u32 sidx = (n & 2) ? 0xEEEE : 0x4444;
    const u32 va = lbase + (u32)(g * 256 + (n & 2) * 64 + (n >> 2) * 32 + (n & 1) * 16);
    const u32 qw = lbase + L_Q + (u32)(wave * 768 + (g >> 1) * 192 + n * 8 + (g & 1) * 4);
    const u32 kw = lbase + L_K + (u32)(wave * 768 + g * 192 + n * 8);
    const u32 qtr = lbase + L_Q + (u32)((lane & 3) * 192 + (lane >> 2) * 8);
    const float ce = 0.08838834764831845f * 1.4426950408889634f;
    STAMPS();
    COUNT0();
#define S_LAST() do { u32 redo; do { redo = 0; s_l(W, va, lbase, sidx, qw, kw, step_tag(NSEG - 1), redo); } while (__builtin_expect(redo, 0)); } while (0)

    if (wave < 2) {
        LANES();
        const int c = 2 * wave + (n >> 3);
        const u32 kxa = lbase + (u32)(((n >> 2) & 1) * 4096 + g * 256 + (4 * c + (n & 3)) * 16);
        const u32 ep = (u32)((n & 7) * 16 + (g & 1) * 8 + (n >> 3) * 4);
        const u32 eo = (u32)((h >> 3) * 32768 + ((h >> 1) & 3) * 1024 + (wave + 2 * (n >> 3)) * 256 + (h & 1) * 128 + (n & 7) * 16 + g * 4);
        const u32 ew = lbase + L_E + (u32)((2 * wave + (g >> 1)) * 128) + ep;
        const u32 ts = slot_v(wp, lane), tk = kq_buf(lane);
        LBAR();
#pragma unroll 1
        for (int s = 0; s + 1 < NSEG; ++s) {
            s_e(W, va, lbase, sidx, qw, kw, step_tag(s), kxa + tab(tk, s), qtr, eo, ew, LIM_X, srd, tab(ts, s) STA);
            nap(256);
            DRAIN();
            *(lds_u32p)(lbase + L_DR + (u32)wave * 4) = step_tag(s);
            FLUSH();
        }
        S_LAST();
    } else if (wave == 2) {
        LANES();
        const u32 po = p_at(h, n & 7, n >> 3, g);
        const u32 er = lbase + L_E + (u32)(g * 128 + (n & 7) * 16);
        const u32x4 ones = {0x3f803f80u, 0x3f803f80u, 0x3f803f80u, 0x3f803f80u};
        const u32 ts = slot_v(wp, lane);
        LBAR();
#pragma unroll 1
        for (int s = 0; s + 1 < NSEG; ++s) {
            const u32 so = tab(ts, s);
            u32 bad;
            s_f(W, va, lbase, sidx, qw, kw, step_tag(s), bad, ones, qtr, er, po, n < 8 ? 1.f : 0.f, n == 8 + 2 * g ? 1.f : 0.f, n == 9 + 2 * g ? 1.f : 0.f, srd, so, ce, LIM_D,
                    0xccccccccccccccccull STA);
            if (__builtin_expect(bad, 0)) s_slow(h, s, rs, so, lbase);
            FLUSH();
        }
        S_LAST();
    } else if (wave == 3) {
        LANES();
        const u32 vm = (u32)lane * 16, cp = (u32)xg ^ (wp >> 1);
        const bool lead = h == 0;
        const u32 tsw = (u32)((lane & 7) * 16 + (lane & 8) * 512 + (lane & 16) * 1024 + (lane >> 5) * SL_2ND);
        const u32 ro = (u32)(lane & 31) * 128, rl = SL_REC + (u32)h * 128;
        LBAR();
        u32 c0, r0, kr = 3950;
        calib(c0, r0);
        u32 pc = c0, pr = r0, te = r0 - 65536u, tn = te + PER_DFLT + GB0, left = SPIN_CAP, own = 0, sp = 0, lim = c0 - 65536u, tg = 0, gb = GB0, irr = 0, hist = ~0u, bump = 0;
#pragma unroll 1
        for (int s = 0; s + 1 < NSEG; ++s) {
            const u32 sm = slot_off(wp ^ 8u, s), so = sp + copy_off(cp, s - 1);
            const u32 mg = s < EARLY_N ? TV(10) : 0u, er = 4096u - kr, ki = 4096u + er + (u32)((int)(er * er) >> 12);
            const u32 off = (TV(0) * ki >> 12) + mg + bump * BUMP_D;
            const u32 lev = min((u32)max((int)(ki - 4200u) * CLK_G >> 12, 0) + (mg >> 4) + bump, 15u);
            u32 poll = ((u32)s + 63u) >> 6, lv;
            s_m(W, va, lbase, sidx, qw, kw, step_tag(s), left, poll, lv, lim, tg, irr, ki, off + TV(14), TV(11), 2000u, TV(7), TV(13), TV(13) * 4400u >> 12, TV(15), sm + copy_off(cp, s) + SL_REC,
                sm + SL_REC + SL_2ND, so + SL_2ND, c0, r0, te, tn, gb, lev, own, TV(2), TV(9), kr, TV(2) + TV(11) + off + 15u, PER_DFLT, vm, so, lbase, srd, sm + rl,
                lead ? sp + SL_TS : OOB, (u32)s, tsw, ro, so + SL_REC, sp + SL_REC + SL_2ND STA);
            if (lv) COUNT(0);
            if (lv > 1) COUNT2();
            own = lv; sp = sm;
            hist = hist * 2 + (irr >> 31);
            if (s > BUMP_S0 && (hist & 3u) == 1u && (hist & BUMP_WIN) && bump < BUMP_MAX) bump += BUMP_N;
            track_rate(c0, r0, pc, pr, kr);
            FLUSH();
        }
        const u32 so = sp + copy_off(cp, NSEG - 2);
        s_g(c0, r0, te, tn, gb, 0u, own, TV(2), TV(9), kr, 0u, 0u, vm, so, lbase, srd, OOB, lead ? sp + SL_TS : OOB, NSEG - 1u, tsw, ro, so + SL_REC, sp + SL_REC + SL_2ND STA);
        for (;;) {
            u32 redo = 0;
            s_l(W, va, lbase, sidx, qw, kw, step_tag(NSEG - 1), redo);
            if (__builtin_expect(!redo, 1)) break;
            if (once_more(left, lbase)) gather(r0, kr, vm, so + SL_2ND, lbase, srd);
        }
        if ((int)left < 0) atomicOr(errw, 1u);
    } else if (wave < 6) {
        LANES();
        const int nt = wave & 1;
        f32x4* msk = (f32x4*)(smem + L_MSK) + nt * 128 + lane;
#pragma unroll
        for (int q = 0; q < 2; ++q) {
            f32x4 m;
#pragma unroll
            for (int i = 0; i < 4; ++i) m[i] = q * 16 + 4 * g + i <= nt * 16 + n ? 0.f : -__builtin_huge_valf();
            msk[q * 64] = m;
        }
        const u32 vl = lbase + (u32)lane * 16;
        const u32 o0 = (u32)OFF_PX + (u32)((h * 2 + nt) * 1024 + lane * 16);
        const u32 o1 = (u32)OFF_PXM + (u32)(((h >> 2) * 2 + nt) * 1024 + ((h & 3) * 16 + n) * 16 + g * 4);
        u32x4 p0 = {0u, 0u, 0u, 0u};
        u32 p1 = 0;
        LBAR();
#pragma unroll 1
        for (int s = 0; s < NSEG; ++s) {
            if (s + 1 < NSEG) {
                const u32 off = s == 0 || (DBG & 8) ? OOB : 0u;
                s_p1(W, va, lbase, sidx, qw, kw, step_tag(s), p0, p1, (o0 + (u32)s * 65536 - 65536) | off, (o1 + (u32)s * 16384 - 16384) | off, srd STA);
                nap(TV(8));
            } else {
                stx4(rs, o0 + (NSEG - 2) * 65536, p0);
                stx1(rs, o1 + (NSEG - 2) * 16384, p1);
                S_LAST();
                stx1(rs, lane == 0 ? (u32)(OFF_FP + (h >> 2) * 128 + ((h & 3) * 2 + nt) * 4) : OOB, 1u);
            }
            px_step(p0, p1, vl + kq_buf(s), vl + kq_buf(s) + 8192 + (u32)nt * 4096, vl + L_MSK + (u32)nt * 2048, qtr + (L_K - L_Q), ce);
            STAMP(3);
            FLUSH();
        }
        stx4(rs, o0 + (NSEG - 1) * 65536, p0);
        stx1(rs, o1 + (NSEG - 1) * 16384, p1);
        DRAIN();
        stx1(rs, lane == 0 ? (u32)(OFF_FZ + ((h >> 2) * 2 + nt) * 128 + (h & 3) * 4) : OOB, 1u);
    } else if (wave == 6) {
        LANES();
        const u32 fl = done_at(wp, 0, (lane & 1) ? h : ((lane & 2) && h < NFILL ? 64 + h : 32 + h)), dv = done_tag(tag);
        u32 cap = SPIN_CAP;
        wait_done(rs, fl + (u32)(lane & 4) * 8192, dv, cap, errw);
#pragma unroll
        for (int i = 0; i < 16; ++i) dma16_sc1(ws + far_off(wp ^ (i < 8 ? KFL : QFL), (i < 8 ? NEAR_KX : NEAR_QX) + (u32)h * 8192) + lane * 16 + (i & 7) * 1024, lbase + L_KQ + i * 1024);
        DRAIN();
        LBAR();
#pragma unroll 1
        for (int s = 0; s + 1 < NSEG; ++s) {
            const u32 pfd = lbase + kq_buf(s + 1);
            const u32 fs = fl + (u32)min(s + 2, NSEG - 1) * 32768;
            s_p0(W, va, lbase, sidx, qw, kw, step_tag(s), (u32)lane * 16, srd, (DBG & 4) ? OOB : far_off(wp ^ KFL, NEAR_KX + (u32)((s + 1) * H_ + h) * 8192), pfd,
                 lane < 4 ? fs : OOB, lbase + 11264u STA);
            nap(TV(4));
            DRAIN();
            if (!__all(*(lds_vu32p)(lbase + 11264u + (u32)(lane & 3) * 4) == dv)) { COUNT4(); wait_done(rs, fs, dv, cap, errw); }
            FLUSH();
        }
        S_LAST();
    } else {
        LANES();
        u32 cap = SPIN_CAP;
        wait_done(rs, done_at(wp, 0, 64 + min(lane, NFILL - 1)), done_tag(tag), cap, errw);
        LBAR();
#pragma unroll 1
        for (int s = 0; s + 1 < NSEG; ++s) {
            const u32 pfd = lbase + kq_buf(s + 1) + 8192;
            s_p2(W, va, lbase, sidx, qw, kw, step_tag(s), (u32)lane * 16, srd, (DBG & 4) ? OOB : far_off(wp ^ QFL, NEAR_QX + (u32)((s + 1) * H_ + h) * 8192), pfd STA);
            FLUSH();
        }
        S_LAST();
    }
    COUNT1(0);
}

__device__ __forceinline__ void v_role(const int wave, const int h, const int xg, const u64 tag, const u16* __restrict__ mem0, const u32x32 (&W)[5],
                                       char* __restrict__ ws, char* smem) {
    const int lane = lane_id(), tid = wave * 64 + lane, n = lane & 15, g = lane >> 4;
    u32* errw = (u32*)(ws + OFF_ERR);
    const rsrc_t rs = mk_rsrc(ws);
    const u32x4 srd = mk_srd(ws);
    const u32 lbase = (u32)(uintptr_t)(__attribute__((address_space(3))) char*)smem;
    TLOAD();
    const u32 wp = wait_init(rs, ws, tag, lane);
    mem0_image(mem0, smem, tid);
    const u32 va = lbase + (u32)(g * 256 + (n & 2) * 64 + (n >> 2) * 32 + (n & 1) * 16);
    const u32 si = n >= 8 ? 0xEEEE : 0x4444, vr = lbase + 12288 + (u32)((lane >> 2) * 16 + (lane & 1) * 8);
    auto zat = [&](int s, int i) {
        const int l = lane_id(), g = l >> 4;
        return z_at(wp, s, (u32)(2 * wave + (g >> 1) + i * 16 + (i < 2 ? (g & 1) * 8 : 0))) + (u32)((h >> 4) * 32768 + (h & 15) * 256 + (l & 15) * 16 + (i < 2 ? 0 : (g & 1) * 8));
    };
    STAMPS();
    COUNT0();
    if (wave == 3) {
        const int lane = lane_id();
        const u32 cp = (u32)xg ^ (wp >> 1), ro = (u32)(lane & 31) * 128;
        u32 left = SPIN_CAP;
        wait_done(rs, done_at(wp, 0, 64 + min(lane, NFILL - 1)), done_tag(tag), left, errw);
        LBAR();
        u32 c0, r0, kr = 3950;
        calib(c0, r0);
        u32 pc = c0, pr = r0, te = r0 - 65536u, tn = te + PER_DFLT + GB0, so = 0, sp = 0, gb = GB0;
#pragma unroll 1
        for (int s = 0; s < NSEG; ++s) {
            const u32 sm = slot_off(wp ^ 8u, s), sn = sm + copy_off(cp, s), l0 = left;
            v_b1(W, va, lbase, si, vr, zat(s, 0), zat(s, 1), zat(s, 2), srd, left, c0, r0, te, tn, gb, (u32)s, TV(2) + TV(3), TV(9), kr, PER_DFLT,
                 (u32)lane * 16, so, so + SL_2ND, lbase, ro, sn + SL_REC, so + SL_REC, sp + SL_REC + SL_2ND STA);
            so = sn; sp = sm;
            if (l0 != left) COUNT(3);
            track_rate(c0, r0, pc, pr, kr);
            FLUSH();
        }
        if ((int)left < 0) atomicOr(errw, 1u);
    } else if (wave < 4) {
        LBAR();
#pragma unroll 1
        for (int s = 0; s < NSEG; ++s) { v_b0(W, va, lbase, si, vr, zat(s, 0), zat(s, 1), zat(s, 2), srd STA); FLUSH(); }
    } else {
        LANES();
        const u32 vw = lbase + 12288 + (u32)((wave & 2) * 256 + (n & 4) * 64 + ((wave & 1) * 2 + (n >> 3)) * 64 + (n & 3) * 16 + g * 4);
        LBAR();
#pragma unroll 1
        for (int s = 0; s < NSEG; ++s) { v_a(W, va, lbase, (n & 2) ? 0xEEEE : 0x4444, vw STA); FLUSH(); }
    }
    DRAIN();
    LBAR();
    stx4(rs, wave == 0 && lane_id() == 0 ? (u32)(OFF_FR + h * 128) : OOB, (u32x4){1u, 1u, 1u, 1u});
    COUNT1(3);
}

__device__ __forceinline__ void r_slow(rsrc_t rs, u32 so, u32 bs, u32 zs, u32 sm, u32 bo, u32x8 za, u32 po, u32 mo, u32 lbase, u32& cap, u32* errw) {
    const int lane = lane_id(), n = lane & 15, g = lane >> 4;
    u32x4 p[8], z[8];
    u32 spins = 0;
#pragma unroll 1
    for (; spins < cap; ++spins) {
        CFENCE();
        bool miss = false;
#pragma unroll
        for (int j = 0; j < 8; ++j) {
            p[j] = ldx4(rs, po + (u32)(j & 3) * 512, so + SL_P + (u32)(j >> 2) * 32768u);
            z[j] = ldx4(rs, za[j], zs);
        }
#pragma unroll
        for (int j = 0; j < 8; ++j) miss |= has_sent(p[j]) | has_sent(z[j]);
        if (!__any(miss)) break;
    }
#pragma unroll
    for (int j = 0; j < 8; ++j)
#pragma unroll
        for (int i = 0; i < 4; ++i) p[j][i] = p[j][i] == MARK ? 0u : p[j][i];
    const u32 uw = lbase + 8192 + (u32)((g >> 1) * 512 + n * 16 + (g & 1) * 8);
#pragma unroll 1
    for (int t = 0; t < 16; ++t) {
        u32x4 e;
#pragma unroll 1
        for (; spins < cap; ++spins) {
            CFENCE();
            e = ldx4(rs, (u32)(lane * 16 + (t & 3) * 1024), so + (u32)(t >> 2) * 32768u);
            if (!__any(has_sent(e))) break;
        }
        const u32x4 x = ldx4(rs, bo + (u32)(t & 3) * 2048, bs + (u32)(t >> 2) * 16384u), y = ldx4(rs, bo + (u32)(t & 3) * 2048 + 1024, bs + (u32)(t >> 2) * 16384u);
        const f32x4 u = smf(e, (u32x4){x[0], y[0], x[2], y[2]}, (u32x4){x[1], y[1], x[3], y[3]}, (f32x4){0.f, 0.f, 0.f, 0.f}, n >= 8 ? 0xEEEE : 0x4444);
        *(lds_u32x2p)(uw + (u32)((t >> 1) * 1024 + (t & 1) * 256)) = (u32x2){pack2(u[0], u[1]), pack2(u[2], u[3])};
    }
    if (spins >= cap) { atomicOr(errw, 2u); cap = 1; }
    f32x4 acc = {0.f, 0.f, 0.f, 0.f};
#pragma unroll
    for (int j = 0; j < 8; ++j) {
        const int k = (WORD >> (4 * j)) & 7, q = k >> 1;
        const u32x4 a = {p[k][2], p[k][3], p[(k + 1) & 7][0], p[(k + 1) & 7][1]};
        const u32 ur = lbase + 8192 + (u32)(q * 2048 + lane * 16);
        acc = smf(a, (k & 1) ? z[2 * q] : *(lds_u32x4p)ur, (k & 1) ? z[2 * q + 1] : *(lds_u32x4p)(ur + 1024), acc, (n & 2) ? 0xEEEE : 0x4444);
    }
    const float a0 = acc[0] + acc[2], a1 = acc[1] + acc[3], o = __shfl_xor(n < 8 ? a1 : a0, 8);
    const u32 r = n < 8 ? pack2(a0, o) : pack2(o, a1);
#pragma unroll
    for (int c = 0; c < 8; ++c) stx1(rs, mo, r, sm + (c & 1) * 4096 + (c & 2) * 8192 + (c >> 2) * SL_2ND);
}

__device__ __forceinline__ void r_role(const int wave, const int ct, const int xg, const u32 wp, const u32 dv, char* __restrict__ ws, char* smem) {
    if (wave == 4 || wave > 5) return;
    const int lane = lane_id(), n = lane & 15, g = lane >> 4;
    u32* errw = (u32*)(ws + OFF_ERR);
    const rsrc_t rs = mk_rsrc(ws);
    const u32x4 srd = mk_srd(ws);
    const u32 lbase = (u32)(uintptr_t)(__attribute__((address_space(3))) char*)smem;
    TLOAD();
    const u32 cp = (u32)xg ^ (wp >> 1), tw = lbase + 6144;
    const u32 te = slot_v(wp, lane) + copy_off(cp, lane);
    u32 cap = SPIN_CAP;
    STAMPS();
    COUNT0();
    if (wave == 5) {
        const u32 ro = (u32)(lane & 31) * 128, rd = SL_REC + ((wp & 8u) ? -0x40000 : 0x40000);
        u32 c0, r0, kr = 4000, nl;
        *(lds_u32x4p)tw = (u32x4){0u, 0u, 0u, 0u};
        wait_done(rs, done_at(wp, 0, 32 + min(lane, 31 + NFILL)), dv, cap, errw);
        calib(c0, r0);
        u32 pc = c0, pr = r0, tx = r0 - 65536u, tb = tx, gb = GB0;
#pragma unroll 1
        for (int s = 0; s + 1 < NSEG; ++s) {
            const u32 so = tab(te, s);
            const u32 g0 = gb;
            u32 tn = tx + PER_DFLT + gb;
            r_t(c0, r0, tx, tn, gb, nl, tw, srd, step_tag(s), kr, tb, TV(12), TV(9), ro, so + rd, so + rd + SL_2ND - copy_off(cp, s) STA);
            tb = tx + TV(2) + g0 + TV(5); tx = tn;
            track_rate(c0, r0, pc, pr, kr);
            FLUSH();
        }
    } else {
        const int q = (wave + 3) & 3;
        const u32 cn = (u32)((n & 7) * 32 + (n >> 3) * 16);
        const u32 bo = (u32)g * 256 + cn, eo = (u32)(q * 32768 + lane * 16), si = n >= 8 ? 0xEEEE : 0x4444;
        const u32 tb = TWO ? far_v(wp ^ 1u, b_at(NEAR_B, RING, lane & (RING - 1), ct)) : far_v(wp, b_at(FAR_BX, NSEG, lane, ct));
        if (wave) {
            const u32 uw = lbase + (u32)(q * 2048 + (g >> 1) * 512 + n * 16 + (g & 1) * 8);
#pragma unroll 1
            for (int s = 0; s + 1 < NSEG; ++s) {
                r_u(bo, eo, si, tw, srd, tab(te, s), (DBG & 1) ? OOB : tab(tb, s) + (u32)q * 16384, step_tag(s), uw, (u32)q STA);
                FLUSH();
            }
        } else {
            const int kp = ct >= 36 ? 10 : ct >> 2;
            const u32 mo = (u32)((kp >> 2) * 32768 + (kp & 3) * 1024 + ((ct & 3) * 2 + ((n >> 2) & 1)) * 128 + (2 * g + (n >> 3)) * 16 + (n & 3) * 4);
            const u32 po = (u32)((lane >> 5) * 2048 + (lane & 31) * 16);
            u32x8 za;
#pragma unroll
            for (int j = 0; j < 8; ++j) { const int hh = 4 * j + ((g & 1) ? 8 : 2) + (g >> 1); za[j] = (u32)((hh & 16) * 2048 + (hh & 15) * 256) + cn; }
            const u32 tm = slot_v(wp ^ 8u, lane), tz = z_at(wp, lane, ct);
#pragma unroll 1
            for (int s = 0; s + 1 < NSEG; ++s) {
                const u32 so = tab(te, s), sm = tab(tm, s), bs = tab(tb, s), zs = tab(tz, s);
                u32 bad;
                r_f(bo, eo, si, tw, srd, so, (DBG & 1) ? OOB : bs + 3 * 16384, step_tag(s), bad, za, po, mo, (n & 2) ? 0xEEEE : 0x4444, lbase + (u32)lane * 16, zs, so + SL_P, sm,
                    min(TV(6), 256u), min(TV(1), 256u) STA);
                if (bad) COUNT(2);
                if (__builtin_expect(bad == 3, 0)) { COUNT3(); r_slow(rs, so, bs, zs, sm, bo, za, po, mo, lbase, cap, errw); }
                FLUSH();
            }
        }
    }
    COUNT1(2);
}

#define FILL_0 6
#define FILL_T 2
template <int PAUSE>
__device__ __forceinline__ void fill_share(rsrc_t rs, const u32 wp, const int s, const int w) {
    const int lane = lane_id(), u = w + lane * 4 * NFILL, pg = u >> 4, hf = pg >= 6;
    const u32 t = u < 224 ? slot_v(wp & ~8u, s) + (u32)((pg - 6 * hf) * 32768 + ((hf ^ (wp >> 3)) & 1) * 262144 + (u & 4) * 1024 + (u & 8) * 2048 + (u & 3) * 1024)
                          : (u < 544 ? far_v(wp ^ 1u, NEAR_Z + (u32)(s * NRED + ((u - 224) >> 3)) * 8192) + (u32)(u & 7) * 1024 : OOB);
#pragma unroll 1
    for (int i = 0; i < 10; ++i) {
        stx4(rs, (u32)lane * 16, (u32x4){SENT, SENT, SENT, SENT}, tab(t, i));
        if (PAUSE) __builtin_amdgcn_s_sleep(PAUSE);
    }
}
__device__ __forceinline__ void fill_first(const int wave, const int f, const u32 wp, const u32 dv, char* __restrict__ ws) {
    const rsrc_t rs = mk_rsrc(ws);
#pragma unroll 1
    for (int s = wave >> 2; s < FILL_0; s += 2) fill_share<0>(rs, wp, s, f * 4 + (wave & 3));
    DRAIN();
    LBAR();
    const int lane = lane_id();
    stx1(rs, wave + 1 < FILL_0 && lane < 32 ? done_at(wp, wave, 64 + f) + lane * 4 : OOB, dv);
}
__device__ __forceinline__ void fill_role(const int wave, const int f, const u32 wp, const u32 dv, char* __restrict__ ws, u32* cnt) {
    const int lane = lane_id();
    const rsrc_t rs = mk_rsrc(ws);
#pragma unroll 1
    for (int s = FILL_0; s <= NSEG; ++s) {
        if (s < NSEG) fill_share<FILL_T>(rs, wp, s, f * 4 + wave);
        DRAIN();
        u32 a = 0;
        if (lane == 0) a = __hip_atomic_fetch_add(cnt + (s & 63), 1u, __ATOMIC_RELAXED, __HIP_MEMORY_SCOPE_WORKGROUP);
        stx1(rs, rfl(a) == 3u && lane < 32 ? done_at(wp, s - 1, 64 + f) + lane * 4 : OOB, dv);
    }
}

__device__ __forceinline__ void relay_role(const u32 wp, const int xg, char* __restrict__ ws) {
    const int lane = lane_id();
    const rsrc_t rs = mk_rsrc(ws);
#pragma unroll 1
    for (int k = 0; k + 1 < NSEG; ++k) {
        const u32 o = slot_off(wp ^ 8u, k) + copy_off((u32)xg ^ (wp >> 1), k) + SL_TS;
        u32 t = 0;
#pragma unroll 1
        for (u32 spins = 0; spins < SPIN_CAP; ++spins) {
            CFENCE();
            const u32x4 f = ldx4(rs, 0u, o);
            t = rfl(f[2]);
            if (rfl(f[1]) == (u32)k + 1u) break;
            __builtin_amdgcn_s_sleep(8);
            if ((spins & 63) == 63 && ldx1(rs, (u32)OFF_ERR) != 0u) return;
        }
        if (k >= 3) stx1(rs, lane < 8 ? (u32)(OFF_RDY + lane * 128) : OOB, (u32)k - 2u);
        for (int q = 0; q < 64 && (int)((u32)__builtin_amdgcn_s_memrealtime() * 24u - t) < 700; ++q) __builtin_amdgcn_s_sleep(2);
    }
}

__device__ __forceinline__ u16* karg(int i) {
    __attribute__((address_space(1))) u16* p;
    asm volatile("s_load_dwordx2 %0, %1, %2\n\ts_waitcnt lgkmcnt(0)" : "=s"(p) : "s"(__builtin_amdgcn_kernarg_segment_ptr()), "s"(i * 8));
    return (u16*)p;
}
#define ARG(i) karg(i)

__device__ __forceinline__ u32 topk_role(const u64 tag, char* __restrict__ ws) {
    const int lane = lane_id();
    const rsrc_t rs = mk_rsrc(ws);
    wait_rows(rs, tag, lane & 31);
    u32 v[32];
#pragma unroll
    for (int i = 0; i < 32; ++i) v[i] = ldx1(rs, (u32)lane * 4, (u32)(OFF_R + i * 256));
#pragma unroll
    for (int i = 0; i < 32; ++i) v[i] = ((v[i] ^ ((u32)((int)v[i] >> 31) | 0x80000000u)) & 0xffff0000u) | (u32)(2047 - i * 64 - lane);
    u32 top = ~0u, res = 0;
#pragma unroll 1
    for (int c = 0; c < TOPC; ++c) {
        u32 d = ~0u;
#pragma unroll
        for (int i = 0; i < 32; ++i) d = min(d, top - v[i]);
        d = min(d, (u32)__builtin_amdgcn_update_dpp(0u, d, 0xb1, 0xf, 0xf, false));
        d = min(d, (u32)__builtin_amdgcn_update_dpp(0u, d, 0x4e, 0xf, 0xf, false));
        d = min(d, (u32)__builtin_amdgcn_update_dpp(0u, d, 0x124, 0xf, 0xf, false));
        d = min(d, (u32)__builtin_amdgcn_update_dpp(0u, d, 0x128, 0xf, 0xf, false));
        const u32 dm = min(min((u32)__builtin_amdgcn_readlane(d, 0), (u32)__builtin_amdgcn_readlane(d, 16)), min((u32)__builtin_amdgcn_readlane(d, 32), (u32)__builtin_amdgcn_readlane(d, 48)));
        top -= dm;
        if (lane == c) res = 2048u - (top & 2047u);
        top -= 1u;
    }
    return res;
}

template <int K0, int K1>
__device__ __forceinline__ void moe_ask(const u16* __restrict__ wp, const int ldw, const int nk, const u32 la, const int lane) {
#pragma unroll
    for (int i = 4 * K0; i < 4 * K1; ++i)
        if (i < 4 * nk) dma16<true>(wp + (size_t)(i * 8 + (lane >> 3)) * ldw + (lane & 7) * 8, la + (i - 4 * K0) * 1024);
}
template <int K0, int K1>
__device__ __forceinline__ void moe_get(const u32 la, const int lane, u32x4 (&r)[7][4]) {
    const u32 a = la + (u32)((lane >> 4) * 1024 + ((lane >> 2) & 3) * 128 + (lane & 3) * 8);
    DRAIN();
#pragma unroll
    for (int k = K0; k < K1; ++k)
#pragma unroll
        for (int t = 0; t < 4; ++t) { const u32x2 u = trd(a + (k - K0) * 4096 + t * 32), v = trd(a + (k - K0) * 4096 + t * 32 + 512); r[k][t] = (u32x4){u[0], u[1], v[0], v[1]}; }
    asm volatile("s_waitcnt lgkmcnt(0)" ::: "memory");
}
constexpr int NGU = FF_ / 32, NDN = D_ / 64;
template <typename F>
__device__ __forceinline__ void moe_part(F&& meanwhile, const int w, const u64 tag, const int wave, char* __restrict__ ws, char* smem) {
    const int lane = lane_id(), n = lane & 15, g = lane >> 4;
    const rsrc_t rs = mk_rsrc(ws);
    const bool down = w >= NGU;
    u32* sel = (u32*)(smem + 512);
    const u32 la = (u32)(uintptr_t)(__attribute__((address_space(3))) char*)smem + 1024 + (u32)wave * 16384;
    const int k0 = down ? wave * 7 : (wave * 5 + 1) >> 1, nk = down ? (wave == 7 ? 5 : 7) : 3 - (wave & 1), ldw = down ? D_ : FF_, c0 = (down ? w - NGU : w >> 1) * 64;
    const u16* src = ARG(down ? 9 : 7 + (w & 1)) + (size_t)k0 * 32 * ldw + c0;
    u32x4 r[7][4], af[7];
    moe_ask<0, 4>(src, ldw, nk, la, lane);
    meanwhile();
    moe_get<0, 4>(la, lane, r);
    moe_ask<4, 7>(src, ldw, nk, la, lane);
    if (wave == 1) {
        const u32 res = topk_role(tag, ws);
        if (lane < TOPC) sel[lane] = res;
        stx1(rs, w == 0 && lane < TOPC ? (u32)(OFF_IDX + lane * 4) : OOB, res);
    }
    if (down) {
        moe_get<4, 7>(la, lane, r);
        bool dead = false;
        if (wave == 0) wait_word(rs, (u32)OFF_CNT, true, (u32)NGU * 2, dead, (u32*)(ws + OFF_ERR), 16u, lane);
    }
    LBAR();
    const u32 row = (sel[n & 7] - 1u) & (S_ - 1);
    const float rv = __uint_as_float(ldx1(rs, (u32)OFF_R + row * 4));
    const u32 ao = (down ? (u32)OFF_HB + (u32)(n & 7) * (FF_ * 2) : (u32)OFF_XN + row * (D_ * 2)) + (u32)(k0 * 32 + g * 8) * 2;
#pragma unroll
    for (int k = 0; k < 7; ++k) af[k] = ldx4(rs, k < nk ? ao + k * 64 : OOB);
    if (down) {
#pragma unroll
        for (int k = 0; k < 7; ++k) {
            const u32x4 u = ldx4(rs, k < nk ? ao + k * 64 + 16 * FF_ : OOB);
#pragma unroll
            for (int i = 0; i < 4; ++i)
                af[k][i] = pack2(__uint_as_float(af[k][i] << 16) * __uint_as_float(u[i] << 16), __uint_as_float(af[k][i] & 0xffff0000u) * __uint_as_float(u[i] & 0xffff0000u));
        }
    }
    f32x4 (*red)[4][64] = (f32x4(*)[4][64])(smem + 1024);
#pragma unroll
    for (int t = 0; t < 4; ++t) {
        f32x4 c = {0.f, 0.f, 0.f, 0.f};
#pragma unroll
        for (int k = 0; k < 7; ++k) if (k < nk) c = mfma16(r[k][t], af[k], c);
        red[wave][t][lane] = c;
    }
    LBAR();
    if (wave < 2) {
        const int l = lane & ~8, t = wave * 2 + (n >> 3);
        const f32x4 y = ((red[0][t][l] + red[1][t][l]) + (red[2][t][l] + red[3][t][l])) + ((red[4][t][l] + red[5][t][l]) + (red[6][t][l] + red[7][t][l]));
        const float rs1 = bfr(1.0f / (1.0f + __expf(-rv)));
        float res[4];
#pragma unroll
        for (int i = 0; i < 4; ++i) {
            const float v = bfr(y[i]);
            res[i] = down ? v * rs1 : ((w & 1) ? v : v / (1.0f + __expf(-v)));
        }
        stx2(rs, (down ? (u32)OFF_UPD : (u32)OFF_HB + (u32)(w & 1) * (16 * FF_)) + (u32)((n & 7) * ldw + c0 + t * 16 + g * 4) * 2, (u32x2){pack2(res[0], res[1]), pack2(res[2], res[3])});
        DRAIN();
        if (lane == 0) __hip_atomic_fetch_add((u32*)(ws + OFF_CNT + (down ? 256 : 0)), 1u, __ATOMIC_RELAXED, __HIP_MEMORY_SCOPE_AGENT);
    }
    LBAR();
}

#define VMQ "8"
#define VMV "10"
#define LOOKST 18
#define NOWV 4
#define NOWIT 1
constexpr int NBIG = (NSEG - 1) * 10, NTASK = NBIG + 80;

struct OutCtx { rsrc_t rs; const u16* x; u16* out; u32* errw; u32 wp; int wid; char* ws; };
template <int OT, int NT>
__device__ __forceinline__ void out_task(const OutCtx& cx, const int sg, const int c0, const int n0, const int tid, const int (&sel_idx)[TOPC], bool& adone, u32& rdy,
                                         bool& dead, char* redp, u32* nextp, u32* tctr) {
    const int wave = __builtin_amdgcn_readfirstlane(tid >> 6), lane = tid & 63, n = lane & 15, g = lane >> 4;
    const rsrc_t rs = cx.rs;
    if (!adone && rdy <= (u32)sg) {
        wait_word(rs, (u32)(OFF_FB + (wave * 4 + (lane & 3)) * 128 + (sg & 1) * 4), true, (u32)(sg >> 1) + 1, dead, cx.errw, 32u, lane);
        u32x2 f = ldx2(rs, (u32)(OFF_FB + (wave * 4 + (lane & 3)) * 128));
        adone = __all(f[0] >= NSEG / 2 && f[1] >= NSEG / 2);
    }
    u32x4 af[OT][5], bq[NT][5];
    const u32 bo = amap((u32)(wave * 4096 + lane * 16));
#pragma unroll
    for (int c = 0; c < OT; ++c) {
        const u32 bb = far_off(cx.wp, b_at(FAR_BX, NSEG, sg, c0) + (u32)c * 32768);
#pragma unroll
        for (int hh = 0; hh < 4; ++hh) af[c][hh] = ldx4(rs, bo + hh * 1024, bb);
    }
    const int ti = (tid >> 6) & (OT * NT - 1);
    const int token = sg * SEGL + (n0 + (ti & (NT - 1))) * 16 + n;
    const int col = (c0 + ti / NT) * 16 + 4 * g;
    int sel = -1;
#pragma unroll
    for (int c = 0; c < TOPC; ++c) if (sel_idx[c] == token) sel = c;
    const u32x2 xv = *(const u32x2*)(cx.x + (size_t)token * D_ + col);
    const u32x2 nv = ldx2(rs, (u32)(OFF_XN + ((size_t)token * D_ + col) * 2));
    if (__any(sel >= 0)) wait_word(rs, (u32)(OFF_CNT + 256), true, (u32)NDN * 2, dead, cx.errw, 256u, lane);
    const u32x2 uv = ldx2(rs, sel >= 0 ? (u32)(OFF_UPD + ((size_t)sel * D_ + col) * 2) : OOB);
    __builtin_amdgcn_sched_barrier(0);
    const bool late = sg + 4 >= NSEG;
    if (late)
        wait_flags(rs, (u32)(OT == 1 ? ((lane & 8) ? OFF_FR + (wave * 4 + (lane & 3)) * 128 : OFF_FZ + (wave * 2 + n0) * 128) : OFF_FP + wave * 128 + (lane & 1) * 16),
                   true, 1u, dead, cx.errw, 64u, lane);
    else if (rdy <= (u32)sg) {
#pragma unroll 1
        for (u32 spins = 0; !dead; ++spins) {
            CFENCE();
            rdy = __builtin_amdgcn_readfirstlane(ldx1(rs, (u32)(OFF_RDY + (cx.wid & 7) * 128)));
            if (rdy > (u32)sg) break;
            __builtin_amdgcn_s_sleep(8);
            if (spins > SPIN_CAP) { if (lane == 0) atomicOr(cx.errw, 64u); dead = true; }
            if ((spins & 63) == 63 && ldx1(rs, (u32)OFF_ERR) != 0u) dead = true;
        }
        CFENCE();
    }
#pragma unroll
    for (int c = 0; c < OT; ++c) af[c][4] = ldx4(rs, (u32)((wave >> 2) * 32768 + (wave & 3) * 1024 + lane * 16), z_at(cx.wp, sg, c0 + c));
#pragma unroll
    for (int nt = 0; nt < NT; ++nt) {
#pragma unroll
        for (int hh = 0; hh < 4; ++hh) bq[nt][hh] = ldx4(rs, (u32)lane * 16, (u32)OFF_PX + (u32)((sg * H_ + wave * 4 + hh) * 2 + n0 + nt) * 1024);
        bq[nt][4] = ldx4(rs, (u32)lane * 16, (u32)OFF_PXM + (u32)((sg * 8 + wave) * 2 + n0 + nt) * 1024);
    }
    CFENCE();
    u32 next = 0;
    if (tid == 0) next = !late ? __hip_atomic_fetch_add(tctr, 1u, __ATOMIC_RELAXED, __HIP_MEMORY_SCOPE_AGENT)
                                 : __hip_atomic_load(tctr, __ATOMIC_RELAXED, __HIP_MEMORY_SCOPE_AGENT);
    f32x4 (*red)[OT * NT][64] = (f32x4(*)[OT * NT][64])redp;
#pragma unroll
    for (int c = 0; c < OT; ++c)
#pragma unroll
        for (int nt = 0; nt < NT; ++nt) {
            f32x4 acc = {0.f, 0.f, 0.f, 0.f};
#pragma unroll
            for (int k = 0; k < 5; ++k) acc = mfma16(af[c][k], bq[nt][k], acc);
            red[wave][c * NT + nt][lane] = acc;
        }
    if (tid == 0) {
        if (late && next < (u32)NTASK) next = __hip_atomic_fetch_add(tctr, 1u, __ATOMIC_RELAXED, __HIP_MEMORY_SCOPE_AGENT);
        *nextp = next;
    }
    LBAR();
    if (tid < OT * NT * 64) {
        f32x4 sum = red[0][ti][lane];
#pragma unroll
        for (int wv = 1; wv < 8; ++wv) sum += red[wv][ti][lane];
        float res[4];
#pragma unroll
        for (int q = 0; q < 4; ++q) {
            float xx = bf2f((u16)((xv[q >> 1] >> ((q & 1) * 16)) & 0xffff));
            float nn = bf2f((u16)((nv[q >> 1] >> ((q & 1) * 16)) & 0xffff));
            float bo2 = nn;
            if (sel >= 0) bo2 = bfr(nn + bf2f((u16)((uv[q >> 1] >> ((q & 1) * 16)) & 0xffff)));
            float gg = bfr(bo2 / (1.0f + __expf(-bo2)));
            float sv = bfr(sum[q]);
            res[q] = bfr(xx + bfr(sv * gg));
        }
        *(u32x2*)(cx.out + (size_t)token * D_ + col) = (u32x2){pack2(res[0], res[1]), pack2(res[2], res[3])};
    }
}

__device__ __forceinline__ u32 xbuf(int b) { return b == 0 ? 90624u : (b == 1 ? 0u : 49664u); }
__device__ __forceinline__ void worker_role(const int wave, const int wid, const u64 tag, const u32 wp, const u32x32 (&W)[5], char* __restrict__ ws, char* smem) {
    const int lane = lane_id(), tid = wave * 64 + lane;
    const int n = lane & 15, g = lane >> 4;
    const int par = (wid >> 2) & 1, kind = (wid >> 3) & 1, h = (wid & 3) * 8 + (wid >> 4), type = kind * 2 + par;
    u32* errw = (u32*)(ws + OFF_ERR);
    const rsrc_t rs = mk_rsrc(ws);
    bool dead = false;
    const bool isqk = type < 2;
    int wslot = 1;

    {
        const u32 lbase = (u32)(uintptr_t)(__attribute__((address_space(3))) char*)smem;
        const u32x4 srd = mk_srd(ws);
        const u32 vm = (u32)lane * 16, xl = lbase + vm, dv = done_tag(tag);
        auto xsrc = [&](int it, int piece) { return (u32)OFF_XNP + (u32)((it + 2 < NSEG / 2 ? it * 2 + 4 : it * 2) + par) * 40960u + (u32)piece * 1024u; };
        const u32 fl = done_at(wp, par, kind * 32 + h) + (u32)lane * 4 - 65536, fc = kind && lane == 32 ? (u32)(OFF_FB + h * 128 + par * 4) : OOB;
        auto flag = [&](u32 v) { stx1(rs, lane < 32 ? fl + v * 65536 : fc, lane < 32 ? dv : v); };
        WSTAMP(4);
        STAMPS();
        if (DBG & 512) { const u32 t0 = (u32)__builtin_amdgcn_s_memrealtime(); while ((u32)__builtin_amdgcn_s_memrealtime() - t0 < 1500u) __builtin_amdgcn_s_sleep(16); }
        if (isqk) {
            const u32 o1 = (u32)((wave & 3) * 64 + lane) * 16;
            if (wave < 4) {
#pragma unroll 1
                for (int it = 0, b3 = 0; it < NSEG / 2; ++it) {
                    const int sg = it * 2 + par;
                    if (it == 8) wait_rows(rs, tag, lane_id() & 31);
                    w_q(W, srd, opaque((u32)(it - NOWIT)) >> 31, xl + xbuf(b3), vm, xsrc(it, wave * 5), lbase + xbuf(b3 == 0 ? 2 : b3 - 1) + (u32)wave * 5120, o1, far_off(wp ^ QFL, NEAR_QX + (u32)(sg * H_ + h) * 8192));
                    b3 = b3 == 2 ? 0 : b3 + 1;
                    if (it < 3 || (it & 3) == 3) WSTAMP(0x10 + it);
                }
                LBAR();
            } else {
                const float ks = 0.08838834764831845f * 1.4426950408889634f;
                f32x16 acc;
                u32 ob = OOB;
                asm volatile("" : "={v[208:223]}"(acc));
                LBAR();
#pragma unroll 1
                for (int it = 0, b3 = 0; it < NSEG / 2; ++it) {
                    if (it == 8) wait_rows(rs, tag, lane_id() & 31);
                    const u32 now = opaque((u32)(it - NOWIT)) >> 31, on = far_off(wp ^ KFL, NEAR_KX + (u32)((it * 2 + par) * H_ + h) * 8192);
                    w_k(W, srd, now, xl + xbuf(b3), vm, xsrc(it, wave * 5), lbase + xbuf(b3 == 0 ? 2 : b3 - 1) + (u32)wave * 5120, on, o1, ob, ks, acc);
                    ob = on | now << 31;
                    b3 = b3 == 2 ? 0 : b3 + 1;
                    if (wave == 4 && it != NOWIT) flag((u32)it + now);
                }
                w_kl(srd, o1, ob, ks, acc);
            }
        } else if (wave < 4) {
#pragma unroll 1
            for (int it = 0, b3 = 0; it < NSEG / 2; ++it) {
                if (it == 8) wait_rows(rs, tag, lane_id() & 31);
                w_v(W, srd, xl + xbuf(b3), vm, xsrc(it, wave * 10), lbase + xbuf(b3 == 0 ? 2 : b3 - 1) + (u32)wave * 10240, xl + ((it & 1) ? 135168u : 40960u) + (u32)wave * 1024 STA);
                b3 = b3 == 2 ? 0 : b3 + 1;
                if (wave == 0 && it && it != NOWV + 1) flag(it <= NOWV ? (u32)it : (u32)it - 1u);
                if (it < 3 || (it & 3) == 3) WSTAMP(0x10 + it);
            }
            LBAR();
            if (wave == 0) flag(NSEG / 2 - 1);
        } else {
            const u32 o1 = amap((u32)h * 1024 + vm) + (u32)(wave & 3) * 131072;
            f32x16 acc;
            u32 ring = RING, n0 = OOB, f0 = OOB;
            const u32 sl = (u32)(lane & 31) * 2 + par, cl = (u32)lane & 32;
            const u32 tn = (DBG & 32) && sl > 3 ? OOB : far_v(wp ^ 1u, b_at(NEAR_B, RING, sl & (RING - 1), cl)), tf = far_v(wp, b_at(FAR_BX, NSEG, sl, cl));
            DRAIN();
            asm volatile("" : "={v[208:223]}"(acc));
#pragma unroll 1
            for (int it = 0; it < NSEG / 2; ++it) {
                if (TWO && __builtin_expect(ring <= (u32)(it * 2 + par), 0)) {
                    wait_word(rs, (u32)(OFF_RDY + (wid & 7) * 128), true, (u32)max(it * 2 + par - RING - 1, 1), dead, errw, 512u, lane);
                    ring = __builtin_amdgcn_readfirstlane(ldx1(rs, (u32)(OFF_RDY + (wid & 7) * 128))) + RING + 2;
                }
                const u32 n2 = tab(tn, it + 32), f2 = tab(tf, it + 32), now = opaque((u32)(it - NOWV)) >> 31;
                w_b(W, srd, now, xl + ((it & 1) ? 135168u : 40960u), tab(tn, it), n2, tab(tf, it), f2, o1, n0, f0, acc STA);
                n0 = n2 | now << 31; f0 = f2 | now << 31;
            }
            w_bl(srd, o1, n0, f0, acc);
        }
        DRAIN();
        LBAR();
        if (wave == 4) flag(NSEG / 2);
    }
    WSTAMP(2);

    {
    const int lane = lane_id(), tid = wave * 64 + lane;
    WSTAMP(5);
    u32* nextp = (u32*)(smem + 131072);
    u32* tctr = (u32*)(ws + OFF_CNT + 512);
    if (tid == 0) nextp[0] = __hip_atomic_fetch_add(tctr, 1u, __ATOMIC_RELAXED, __HIP_MEMORY_SCOPE_AGENT);
    wait_flags(rs, (u32)OFF_IDX + (lane & 1) * 16, true, 1u, dead, errw, 256u, lane);
    __syncthreads();
    int sel_idx[TOPC];
    {
        u32x4 i0 = ldx4(rs, (u32)OFF_IDX), i1 = ldx4(rs, (u32)OFF_IDX + 16);
#pragma unroll
        for (int c = 0; c < 4; ++c) { sel_idx[c] = (int)i0[c] - 1; sel_idx[4 + c] = (int)i1[c] - 1; }
    }
    WSTAMP(3);
    const OutCtx cx = {rs, ARG(0), ARG(11), errw, wp, wid, ws};
    bool adone = false;
    u32 rdy = 0;
#pragma unroll 1
    for (int tc = 0;; ++tc) {
        const int task = __builtin_amdgcn_readfirstlane(nextp[tc & 1]);
        if (task >= NTASK) break;
        char* redp = smem + (tc & 1) * 65536;
        if ((DBG & 128) && task < 500) { if (tid == 0) nextp[(tc + 1) & 1] = __hip_atomic_fetch_add(tctr, 1u, __ATOMIC_RELAXED, __HIP_MEMORY_SCOPE_AGENT); LBAR(); }
        else if (task < NBIG) {
            WSTAMP(0x100 + task / 10);
            out_task<4, 2>(cx, task / 10, (task % 10) * 4, 0, tid, sel_idx, adone, rdy, dead, redp, nextp + ((tc + 1) & 1), tctr);
        } else {
            WSTAMP(0x100 + NSEG - 1);
            out_task<1, 1>(cx, NSEG - 1, (task - NBIG) >> 1, (task - NBIG) & 1, tid, sel_idx, adone, rdy, dead, redp, nextp + ((tc + 1) & 1), tctr);
        }
        WSTAMP(0x300);
        if (task >= NBIG && task < NBIG + NSEG && tid < 64 + NFILL) stx1(rs, done_at(wp, task - NBIG, tid), 0u);
        if (task == NTASK - 1) {
            if (tid < 256) stx2(rs, (u32)(OFF_INI + tid * 8), (u32x2){~(u32)tag, ~cal_tag(tag, ~(u32)tag)});
        }
    }
    }
}

constexpr int FUSED_LDS = 140 * 1024;
#define ORD_A 0x143210
#define ORD_C 0x101234
#define KERNEL_SYM "fused_kernel"
#define CODE_4K 19u
extern "C" __global__ __launch_bounds__(512, 2) void fused_kernel(const u16*, const u16*, const u16*, const u16*, const u16*, const u16*, const u16*, const u16*,
                                                                  const u16*, const u16*, char*, u16*) {
    extern __shared__ __attribute__((aligned(16))) char smem[];
    const int wave = __builtin_amdgcn_readfirstlane(threadIdx.x >> 6);
    char* __restrict__ ws = (char*)ARG(10);
    u64 gen = 0, tag = 0, old = 0;
    u32 xcc, se;
    asm volatile("s_getreg_b32 %0, hwreg(HW_REG_XCC_ID, 0, 3)" : "=s"(xcc));
    asm volatile("s_getreg_b32 %0, hwreg(HW_REG_HW_ID, 13, 2)" : "=s"(se));
    u32 bid = blockIdx.x;
    asm volatile("" : "+s"(bid));
    const bool first = (int)bid >= 0;
    u32 bq = first ? (bid & ~31u) + se * 8u + xcc : bid & 255u;
    asm volatile("" : "+s"(bq));
    const int b = (int)bq, k = b >> 3, xg = b & 3;
    if (threadIdx.x >= 448 && threadIdx.x < 453) {
        const int lane = threadIdx.x & 63;
        const u32 pg = (bid >> 3 & 31u) + 32u * (u32)lane;
        const char* tp = lane < 4 ? ws + min(pg << 21, (u32)WS_TOTAL - 4u) : (const char*)ARG(11) + (size_t)(bid >> 3 & 3u) * 655360;
        u32 sv;
        asm volatile("s_mov_b32 %0, m0\n\ts_mov_b32 m0, %2\n\ts_nop 0\n\tglobal_load_lds_dword %1, off\n\ts_mov_b32 m0, %0"
                     : "=&s"(sv) : "v"(tp), "s"((u32)(uintptr_t)(__attribute__((address_space(3))) char*)smem + 133120u) : "memory");
    }
    if (wave == 6) {
        u64 pc;
        u32 sv;
        asm volatile("s_getpc_b64 %0" : "=s"(pc));
        const u32 o = (bid >> 3 & 31u) % CODE_4K * 4096u + (u32)(threadIdx.x & 31) * 128u;
        asm volatile("s_mov_b32 %0, m0\n\ts_mov_b32 m0, %3\n\ts_nop 0\n\tglobal_load_lds_dword %1, %2\n\ts_mov_b32 m0, %0"
                     : "=&s"(sv) : "v"(o), "s"(pc), "s"((u32)(uintptr_t)(__attribute__((address_space(3))) char*)smem + 133376u) : "memory");
    }
    u64* claim = (u64*)(ws + OFF_CLAIM);
    u32* sh = (u32*)(smem + 132608);
    auto try_claim = [&] {
        if (wave == 0 && first) {
            asm volatile("s_load_dwordx2 %0, %1, 0x0\n\ts_waitcnt lgkmcnt(0)" : "=s"(gen) : "s"(ws + OFF_GEN) : "memory");
            tag = (gen + 0x9E3779B97F4A7C15ull) * 0xBF58476D1CE4E5B9ull;
            tag ^= tag >> 31;
            tag *= 0x94D049BB133111EBull;
            tag ^= tag >> 29;
            if (lane_id() == 0) {
                old = __hip_atomic_exchange(claim + b * 8, tag, __ATOMIC_RELAXED, __HIP_MEMORY_SCOPE_AGENT);
                sh[1] = (u32)tag; sh[2] = (u32)(tag >> 32); sh[3] = (u32)gen; sh[4] = (u32)(gen >> 32);
            }
        }
    };
    auto settle = [&] {
        const int tid = wave * 64 + lane_id();
        if (tid == 0 && first) {
            u32 vb = (u32)b;
#pragma unroll 1
            for (int i = 0; i < 256 && old == tag; ++i) {
                vb = (vb + 1) & 255u;
                old = __hip_atomic_exchange(claim + vb * 8, tag, __ATOMIC_RELAXED, __HIP_MEMORY_SCOPE_AGENT);
            }
            sh[0] = vb;
        }
        LBAR();
        const u32 bc = __builtin_amdgcn_readfirstlane(sh[0]);
        asm volatile("s_cmp_eq_u32 %0, %1\n\ts_cbranch_scc1 Lgo%=\n\t"
                     "s_or_b32 s2, %0, 0x80000000\n\ts_mov_b64 s[0:1], %2\n\tv_mov_b32 v0, %3\n\ts_waitcnt vmcnt(0) lgkmcnt(0)\n\t"
                     "s_getpc_b64 s[4:5]\nLpc%=:\n\ts_sub_u32 s4, s4, Lpc%=-" KERNEL_SYM "\n\ts_subb_u32 s5, s5, 0\n\ts_setpc_b64 s[4:5]\nLgo%=:"
                     :: "s"(bc), "s"(b), "s"(__builtin_amdgcn_kernarg_segment_ptr()), "v"(tid) : "s2", "scc", "memory");
        tag = (u64)(u32)__builtin_amdgcn_readfirstlane(sh[1]) | ((u64)(u32)__builtin_amdgcn_readfirstlane(sh[2]) << 32);
    };
    if (b & 4) {
        const int wid = k * 4 + xg, h = (wid & 3) * 8 + (wid >> 4), par = (wid >> 2) & 1;
        const bool isqk = ((wid >> 3) & 1) == 0;
        const int lane = lane_id();
        u32x32 W[5];
        u32 wp = 0;
        u32x4 f0, f1;
        u64 tg2 = 0;
        bool got = false;
        auto xtile = [&](int t) {
#pragma unroll
            for (int i = 0; i < 5; ++i)
                dma16_sc1(ws + OFF_XNP + (size_t)(par + 2 * t) * 40960 + (wave * 5 + i) * 1024 + lane * 16,
                          __builtin_amdgcn_readfirstlane((u32)(uintptr_t)(__attribute__((address_space(3))) char*)smem + xbuf(t) + (u32)(wave * 5 + i) * 1024));
        };
        load_weights<ORD_A>([&](int st) {
            if (st == 0) try_claim();
            if (st == LOOKST) {
                asm volatile("global_load_dwordx4 %0, %2, off sc1\n\tglobal_load_dwordx4 %1, %2, off offset:1024 sc1"
                             : "=&v"(f0), "=&v"(f1) : "v"(ws + OFF_INI + lane * 16) : "memory");
                tg2 = (u64)(u32)__builtin_amdgcn_readfirstlane(sh[1]) | ((u64)(u32)__builtin_amdgcn_readfirstlane(sh[2]) << 32);
            }
            if (st == 3) {
                asm volatile("" : "+v"(f0), "+v"(f1));
                got = __all(lane >= 16 || rows_ok(f1, tg2));
                if (got) xtile(0);
                if (__all(lane < 48 || ini_ok(f0, tg2))) wp = wp_of(ws, f0);
            }
        }, ARG(isqk ? 3 : 5) + h * DH_, ARG(isqk ? 4 : 6) + (size_t)h * DH_ * (isqk ? 1 : D_), !isqk, smem, wave, lane,
           wave >> 2, 2 * (wave & 3), wave >> 2, 2 * (wave & 3) + 1, W);
        settle();
        const rsrc_t rs = mk_rsrc(ws);
        if (!got) { wait_rows(rs, tag, lane & 15); xtile(0); }
        if (wp == 0) wp = wait_init(rs, ws, tag, lane);
        DRAIN();
        xtile(1);
        LBAR();
        PIN_W(W);
        worker_role(wave, wid, tag, wp, W, ws, smem);
        __builtin_amdgcn_endpgm();
    }
    const int hl = ((k >> 1) ^ (xg >> 1)) & 1, j = (k >> 2) * 2 + (k & 1);
    if (j < 8) {
        const int h = xg * 8 + hl * 4 + (j & 3);
        const bool iss = j < 4;
        u32x32 W[5];
        load_weights<ORD_C>([&](int st) {
            if (st == 0) try_claim();
        }, ARG(iss ? 3 : 5) + h * DH_, ARG(iss ? 4 : 6) + (size_t)h * DH_ * (iss ? 1 : D_), !iss, smem, wave, lane_id(),
           iss ? 0 : wave < 4, iss ? wave : (wave < 4 ? 2 * wave : wave - 4), iss ? 1 : wave < 4, iss || wave >= 4 ? wave : 2 * wave + 1, W, !iss && wave < 4);
        settle();
        PIN_W(W);
        if (iss) s_role(wave, h, xg, tag, ARG(1), W, ws, smem);
        else v_role(wave, h, xg, tag, ARG(1), W, ws, smem);
        __builtin_amdgcn_endpgm();
    }
    try_claim();
    const int pi = xg * 8 + hl * 4 + (j & 3);
    if (j < 12) {
        u32 votes = 0;
        if (wave == 1) votes = probe(pi, xcc, ((se ^ xcc) >> 1) & 1u, ws);
        settle();
        if (wave == 1) {
            const u32 c = (u32)pi * 64 + lane_id();
            stx4(mk_rsrc(ws), c < INI_ZERO_CHUNKS ? (u32)ZERO_BEGIN + c * 16 : OOB, (u32x4){0u, 0u, 0u, 0u});
            DRAIN();
            stx2(mk_rsrc(ws), lane_id() == 0 ? (u32)(OFF_INI + (96 + pi) * 8) : OOB, (u32x2){votes, cal_tag(tag, votes)});
        }
    }
    {
        const int lane = lane_id(), li = pi + (j < 12 ? 32 : 0);
        const rsrc_t rs = mk_rsrc(ws);
        u32x4 xv[5], wv[5];
        ln_load(li * 32 + wave * 4, lane, ARG(0), ARG(2), xv, wv);
        if (wave < 2) ((u32*)smem)[wave * 64 + lane] = 0u;
        ln_regs(xv);
        ln_store(li * 32 + wave * 4, lane, xv, wv, rs);
        DRAIN();
        if (j < 12) LBAR(); else settle();
        stx2(rs, wave == 0 && lane == 0 ? (u32)(OFF_INI + (128 + li) * 8) : OOB, (u32x2){(u32)tag, (u32)(tag >> 32)});
    }
    u32 wp = 0;
    const int mi = (j - 8) * 8 + xg * 2 + hl;
    moe_part([&] {
        wp = wait_init(mk_rsrc(ws), ws, tag, lane_id());
        if (j >= 13 && mi < NGU) fill_first(wave, mi - 40, wp, done_tag(tag), ws);
    }, mi, tag, wave, ws, smem);
    if (j < 13) {
        r_role(wave, (int)(((hl ? 0x85410u : 0x97632u) >> ((j - 8) * 4)) & 15u) * 4 + xg, xg, wp, done_tag(tag), ws, smem);
        __builtin_amdgcn_endpgm();
    }
    gen = (u64)sh[3] | ((u64)sh[4] << 32);
    if (mi == 40) {
        u32 cap = SPIN_CAP;
        if (wave == 0) wait_done(mk_rsrc(ws), done_at(wp, 0, 64 + min(lane_id(), NFILL - 1)), done_tag(tag), cap, (u32*)(ws + OFF_ERR));
        LBAR();
    }
    if (wave >= 4) { if (mi < NGU) fill_role(wave & 3, mi - 40, wp, done_tag(tag), ws, (u32*)smem); }
    else if (mi == 40) { if (wave == 0) relay_role(wp, xg, ws); }
    else if (mi == 41 && wave == 0) {
        const rsrc_t rs = mk_rsrc(ws);
        const int lane = lane_id();
#pragma unroll 1
        for (u32 spins = 0; spins < SPIN_CAP; ++spins) {
            CFENCE();
            bool ok = true;
#pragma unroll
            for (int q = 0; q < 4; ++q) {
                u32x2 f = ldx2(rs, (u32)(OFF_CLAIM + (q * 64 + lane) * 64));
                ok = ok && f[0] == (u32)tag && f[1] == (u32)(tag >> 32);
            }
            if (__all(ok)) break;
            __builtin_amdgcn_s_sleep(32);
        }
        if (lane == 0) __hip_atomic_store((u64*)(ws + OFF_GEN), gen + 1ull, __ATOMIC_RELAXED, __HIP_MEMORY_SCOPE_AGENT);
#pragma unroll
        for (int q = 0; q < 4; ++q) stx2(rs, (u32)(OFF_CLAIM + (q * 64 + lane) * 64), (u32x2){~(u32)tag, 0u});
    }
}

extern "C" void kernel_launch(void* const* d_in, const int* in_sizes, int n_in, void* d_out, int out_size,
                              void* d_ws, size_t ws_size, hipStream_t stream) {
    const u16* x = (const u16*)d_in[0];
    const u16* mem0 = (const u16*)d_in[1];
    const u16* Wq = (const u16*)d_in[2];
    const u16* Wk = (const u16*)d_in[3];
    const u16* Wv = (const u16*)d_in[4];
    const u16* Wo = (const u16*)d_in[5];
    const u16* w_r = (const u16*)d_in[8];
    const u16* w_gate = (const u16*)d_in[9];
    const u16* w_up = (const u16*)d_in[10];
    const u16* w_down = (const u16*)d_in[11];
    char* ws = (char*)d_ws;
    u16* out = (u16*)d_out;

    hipFuncSetAttribute((const void*)fused_kernel, hipFuncAttributeMaxDynamicSharedMemorySize, FUSED_LDS);
    fused_kernel<<<256, 512, FUSED_LDS, stream>>>(x, mem0, w_r, Wq, Wk, Wv, Wo, w_gate, w_up, w_down, ws, out);
}
```

```cpp
#include <hip/hip_runtime.h>
#include <hip/hip_bf16.h>
#include <stdint.h>

constexpr int S_ = 2048, D_ = 640, H_ = 32, DH_ = 128, NSEG = 64, SEGL = 32, FF_ = 1728, TOPC = 8;
constexpr int HD_ = H_ * DH_;
constexpr int NRED = 40;
constexpr int NFILL = 14;

typedef unsigned short u16;
typedef unsigned int u32;
typedef unsigned long long u64;
typedef __attribute__((ext_vector_type(8))) __bf16 bf16x8;
typedef __attribute__((ext_vector_type(16))) __bf16 bf16x16;
typedef __attribute__((ext_vector_type(4))) float f32x4;
typedef __attribute__((ext_vector_type(4))) unsigned int u32x4;
typedef __attribute__((ext_vector_type(8))) unsigned int u32x8;
typedef __attribute__((ext_vector_type(2))) unsigned int u32x2;

__device__ __forceinline__ float bf2f(u16 h) { return __uint_as_float(((u32)h) << 16); }
typedef __bf16 bf16x2_t __attribute__((ext_vector_type(2)));
typedef float f32x2_t __attribute__((ext_vector_type(2)));
__device__ __forceinline__ u16 f2bf(float f) {
    __bf16 h = (__bf16)f;
    return __builtin_bit_cast(u16, h);
}
__device__ __forceinline__ float bfr(float f) { return (float)(__bf16)f; }
__device__ __forceinline__ u32 pack2(float a, float b) {
    f32x2_t f = {a, b};
    bf16x2_t h = __builtin_convertvector(f, bf16x2_t);
    return __builtin_bit_cast(u32, h);
}
__device__ __forceinline__ bf16x8 as_bf(u32x4 v) { return __builtin_bit_cast(bf16x8, v); }
__device__ __forceinline__ f32x4 mfma16(u32x4 a, u32x4 b, f32x4 c) {
    return __builtin_amdgcn_mfma_f32_16x16x32_bf16(as_bf(a), as_bf(b), c, 0, 0, 0);
}
__device__ __forceinline__ f32x4 smf(u32x4 a, u32x4 b0, u32x4 b1, f32x4 c, int idx) {
    u32x8 b = {b0[0], b0[1], b0[2], b0[3], b1[0], b1[1], b1[2], b1[3]};
    return __builtin_amdgcn_smfmac_f32_16x16x64_bf16(as_bf(a), __builtin_bit_cast(bf16x16, b), c, idx, 0, 0);
}
__device__ __forceinline__ int dmap(int p) {
    int q = p & 15;
    return (p & ~15) | (q & 1) | ((q & 2) << 2) | ((q & 4) >> 1) | ((q & 8) >> 1);
}

constexpr size_t al256(size_t x) { return (x + 255) & ~(size_t)255; }
constexpr size_t OFF_XN = 0;
constexpr size_t OFF_XNP = OFF_XN + al256((size_t)S_ * D_ * 2);
constexpr size_t OFF_R = OFF_XNP + al256((size_t)S_ * D_ * 2);
constexpr size_t OFF_PX = OFF_R + al256((size_t)S_ * 4);
constexpr size_t OFF_PXM = OFF_PX + al256((size_t)NSEG * H_ * 2048);
constexpr size_t OFF_HB = OFF_PXM + al256((size_t)NSEG * 8 * 2048);
constexpr size_t OFF_UPD = OFF_HB + al256((size_t)16 * FF_ * 2);
constexpr size_t OFF_GEN = OFF_UPD + al256((size_t)8 * D_ * 2);
constexpr size_t OFF_CLAIM = OFF_GEN + 256;
constexpr size_t OFF_INI = OFF_CLAIM + 16384;
constexpr size_t OFF_ERR = OFF_INI + 2048;
constexpr size_t OFF_FB = OFF_ERR + 256;
constexpr size_t OFF_FP = OFF_FB + 4096;
constexpr size_t OFF_FZ = OFF_FP + 1024;
constexpr size_t OFF_FR = OFF_FZ + 2048;
constexpr size_t OFF_RDY = OFF_FR + 4096;
constexpr size_t OFF_IDX = OFF_RDY + 1024;
constexpr size_t OFF_CNT = OFF_IDX + 256;
constexpr size_t ZERO_BEGIN = OFF_ERR, ZERO_END = OFF_CNT + 1024;
constexpr size_t OFF_WIN = (size_t)12 << 20;
constexpr size_t WS_TOTAL = OFF_WIN + ((size_t)240 << 20);
static_assert(ZERO_END <= OFF_WIN && WS_TOTAL <= ((size_t)256 << 20), "ws");

constexpr u32 SLOT0 = 20u << 20;
constexpr u32 SL_P = 4 * 32768, SL_TS = 2 * 32768 + 1024, SL_REC = 3 * 32768, SL_2ND = 4 * 32768;
constexpr u32 L_M2 = 81920;
constexpr u32 FAR_BX = 1u << 20;
constexpr u32 NEAR_Z = 26u << 20;
#define RING 16
constexpr u32 NEAR_B = NEAR_Z + NSEG * NRED * 8192;
constexpr u32 NEAR_QX = NEAR_B + 32 * NRED * 32768;
constexpr u32 NEAR_KX = NEAR_QX + NSEG * H_ * 8192;
#define QFL 0u
#define KFL 1u
static_assert(2 * ((size_t)NEAR_KX + NSEG * H_ * 8192) + ((size_t)2 << 20) <= WS_TOTAL - OFF_WIN && SLOT0 + (32u << 20) <= 2 * NEAR_Z && NEAR_B % (1u << 20) == 0 && FAR_BX + NSEG * NRED * 32768 <= NEAR_QX, "window");
__device__ __forceinline__ u32 b_at(u32 base, u32 n, u32 sg, u32 ct) { return base + (ct < 32 ? (sg << 20) + (ct << 15) : (n << 20) + (sg << 18) + ((ct - 32) << 15)); }
__device__ __forceinline__ u32 slot_off(u32 wp, int s) {
    u32 r, t;
    asm("s_lshr_b32 %0, %3, 2\n\ts_xor_b32 %0, %0, %2\n\ts_and_b32 %0, %0, 1\n\ts_lshl_b32 %0, %0, 13\n\ts_lshl_b32 %1, %3, 19\n\t"
        "s_add_u32 %0, %0, %1\n\ts_andn2_b32 %1, %2, 15\n\ts_add_u32 %0, %0, %1\n\ts_and_b32 %1, %2, 8\n\ts_lshl_b32 %1, %1, 15\n\ts_add_u32 %0, %0, %1\n\t"
        "s_add_u32 %0, %0, %4" : "=&s"(r), "=&s"(t) : "s"(wp), "s"(s), "n"(SLOT0) : "scc");
    return r;
}
__device__ __forceinline__ u32 slot_v(u32 wp, u32 s) { return (wp & ~15u) + SLOT0 + (s << 19) + (((wp ^ (s >> 2)) & 1u) << 13) + ((wp & 8u) << 15); }
__device__ __forceinline__ u32 far_v(u32 wp, u32 L) { return (wp & ~15u) + 2 * L + ((~(wp ^ (L >> 20)) & 1u) << 13); }
__device__ __forceinline__ u32 tab(u32 t, int s) { return __builtin_amdgcn_readlane(t, s); }
__device__ __forceinline__ u32 copy_off(u32 c, int s) { const u32 v = c ^ (u32)s; return ((v & 1u) << 14) | ((v & 2u) << 11); }
__device__ __forceinline__ u32 done_at(u32 wp, u32 sg, u32 p) { return far_v(wp ^ 1u, (sg * 2 + (p >> 6)) * 8192) + (p & 63u) * 128; }
__device__ __forceinline__ u32 z_at(u32 wp, u32 s, u32 ct) {
    const u32 X = 2 * NEAR_Z + (s * 10 + (ct >> 2)) * 65536;
    return (wp & ~15u) + X + (((wp ^ (X >> 21)) & 1u) << 13) + copy_off(ct ^ (wp >> 1) ^ 1u, (int)(X >> 19));
}
__device__ __forceinline__ u32 far_off(u32 wp, u32 L) {
    u32 r, t;
    asm("s_lshr_b32 %0, %3, 20\n\ts_xnor_b32 %0, %0, %2\n\ts_and_b32 %0, %0, 1\n\ts_lshl_b32 %0, %0, 13\n\ts_lshl_b32 %1, %3, 1\n\t"
        "s_add_u32 %0, %0, %1\n\ts_andn2_b32 %1, %2, 15\n\ts_add_u32 %0, %0, %1" : "=&s"(r), "=&s"(t) : "s"(wp), "s"(L) : "scc");
    return r;
}
constexpr u32 SENT = 0xffbfffbfu;
constexpr u32 MARK = 0xffbeffbeu;
constexpr u32 OOB = 0x80000000u;
__device__ __forceinline__ u32 amap(u32 L) { return ((L >> 13) << 14) | (L & 8191u); }
__device__ __forceinline__ u32 win_off(const char* ws) {
    u64 a = (u64)(uintptr_t)ws;
    u64 w = (a + OFF_WIN + ((u64)2 << 20) - 1) & ~(((u64)2 << 20) - 1);
    return (u32)(w - a);
}

constexpr int AUX_SC1 = 16;
typedef __amdgpu_buffer_rsrc_t rsrc_t;
__device__ __forceinline__ rsrc_t mk_rsrc(const void* p) {
    return __builtin_amdgcn_make_buffer_rsrc((void*)p, 0, 0x7fffffff, 0x00020000);
}
__device__ __forceinline__ u32x4 ldx4(rsrc_t r, u32 off, u32 so = 0) { return __builtin_amdgcn_raw_buffer_load_b128(r, off, so, AUX_SC1); }
__device__ __forceinline__ u32x2 ldx2(rsrc_t r, u32 off, u32 so = 0) { return __builtin_amdgcn_raw_buffer_load_b64(r, off, so, AUX_SC1); }
__device__ __forceinline__ u32 ldx1(rsrc_t r, u32 off, u32 so = 0) { return __builtin_amdgcn_raw_buffer_load_b32(r, off, so, AUX_SC1); }
__device__ __forceinline__ void stx4(rsrc_t r, u32 off, u32x4 v, u32 so = 0) { __builtin_amdgcn_raw_buffer_store_b128(v, r, off + so, 0, AUX_SC1); }
__device__ __forceinline__ void stx2(rsrc_t r, u32 off, u32x2 v, u32 so = 0) { __builtin_amdgcn_raw_buffer_store_b64(v, r, off, so, AUX_SC1); }
__device__ __forceinline__ void stx1(rsrc_t r, u32 off, u32 v, u32 so = 0) { __builtin_amdgcn_raw_buffer_store_b32(v, r, off, so, AUX_SC1); }
#define DRAIN() asm volatile("s_waitcnt vmcnt(0)" ::: "memory")
#define CFENCE() asm volatile("" ::: "memory")
#define LBAR() asm volatile("s_waitcnt lgkmcnt(0)\n\ts_barrier" ::: "memory")

constexpr u32 SPIN_CAP = 1u << 18;
__device__ __forceinline__ int lane_id() {
    int l;
    asm volatile("v_mbcnt_lo_u32_b32 %0, -1, 0\n\tv_mbcnt_hi_u32_b32 %0, -1, %0" : "=v"(l));
    __builtin_assume(l >= 0 && l < 64);
    return l;
}

__device__ __forceinline__ float xmax16(float x) {
    u32 a = __float_as_uint(x);
    auto r = __builtin_amdgcn_permlane16_swap(a, a, false, false);
    return fmaxf(__uint_as_float(r[0]), __uint_as_float(r[1]));
}
__device__ __forceinline__ float xmax32(float x) {
    u32 a = __float_as_uint(x);
    auto r = __builtin_amdgcn_permlane32_swap(a, a, false, false);
    return fmaxf(__uint_as_float(r[0]), __uint_as_float(r[1]));
}
__device__ __forceinline__ float xadd16(float x) {
    u32 a = __float_as_uint(x);
    auto r = __builtin_amdgcn_permlane16_swap(a, a, false, false);
    return __uint_as_float(r[0]) + __uint_as_float(r[1]);
}
__device__ __forceinline__ float xadd32(float x) {
    u32 a = __float_as_uint(x);
    auto r = __builtin_amdgcn_permlane32_swap(a, a, false, false);
    return __uint_as_float(r[0]) + __uint_as_float(r[1]);
}
__device__ __forceinline__ u32 umax3(u32 a, u32 b, u32 c) { return max(max(a, b), c); }

__device__ __forceinline__ void wait_flags(rsrc_t rs, u32 off, bool act, u32 target, bool& dead, u32* errw, u32 code, int lane) {
    u32 spins = 0;
#pragma unroll 1
    while (!dead) {
        CFENCE();
        u32x4 f = ldx4(rs, off);
        bool ok = !act || ((f[0] >= target) & (f[1] >= target) & (f[2] >= target) & (f[3] >= target));
        if (__all(ok)) break;
        __builtin_amdgcn_s_sleep(2);
        if (++spins > SPIN_CAP) { if (lane == 0) atomicOr(errw, code); dead = true; }
        if ((spins & 63) == 0 && ldx1(rs, (u32)OFF_ERR) != 0u) dead = true;
    }
    CFENCE();
}
__device__ __forceinline__ void wait_word(rsrc_t rs, u32 off, bool act, u32 target, bool& dead, u32* errw, u32 code, int lane) {
    u32 spins = 0;
#pragma unroll 1
    while (!dead) {
        CFENCE();
        u32 f = ldx1(rs, off);
        if (__all(!act || f >= target)) break;
        __builtin_amdgcn_s_sleep(4);
        if (++spins > SPIN_CAP) { if (lane == 0) atomicOr(errw, code); dead = true; }
        if ((spins & 63) == 0 && ldx1(rs, (u32)OFF_ERR) != 0u) dead = true;
    }
    CFENCE();
}

__device__ __forceinline__ u32 cal_tag(u64 tag, u32 v) { return ((u32)tag ^ (u32)(tag >> 32)) + v * 0x9E3779B1u; }
#define WDS 6
__device__ __forceinline__ void wait_done(rsrc_t rs, u32 off, u32 dv, u32& cap, u32* errw) {
    u32 spins = 0;
#pragma unroll 1
    for (; spins < cap; ++spins) {
        CFENCE();
        if (__all(ldx1(rs, off) == dv)) break;
        __builtin_amdgcn_s_sleep(WDS);
    }
    if (spins >= cap) { atomicOr(errw, 16u); cap = 1; }
    CFENCE();
}
__device__ __forceinline__ u32 done_tag(u64 tag) { return cal_tag(tag, 1u) | 1u; }
__device__ __forceinline__ bool rows_ok(u32x4 f, u64 tag) { return (f[0] == (u32)tag) & (f[1] == (u32)(tag >> 32)) & (f[2] == (u32)tag) & (f[3] == (u32)(tag >> 32)); }
__device__ __forceinline__ bool ini_ok(u32x4 c, u64 tag) { return (c[1] == cal_tag(tag, c[0])) & (c[3] == cal_tag(tag, c[2])); }
__device__ __forceinline__ void wait_rows(rsrc_t rs, u64 tag, int lane) {
#pragma unroll 1
    for (u32 spins = 0; spins < SPIN_CAP; ++spins) {
        CFENCE();
        u32x4 f = ldx4(rs, (u32)(OFF_INI + 1024 + lane * 16));
        if (__all(rows_ok(f, tag))) break;
        __builtin_amdgcn_s_sleep(4);
    }
    CFENCE();
}
__device__ __forceinline__ u32 to_sgpr(u32 x) {
    u32 r;
    asm volatile("s_nop 0\n\tv_readfirstlane_b32 %0, %1\n\ts_nop 4" : "=s"(r) : "v"(x));
    return r;
}
__device__ __forceinline__ u32 wp_of(const char* ws, u32x4 c) {
    u32 v = c[0] + c[2];
    v += __builtin_amdgcn_update_dpp(0u, v, 0xb1, 0xf, 0xf, false);
    v += __builtin_amdgcn_update_dpp(0u, v, 0x4e, 0xf, 0xf, false);
    v += __builtin_amdgcn_update_dpp(0u, v, 0x124, 0xf, 0xf, false);
    v += __builtin_amdgcn_update_dpp(0u, v, 0x128, 0xf, 0xf, false);
    const u32 r = __builtin_amdgcn_readlane(v, 48) + 0x5f5f5f5fu;
    return to_sgpr(win_off(ws) | ((r >> 7) & 1u) | ((r >> 14) & 2u) | ((r >> 21) & 4u) | ((r >> 28) & 8u));
}
__device__ __forceinline__ u32 wait_init(rsrc_t rs, const char* ws, u64 tag, int lane) {
    u32x4 c = {0u, 0u, 0u, 0u};
#pragma unroll 1
    for (u32 spins = 0; spins < SPIN_CAP; ++spins) {
        CFENCE();
        c = ldx4(rs, (u32)(OFF_INI + (lane | 48) * 16));
        if (__all(ini_ok(c, tag))) break;
        __builtin_amdgcn_s_sleep(4);
    }
    return wp_of(ws, c);
}

#define STAMPS() do { } while (0)
#define STAMP(k) do { } while (0)
#define RSTAMP(k) do { } while (0)
#define STAMP2(k, r) do { } while (0)
#define FLUSH() do { } while (0)
#define WSTAMP(id) do { } while (0)
#define WDUMP() do { } while (0)
#define CSTAMP(id) do { } while (0)
constexpr u32 INI_ZERO_CHUNKS = (u32)((ZERO_END - ZERO_BEGIN) / 16);
static_assert(INI_ZERO_CHUNKS <= 32 * 64, "reset");

__device__ __forceinline__ float rsum16(float x) {
    x += __uint_as_float(__builtin_amdgcn_update_dpp(0u, __float_as_uint(x), 0xb1, 0xf, 0xf, false));
    x += __uint_as_float(__builtin_amdgcn_update_dpp(0u, __float_as_uint(x), 0x4e, 0xf, 0xf, false));
    x += __uint_as_float(__builtin_amdgcn_update_dpp(0u, __float_as_uint(x), 0x124, 0xf, 0xf, false));
    x += __uint_as_float(__builtin_amdgcn_update_dpp(0u, __float_as_uint(x), 0x128, 0xf, 0xf, false));
    return x;
}

__device__ __forceinline__ void ln_regs(u32x4 (&xv)[5]) {
    float sum = 0.f;
#pragma unroll
    for (int i = 0; i < 5; ++i)
#pragma unroll
        for (int j = 0; j < 4; ++j) sum += __uint_as_float(xv[i][j] << 16) + __uint_as_float(xv[i][j] & 0xffff0000u);
    const float mean = rsum16(sum) * (1.0f / D_);
    float vs = 0.f;
#pragma unroll
    for (int i = 0; i < 5; ++i)
#pragma unroll
        for (int j = 0; j < 4; ++j) {
            float d0 = __uint_as_float(xv[i][j] << 16) - mean, d1 = __uint_as_float(xv[i][j] & 0xffff0000u) - mean;
            vs += d0 * d0 + d1 * d1;
        }
    const float var = bfr(rsum16(vs) * (1.0f / D_));
    const float m_bf = bfr(mean);
    const float rden = 1.0f / bfr(sqrtf(bfr(var + 1e-05f)));
#pragma unroll
    for (int i = 0; i < 5; ++i)
#pragma unroll
        for (int j = 0; j < 4; ++j)
            xv[i][j] = pack2(bfr(__uint_as_float(xv[i][j] << 16) - m_bf) * rden, bfr(__uint_as_float(xv[i][j] & 0xffff0000u) - m_bf) * rden);
}
__device__ __forceinline__ u32x4 ln_perm(const u32x4 o, const int n) {
    const u32 s0 = (n & 1) ? o[0] : o[2], s1 = (n & 1) ? o[1] : o[3];
    const u32 t0 = __builtin_amdgcn_update_dpp(0u, s0, 0xb1, 0xf, 0xf, false), t1 = __builtin_amdgcn_update_dpp(0u, s1, 0xb1, 0xf, 0xf, false);
    return (n & 1) ? (u32x4){t0, o[2], t1, o[3]} : (u32x4){o[0], t0, o[1], t1};
}
__device__ __forceinline__ u32 img_off(const int row, const int n, const int i) {
    return (u32)((row >> 4) * 20480 + (4 * i + (n >> 2)) * 1024 + (n & 3) * 256 + (row & 15) * 16);
}
__device__ __forceinline__ void ln_load(const int row0, const int lane, const u16* __restrict__ x, const u16* __restrict__ w_r, u32x4 (&xv)[5], u32x4 (&wv)[5]) {
    const int n = lane & 15, row = row0 + (lane >> 4);
#pragma unroll
    for (int i = 0; i < 5; ++i) xv[i] = *(const u32x4*)(x + (size_t)row * D_ + (n + 16 * i) * 8);
#pragma unroll
    for (int i = 0; i < 5; ++i) wv[i] = *(const u32x4*)(w_r + (n + 16 * i) * 8);
}
__device__ __forceinline__ void ln_store(const int row0, const int lane, const u32x4 (&xv)[5], const u32x4 (&wv)[5], rsrc_t rs) {
    const int n = lane & 15, row = row0 + (lane >> 4);
#pragma unroll
    for (int i = 0; i < 5; ++i) stx4(rs, (u32)OFF_XNP + img_off(row, n, i), ln_perm(xv[i], n));
    CFENCE();
    float dot = 0.f;
#pragma unroll
    for (int i = 0; i < 5; ++i) {
        stx4(rs, (u32)OFF_XN + (u32)(row * D_ + (n + 16 * i) * 8) * 2, xv[i]);
#pragma unroll
        for (int j = 0; j < 4; ++j)
            dot += __uint_as_float(xv[i][j] << 16) * __uint_as_float(wv[i][j] << 16) + __uint_as_float(xv[i][j] & 0xffff0000u) * __uint_as_float(wv[i][j] & 0xffff0000u);
    }
    dot = rsum16(dot);
    stx1(rs, n == 0 ? (u32)(OFF_R + row * 4) : OOB, __float_as_uint(bfr(dot)));
}
__device__ __forceinline__ u32x4 mk_srd(const char* ws) {
    const u64 a = (u64)(uintptr_t)ws;
    return (u32x4){(u32)a, (u32)(a >> 32), 0x7fffffffu, 0x00020000u};
}

__device__ __forceinline__ u32 probe(const int pi, const u32 xcc, const u32 hl, char* __restrict__ ws) {
    const u32x4 srd = mk_srd(ws);
    const u32 base = win_off(ws) + SLOT0 + (56u << 19) + (u32)(6 + (pi >> 4)) * 32768u + (u32)(pi & 15) * 128u;
    const int lane = lane_id();
    const u32 zero = 0;
    asm volatile("buffer_atomic_or %0, %1, %2, %3 offen sc1\n\ts_waitcnt vmcnt(0)" :: "v"(zero), "v"(lane < 16 ? (u32)lane * 2048u : OOB), "s"(srd), "s"(base) : "memory");
    const u32 one = lane == 0 ? 0u : OOB;
    auto lat = [&](u32 q) {
        u64 t0, t1, t2;
        asm volatile("s_memtime %0\n\tbuffer_atomic_or %3, %4, %5, %6 offen sc1\n\ts_waitcnt vmcnt(0)\n\ts_memtime %1\n\t"
                     "buffer_atomic_or %3, %4, %5, %6 offen sc1\n\ts_waitcnt vmcnt(0)\n\ts_memtime %2\n\ts_waitcnt lgkmcnt(0)"
                     : "=&s"(t0), "=&s"(t1), "=&s"(t2) : "v"(zero), "v"(one), "s"(srd), "s"(base + q * 2048u) : "memory");
        return min((u32)t1 - (u32)t0, (u32)t2 - (u32)t1);
    };
    u32 q = (u32)pi * 5u & 15u, best = lat(q), votes = 0x01010101u;
    const u32 flip = (xcc >> 2) | ((xcc & 3u) << 1) | (hl << 3);
#pragma unroll 1
    for (u32 r = 0; r < 2; ++r) {
        const u32 i = r * (1u + ((u32)pi >> 2) % 3u + ((u32)pi & 3u)) % 3u + r, bit = (0x1284u >> (4 * i)) & 15u;
        u32 t = lat(q ^ bit);
        if (r) { best = min(best, lat(q)); t = min(t, lat(q ^ bit)); }
        if ((t + 6u < best || best + 6u < t) && !(r && xcc >= 4)) votes += ((((q & bit) != 0) ^ (t < best) ^ ((flip >> i) & 1u)) * 2u - 1u) << (8 * i);
        if (t < best) { best = t; q ^= bit; }
    }
    return votes;
}

typedef __attribute__((ext_vector_type(32))) unsigned int u32x32;
__device__ __forceinline__ u32x4 wfrag(const u32x32 (&W)[5], const int i) {
    return (u32x4){W[i >> 3][(i & 7) * 4], W[i >> 3][(i & 7) * 4 + 1], W[i >> 3][(i & 7) * 4 + 2], W[i >> 3][(i & 7) * 4 + 3]};
}
#define PIN_W(W) asm volatile("" : "+{v[16:47]}"(W[0]), "+{v[48:79]}"(W[1]), "+{v[80:111]}"(W[2]), "+{v[112:143]}"(W[3]), "+{v[144:175]}"(W[4]))
typedef __attribute__((ext_vector_type(4))) short s16x4;
typedef __attribute__((address_space(3))) s16x4* lds_s16x4p;
__device__ __forceinline__ u32x2 trd(u32 ldsaddr) {
    return __builtin_bit_cast(u32x2, __builtin_amdgcn_ds_read_tr16_b64_v4i16((lds_s16x4p)ldsaddr));
}
template <bool NT>
__device__ __forceinline__ void dma16(const void* g, u32 lds_base) {
    u32 sv;
    if (NT)
        asm volatile("s_mov_b32 %0, m0\n\ts_mov_b32 m0, %2\n\ts_nop 0\n\tglobal_load_lds_dwordx4 %1, off nt\n\ts_mov_b32 m0, %0"
                     : "=&s"(sv) : "v"(g), "s"(lds_base) : "memory");
    else
        asm volatile("s_mov_b32 %0, m0\n\ts_mov_b32 m0, %2\n\ts_nop 0\n\tglobal_load_lds_dwordx4 %1, off\n\ts_mov_b32 m0, %0"
                     : "=&s"(sv) : "v"(g), "s"(lds_base) : "memory");
}
__device__ __forceinline__ void dma16_sc1(const void* g, u32 lds_base) {
    u32 sv;
    asm volatile("s_mov_b32 %0, m0\n\ts_mov_b32 m0, %2\n\ts_nop 0\n\tglobal_load_lds_dwordx4 %1, off sc1\n\ts_mov_b32 m0, %0"
                 : "=&s"(sv) : "v"(g), "s"(lds_base) : "memory");
}
template <int ORD, typename F>
__device__ __forceinline__ void load_weights(F&& after, const u16* __restrict__ p0, const u16* __restrict__ p1, const bool wo1, char* smem,
                                             const int wave, const int lane, const int sa, const int ta, const int sb, const int tb,
                                             u32x32 (&W)[5], const bool mix = false) {
    const u32 lbase = (u32)(uintptr_t)(__attribute__((address_space(3))) char*)smem;
    const int strm = wave >> 2;
    const bool wo = wo1 && strm == 1;
    const u16* src = strm ? p1 : p0;
    const u32 sstep = wo ? 128u : 128u * HD_;
    u32 so[8];
#pragma unroll
    for (int i = 0; i < 8; ++i) {
        const int j = (wave & 3) * 8 + i, r = 4 * j + (lane >> 4);
        const int f = (lane >> 4) | (((j >> 1) & 1) << 2);
        const int ch = (lane & 15) ^ (f << 1);
        const int e = (r & ~31) | (((r >> 2) & 1) << 4) | (((r >> 3) & 3) << 2) | (r & 3);
        so[i] = wo ? (u32)(e * D_ + ch * 8) : (u32)(dmap(r) * HD_ + ch * 8);
    }
    auto issue = [&](int st) {
        const u32 dst = __builtin_amdgcn_readfirstlane(lbase + (u32)((st & 1) * 65536 + strm * 32768 + (wave & 3) * 8192));
#pragma unroll
        for (int i = 0; i < 8; ++i) dma16<((ORD >> 20) & 1) != 0>(src + so[i] + (size_t)((ORD >> (4 * st)) & 15) * sstep, dst + i * 1024);
    };
    const int g = lane >> 4, q = (lane >> 2) & 3, p = lane & 3;
    const int f = q | ((g & 1) << 2);
    const u32 ra = lbase + (u32)(sa * 32768 + (8 * g + q) * 256 + (((ta ^ f) << 1) | (p >> 1)) * 16 + (p & 1) * 8);
    const u32 rb = lbase + (u32)(sb * 32768 + (8 * g + q) * 256 + (((tb ^ f) << 1) | (p >> 1)) * 16 + (p & 1) * 8);
    issue(0);
    issue(1);
#pragma unroll
    for (int st = 0; st < 5; ++st) {
        if (st < 4) asm volatile("s_waitcnt vmcnt(8)" ::: "memory"); else asm volatile("s_waitcnt vmcnt(0)" ::: "memory");
        LBAR();
#pragma unroll
        for (int k = 0; k < 4; ++k) {
            const u32 o = (u32)((st & 1) * 65536 + k * 8192);
            u32x2 a0 = trd(ra + o), a1 = trd(ra + o + 1024), b0 = trd(rb + o), b1 = trd(rb + o + 1024);
            const int ia = (((ORD >> (4 * st)) & 15) * 4 + k) * 4, ib = 80 + ia;
            if (mix) {
                const int ic = 2 * ia;
                W[ic >> 5][ic & 31] = a0[0]; W[ic >> 5][(ic & 31) + 2] = a0[1]; W[ic >> 5][(ic & 31) + 4] = a1[0]; W[ic >> 5][(ic & 31) + 6] = a1[1];
                W[ic >> 5][(ic & 31) + 1] = b0[0]; W[ic >> 5][(ic & 31) + 3] = b0[1]; W[ic >> 5][(ic & 31) + 5] = b1[0]; W[ic >> 5][(ic & 31) + 7] = b1[1];
                continue;
            }
            W[ia >> 5][ia & 31] = a0[0]; W[ia >> 5][(ia & 31) + 1] = a0[1]; W[ia >> 5][(ia & 31) + 2] = a1[0]; W[ia >> 5][(ia & 31) + 3] = a1[1];
            W[ib >> 5][ib & 31] = b0[0]; W[ib >> 5][(ib & 31) + 1] = b0[1]; W[ib >> 5][(ib & 31) + 2] = b1[0]; W[ib >> 5][(ib & 31) + 3] = b1[1];
        }
        LBAR();
        after(st + 16);
        if (st + 2 < 5) issue(st + 2);
        after(st);
    }
}

typedef __attribute__((ext_vector_type(16))) unsigned int u32x16;
typedef __attribute__((ext_vector_type(16))) float f32x16;
constexpr int TWO = 2;
constexpr u32 WORD = 0x54321076;
#define STP
#define STA
__device__ __forceinline__ void s_p0(const u32x32 (&W)[5], u32 va, u32 lz, u32 sidx, u32 qw, u32 kw, u32 fv, u32 vm, u32x4 rs, u32 pfo, u32 pfd, u32 fo, u32 fd, u32 fb, u32 dv, u32& left STP) {
    u32 dl;
    asm volatile(
        ".p2align 6\n\t"
        "v_mov_b32 v206, %[va]\n\t"
        "s_mov_b32 %[dl], 81920\n\t"
        "v_mov_b64 v[248:249], 0\n\t"
        "v_mov_b64 v[250:251], 0\n\t"
        "v_mov_b64 v[252:253], 0\n\t"
        "v_mov_b64 v[254:255], 0\n\t"
        "v_mov_b32 v205, %[fv]\n\t"
        "s_mov_b32 s92, m0\n\t"
        "s_mov_b32 m0, %[pfd]\n\t"
        "s_add_u32 s93, %[pfo], 0x1000\n\t"
        "buffer_load_dwordx4 %[vm], %[rs], %[pfo] offen offset:0 sc1 lds\n\t"
        "buffer_load_dwordx4 %[vm], %[rs], %[pfo] offen offset:1024 sc1 lds\n\t"
        "buffer_load_dwordx4 %[vm], %[rs], %[pfo] offen offset:2048 sc1 lds\n\t"
        "buffer_load_dwordx4 %[vm], %[rs], %[pfo] offen offset:3072 sc1 lds\n\t"
        "s_add_u32 m0, m0, 0x1000\n\t"
        "s_nop 0\n\t"
        "buffer_load_dwordx4 %[vm], %[rs], s93 offen offset:0 sc1 lds\n\t"
        "buffer_load_dwordx4 %[vm], %[rs], s93 offen offset:1024 sc1 lds\n\t"
        "buffer_load_dwordx4 %[vm], %[rs], s93 offen offset:2048 sc1 lds\n\t"
        "buffer_load_dwordx4 %[vm], %[rs], s93 offen offset:3072 sc1 lds\n\t"
        "s_mov_b32 m0, s92\n\t"
        "s_setprio 3\n\t"
        "Lb1%=:\n\t"
        "s_barrier\n\t"
        "buffer_load_dword v199, %[fb], %[rs], 0 offen sc1\n\t"
        "ds_read_b128 v[208:211], v206 offset:0\n\t"
        "ds_read_b128 v[212:215], v206 offset:1024\n\t"
        "ds_read_b128 v[216:219], v206 offset:2048\n\t"
        "ds_read_b128 v[220:223], v206 offset:3072\n\t"
        "ds_read_b128 v[224:227], v206 offset:4096\n\t"
        "ds_read_b128 v[228:231], v206 offset:5120\n\t"
        "s_waitcnt lgkmcnt(5)\n\t"
        "v_smfmac_f32_16x16x64_bf16 v[248:251], v[208:211], v[16:23], %[sidx]\n\t"
        "ds_read_b128 v[232:235], v206 offset:6144\n\t"
        "s_waitcnt lgkmcnt(5)\n\t"
        "v_smfmac_f32_16x16x64_bf16 v[248:251], v[212:215], v[24:31], %[sidx]\n\t"
        "ds_read_b128 v[236:239], v206 offset:7168\n\t"
        "s_waitcnt lgkmcnt(5)\n\t"
        "v_smfmac_f32_16x16x64_bf16 v[248:251], v[216:219], v[32:39], %[sidx]\n\t"
        "ds_read_b128 v[240:243], v206 offset:8192\n\t"
        "s_waitcnt lgkmcnt(5)\n\t"
        "v_smfmac_f32_16x16x64_bf16 v[248:251], v[220:223], v[40:47], %[sidx]\n\t"
        "ds_read_b128 v[244:247], v206 offset:10240\n\t"
        "s_waitcnt lgkmcnt(5)\n\t"
        "v_smfmac_f32_16x16x64_bf16 v[248:251], v[224:227], v[48:55], %[sidx]\n\t"
        "s_waitcnt lgkmcnt(4)\n\t"
        "v_smfmac_f32_16x16x64_bf16 v[248:251], v[228:231], v[56:63], %[sidx]\n\t"
        "s_waitcnt lgkmcnt(3)\n\t"
        "v_smfmac_f32_16x16x64_bf16 v[248:251], v[232:235], v[64:71], %[sidx]\n\t"
        "s_waitcnt lgkmcnt(2)\n\t"
        "v_smfmac_f32_16x16x64_bf16 v[248:251], v[236:239], v[72:79], %[sidx]\n\t"
        "s_waitcnt lgkmcnt(1)\n\t"
        "v_smfmac_f32_16x16x64_bf16 v[248:251], v[240:243], v[80:87], %[sidx]\n\t"
        "s_waitcnt lgkmcnt(0)\n\t"
        "v_smfmac_f32_16x16x64_bf16 v[248:251], v[244:247], v[88:95], %[sidx]\n\t"
        "s_setprio 0\n\t"
        "s_nop 6\n\t"
        "v_cvt_pk_bf16_f32 v200, v248, v249\n\t"
        "v_cvt_pk_bf16_f32 v201, v250, v251\n\t"
        "ds_write2_b32 %[qw], v200, v201 offset1:96\n\t"
        "v_fma_f32 v202, v248, v249, v250\n\t"
        "v_cmp_u_f32 vcc, v202, v251\n\t"
        "s_cbranch_vccnz Lnan%=\n\t"
        "Lok%=:\n\t"
        "s_mov_b32 m0, %[fd]\n\t"
        "s_nop 0\n\t"
        "buffer_load_dword %[fo], %[rs], 0 offen sc1 lds\n\t"
        "s_mov_b32 m0, s92\n\t"
        "s_waitcnt lgkmcnt(0)\n\t"
        "s_barrier\n\t"
        "v_smfmac_f32_16x16x64_bf16 v[252:255], v[208:211], v[96:103], %[sidx]\n\t"
        "v_smfmac_f32_16x16x64_bf16 v[252:255], v[212:215], v[104:111], %[sidx]\n\t"
        "v_smfmac_f32_16x16x64_bf16 v[252:255], v[216:219], v[112:119], %[sidx]\n\t"
        "v_smfmac_f32_16x16x64_bf16 v[252:255], v[220:223], v[120:127], %[sidx]\n\t"
        "v_smfmac_f32_16x16x64_bf16 v[252:255], v[224:227], v[128:135], %[sidx]\n\t"
        "v_smfmac_f32_16x16x64_bf16 v[252:255], v[228:231], v[136:143], %[sidx]\n\t"
        "v_smfmac_f32_16x16x64_bf16 v[252:255], v[232:235], v[144:151], %[sidx]\n\t"
        "v_smfmac_f32_16x16x64_bf16 v[252:255], v[236:239], v[152:159], %[sidx]\n\t"
        "v_smfmac_f32_16x16x64_bf16 v[252:255], v[240:243], v[160:167], %[sidx]\n\t"
        "v_smfmac_f32_16x16x64_bf16 v[252:255], v[244:247], v[168:175], %[sidx]\n\t"
        "s_waitcnt vmcnt(1)\n\t"
        "v_cmp_ne_u32 vcc, %[dv], v199\n\t"
        "s_nop 5\n\t"
        "v_add_f32 v252, v252, v254\n\t"
        "v_add_f32 v253, v253, v255\n\t"
        "v_cvt_pk_bf16_f32 v204, v252, v253\n\t"
        "ds_write_b64 %[kw], v[204:205]\n\t"
        "s_cbranch_vccnz Lw%=\n\t"
        "Lv%=:\n\t"
        "s_barrier\n\t"
        "s_branch Lend%=\n\t"
        "Lw%=:\n\t"
        "s_sub_u32 %[left], %[left], 1\n\t"
        "s_cmp_lt_i32 %[left], 0\n\t"
        "s_cbranch_scc1 Lv%=\n\t"
        "buffer_load_dword v199, %[fb], %[rs], 0 offen sc1\n\t"
        "s_waitcnt vmcnt(0)\n\t"
        "v_cmp_ne_u32 vcc, %[dv], v199\n\t"
        "s_cbranch_vccz Lv%=\n\t"
        "s_sleep 4\n\t"
        "s_branch Lw%=\n\t"
        "Lnan%=:\n\t"
        "v_mbcnt_lo_u32_b32 v202, -1, 0\n\t"
        "v_mbcnt_hi_u32_b32 v202, -1, v202\n\t"
        "v_lshl_add_u32 v207, v202, 7, %[lz]\n\t"
        "v_and_b32 v202, 31, v202\n\t"
        "v_lshl_add_u32 v207, v202, 2, v207\n\t"
        "ds_read2st64_b32 v[202:203], v207 offset1:32\n\t"
        "s_waitcnt lgkmcnt(0)\n\t"
        "s_mov_b64 exec, 0x00ff00ff\n\t"
        "v_max_u32 v202, v202, v203\n\t"
        "s_mov_b64 exec, -1\n\t"
        "v_cmp_eq_u32 vcc, 0xffbfffbf, v202\n\t"
        "s_cbranch_vccnz Lredo%=\n\t"
        "v_max_u32 v207, v208, v209\n\t"
        "v_max3_u32 v207, v207, v210, v211\n\t"
        "v_max3_u32 v207, v207, v212, v213\n\t"
        "v_max3_u32 v207, v207, v214, v215\n\t"
        "v_max3_u32 v207, v207, v216, v217\n\t"
        "v_max3_u32 v207, v207, v218, v219\n\t"
        "v_max3_u32 v207, v207, v220, v221\n\t"
        "v_max3_u32 v207, v207, v222, v223\n\t"
        "v_max3_u32 v207, v207, v224, v225\n\t"
        "v_max3_u32 v207, v207, v226, v227\n\t"
        "v_max3_u32 v207, v207, v228, v229\n\t"
        "v_max3_u32 v207, v207, v230, v231\n\t"
        "v_max3_u32 v207, v207, v232, v233\n\t"
        "v_max3_u32 v207, v207, v234, v235\n\t"
        "v_max3_u32 v207, v207, v236, v237\n\t"
        "v_max3_u32 v207, v207, v238, v239\n\t"
        "v_max3_u32 v207, v207, v240, v241\n\t"
        "v_max3_u32 v207, v207, v242, v243\n\t"
        "v_max3_u32 v207, v207, v244, v245\n\t"
        "v_max3_u32 v207, v207, v246, v247\n\t"
        "v_cmp_eq_u32 vcc, 0xffbfffbf, v207\n\t"
        "s_cbranch_vccz Lok%=\n\t"
        "Lredo%=:\n\t"
        "s_waitcnt lgkmcnt(0)\n\t"
        "v_add_u32 v206, %[dl], v206\n\t"
        "v_mov_b64 v[248:249], 0\n\t"
        "v_mov_b64 v[250:251], 0\n\t"
        "v_mov_b64 v[252:253], 0\n\t"
        "v_mov_b64 v[254:255], 0\n\t"
        "s_add_u32 %[lz], %[lz], %[dl]\n\t"
        "s_sub_u32 %[dl], 0, %[dl]\n\t"
        "s_setprio 3\n\t"
        "s_barrier\n\t"
        "s_branch Lb1%=\n\t"
        "Lend%=:\n\t"
        : [lz] "+&s"(lz), [dl] "=&s"(dl), [left] "+&s"(left)
        : [w0] "{v[16:47]}"(W[0]), [w1] "{v[48:79]}"(W[1]), [w2] "{v[80:111]}"(W[2]), [w3] "{v[112:143]}"(W[3]), [w4] "{v[144:175]}"(W[4]), [va] "v"(va), [sidx] "v"(sidx), [qw] "v"(qw), [kw] "v"(kw), [fv] "s"(fv), [vm] "v"(vm), [rs] "s"(rs), [pfo] "s"(pfo), [pfd] "s"(pfd), [fo] "v"(fo), [fd] "s"(fd), [fb] "v"(fb), [dv] "s"(dv)
        : "s92", "s93", "v199", "v200", "v201", "v202", "v203", "v204", "v205", "v206", "v207", "v208", "v209", "v210", "v211", "v212", "v213", "v214", "v215", "v216", "v217", "v218", "v219", "v220", "v221", "v222", "v223", "v224", "v225", "v226", "v227", "v228", "v229", "v230", "v231", "v232", "v233", "v234", "v235", "v236", "v237", "v238", "v239", "v240", "v241", "v242", "v243", "v244", "v245", "v246", "v247", "v248", "v249", "v250", "v251", "v252", "v253", "v254", "v255", "vcc", "scc", "memory");
}

__device__ __forceinline__ void s_p1(const u32x32 (&W)[5], u32 va, u32 lz, u32 sidx, u32 qw, u32 kw, u32 fv, u32x4 p0, u32 p1, u32 o0, u32 o1, u32x4 rs STP) {
    u32 dl;
    asm volatile(
        ".p2align 6\n\t"
        "v_mov_b32 v206, %[va]\n\t"
        "s_mov_b32 %[dl], 81920\n\t"
        "v_mov_b64 v[248:249], 0\n\t"
        "v_mov_b64 v[250:251], 0\n\t"
        "v_mov_b64 v[252:253], 0\n\t"
        "v_mov_b64 v[254:255], 0\n\t"
        "v_mov_b32 v205, %[fv]\n\t"
        "s_setprio 3\n\t"
        "s_waitcnt vmcnt(0)\n\t"
        "Lb1%=:\n\t"
        "s_barrier\n\t"
        "ds_read_b128 v[208:211], v206 offset:0\n\t"
        "ds_read_b128 v[212:215], v206 offset:1024\n\t"
        "ds_read_b128 v[216:219], v206 offset:2048\n\t"
        "ds_read_b128 v[220:223], v206 offset:3072\n\t"
        "ds_read_b128 v[224:227], v206 offset:4096\n\t"
        "ds_read_b128 v[228:231], v206 offset:5120\n\t"
        "s_waitcnt lgkmcnt(5)\n\t"
        "v_smfmac_f32_16x16x64_bf16 v[248:251], v[208:211], v[16:23], %[sidx]\n\t"
        "ds_read_b128 v[232:235], v206 offset:6144\n\t"
        "s_waitcnt lgkmcnt(5)\n\t"
        "v_smfmac_f32_16x16x64_bf16 v[248:251], v[212:215], v[24:31], %[sidx]\n\t"
        "ds_read_b128 v[236:239], v206 offset:7168\n\t"
        "s_waitcnt lgkmcnt(5)\n\t"
        "v_smfmac_f32_16x16x64_bf16 v[248:251], v[216:219], v[32:39], %[sidx]\n\t"
        "ds_read_b128 v[240:243], v206 offset:8192\n\t"
        "s_waitcnt lgkmcnt(5)\n\t"
        "v_smfmac_f32_16x16x64_bf16 v[248:251], v[220:223], v[40:47], %[sidx]\n\t"
        "ds_read_b128 v[244:247], v206 offset:10240\n\t"
        "s_waitcnt lgkmcnt(5)\n\t"
        "v_smfmac_f32_16x16x64_bf16 v[248:251], v[224:227], v[48:55], %[sidx]\n\t"
        "s_waitcnt lgkmcnt(4)\n\t"
        "v_smfmac_f32_16x16x64_bf16 v[248:251], v[228:231], v[56:63], %[sidx]\n\t"
        "s_waitcnt lgkmcnt(3)\n\t"
        "v_smfmac_f32_16x16x64_bf16 v[248:251], v[232:235], v[64:71], %[sidx]\n\t"
        "s_waitcnt lgkmcnt(2)\n\t"
        "v_smfmac_f32_16x16x64_bf16 v[248:251], v[236:239], v[72:79], %[sidx]\n\t"
        "s_waitcnt lgkmcnt(1)\n\t"
        "v_smfmac_f32_16x16x64_bf16 v[248:251], v[240:243], v[80:87], %[sidx]\n\t"
        "s_waitcnt lgkmcnt(0)\n\t"
        "v_smfmac_f32_16x16x64_bf16 v[248:251], v[244:247], v[88:95], %[sidx]\n\t"
        "s_setprio 0\n\t"
        "s_nop 6\n\t"
        "v_cvt_pk_bf16_f32 v200, v248, v249\n\t"
        "v_cvt_pk_bf16_f32 v201, v250, v251\n\t"
        "ds_write2_b32 %[qw], v200, v201 offset1:96\n\t"
        "v_fma_f32 v202, v248, v249, v250\n\t"
        "v_cmp_u_f32 vcc, v202, v251\n\t"
        "s_cbranch_vccnz Lnan%=\n\t"
        "Lok%=:\n\t"
        "buffer_store_dwordx4 v[192:195], %[o0], %[rs], 0 offen sc1\n\t"
        "buffer_store_dword v196, %[o1], %[rs], 0 offen sc1\n\t"
        "s_waitcnt lgkmcnt(0)\n\t"
        "s_barrier\n\t"
        "s_nop 15\n\t"
        "s_nop 15\n\t"
        "s_nop 15\n\t"
        "s_nop 3\n\t"
        "v_smfmac_f32_16x16x64_bf16 v[252:255], v[208:211], v[96:103], %[sidx]\n\t"
        "v_smfmac_f32_16x16x64_bf16 v[252:255], v[212:215], v[104:111], %[sidx]\n\t"
        "v_smfmac_f32_16x16x64_bf16 v[252:255], v[216:219], v[112:119], %[sidx]\n\t"
        "v_smfmac_f32_16x16x64_bf16 v[252:255], v[220:223], v[120:127], %[sidx]\n\t"
        "v_smfmac_f32_16x16x64_bf16 v[252:255], v[224:227], v[128:135], %[sidx]\n\t"
        "v_smfmac_f32_16x16x64_bf16 v[252:255], v[228:231], v[136:143], %[sidx]\n\t"
        "v_smfmac_f32_16x16x64_bf16 v[252:255], v[232:235], v[144:151], %[sidx]\n\t"
        "v_smfmac_f32_16x16x64_bf16 v[252:255], v[236:239], v[152:159], %[sidx]\n\t"
        "v_smfmac_f32_16x16x64_bf16 v[252:255], v[240:243], v[160:167], %[sidx]\n\t"
        "v_smfmac_f32_16x16x64_bf16 v[252:255], v[244:247], v[168:175], %[sidx]\n\t"
        "s_nop 7\n\t"
        "v_add_f32 v252, v252, v254\n\t"
        "v_add_f32 v253, v253, v255\n\t"
        "v_cvt_pk_bf16_f32 v204, v252, v253\n\t"
        "ds_write_b64 %[kw], v[204:205]\n\t"
        "s_barrier\n\t"
        "s_branch Lend%=\n\t"
        "Lnan%=:\n\t"
        "v_mbcnt_lo_u32_b32 v202, -1, 0\n\t"
        "v_mbcnt_hi_u32_b32 v202, -1, v202\n\t"
        "v_lshl_add_u32 v207, v202, 7, %[lz]\n\t"
        "v_and_b32 v202, 31, v202\n\t"
        "v_lshl_add_u32 v207, v202, 2, v207\n\t"
        "ds_read2st64_b32 v[202:203], v207 offset1:32\n\t"
        "s_waitcnt lgkmcnt(0)\n\t"
        "s_mov_b64 exec, 0x00ff00ff\n\t"
        "v_max_u32 v202, v202, v203\n\t"
        "s_mov_b64 exec, -1\n\t"
        "v_cmp_eq_u32 vcc, 0xffbfffbf, v202\n\t"
        "s_cbranch_vccnz Lredo%=\n\t"
        "v_max_u32 v207, v208, v209\n\t"
        "v_max3_u32 v207, v207, v210, v211\n\t"
        "v_max3_u32 v207, v207, v212, v213\n\t"
        "v_max3_u32 v207, v207, v214, v215\n\t"
        "v_max3_u32 v207, v207, v216, v217\n\t"
        "v_max3_u32 v207, v207, v218, v219\n\t"
        "v_max3_u32 v207, v207, v220, v221\n\t"
        "v_max3_u32 v207, v207, v222, v223\n\t"
        "v_max3_u32 v207, v207, v224, v225\n\t"
        "v_max3_u32 v207, v207, v226, v227\n\t"
        "v_max3_u32 v207, v207, v228, v229\n\t"
        "v_max3_u32 v207, v207, v230, v231\n\t"
        "v_max3_u32 v207, v207, v232, v233\n\t"
        "v_max3_u32 v207, v207, v234, v235\n\t"
        "v_max3_u32 v207, v207, v236, v237\n\t"
        "v_max3_u32 v207, v207, v238, v239\n\t"
        "v_max3_u32 v207, v207, v240, v241\n\t"
        "v_max3_u32 v207, v207, v242, v243\n\t"
        "v_max3_u32 v207, v207, v244, v245\n\t"
        "v_max3_u32 v207, v207, v246, v247\n\t"
        "v_cmp_eq_u32 vcc, 0xffbfffbf, v207\n\t"
        "s_cbranch_vccz Lok%=\n\t"
        "Lredo%=:\n\t"
        "s_waitcnt lgkmcnt(0)\n\t"
        "v_add_u32 v206, %[dl], v206\n\t"
        "v_mov_b64 v[248:249], 0\n\t"
        "v_mov_b64 v[250:251], 0\n\t"
        "v_mov_b64 v[252:253], 0\n\t"
        "v_mov_b64 v[254:255], 0\n\t"
        "s_add_u32 %[lz], %[lz], %[dl]\n\t"
        "s_sub_u32 %[dl], 0, %[dl]\n\t"
        "s_setprio 3\n\t"
        "s_barrier\n\t"
        "s_branch Lb1%=\n\t"
        "Lend%=:\n\t"
        : [lz] "+&s"(lz), [dl] "=&s"(dl)
        : [w0] "{v[16:47]}"(W[0]), [w1] "{v[48:79]}"(W[1]), [w2] "{v[80:111]}"(W[2]), [w3] "{v[112:143]}"(W[3]), [w4] "{v[144:175]}"(W[4]), [va] "v"(va), [sidx] "v"(sidx), [qw] "v"(qw), [kw] "v"(kw), [fv] "s"(fv), [p0] "{v[192:195]}"(p0), [p1] "{v196}"(p1), [o0] "v"(o0), [o1] "v"(o1), [rs] "s"(rs)
        : "v200", "v201", "v202", "v203", "v204", "v205", "v206", "v207", "v208", "v209", "v210", "v211", "v212", "v213", "v214", "v215", "v216", "v217", "v218", "v219", "v220", "v221", "v222", "v223", "v224", "v225", "v226", "v227", "v228", "v229", "v230", "v231", "v232", "v233", "v234", "v235", "v236", "v237", "v238", "v239", "v240", "v241", "v242", "v243", "v244", "v245", "v246", "v247", "v248", "v249", "v250", "v251", "v252", "v253", "v254", "v255", "vcc", "scc", "memory");
}

__device__ __forceinline__ void s_p2(const u32x32 (&W)[5], u32 va, u32 lz, u32 sidx, u32 qw, u32 kw, u32 fv, u32 vm, u32x4 rs, u32 pfo, u32 pfd STP) {
    u32 dl;
    asm volatile(
        ".p2align 6\n\t"
        "v_mov_b32 v206, %[va]\n\t"
        "s_mov_b32 %[dl], 81920\n\t"
        "v_mov_b64 v[248:249], 0\n\t"
        "v_mov_b64 v[250:251], 0\n\t"
        "v_mov_b64 v[252:253], 0\n\t"
        "v_mov_b64 v[254:255], 0\n\t"
        "v_mov_b32 v205, %[fv]\n\t"
        "s_setprio 3\n\t"
        "s_waitcnt vmcnt(0)\n\t"
        "Lb1%=:\n\t"
        "s_barrier\n\t"
        "ds_read_b128 v[208:211], v206 offset:0\n\t"
        "ds_read_b128 v[212:215], v206 offset:1024\n\t"
        "ds_read_b128 v[216:219], v206 offset:2048\n\t"
        "ds_read_b128 v[220:223], v206 offset:3072\n\t"
        "ds_read_b128 v[224:227], v206 offset:4096\n\t"
        "ds_read_b128 v[228:231], v206 offset:5120\n\t"
        "s_waitcnt lgkmcnt(5)\n\t"
        "v_smfmac_f32_16x16x64_bf16 v[248:251], v[208:211], v[16:23], %[sidx]\n\t"
        "ds_read_b128 v[232:235], v206 offset:6144\n\t"
        "s_waitcnt lgkmcnt(5)\n\t"
        "v_smfmac_f32_16x16x64_bf16 v[248:251], v[212:215], v[24:31], %[sidx]\n\t"
        "ds_read_b128 v[236:239], v206 offset:7168\n\t"
        "s_waitcnt lgkmcnt(5)\n\t"
        "v_smfmac_f32_16x16x64_bf16 v[248:251], v[216:219], v[32:39], %[sidx]\n\t"
        "ds_read_b128 v[240:243], v206 offset:8192\n\t"
        "s_waitcnt lgkmcnt(5)\n\t"
        "v_smfmac_f32_16x16x64_bf16 v[248:251], v[220:223], v[40:47], %[sidx]\n\t"
        "ds_read_b128 v[244:247], v206 offset:10240\n\t"
        "s_waitcnt lgkmcnt(5)\n\t"
        "v_smfmac_f32_16x16x64_bf16 v[248:251], v[224:227], v[48:55], %[sidx]\n\t"
        "s_waitcnt lgkmcnt(4)\n\t"
        "v_smfmac_f32_16x16x64_bf16 v[248:251], v[228:231], v[56:63], %[sidx]\n\t"
        "s_waitcnt lgkmcnt(3)\n\t"
        "v_smfmac_f32_16x16x64_bf16 v[248:251], v[232:235], v[64:71], %[sidx]\n\t"
        "s_waitcnt lgkmcnt(2)\n\t"
        "v_smfmac_f32_16x16x64_bf16 v[248:251], v[236:239], v[72:79], %[sidx]\n\t"
        "s_waitcnt lgkmcnt(1)\n\t"
        "v_smfmac_f32_16x16x64_bf16 v[248:251], v[240:243], v[80:87], %[sidx]\n\t"
        "s_waitcnt lgkmcnt(0)\n\t"
        "v_smfmac_f32_16x16x64_bf16 v[248:251], v[244:247], v[88:95], %[sidx]\n\t"
        "s_setprio 0\n\t"
        "s_nop 6\n\t"
        "v_cvt_pk_bf16_f32 v200, v248, v249\n\t"
        "v_cvt_pk_bf16_f32 v201, v250, v251\n\t"
        "ds_write2_b32 %[qw], v200, v201 offset1:96\n\t"
        "v_fma_f32 v202, v248, v249, v250\n\t"
        "v_cmp_u_f32 vcc, v202, v251\n\t"
        "s_cbranch_vccnz Lnan%=\n\t"
        "Lok%=:\n\t"
        "s_mov_b32 s92, m0\n\t"
        "s_mov_b32 m0, %[pfd]\n\t"
        "s_add_u32 s93, %[pfo], 0x1000\n\t"
        "buffer_load_dwordx4 %[vm], %[rs], %[pfo] offen offset:0 sc1 lds\n\t"
        "buffer_load_dwordx4 %[vm], %[rs], %[pfo] offen offset:1024 sc1 lds\n\t"
        "buffer_load_dwordx4 %[vm], %[rs], %[pfo] offen offset:2048 sc1 lds\n\t"
        "buffer_load_dwordx4 %[vm], %[rs], %[pfo] offen offset:3072 sc1 lds\n\t"
        "s_add_u32 m0, m0, 0x1000\n\t"
        "s_nop 0\n\t"
        "buffer_load_dwordx4 %[vm], %[rs], s93 offen offset:0 sc1 lds\n\t"
        "buffer_load_dwordx4 %[vm], %[rs], s93 offen offset:1024 sc1 lds\n\t"
        "buffer_load_dwordx4 %[vm], %[rs], s93 offen offset:2048 sc1 lds\n\t"
        "buffer_load_dwordx4 %[vm], %[rs], s93 offen offset:3072 sc1 lds\n\t"
        "s_mov_b32 m0, s92\n\t"
        "s_waitcnt lgkmcnt(0)\n\t"
        "s_barrier\n\t"
        "v_smfmac_f32_16x16x64_bf16 v[252:255], v[208:211], v[96:103], %[sidx]\n\t"
        "v_smfmac_f32_16x16x64_bf16 v[252:255], v[212:215], v[104:111], %[sidx]\n\t"
        "v_smfmac_f32_16x16x64_bf16 v[252:255], v[216:219], v[112:119], %[sidx]\n\t"
        "v_smfmac_f32_16x16x64_bf16 v[252:255], v[220:223], v[120:127], %[sidx]\n\t"
        "v_smfmac_f32_16x16x64_bf16 v[252:255], v[224:227], v[128:135], %[sidx]\n\t"
        "v_smfmac_f32_16x16x64_bf16 v[252:255], v[228:231], v[136:143], %[sidx]\n\t"
        "v_smfmac_f32_16x16x64_bf16 v[252:255], v[232:235], v[144:151], %[sidx]\n\t"
        "v_smfmac_f32_16x16x64_bf16 v[252:255], v[236:239], v[152:159], %[sidx]\n\t"
        "v_smfmac_f32_16x16x64_bf16 v[252:255], v[240:243], v[160:167], %[sidx]\n\t"
        "v_smfmac_f32_16x16x64_bf16 v[252:255], v[244:247], v[168:175], %[sidx]\n\t"
        "s_nop 7\n\t"
        "v_add_f32 v252, v252, v254\n\t"
        "v_add_f32 v253, v253, v255\n\t"
        "v_cvt_pk_bf16_f32 v204, v252, v253\n\t"
        "ds_write_b64 %[kw], v[204:205]\n\t"
        "s_barrier\n\t"
        "s_branch Lend%=\n\t"
        "Lnan%=:\n\t"
        "v_mbcnt_lo_u32_b32 v202, -1, 0\n\t"
        "v_mbcnt_hi_u32_b32 v202, -1, v202\n\t"
        "v_lshl_add_u32 v207, v202, 7, %[lz]\n\t"
        "v_and_b32 v202, 31, v202\n\t"
        "v_lshl_add_u32 v207, v202, 2, v207\n\t"
        "ds_read2st64_b32 v[202:203], v207 offset1:32\n\t"
        "s_waitcnt lgkmcnt(0)\n\t"
        "s_mov_b64 exec, 0x00ff00ff\n\t"
        "v_max_u32 v202, v202, v203\n\t"
        "s_mov_b64 exec, -1\n\t"
        "v_cmp_eq_u32 vcc, 0xffbfffbf, v202\n\t"
        "s_cbranch_vccnz Lredo%=\n\t"
        "v_max_u32 v207, v208, v209\n\t"
        "v_max3_u32 v207, v207, v210, v211\n\t"
        "v_max3_u32 v207, v207, v212, v213\n\t"
        "v_max3_u32 v207, v207, v214, v215\n\t"
        "v_max3_u32 v207, v207, v216, v217\n\t"
        "v_max3_u32 v207, v207, v218, v219\n\t"
        "v_max3_u32 v207, v207, v220, v221\n\t"
        "v_max3_u32 v207, v207, v222, v223\n\t"
        "v_max3_u32 v207, v207, v224, v225\n\t"
        "v_max3_u32 v207, v207, v226, v227\n\t"
        "v_max3_u32 v207, v207, v228, v229\n\t"
        "v_max3_u32 v207, v207, v230, v231\n\t"
        "v_max3_u32 v207, v207, v232, v233\n\t"
        "v_max3_u32 v207, v207, v234, v235\n\t"
        "v_max3_u32 v207, v207, v236, v237\n\t"
        "v_max3_u32 v207, v207, v238, v239\n\t"
        "v_max3_u32 v207, v207, v240, v241\n\t"
        "v_max3_u32 v207, v207, v242, v243\n\t"
        "v_max3_u32 v207, v207, v244, v245\n\t"
        "v_max3_u32 v207, v207, v246, v247\n\t"
        "v_cmp_eq_u32 vcc, 0xffbfffbf, v207\n\t"
        "s_cbranch_vccz Lok%=\n\t"
        "Lredo%=:\n\t"
        "s_waitcnt lgkmcnt(0)\n\t"
        "v_add_u32 v206, %[dl], v206\n\t"
        "v_mov_b64 v[248:249], 0\n\t"
        "v_mov_b64 v[250:251], 0\n\t"
        "v_mov_b64 v[252:253], 0\n\t"
        "v_mov_b64 v[254:255], 0\n\t"
        "s_add_u32 %[lz], %[lz], %[dl]\n\t"
        "s_sub_u32 %[dl], 0, %[dl]\n\t"
        "s_setprio 3\n\t"
        "s_barrier\n\t"
        "s_branch Lb1%=\n\t"
        "Lend%=:\n\t"
        : [lz] "+&s"(lz), [dl] "=&s"(dl)
        : [w0] "{v[16:47]}"(W[0]), [w1] "{v[48:79]}"(W[1]), [w2] "{v[80:111]}"(W[2]), [w3] "{v[112:143]}"(W[3]), [w4] "{v[144:175]}"(W[4]), [va] "v"(va), [sidx] "v"(sidx), [qw] "v"(qw), [kw] "v"(kw), [fv] "s"(fv), [vm] "v"(vm), [rs] "s"(rs), [pfo] "s"(pfo), [pfd] "s"(pfd)
        : "s92", "s93", "v200", "v201", "v202", "v203", "v204", "v205", "v206", "v207", "v208", "v209", "v210", "v211", "v212", "v213", "v214", "v215", "v216", "v217", "v218", "v219", "v220", "v221", "v222", "v223", "v224", "v225", "v226", "v227", "v228", "v229", "v230", "v231", "v232", "v233", "v234", "v235", "v236", "v237", "v238", "v239", "v240", "v241", "v242", "v243", "v244", "v245", "v246", "v247", "v248", "v249", "v250", "v251", "v252", "v253", "v254", "v255", "vcc", "scc", "memory");
}

__device__ __forceinline__ void s_e(const u32x32 (&W)[5], u32 va, u32 lz, u32 sidx, u32 qw, u32 kw, u32 fv, u32 kxa, u32 qtr, u32 eo, u32 ew, float lim, u32x4 rs, u32 so STP) {
    u32 dl;
    asm volatile(
        ".p2align 6\n\t"
        "v_mov_b32 v206, %[va]\n\t"
        "s_mov_b32 %[dl], 81920\n\t"
        "v_mov_b64 v[248:249], 0\n\t"
        "v_mov_b64 v[250:251], 0\n\t"
        "v_mov_b64 v[252:253], 0\n\t"
        "v_mov_b64 v[254:255], 0\n\t"
        "v_mov_b32 v205, %[fv]\n\t"
        "ds_read_b128 v[176:179], %[kxa] offset:0\n\t"
        "ds_read_b128 v[180:183], %[kxa] offset:1024\n\t"
        "ds_read_b128 v[184:187], %[kxa] offset:2048\n\t"
        "ds_read_b128 v[188:191], %[kxa] offset:3072\n\t"
        "s_add_u32 s92, %[so], 0x1000\n\t"
        "s_add_u32 s93, %[so], 0x4000\n\t"
        "s_add_u32 s94, %[so], 0x5000\n\t"
        "s_setprio 2\n\t"
        "s_waitcnt lgkmcnt(0)\n\t"
        "Lb1%=:\n\t"
        "s_barrier\n\t"
        "s_nop 11\n\t"
        "ds_read_b128 v[208:211], v206 offset:0\n\t"
        "ds_read_b128 v[212:215], v206 offset:1024\n\t"
        "ds_read_b128 v[216:219], v206 offset:2048\n\t"
        "ds_read_b128 v[220:223], v206 offset:3072\n\t"
        "ds_read_b128 v[224:227], v206 offset:4096\n\t"
        "ds_read_b128 v[228:231], v206 offset:5120\n\t"
        "s_waitcnt lgkmcnt(5)\n\t"
        "v_smfmac_f32_16x16x64_bf16 v[248:251], v[208:211], v[16:23], %[sidx]\n\t"
        "ds_read_b128 v[232:235], v206 offset:6144\n\t"
        "s_waitcnt lgkmcnt(5)\n\t"
        "v_smfmac_f32_16x16x64_bf16 v[248:251], v[212:215], v[24:31], %[sidx]\n\t"
        "ds_read_b128 v[236:239], v206 offset:7168\n\t"
        "s_waitcnt lgkmcnt(5)\n\t"
        "v_smfmac_f32_16x16x64_bf16 v[248:251], v[216:219], v[32:39], %[sidx]\n\t"
        "ds_read_b128 v[240:243], v206 offset:8192\n\t"
        "s_waitcnt lgkmcnt(5)\n\t"
        "v_smfmac_f32_16x16x64_bf16 v[248:251], v[220:223], v[40:47], %[sidx]\n\t"
        "ds_read_b128 v[244:247], v206 offset:10240\n\t"
        "s_waitcnt lgkmcnt(5)\n\t"
        "v_smfmac_f32_16x16x64_bf16 v[248:251], v[224:227], v[48:55], %[sidx]\n\t"
        "s_waitcnt lgkmcnt(4)\n\t"
        "v_smfmac_f32_16x16x64_bf16 v[248:251], v[228:231], v[56:63], %[sidx]\n\t"
        "s_waitcnt lgkmcnt(3)\n\t"
        "v_smfmac_f32_16x16x64_bf16 v[248:251], v[232:235], v[64:71], %[sidx]\n\t"
        "s_waitcnt lgkmcnt(2)\n\t"
        "v_smfmac_f32_16x16x64_bf16 v[248:251], v[236:239], v[72:79], %[sidx]\n\t"
        "s_waitcnt lgkmcnt(1)\n\t"
        "v_smfmac_f32_16x16x64_bf16 v[248:251], v[240:243], v[80:87], %[sidx]\n\t"
        "s_waitcnt lgkmcnt(0)\n\t"
        "v_smfmac_f32_16x16x64_bf16 v[248:251], v[244:247], v[88:95], %[sidx]\n\t"
        "v_smfmac_f32_16x16x64_bf16 v[252:255], v[208:211], v[96:103], %[sidx]\n\t"
        "v_smfmac_f32_16x16x64_bf16 v[252:255], v[212:215], v[104:111], %[sidx]\n\t"
        "v_cvt_pk_bf16_f32 v200, v248, v249\n\t"
        "v_cvt_pk_bf16_f32 v201, v250, v251\n\t"
        "ds_write2_b32 %[qw], v200, v201 offset1:96\n\t"
        "v_fma_f32 v202, v248, v249, v250\n\t"
        "v_smfmac_f32_16x16x64_bf16 v[252:255], v[216:219], v[112:119], %[sidx]\n\t"
        "v_cmp_u_f32 vcc, v202, v251\n\t"
        "v_smfmac_f32_16x16x64_bf16 v[252:255], v[220:223], v[120:127], %[sidx]\n\t"
        "s_waitcnt lgkmcnt(0)\n\t"
        "s_cbranch_vccnz Lnan%=\n\t"
        "Lok%=:\n\t"
        "s_barrier\n\t"
        "ds_read_b64_tr_b16 v[192:193], %[qtr] offset:0\n\t"
        "ds_read_b64_tr_b16 v[194:195], %[qtr] offset:768\n\t"
        "v_smfmac_f32_16x16x64_bf16 v[252:255], v[224:227], v[128:135], %[sidx]\n\t"
        "ds_read_b64_tr_b16 v[196:197], %[qtr] offset:1536\n\t"
        "ds_read_b64_tr_b16 v[198:199], %[qtr] offset:2304\n\t"
        "v_smfmac_f32_16x16x64_bf16 v[252:255], v[228:231], v[136:143], %[sidx]\n\t"
        "ds_read_b64_tr_b16 v[208:209], %[qtr] offset:3072\n\t"
        "ds_read_b64_tr_b16 v[210:211], %[qtr] offset:3840\n\t"
        "v_smfmac_f32_16x16x64_bf16 v[252:255], v[232:235], v[144:151], %[sidx]\n\t"
        "ds_read_b64_tr_b16 v[212:213], %[qtr] offset:4608\n\t"
        "ds_read_b64_tr_b16 v[214:215], %[qtr] offset:5376\n\t"
        "v_smfmac_f32_16x16x64_bf16 v[252:255], v[236:239], v[152:159], %[sidx]\n\t"
        "v_mov_b32 v203, 0xffbfffbf\n\t"
        "s_waitcnt lgkmcnt(6)\n\t"
        "v_mfma_f32_16x16x32_bf16 v[248:251], v[176:179], v[192:195], 0\n\t"
        "s_waitcnt lgkmcnt(4)\n\t"
        "v_mfma_f32_16x16x32_bf16 v[248:251], v[180:183], v[196:199], v[248:251]\n\t"
        "s_waitcnt lgkmcnt(2)\n\t"
        "v_mfma_f32_16x16x32_bf16 v[248:251], v[184:187], v[208:211], v[248:251]\n\t"
        "s_waitcnt lgkmcnt(0)\n\t"
        "v_mfma_f32_16x16x32_bf16 v[248:251], v[188:191], v[212:215], v[248:251]\n\t"
        "v_smfmac_f32_16x16x64_bf16 v[252:255], v[240:243], v[160:167], %[sidx]\n\t"
        "v_smfmac_f32_16x16x64_bf16 v[252:255], v[244:247], v[168:175], %[sidx]\n\t"
        "v_add_f32_dpp v192, v248, v248 row_ror:8 row_mask:0xf bank_mask:0x3\n\t"
        "v_add_f32_dpp v193, v249, v249 row_ror:8 row_mask:0xf bank_mask:0x3\n\t"
        "v_add_f32_dpp v192, v250, v250 row_ror:8 row_mask:0xf bank_mask:0xc\n\t"
        "v_add_f32_dpp v193, v251, v251 row_ror:8 row_mask:0xf bank_mask:0xc\n\t"
        "v_max_f32_e64 v196, |v192|, |v193|\n\t"
        "v_exp_f32 v194, v192\n\t"
        "v_exp_f32 v195, v193\n\t"
        "v_cmp_le_f32 vcc, v196, %[lim]\n\t"
        "v_cvt_pk_bf16_f32 v196, v194, v195\n\t"
        "s_nop 0\n\t"
        "v_cndmask_b32 v194, v203, v196, vcc\n\t"
        "v_cndmask_b32 v195, v203, v196, vcc\n\t"
        "ds_write_b32 %[ew], v194\n\t"
        "s_nop 0\n\t"
        "v_permlane16_swap_b32 v194, v195\n\t"
        "buffer_store_dwordx2 v[194:195], %[eo], %[rs], %[so] offen sc1\n\t"
        "buffer_store_dwordx2 v[194:195], %[eo], %[rs], s93 offen sc1\n\t"
        "v_add_f32 v252, v252, v254\n\t"
        "v_add_f32 v253, v253, v255\n\t"
        "v_cvt_pk_bf16_f32 v204, v252, v253\n\t"
        "ds_write_b64 %[kw], v[204:205]\n\t"
        "s_barrier\n\t"
        "s_branch Lend%=\n\t"
        "Lnan%=:\n\t"
        "v_mbcnt_lo_u32_b32 v202, -1, 0\n\t"
        "v_mbcnt_hi_u32_b32 v202, -1, v202\n\t"
        "v_lshl_add_u32 v207, v202, 7, %[lz]\n\t"
        "v_and_b32 v202, 31, v202\n\t"
        "v_lshl_add_u32 v207, v202, 2, v207\n\t"
        "ds_read2st64_b32 v[202:203], v207 offset1:32\n\t"
        "s_waitcnt lgkmcnt(0)\n\t"
        "s_mov_b64 exec, 0x00ff00ff\n\t"
        "v_max_u32 v202, v202, v203\n\t"
        "s_mov_b64 exec, -1\n\t"
        "v_cmp_eq_u32 vcc, 0xffbfffbf, v202\n\t"
        "s_cbranch_vccnz Lredo%=\n\t"
        "v_max_u32 v207, v208, v209\n\t"
        "v_max3_u32 v207, v207, v210, v211\n\t"
        "v_max3_u32 v207, v207, v212, v213\n\t"
        "v_max3_u32 v207, v207, v214, v215\n\t"
        "v_max3_u32 v207, v207, v216, v217\n\t"
        "v_max3_u32 v207, v207, v218, v219\n\t"
        "v_max3_u32 v207, v207, v220, v221\n\t"
        "v_max3_u32 v207, v207, v222, v223\n\t"
        "v_max3_u32 v207, v207, v224, v225\n\t"
        "v_max3_u32 v207, v207, v226, v227\n\t"
        "v_max3_u32 v207, v207, v228, v229\n\t"
        "v_max3_u32 v207, v207, v230, v231\n\t"
        "v_max3_u32 v207, v207, v232, v233\n\t"
        "v_max3_u32 v207, v207, v234, v235\n\t"
        "v_max3_u32 v207, v207, v236, v237\n\t"
        "v_max3_u32 v207, v207, v238, v239\n\t"
        "v_max3_u32 v207, v207, v240, v241\n\t"
        "v_max3_u32 v207, v207, v242, v243\n\t"
        "v_max3_u32 v207, v207, v244, v245\n\t"
        "v_max3_u32 v207, v207, v246, v247\n\t"
        "v_cmp_eq_u32 vcc, 0xffbfffbf, v207\n\t"
        "s_cbranch_vccz Lok%=\n\t"
        "Lredo%=:\n\t"
        "s_waitcnt lgkmcnt(0)\n\t"
        "v_add_u32 v206, %[dl], v206\n\t"
        "v_mov_b64 v[248:249], 0\n\t"
        "v_mov_b64 v[250:251], 0\n\t"
        "v_mov_b64 v[252:253], 0\n\t"
        "v_mov_b64 v[254:255], 0\n\t"
        "s_add_u32 %[lz], %[lz], %[dl]\n\t"
        "s_sub_u32 %[dl], 0, %[dl]\n\t"
        "s_setprio 2\n\t"
        "s_barrier\n\t"
        "s_branch Lb1%=\n\t"
        "Lend%=:\n\t"
        : [lz] "+&s"(lz), [dl] "=&s"(dl)
        : [w0] "{v[16:47]}"(W[0]), [w1] "{v[48:79]}"(W[1]), [w2] "{v[80:111]}"(W[2]), [w3] "{v[112:143]}"(W[3]), [w4] "{v[144:175]}"(W[4]), [va] "v"(va), [sidx] "v"(sidx), [qw] "v"(qw), [kw] "v"(kw), [fv] "s"(fv), [kxa] "v"(kxa), [qtr] "v"(qtr), [eo] "v"(eo), [ew] "v"(ew), [lim] "v"(lim), [rs] "s"(rs), [so] "s"(so)
        : "s92", "s93", "s94", "v176", "v177", "v178", "v179", "v180", "v181", "v182", "v183", "v184", "v185", "v186", "v187", "v188", "v189", "v190", "v191", "v192", "v193", "v194", "v195", "v196", "v197", "v198", "v199", "v200", "v201", "v202", "v203", "v204", "v205", "v206", "v207", "v208", "v209", "v210", "v211", "v212", "v213", "v214", "v215", "v216", "v217", "v218", "v219", "v220", "v221", "v222", "v223", "v224", "v225", "v226", "v227", "v228", "v229", "v230", "v231", "v232", "v233", "v234", "v235", "v236", "v237", "v238", "v239", "v240", "v241", "v242", "v243", "v244", "v245", "v246", "v247", "v248", "v249", "v250", "v251", "v252", "v253", "v254", "v255", "vcc", "scc", "memory");
}

__device__ __forceinline__ void s_f(const u32x32 (&W)[5], u32 va, u32 lz, u32 sidx, u32 qw, u32 kw, u32 fv, u32& bad, u32x4 ones, u32 qtr, u32 er, u32 po, float c1, float ind0, float ind1, u32x4 rs, u32 so, float ce, float big, u64 jm STP) {
    u32 dl;
    asm volatile(
        ".p2align 6\n\t"
        "v_mov_b32 v206, %[va]\n\t"
        "s_mov_b32 %[dl], 81920\n\t"
        "v_mov_b64 v[248:249], 0\n\t"
        "v_mov_b64 v[250:251], 0\n\t"
        "v_mov_b64 v[252:253], 0\n\t"
        "v_mov_b64 v[254:255], 0\n\t"
        "v_mov_b32 v205, %[fv]\n\t"
        "s_add_u32 s92, %[so], 0x1000\n\t"
        "s_add_u32 s93, %[so], 0x4000\n\t"
        "s_add_u32 s94, %[so], 0x5000\n\t"
        "s_mov_b32 %[bad], 0\n\t"
        "s_setprio 2\n\t"
        "Lb1%=:\n\t"
        "s_barrier\n\t"
        "s_nop 11\n\t"
        "ds_read_b128 v[208:211], v206 offset:0\n\t"
        "ds_read_b128 v[212:215], v206 offset:1024\n\t"
        "ds_read_b128 v[216:219], v206 offset:2048\n\t"
        "ds_read_b128 v[220:223], v206 offset:3072\n\t"
        "ds_read_b128 v[224:227], v206 offset:4096\n\t"
        "ds_read_b128 v[228:231], v206 offset:5120\n\t"
        "s_waitcnt lgkmcnt(5)\n\t"
        "v_smfmac_f32_16x16x64_bf16 v[248:251], v[208:211], v[16:23], %[sidx]\n\t"
        "ds_read_b128 v[232:235], v206 offset:6144\n\t"
        "s_waitcnt lgkmcnt(5)\n\t"
        "v_smfmac_f32_16x16x64_bf16 v[248:251], v[212:215], v[24:31], %[sidx]\n\t"
        "ds_read_b128 v[236:239], v206 offset:7168\n\t"
        "s_waitcnt lgkmcnt(5)\n\t"
        "v_smfmac_f32_16x16x64_bf16 v[248:251], v[216:219], v[32:39], %[sidx]\n\t"
        "ds_read_b128 v[240:243], v206 offset:8192\n\t"
        "s_waitcnt lgkmcnt(5)\n\t"
        "v_smfmac_f32_16x16x64_bf16 v[248:251], v[220:223], v[40:47], %[sidx]\n\t"
        "ds_read_b128 v[244:247], v206 offset:10240\n\t"
        "s_waitcnt lgkmcnt(5)\n\t"
        "v_smfmac_f32_16x16x64_bf16 v[248:251], v[224:227], v[48:55], %[sidx]\n\t"
        "s_waitcnt lgkmcnt(4)\n\t"
        "v_smfmac_f32_16x16x64_bf16 v[248:251], v[228:231], v[56:63], %[sidx]\n\t"
        "s_waitcnt lgkmcnt(3)\n\t"
        "v_smfmac_f32_16x16x64_bf16 v[248:251], v[232:235], v[64:71], %[sidx]\n\t"
        "s_waitcnt lgkmcnt(2)\n\t"
        "v_smfmac_f32_16x16x64_bf16 v[248:251], v[236:239], v[72:79], %[sidx]\n\t"
        "s_waitcnt lgkmcnt(1)\n\t"
        "v_smfmac_f32_16x16x64_bf16 v[248:251], v[240:243], v[80:87], %[sidx]\n\t"
        "s_waitcnt lgkmcnt(0)\n\t"
        "v_smfmac_f32_16x16x64_bf16 v[248:251], v[244:247], v[88:95], %[sidx]\n\t"
        "v_smfmac_f32_16x16x64_bf16 v[252:255], v[208:211], v[96:103], %[sidx]\n\t"
        "v_smfmac_f32_16x16x64_bf16 v[252:255], v[212:215], v[104:111], %[sidx]\n\t"
        "v_cvt_pk_bf16_f32 v200, v248, v249\n\t"
        "v_cvt_pk_bf16_f32 v201, v250, v251\n\t"
        "ds_write2_b32 %[qw], v200, v201 offset1:96\n\t"
        "v_fma_f32 v202, v248, v249, v250\n\t"
        "v_smfmac_f32_16x16x64_bf16 v[252:255], v[216:219], v[112:119], %[sidx]\n\t"
        "v_cmp_u_f32 vcc, v202, v251\n\t"
        "v_smfmac_f32_16x16x64_bf16 v[252:255], v[220:223], v[120:127], %[sidx]\n\t"
        "s_waitcnt lgkmcnt(0)\n\t"
        "s_cbranch_vccnz Lnan%=\n\t"
        "Lok%=:\n\t"
        "s_barrier\n\t"
        "v_smfmac_f32_16x16x64_bf16 v[252:255], v[224:227], v[128:135], %[sidx]\n\t"
        "v_smfmac_f32_16x16x64_bf16 v[252:255], v[228:231], v[136:143], %[sidx]\n\t"
        "v_smfmac_f32_16x16x64_bf16 v[252:255], v[232:235], v[144:151], %[sidx]\n\t"
        "v_smfmac_f32_16x16x64_bf16 v[252:255], v[236:239], v[152:159], %[sidx]\n\t"
        "v_smfmac_f32_16x16x64_bf16 v[252:255], v[240:243], v[160:167], %[sidx]\n\t"
        "v_smfmac_f32_16x16x64_bf16 v[252:255], v[244:247], v[168:175], %[sidx]\n\t"
        "s_nop 7\n\t"
        "v_add_f32 v252, v252, v254\n\t"
        "v_add_f32 v253, v253, v255\n\t"
        "v_cvt_pk_bf16_f32 v204, v252, v253\n\t"
        "ds_write_b64 %[kw], v[204:205]\n\t"
        "v_mov_b32 v237, 0\n\t"
        "ds_read_b64_tr_b16 v[192:193], %[qtr] offset:0\n\t"
        "ds_read_b64_tr_b16 v[194:195], %[qtr] offset:768\n\t"
        "ds_read_b64_tr_b16 v[196:197], %[qtr] offset:1536\n\t"
        "ds_read_b64_tr_b16 v[198:199], %[qtr] offset:2304\n\t"
        "ds_read_b64_tr_b16 v[208:209], %[qtr] offset:3072\n\t"
        "ds_read_b64_tr_b16 v[210:211], %[qtr] offset:3840\n\t"
        "ds_read_b64_tr_b16 v[212:213], %[qtr] offset:4608\n\t"
        "ds_read_b64_tr_b16 v[214:215], %[qtr] offset:5376\n\t"
        "s_nop 15\n\t"
        "s_nop 5\n\t"
        "ds_read_b64_tr_b16 v[224:225], %[qtr] offset:7680\n\t"
        "ds_read_b64_tr_b16 v[226:227], %[qtr] offset:8448\n\t"
        "ds_read_b64_tr_b16 v[228:229], %[qtr] offset:10752\n\t"
        "ds_read_b64_tr_b16 v[230:231], %[qtr] offset:11520\n\t"
        "s_waitcnt lgkmcnt(2)\n\t"
        "v_mfma_f32_16x16x32_bf16 v[248:251], v[224:227], v[196:199], 0\n\t"
        "s_waitcnt lgkmcnt(0)\n\t"
        "v_mfma_f32_16x16x32_bf16 v[248:251], v[228:231], v[212:215], v[248:251]\n\t"
        "v_min3_u32 v203, v224, v226, v228\n\t"
        "v_min_u32 v203, v203, v230\n\t"
        "s_setprio 3\n\t"
        "s_barrier\n\t"
        "ds_read_b64_tr_b16 v[224:225], %[qtr] offset:6144\n\t"
        "ds_read_b64_tr_b16 v[226:227], %[qtr] offset:6912\n\t"
        "ds_read_b64_tr_b16 v[228:229], %[qtr] offset:9216\n\t"
        "ds_read_b64_tr_b16 v[230:231], %[qtr] offset:9984\n\t"
        "ds_read_b128 v[240:243], %[er]\n\t"
        "s_waitcnt lgkmcnt(3)\n\t"
        "v_mfma_f32_16x16x32_bf16 v[248:251], v[224:227], v[192:195], v[248:251]\n\t"
        "s_waitcnt lgkmcnt(1)\n\t"
        "v_mfma_f32_16x16x32_bf16 v[248:251], v[228:231], v[208:211], v[248:251]\n\t"
        "v_min3_u32 v203, v224, v226, v203\n\t"
        "s_waitcnt lgkmcnt(0)\n\t"
        "Lgo%=:\n\t"
        "v_mfma_f32_16x16x32_bf16 v[244:247], v[176:179], v[240:243], 0\n\t"
        "v_min3_u32 v203, v228, v230, v203\n\t"
        "v_cmp_ne_u32 s[96:97], %[fv], v203\n\t"
        "s_nop 0\n\t"
        "v_add_f32_dpp v248, v248, v248 row_ror:8 row_mask:0xf bank_mask:0xf\n\t"
        "v_add_f32_dpp v249, v249, v249 row_ror:8 row_mask:0xf bank_mask:0xf\n\t"
        "v_mul_f32 v248, %[ce], v248\n\t"
        "v_mul_f32 v249, %[ce], v249\n\t"
        "v_exp_f32 v250, v248\n\t"
        "v_exp_f32 v251, v249\n\t"
        "s_and_b64 s[96:97], s[96:97], %[jm]\n\t"
        "v_cvt_pk_bf16_f32 v236, v250, v251\n\t"
        "v_fma_f32 v250, v250, %[c1], %[ind0]\n\t"
        "v_fma_f32 v251, v251, %[c1], %[ind1]\n\t"
        "v_mfma_f32_16x16x16_bf16 v[244:247], v[176:177], v[236:237], v[244:247]\n\t"
        "s_cbranch_scc1 Lt%=\n\t"
        "s_nop 6\n\t"
        "v_rcp_f32 v246, v244\n\t"
        "v_cmp_gt_f32 vcc, %[big], v244\n\t"
        "v_pk_mul_f32 v[250:251], v[250:251], v[246:247] op_sel_hi:[1,0]\n\t"
        "v_cvt_pk_bf16_f32 v248, v250, v251\n\t"
        "v_cvt_pk_bf16_f32 v249, v250, v251\n\t"
        "s_nop 1\n\t"
        "v_permlane32_swap_b32 v248, v249\n\t"
        "s_mov_b64 exec, vcc\n\t"
        "buffer_store_dwordx2 v[248:249], %[po], %[rs], %[so] offen sc1\n\t"
        "buffer_store_dwordx2 v[248:249], %[po], %[rs], s93 offen sc1\n\t"
        "s_cmp_lg_u64 exec, -1\n\t"
        "s_mov_b64 exec, -1\n\t"
        "s_cselect_b32 %[bad], 1, 0\n\t"
        "s_branch Lend%=\n\t"
        "Lt%=:\n\t"
        "s_nop 7\n\t"
        "ds_read_b64_tr_b16 v[216:217], %[qtr] offset:6144\n\t"
        "ds_read_b64_tr_b16 v[218:219], %[qtr] offset:6912\n\t"
        "ds_read_b64_tr_b16 v[220:221], %[qtr] offset:7680\n\t"
        "ds_read_b64_tr_b16 v[222:223], %[qtr] offset:8448\n\t"
        "ds_read_b64_tr_b16 v[224:225], %[qtr] offset:9216\n\t"
        "ds_read_b64_tr_b16 v[226:227], %[qtr] offset:9984\n\t"
        "ds_read_b64_tr_b16 v[228:229], %[qtr] offset:10752\n\t"
        "ds_read_b64_tr_b16 v[230:231], %[qtr] offset:11520\n\t"
        "ds_read_b128 v[240:243], %[er]\n\t"
        "s_waitcnt lgkmcnt(0)\n\t"
        "v_mfma_f32_16x16x32_bf16 v[248:251], v[216:219], v[192:195], 0\n\t"
        "v_mfma_f32_16x16x32_bf16 v[248:251], v[220:223], v[196:199], v[248:251]\n\t"
        "v_mfma_f32_16x16x32_bf16 v[248:251], v[224:227], v[208:211], v[248:251]\n\t"
        "v_mfma_f32_16x16x32_bf16 v[248:251], v[228:231], v[212:215], v[248:251]\n\t"
        "v_min3_u32 v203, v216, v218, v220\n\t"
        "v_min3_u32 v203, v222, v224, v203\n\t"
        "v_min_u32 v203, v226, v203\n\t"
        "s_branch Lgo%=\n\t"
        "Lnan%=:\n\t"
        "v_mbcnt_lo_u32_b32 v202, -1, 0\n\t"
        "v_mbcnt_hi_u32_b32 v202, -1, v202\n\t"
        "v_lshl_add_u32 v207, v202, 7, %[lz]\n\t"
        "v_and_b32 v202, 31, v202\n\t"
        "v_lshl_add_u32 v207, v202, 2, v207\n\t"
        "ds_read2st64_b32 v[202:203], v207 offset1:32\n\t"
        "s_waitcnt lgkmcnt(0)\n\t"
        "s_mov_b64 exec, 0x00ff00ff\n\t"
        "v_max_u32 v202, v202, v203\n\t"
        "s_mov_b64 exec, -1\n\t"
        "v_cmp_eq_u32 vcc, 0xffbfffbf, v202\n\t"
        "s_cbranch_vccnz Lredo%=\n\t"
        "v_max_u32 v207, v208, v209\n\t"
        "v_max3_u32 v207, v207, v210, v211\n\t"
        "v_max3_u32 v207, v207, v212, v213\n\t"
        "v_max3_u32 v207, v207, v214, v215\n\t"
        "v_max3_u32 v207, v207, v216, v217\n\t"
        "v_max3_u32 v207, v207, v218, v219\n\t"
        "v_max3_u32 v207, v207, v220, v221\n\t"
        "v_max3_u32 v207, v207, v222, v223\n\t"
        "v_max3_u32 v207, v207, v224, v225\n\t"
        "v_max3_u32 v207, v207, v226, v227\n\t"
        "v_max3_u32 v207, v207, v228, v229\n\t"
        "v_max3_u32 v207, v207, v230, v231\n\t"
        "v_max3_u32 v207, v207, v232, v233\n\t"
        "v_max3_u32 v207, v207, v234, v235\n\t"
        "v_max3_u32 v207, v207, v236, v237\n\t"
        "v_max3_u32 v207, v207, v238, v239\n\t"
        "v_max3_u32 v207, v207, v240, v241\n\t"
        "v_max3_u32 v207, v207, v242, v243\n\t"
        "v_max3_u32 v207, v207, v244, v245\n\t"
        "v_max3_u32 v207, v207, v246, v247\n\t"
        "v_cmp_eq_u32 vcc, 0xffbfffbf, v207\n\t"
        "s_cbranch_vccz Lok%=\n\t"
        "Lredo%=:\n\t"
        "s_waitcnt lgkmcnt(0)\n\t"
        "v_add_u32 v206, %[dl], v206\n\t"
        "v_mov_b64 v[248:249], 0\n\t"
        "v_mov_b64 v[250:251], 0\n\t"
        "v_mov_b64 v[252:253], 0\n\t"
        "v_mov_b64 v[254:255], 0\n\t"
        "s_add_u32 %[lz], %[lz], %[dl]\n\t"
        "s_sub_u32 %[dl], 0, %[dl]\n\t"
        "s_setprio 2\n\t"
        "s_barrier\n\t"
        "s_branch Lb1%=\n\t"
        "Lend%=:\n\t"
        : [lz] "+&s"(lz), [dl] "=&s"(dl), [bad] "=&s"(bad)
        : [w0] "{v[16:47]}"(W[0]), [w1] "{v[48:79]}"(W[1]), [w2] "{v[80:111]}"(W[2]), [w3] "{v[112:143]}"(W[3]), [w4] "{v[144:175]}"(W[4]), [va] "v"(va), [sidx] "v"(sidx), [qw] "v"(qw), [kw] "v"(kw), [fv] "s"(fv), [ones] "{v[176:179]}"(ones), [qtr] "v"(qtr), [er] "v"(er), [po] "v"(po), [c1] "v"(c1), [ind0] "v"(ind0), [ind1] "v"(ind1), [rs] "s"(rs), [so] "s"(so), [ce] "s"(ce), [big] "s"(big), [jm] "s"(jm)
        : "s92", "s93", "s94", "s95", "s96", "s97", "v192", "v193", "v194", "v195", "v196", "v197", "v198", "v199", "v200", "v201", "v202", "v203", "v204", "v205", "v206", "v207", "v208", "v209", "v210", "v211", "v212", "v213", "v214", "v215", "v216", "v217", "v218", "v219", "v220", "v221", "v222", "v223", "v224", "v225", "v226", "v227", "v228", "v229", "v230", "v231", "v232", "v233", "v234", "v235", "v236", "v237", "v238", "v239", "v240", "v241", "v242", "v243", "v244", "v245", "v246", "v247", "v248", "v249", "v250", "v251", "v252", "v253", "v254", "v255", "vcc", "scc", "memory");
}

__device__ __forceinline__ void s_m(const u32x32 (&W)[5], u32 va, u32 lz, u32 sidx, u32 qw, u32 kw, u32 fv, u32& left, u32& poll, u32& lv, u32& lim, u32& tg, u32& irr, u32 ki, u32 off, u32 st, u32 tol, u32 tol2, u32 qp, u32 qg, u32 esc, u32 rn, u32 rkn, u32 so2, u32& c0, u32& r0, u32& te, u32& tn, u32& gb, u32 gx, u32 lev, u32 own, u32 gp, u32 lk, u32 kr, u32 dw, u32 df, u32 vm, u32 so, u32 lb, u32x4 rs, u32 tso, u32 tsl, u32 sn, u32 tsw, u32 ro, u32 rb, u32 rk STP) {
    u32 dl;
    asm volatile(
        ".p2align 6\n\t"
        "s_mov_b32 s94, m0\n\t"
        "s_add_u32 s92, %[so], 0x8000\n\t"
        "s_add_u32 s93, %[so], 0x10000\n\t"
        "s_mov_b32 m0, %[lb]\n\t"
        "s_setprio 2\n\t"
        "s_mov_b32 %[lv], 0\n\t"
        "s_cmp_lg_u32 %[poll], 1\n\t"
        "s_cbranch_scc1 Lpb%=\n\t"
        "v_lshrrev_b32 v201, 2, %[vm]\n\t"
        "v_add_u32 v201, %[lb], v201\n\t"
        "s_waitcnt vmcnt(0)\n\t"
        "ds_read_b32 v200, v201 offset:29696\n\t"
        "s_cmp_lg_u32 %[own], 0\n\t"
        "s_mov_b64 s[96:97], 0\n\t"
        "s_mov_b32 s98, %[rb]\n\t"
        "s_mov_b32 s99, 0x2000\n\t"
        "s_waitcnt lgkmcnt(0)\n\t"
        "v_cmp_gt_i32 vcc, s96, v200\n\t"
        "s_cbranch_scc1 Ls%=\n\t"
        "s_cbranch_vccnz Ls%=\n\t"
        "Lg%=:\n\t"
        "s_cmp_lg_u32 s96, 0\n\t"
        "s_cselect_b32 s95, %[gx], 0\n\t"
        "s_add_u32 s95, s95, %[te]\n\t"
        "s_add_u32 s95, s95, %[gp]\n\t"
        "s_add_u32 s95, s95, %[gb]\n\t"
        "Lda_%=:\n\t"
        "s_memrealtime s[90:91]\n\t"
        "s_waitcnt lgkmcnt(0)\n\t"
        "s_mul_i32 s90, s90, 24\n\t"
        "s_sub_u32 s88, s95, s90\n\t"
        "s_sub_u32 s89, s88, 620\n\t"
        "s_cmp_gt_u32 s89, 0x4000\n\t"
        "s_cbranch_scc1 Lea_%=\n\t"
        "s_sleep 1\n\t"
        "s_branch Lda_%=\n\t"
        "Lea_%=:\n\t"
        "s_memrealtime s[64:65]\n\t"
        "s_nop 0\n\t"
        "s_memrealtime s[66:67]\n\t"
        "s_nop 0\n\t"
        "s_memrealtime s[68:69]\n\t"
        "s_nop 0\n\t"
        "s_memrealtime s[70:71]\n\t"
        "s_nop 0\n\t"
        "s_memrealtime s[72:73]\n\t"
        "s_nop 0\n\t"
        "s_memrealtime s[74:75]\n\t"
        "s_memtime s[90:91]\n\t"
        "s_memrealtime s[76:77]\n\t"
        "s_nop 0\n\t"
        "s_memrealtime s[78:79]\n\t"
        "s_nop 0\n\t"
        "s_memrealtime s[80:81]\n\t"
        "s_nop 0\n\t"
        "s_memrealtime s[82:83]\n\t"
        "s_nop 0\n\t"
        "s_memrealtime s[84:85]\n\t"
        "s_nop 0\n\t"
        "s_memrealtime s[86:87]\n\t"
        "s_nop 0\n\t"
        "s_nop 7\n\t"
        "s_waitcnt lgkmcnt(0)\n\t"
        "s_add_u32 s64, s64, s66\n\t"
        "s_add_u32 s64, s64, s68\n\t"
        "s_add_u32 s64, s64, s70\n\t"
        "s_add_u32 s64, s64, s72\n\t"
        "s_add_u32 s64, s64, s74\n\t"
        "s_add_u32 s64, s64, s76\n\t"
        "s_add_u32 s64, s64, s78\n\t"
        "s_add_u32 s64, s64, s80\n\t"
        "s_add_u32 s64, s64, s82\n\t"
        "s_add_u32 s64, s64, s84\n\t"
        "s_add_u32 s64, s64, s86\n\t"
        "s_lshl_b32 s64, s64, 1\n\t"
        "s_mov_b32 %[c0], s90\n\t"
        "s_mov_b32 %[r0], s64\n\t"
        "s_sub_u32 s88, s95, s64\n\t"
        "s_mul_i32 s88, s88, %[kr]\n\t"
        "s_ashr_i32 s88, s88, 12\n\t"
        "s_sub_u32 s88, s88, 244\n\t"
        "s_nop 1\n\t"
        "s_max_i32 s88, s88, 0\n\t"
        "s_min_i32 s88, s88, 384\n\t"
        "s_andn2_b32 s88, s88, 3\n\t"
        "s_getpc_b64 s[90:91]\n\t"
        "Lga_%=:\n\t"
        "s_sub_u32 s90, s90, s88\n\t"
        "s_subb_u32 s91, s91, 0\n\t"
        "s_add_u32 s90, s90, Lza_%=-Lga_%=\n\t"
        "s_addc_u32 s91, s91, 0\n\t"
        "s_setpc_b64 s[90:91]\n\t"
        "s_nop 0\n\ts_nop 0\n\ts_nop 0\n\ts_nop 0\n\t"
        "s_nop 0\n\ts_nop 0\n\ts_nop 0\n\ts_nop 0\n\t"
        "s_nop 0\n\ts_nop 0\n\ts_nop 0\n\ts_nop 0\n\t"
        "s_nop 0\n\ts_nop 0\n\ts_nop 0\n\ts_nop 0\n\t"
        "s_nop 0\n\ts_nop 0\n\ts_nop 0\n\ts_nop 0\n\t"
        "s_nop 0\n\ts_nop 0\n\ts_nop 0\n\ts_nop 0\n\t"
        "s_nop 0\n\ts_nop 0\n\ts_nop 0\n\ts_nop 0\n\t"
        "s_nop 0\n\ts_nop 0\n\ts_nop 0\n\ts_nop 0\n\t"
        "s_nop 0\n\ts_nop 0\n\ts_nop 0\n\ts_nop 0\n\t"
        "s_nop 0\n\ts_nop 0\n\ts_nop 0\n\ts_nop 0\n\t"
        "s_nop 0\n\ts_nop 0\n\ts_nop 0\n\ts_nop 0\n\t"
        "s_nop 0\n\ts_nop 0\n\ts_nop 0\n\ts_nop 0\n\t"
        "s_nop 0\n\ts_nop 0\n\ts_nop 0\n\ts_nop 0\n\t"
        "s_nop 0\n\ts_nop 0\n\ts_nop 0\n\ts_nop 0\n\t"
        "s_nop 0\n\ts_nop 0\n\ts_nop 0\n\ts_nop 0\n\t"
        "s_nop 0\n\ts_nop 0\n\ts_nop 0\n\ts_nop 0\n\t"
        "s_nop 0\n\ts_nop 0\n\ts_nop 0\n\ts_nop 0\n\t"
        "s_nop 0\n\ts_nop 0\n\ts_nop 0\n\ts_nop 0\n\t"
        "s_nop 0\n\ts_nop 0\n\ts_nop 0\n\ts_nop 0\n\t"
        "s_nop 0\n\ts_nop 0\n\ts_nop 0\n\ts_nop 0\n\t"
        "s_nop 0\n\ts_nop 0\n\ts_nop 0\n\ts_nop 0\n\t"
        "s_nop 0\n\ts_nop 0\n\ts_nop 0\n\ts_nop 0\n\t"
        "s_nop 0\n\ts_nop 0\n\ts_nop 0\n\ts_nop 0\n\t"
        "s_nop 0\n\ts_nop 0\n\ts_nop 0\n\ts_nop 0\n\t"
        "Lza_%=:\n\t"
        "buffer_load_dwordx4 %[vm], %[rs], %[so] offen offset:0 sc1 lds\n\t"
        "buffer_load_dwordx4 %[vm], %[rs], %[so] offen offset:1024 sc1 lds\n\t"
        "buffer_load_dwordx4 %[vm], %[rs], %[so] offen offset:2048 sc1 lds\n\t"
        "buffer_load_dwordx4 %[vm], %[rs], %[so] offen offset:3072 sc1 lds\n\t"
        "s_add_u32 m0, m0, 0x1000\n\t"
        "s_nop 0\n\t"
        "buffer_load_dwordx4 %[vm], %[rs], s92 offen offset:0 sc1 lds\n\t"
        "buffer_load_dwordx4 %[vm], %[rs], s92 offen offset:1024 sc1 lds\n\t"
        "buffer_load_dwordx4 %[vm], %[rs], s92 offen offset:2048 sc1 lds\n\t"
        "buffer_load_dwordx4 %[vm], %[rs], s92 offen offset:3072 sc1 lds\n\t"
        "s_add_u32 m0, m0, 0x1000\n\t"
        "s_nop 0\n\t"
        "buffer_load_dwordx4 %[vm], %[rs], s93 offen offset:0 sc1 lds\n\t"
        "buffer_load_dwordx4 %[vm], %[rs], s93 offen offset:2048 sc1 lds\n\t"
        "s_cmp_lg_u32 s96, 0\n\t"
        "s_cbranch_scc1 Lnn%=\n\t"
        "v_readlane_b32 %[gb], v200, 0\n\t"
        "s_and_b32 %[gb], %[gb], 15\n\t"
        "s_lshl_b32 %[gb], %[gb], 4\n\t"
        "v_subrev_u32 v200, %[tn], v200\n\t"
        "v_bfe_i32 v200, v200, 0, 30\n\t"
        "s_nop 1\n\t"
        "v_max_i32_dpp v200, v200, v200 quad_perm:[1,0,3,2] row_mask:0xf bank_mask:0xf\n\t"
        "s_nop 1\n\t"
        "v_max_i32_dpp v200, v200, v200 quad_perm:[2,3,0,1] row_mask:0xf bank_mask:0xf\n\t"
        "s_nop 1\n\t"
        "v_max_i32_dpp v200, v200, v200 row_ror:4 row_mask:0xf bank_mask:0xf\n\t"
        "s_nop 1\n\t"
        "v_max_i32_dpp v200, v200, v200 row_ror:8 row_mask:0xf bank_mask:0xf\n\t"
        "s_nop 0\n\t"
        "v_readlane_b32 s88, v200, 0\n\t"
        "v_readlane_b32 s89, v200, 16\n\t"
        "s_max_i32 s88, s88, s89\n\t"
        "s_add_u32 %[tn], %[tn], s88\n\t"
        "Lnn%=:\n\t"
        "s_add_u32 s88, %[tn], %[dw]\n\t"
        "s_add_u32 s88, s88, %[gb]\n\t"
        "s_add_u32 s89, %[tn], %[gp]\n\t"
        "s_sub_u32 s88, s88, %[lev]\n\t"
        "s_and_b32 s88, s88, 0x3ffffff0\n\t"
        "s_or_b32 s88, s88, %[lev]\n\t"
        "v_mov_b32 v193, %[sn]\n\t"
        "v_mov_b32 v194, s89\n\t"
        "v_mov_b32 v196, s88\n\t"
        "s_mov_b32 exec_hi, 0\n\t"
        "buffer_store_dwordx4 v[196:199], %[tsw], %[rs], %[tso] offen sc1\n\t"
        "buffer_store_dwordx4 v[192:195], %[tsw], %[rs], %[tsl] offen sc1\n\t"
        "s_mov_b32 exec_hi, -1\n\t"
        "s_mov_b32 %[te], %[tn]\n\t"
        "s_add_u32 %[tn], %[tn], %[df]\n\t"
        "s_add_u32 %[tn], %[tn], %[gb]\n\t"
        "s_mov_b32 %[irr], s96\n\t"
        "s_cmp_lg_u32 s96, 0\n\t"
        "s_cselect_b32 s88, %[esc], 0\n\t"
        "s_add_u32 %[qg], %[qg], s88\n\t"
        "s_add_u32 %[qp], %[qp], s88\n\t"
        "s_add_u32 %[tg], s95, %[st]\n\t"
        "s_sub_u32 %[lim], %[tg], %[r0]\n\t"
        "s_mul_i32 %[lim], %[lim], %[kr]\n\t"
        "s_ashr_i32 %[lim], %[lim], 12\n\t"
        "s_add_u32 %[lim], %[lim], %[c0]\n\t"
        "Lpb%=:\n\t"
        "s_mov_b64 s[96:97], 0xffffffff\n\t"
        "s_mov_b32 s97, 0\n\t"
        "v_mov_b32 v206, %[va]\n\t"
        "s_mov_b32 %[dl], 81920\n\t"
        "v_mov_b64 v[248:249], 0\n\t"
        "v_mov_b64 v[250:251], 0\n\t"
        "v_mov_b64 v[252:253], 0\n\t"
        "v_mov_b64 v[254:255], 0\n\t"
        "v_mov_b32 v205, %[fv]\n\t"
        "v_mbcnt_lo_u32_b32 v202, -1, 0\n\t"
        "v_mbcnt_hi_u32_b32 v202, -1, v202\n\t"
        "v_lshl_add_u32 v187, v202, 7, %[lz]\n\t"
        "v_and_b32 v202, 31, v202\n\t"
        "v_lshl_add_u32 v187, v202, 2, v187\n\t"
        "s_add_u32 s88, %[te], %[qg]\n\t"
        "s_and_b32 s88, s88, 0x3ffffffe\n\t"
        "s_bitset1_b32 s88, 31\n\t"
        "s_cmp_lt_u32 %[lv], 4\n\t"
        "s_cselect_b32 s88, s88, 0xbfffffff\n\t"
        "v_mov_b32 v188, s88\n\t"
        "s_add_u32 s95, %[lim], %[tol]\n\t"
        "s_mov_b32 m0, s94\n\t"
        "s_waitcnt vmcnt(2)\n\t"
        "Lb1%=:\n\t"
        "ds_read2st64_b32 v[202:203], v187 offset1:32\n\t"
        "s_barrier\n\t"
        "s_memtime s[90:91]\n\t"
        "s_waitcnt lgkmcnt(0)\n\t"
        "s_mov_b64 exec, 0x00ff00ff\n\t"
        "v_max_u32 v202, v202, v203\n\t"
        "s_mov_b64 exec, -1\n\t"
        "v_cmp_eq_u32 vcc, 0xffbfffbf, v202\n\t"
        "s_sub_u32 s88, s95, s90\n\t"
        "s_cmp_lt_i32 s88, 0\n\t"
        "ds_read_b128 v[208:211], v206 offset:0\n\t"
        "ds_read_b128 v[212:215], v206 offset:1024\n\t"
        "s_cbranch_vccnz Lbad%=\n\t"
        "s_cbranch_scc1 Llate%=\n\t"
        "Lfr%=:\n\t"
        "ds_read_b128 v[216:219], v206 offset:2048\n\t"
        "ds_read_b128 v[220:223], v206 offset:3072\n\t"
        "ds_read_b128 v[224:227], v206 offset:4096\n\t"
        "ds_read_b128 v[228:231], v206 offset:5120\n\t"
        "s_waitcnt lgkmcnt(5)\n\t"
        "v_smfmac_f32_16x16x64_bf16 v[248:251], v[208:211], v[16:23], %[sidx]\n\t"
        "ds_read_b128 v[232:235], v206 offset:6144\n\t"
        "s_waitcnt lgkmcnt(5)\n\t"
        "v_smfmac_f32_16x16x64_bf16 v[248:251], v[212:215], v[24:31], %[sidx]\n\t"
        "ds_read_b128 v[236:239], v206 offset:7168\n\t"
        "s_waitcnt lgkmcnt(5)\n\t"
        "v_smfmac_f32_16x16x64_bf16 v[248:251], v[216:219], v[32:39], %[sidx]\n\t"
        "ds_read_b128 v[240:243], v206 offset:8192\n\t"
        "s_waitcnt lgkmcnt(5)\n\t"
        "v_smfmac_f32_16x16x64_bf16 v[248:251], v[220:223], v[40:47], %[sidx]\n\t"
        "ds_read_b128 v[244:247], v206 offset:10240\n\t"
        "s_waitcnt lgkmcnt(5)\n\t"
        "v_smfmac_f32_16x16x64_bf16 v[248:251], v[224:227], v[48:55], %[sidx]\n\t"
        "s_waitcnt lgkmcnt(4)\n\t"
        "v_smfmac_f32_16x16x64_bf16 v[248:251], v[228:231], v[56:63], %[sidx]\n\t"
        "s_waitcnt lgkmcnt(3)\n\t"
        "v_smfmac_f32_16x16x64_bf16 v[248:251], v[232:235], v[64:71], %[sidx]\n\t"
        "s_waitcnt lgkmcnt(2)\n\t"
        "v_smfmac_f32_16x16x64_bf16 v[248:251], v[236:239], v[72:79], %[sidx]\n\t"
        "s_waitcnt lgkmcnt(1)\n\t"
        "v_smfmac_f32_16x16x64_bf16 v[248:251], v[240:243], v[80:87], %[sidx]\n\t"
        "s_waitcnt lgkmcnt(0)\n\t"
        "v_smfmac_f32_16x16x64_bf16 v[248:251], v[244:247], v[88:95], %[sidx]\n\t"
        "v_smfmac_f32_16x16x64_bf16 v[252:255], v[208:211], v[96:103], %[sidx]\n\t"
        "v_smfmac_f32_16x16x64_bf16 v[252:255], v[212:215], v[104:111], %[sidx]\n\t"
        "v_cvt_pk_bf16_f32 v200, v248, v249\n\t"
        "v_cvt_pk_bf16_f32 v201, v250, v251\n\t"
        "ds_write2_b32 %[qw], v200, v201 offset1:96\n\t"
        "v_fma_f32 v202, v248, v249, v250\n\t"
        "v_smfmac_f32_16x16x64_bf16 v[252:255], v[216:219], v[112:119], %[sidx]\n\t"
        "v_cmp_u_f32 vcc, v202, v251\n\t"
        "v_smfmac_f32_16x16x64_bf16 v[252:255], v[220:223], v[120:127], %[sidx]\n\t"
        "s_waitcnt lgkmcnt(0)\n\t"
        "s_cbranch_vccnz Lnan%=\n\t"
        "Lok%=:\n\t"
        "s_barrier\n\t"
        "v_smfmac_f32_16x16x64_bf16 v[252:255], v[224:227], v[128:135], %[sidx]\n\t"
        "v_smfmac_f32_16x16x64_bf16 v[252:255], v[228:231], v[136:143], %[sidx]\n\t"
        "v_smfmac_f32_16x16x64_bf16 v[252:255], v[232:235], v[144:151], %[sidx]\n\t"
        "v_smfmac_f32_16x16x64_bf16 v[252:255], v[236:239], v[152:159], %[sidx]\n\t"
        "v_smfmac_f32_16x16x64_bf16 v[252:255], v[240:243], v[160:167], %[sidx]\n\t"
        "v_smfmac_f32_16x16x64_bf16 v[252:255], v[244:247], v[168:175], %[sidx]\n\t"
        "s_add_u32 m0, %[lb], 29696\n\t"
        "s_cmp_lg_u32 %[lv], 0\n\t"
        "buffer_load_dword %[ro], %[rs], %[rn] offen sc1 lds\n\t"
        "s_nop 4\n\t"
        "v_add_f32 v252, v252, v254\n\t"
        "v_add_f32 v253, v253, v255\n\t"
        "v_cvt_pk_bf16_f32 v204, v252, v253\n\t"
        "ds_write_b64 %[kw], v[204:205]\n\t"
        "s_barrier\n\t"
        "s_cbranch_scc1 Lmore%=\n\t"
        "s_mov_b32 m0, s94\n\t"
        "s_branch Lend%=\n\t"
        "Lbad%=:\n\t"
        "s_cmp_gt_u32 %[lv], 4\n\t"
        "s_cselect_b32 s88, 0x80000000, %[tso]\n\t"
        "s_mov_b64 exec, s[96:97]\n\t"
        "buffer_store_dwordx4 v[188:191], %[tsw], %[rs], s88 offen sc1\n\t"
        "s_mov_b64 exec, -1\n\t"
        "s_mov_b32 s92, %[so]\n\t"
        "s_mov_b32 %[so], %[so2]\n\t"
        "s_mov_b32 %[so2], s92\n\t"
        "s_add_u32 s92, %[so], 0x8000\n\t"
        "s_add_u32 s93, %[so], 0x10000\n\t"
        "s_add_u32 m0, %[lz], %[dl]\n\t"
        "s_sub_i32 %[left], %[left], 1\n\t"
        "s_cmp_lt_i32 %[left], 0\n\t"
        "s_cbranch_scc1 Lgu%=\n\t"
        "buffer_load_dwordx4 %[vm], %[rs], %[so] offen offset:0 sc1 lds\n\t"
        "buffer_load_dwordx4 %[vm], %[rs], %[so] offen offset:1024 sc1 lds\n\t"
        "buffer_load_dwordx4 %[vm], %[rs], %[so] offen offset:2048 sc1 lds\n\t"
        "buffer_load_dwordx4 %[vm], %[rs], %[so] offen offset:3072 sc1 lds\n\t"
        "s_add_u32 m0, m0, 0x1000\n\t"
        "s_nop 0\n\t"
        "buffer_load_dwordx4 %[vm], %[rs], s92 offen offset:0 sc1 lds\n\t"
        "buffer_load_dwordx4 %[vm], %[rs], s92 offen offset:1024 sc1 lds\n\t"
        "buffer_load_dwordx4 %[vm], %[rs], s92 offen offset:2048 sc1 lds\n\t"
        "buffer_load_dwordx4 %[vm], %[rs], s92 offen offset:3072 sc1 lds\n\t"
        "s_add_u32 m0, m0, 0x1000\n\t"
        "s_nop 0\n\t"
        "buffer_load_dwordx4 %[vm], %[rs], s93 offen offset:0 sc1 lds\n\t"
        "buffer_load_dwordx4 %[vm], %[rs], s93 offen offset:2048 sc1 lds\n\t"
        "Lgv%=:\n\t"
        "s_mov_b32 m0, s94\n\t"
        "s_add_u32 %[lim], %[lim], %[qp]\n\t"
        "s_add_u32 %[lv], %[lv], 1\n\t"
        "s_cmp_gt_u32 %[lv], 4\n\t"
        "s_cselect_b32 s88, 0, %[qg]\n\t"
        "s_cselect_b32 %[lim], %[c0], %[lim]\n\t"
        "s_add_u32 %[te], %[te], s88\n\t"
        "s_add_u32 %[tn], %[tn], s88\n\t"
        "s_cmp_eq_u32 %[lv], 1\n\t"
        "s_cselect_b32 s88, 700, 0\n\t"
        "s_add_u32 %[qg], %[qg], s88\n\t"
        "s_add_u32 %[qp], %[qp], s88\n\t"
        "s_mov_b32 %[tol], %[tol2]\n\t"
        "s_min_u32 s89, %[lv], 4\n\t"
        "s_lshl_b32 s89, s89, 3\n\t"
        "s_add_u32 s89, s89, 24\n\t"
        "s_lshl_b64 s[96:97], 0xff, s89\n\t"
        "s_add_u32 s88, %[te], %[qg]\n\t"
        "s_and_b32 s88, s88, 0x3ffffffe\n\t"
        "s_bitset1_b32 s88, 31\n\t"
        "s_cmp_lt_u32 %[lv], 4\n\t"
        "s_cselect_b32 s88, s88, 0xbfffffff\n\t"
        "v_mov_b32 v188, s88\n\t"
        "s_add_u32 s95, %[lim], %[tol]\n\t"
        "s_branch Lredo%=\n\t"
        "Llate%=:\n\t"
        "s_cmp_gt_u32 %[lv], 4\n\t"
        "s_cselect_b32 s91, 1536, 0\n\t"
        "s_cmp_eq_u32 %[sn], 0\n\t"
        "s_cselect_b32 s91, 1024, s91\n\t"
        "s_sub_u32 s88, s90, %[c0]\n\t"
        "s_min_u32 s88, s88, 0x3ffff\n\t"
        "s_mul_i32 s88, s88, %[ki]\n\t"
        "s_lshr_b32 s88, s88, 12\n\t"
        "s_add_u32 s88, s88, %[r0]\n\t"
        "s_add_u32 s88, s88, %[off]\n\t"
        "s_add_u32 s88, s88, s91\n\t"
        "s_sub_u32 s88, s88, %[te]\n\t"
        "s_max_i32 s88, s88, 500\n\t"
        "s_add_u32 s88, s88, %[te]\n\t"
        "s_and_b32 s89, s88, 0x3ffffffd\n\t"
        "s_or_b32 s89, s89, 0x80000001\n\t"
        "v_mov_b32 v188, s89\n\t"
        "s_mov_b64 exec, s[96:97]\n\t"
        "buffer_store_dwordx4 v[188:191], %[tsw], %[rs], %[tso] offen sc1\n\t"
        "s_mov_b64 exec, -1\n\t"
        "s_mov_b32 %[poll], 4\n\t"
        "s_sub_u32 s89, s88, %[te]\n\t"
        "s_max_i32 s89, s89, 0\n\t"
        "s_add_u32 %[te], %[te], s89\n\t"
        "s_add_u32 %[tn], %[tn], s89\n\t"
        "s_branch Lfr%=\n\t"
        "Lmore%=:\n\t"
        "s_add_u32 m0, m0, 0x100\n\t"
        "s_add_u32 s88, %[rkn], 0\n\t"
        "buffer_load_dword %[ro], %[rs], s88 offen sc1 lds\n\t"
        "s_cmp_eq_u32 %[lv], 1\n\t"
        "s_cbranch_scc1 Lmo%=\n\t"
        "s_add_u32 m0, m0, 0x100\n\t"
        "s_add_u32 s88, %[rkn], 4096\n\t"
        "buffer_load_dword %[ro], %[rs], s88 offen sc1 lds\n\t"
        "s_cmp_eq_u32 %[lv], 2\n\t"
        "s_cbranch_scc1 Lmo%=\n\t"
        "s_add_u32 m0, m0, 0x100\n\t"
        "s_add_u32 s88, %[rkn], 16384\n\t"
        "buffer_load_dword %[ro], %[rs], s88 offen sc1 lds\n\t"
        "s_cmp_eq_u32 %[lv], 3\n\t"
        "s_cbranch_scc1 Lmo%=\n\t"
        "s_add_u32 m0, m0, 0x100\n\t"
        "s_add_u32 s88, %[rkn], 20480\n\t"
        "buffer_load_dword %[ro], %[rs], s88 offen sc1 lds\n\t"
        "Lmo%=:\n\t"
        "s_mov_b32 m0, s94\n\t"
        "s_branch Lend%=\n\t"
        "Lnan%=:\n\t"
        "v_max_u32 v207, v208, v209\n\t"
        "v_max3_u32 v207, v207, v210, v211\n\t"
        "v_max3_u32 v207, v207, v212, v213\n\t"
        "v_max3_u32 v207, v207, v214, v215\n\t"
        "v_max3_u32 v207, v207, v216, v217\n\t"
        "v_max3_u32 v207, v207, v218, v219\n\t"
        "v_max3_u32 v207, v207, v220, v221\n\t"
        "v_max3_u32 v207, v207, v222, v223\n\t"
        "v_max3_u32 v207, v207, v224, v225\n\t"
        "v_max3_u32 v207, v207, v226, v227\n\t"
        "v_max3_u32 v207, v207, v228, v229\n\t"
        "v_max3_u32 v207, v207, v230, v231\n\t"
        "v_max3_u32 v207, v207, v232, v233\n\t"
        "v_max3_u32 v207, v207, v234, v235\n\t"
        "v_max3_u32 v207, v207, v236, v237\n\t"
        "v_max3_u32 v207, v207, v238, v239\n\t"
        "v_max3_u32 v207, v207, v240, v241\n\t"
        "v_max3_u32 v207, v207, v242, v243\n\t"
        "v_max3_u32 v207, v207, v244, v245\n\t"
        "v_max3_u32 v207, v207, v246, v247\n\t"
        "v_cmp_eq_u32 vcc, 0xffbfffbf, v207\n\t"
        "s_cbranch_vccz Lok%=\n\t"
        "s_branch Lbad%=\n\t"
        "Lredo%=:\n\t"
        "s_waitcnt lgkmcnt(0)\n\t"
        "v_add_u32 v206, %[dl], v206\n\t"
        "v_add_u32 v187, %[dl], v187\n\t"
        "v_mov_b64 v[248:249], 0\n\t"
        "v_mov_b64 v[250:251], 0\n\t"
        "v_mov_b64 v[252:253], 0\n\t"
        "v_mov_b64 v[254:255], 0\n\t"
        "s_add_u32 %[lz], %[lz], %[dl]\n\t"
        "s_sub_u32 %[dl], 0, %[dl]\n\t"
        "s_setprio 2\n\t"
        "s_barrier\n\t"
        "s_waitcnt vmcnt(0)\n\t"
        "s_branch Lb1%=\n\t"
        "Lgu%=:\n\t"
        "v_add_u32 v207, m0, %[vm]\n\t"
        "v_mov_b64 v[196:197], 0\n\t"
        "v_mov_b64 v[198:199], 0\n\t"
        "ds_write_b128 v207, v[196:199] offset:0\n\t"
        "ds_write_b128 v207, v[196:199] offset:1024\n\t"
        "ds_write_b128 v207, v[196:199] offset:2048\n\t"
        "ds_write_b128 v207, v[196:199] offset:3072\n\t"
        "ds_write_b128 v207, v[196:199] offset:4096\n\t"
        "ds_write_b128 v207, v[196:199] offset:5120\n\t"
        "ds_write_b128 v207, v[196:199] offset:6144\n\t"
        "ds_write_b128 v207, v[196:199] offset:7168\n\t"
        "ds_write_b128 v207, v[196:199] offset:8192\n\t"
        "ds_write_b128 v207, v[196:199] offset:9216\n\t"
        "ds_write_b128 v207, v[196:199] offset:10240\n\t"
        "s_branch Lgv%=\n\t"
        "Ls%=:\n\t"
        "s_mov_b32 s90, 0xbfffffff\n\t"
        "s_cmp_eq_u32 s97, 0\n\t"
        "s_cselect_b32 s90, 0xffbfffbf, s90\n\t"
        "s_mov_b32 s96, 0xc0000000\n\t"
        "s_waitcnt vmcnt(0) lgkmcnt(0)\n\t"
        "v_cmp_eq_u32 s[88:89], s90, v200\n\t"
        "v_cmp_gt_i32 vcc, s96, v200\n\t"
        "v_and_b32 v202, 0xc0000001, v200\n\t"
        "v_subrev_u32 v200, %[te], v200\n\t"
        "s_sub_u32 s99, s99, 1\n\t"
        "s_cselect_b32 s88, 0, s88\n\t"
        "s_or_b32 s89, s88, vcc_lo\n\t"
        "s_cbranch_scc0 Lu%=\n\t"
        "v_bfe_i32 v200, v200, 0, 30\n\t"
        "s_cmp_lg_u32 s88, 0\n\t"
        "v_cndmask_b32 v200, 0, v200, vcc\n\t"
        "s_cbranch_scc1 Lq%=\n\t"
        "v_cmp_eq_u32 vcc, 0x80000000, v202\n\t"
        "s_and_b32 s98, s97, 1\n\t"
        "v_max_i32_dpp v200, v200, v200 quad_perm:[1,0,3,2] row_mask:0xf bank_mask:0xf\n\t"
        "s_lshl_b32 s98, s98, 12\n\t"
        "s_lshr_b32 s88, s97, 1\n\t"
        "v_max_i32_dpp v200, v200, v200 quad_perm:[2,3,0,1] row_mask:0xf bank_mask:0xf\n\t"
        "s_lshl_b32 s88, s88, 14\n\t"
        "s_add_u32 s98, s98, s88\n\t"
        "v_max_i32_dpp v200, v200, v200 row_ror:4 row_mask:0xf bank_mask:0xf\n\t"
        "s_add_u32 s98, s98, %[rk]\n\t"
        "s_add_u32 s97, s97, 1\n\t"
        "v_max_i32_dpp v200, v200, v200 row_ror:8 row_mask:0xf bank_mask:0xf\n\t"
        "s_nop 0\n\t"
        "v_readlane_b32 s88, v200, 0\n\t"
        "v_readlane_b32 s89, v200, 16\n\t"
        "s_max_i32 s88, s88, s89\n\t"
        "s_max_i32 s88, s88, 0\n\t"
        "s_add_u32 %[te], %[te], s88\n\t"
        "s_add_u32 %[tn], %[tn], s88\n\t"
        "s_cbranch_vccz Lu%=\n\t"
        "s_cmp_gt_u32 s97, 4\n\t"
        "s_cbranch_scc1 Lu%=\n\t"
        "v_add_u32 v201, 0x100, v201\n\t"
        "s_cmp_le_u32 s97, %[own]\n\t"
        "ds_read_b32 v200, v201 offset:29696\n\t"
        "s_cbranch_scc1 Ls%=\n\t"
        "s_sub_u32 s90, %[te], %[lk]\n\t"
        "Lt%=:\n\t"
        "s_memrealtime s[88:89]\n\t"
        "s_waitcnt lgkmcnt(0)\n\t"
        "s_mul_i32 s88, s88, 24\n\t"
        "s_sub_u32 s88, s90, s88\n\t"
        "s_cmp_lt_u32 s88, 0x4000\n\t"
        "s_cbranch_scc1 Lt%=\n\t"
        "s_add_u32 s89, %[rk], 20480\n\t"
        "s_cmp_lt_i32 s88, -6500\n\t"
        "s_cselect_b32 s97, 4, s97\n\t"
        "s_cselect_b32 s98, s89, s98\n\t"
        "v_mov_b32 v200, 0\n\t"
        "buffer_load_dword v200, %[ro], %[rs], s98 offen sc1\n\t"
        "s_branch Ls%=\n\t"
        "Lq%=:\n\t"
        "s_sleep 6\n\t"
        "buffer_load_dword v200, %[ro], %[rs], s98 offen sc1\n\t"
        "s_branch Ls%=\n\t"
        "Lu%=:\n\t"
        "v_mov_b32 v200, 0\n\t"
        "s_branch Lg%=\n\t"
        "Lend%=:\n\t"
        : [poll] "+&s"(poll), [lim] "+&s"(lim), [tg] "+&s"(tg), [tol] "+&s"(tol), [qg] "+&s"(qg), [qp] "+&s"(qp), [lv] "=&s"(lv), [so] "+&s"(so), [c0] "+&s"(c0), [r0] "+&s"(r0), [te] "+&s"(te), [tn] "+&s"(tn), [gb] "+&s"(gb), [irr] "+&s"(irr), [lz] "+&s"(lz), [dl] "=&s"(dl), [so2] "+&s"(so2), [left] "+&s"(left)
        : [ki] "s"(ki), [st] "s"(st), [tol2] "s"(tol2), [off] "s"(off), [rn] "s"(rn), [rkn] "s"(rkn), [esc] "s"(esc), [vm] "v"(vm), [lb] "s"(lb), [rs] "s"(rs), [own] "s"(own), [gp] "s"(gp), [kr] "s"(kr), [dw] "s"(dw), [df] "s"(df), [tso] "s"(tso), [tsl] "s"(tsl), [sn] "s"(sn), [tsw] "v"(tsw), [ro] "v"(ro), [rb] "s"(rb), [rk] "s"(rk), [lk] "s"(lk), [lev] "s"(lev), [gx] "s"(gx), [w0] "{v[16:47]}"(W[0]), [w1] "{v[48:79]}"(W[1]), [w2] "{v[80:111]}"(W[2]), [w3] "{v[112:143]}"(W[3]), [w4] "{v[144:175]}"(W[4]), [va] "v"(va), [sidx] "v"(sidx), [qw] "v"(qw), [kw] "v"(kw), [fv] "s"(fv)
        : "s64", "s65", "s66", "s67", "s68", "s69", "s70", "s71", "s72", "s73", "s74", "s75", "s76", "s77", "s78", "s79", "s80", "s81", "s82", "s83", "s84", "s85", "s86", "s87", "s88", "s89", "s90", "s91", "s92", "s93", "s94", "s95", "s96", "s97", "s98", "s99", "v187", "v188", "v189", "v190", "v191", "v192", "v193", "v194", "v195", "v196", "v197", "v198", "v199", "v200", "v201", "v202", "v203", "v204", "v205", "v206", "v207", "v208", "v209", "v210", "v211", "v212", "v213", "v214", "v215", "v216", "v217", "v218", "v219", "v220", "v221", "v222", "v223", "v224", "v225", "v226", "v227", "v228", "v229", "v230", "v231", "v232", "v233", "v234", "v235", "v236", "v237", "v238", "v239", "v240", "v241", "v242", "v243", "v244", "v245", "v246", "v247", "v248", "v249", "v250", "v251", "v252", "v253", "v254", "v255", "vcc", "scc", "memory");
}

__device__ __forceinline__ void s_g(u32& c0, u32& r0, u32& te, u32& tn, u32& gb, u32 gx, u32 lev, u32 own, u32 gp, u32 lk, u32 kr, u32 dw, u32 df, u32 vm, u32 so, u32 lb, u32x4 rs, u32 tso, u32 tsl, u32 sn, u32 tsw, u32 ro, u32 rb, u32 rk STP) {
    asm volatile(
        ".p2align 6\n\t"
        "s_mov_b32 s94, m0\n\t"
        "s_add_u32 s92, %[so], 0x8000\n\t"
        "s_add_u32 s93, %[so], 0x10000\n\t"
        "s_mov_b32 m0, %[lb]\n\t"
        "v_lshrrev_b32 v201, 2, %[vm]\n\t"
        "v_add_u32 v201, %[lb], v201\n\t"
        "s_waitcnt vmcnt(0)\n\t"
        "ds_read_b32 v200, v201 offset:29696\n\t"
        "s_cmp_lg_u32 %[own], 0\n\t"
        "s_mov_b64 s[96:97], 0\n\t"
        "s_mov_b32 s98, %[rb]\n\t"
        "s_mov_b32 s99, 0x2000\n\t"
        "s_waitcnt lgkmcnt(0)\n\t"
        "v_cmp_gt_i32 vcc, s96, v200\n\t"
        "s_cbranch_scc1 Ls%=\n\t"
        "s_cbranch_vccnz Ls%=\n\t"
        "Lg%=:\n\t"
        "s_cmp_lg_u32 s96, 0\n\t"
        "s_cselect_b32 s95, %[gx], 0\n\t"
        "s_add_u32 s95, s95, %[te]\n\t"
        "s_add_u32 s95, s95, %[gp]\n\t"
        "s_add_u32 s95, s95, %[gb]\n\t"
        "Lda_%=:\n\t"
        "s_memrealtime s[90:91]\n\t"
        "s_waitcnt lgkmcnt(0)\n\t"
        "s_mul_i32 s90, s90, 24\n\t"
        "s_sub_u32 s88, s95, s90\n\t"
        "s_sub_u32 s89, s88, 620\n\t"
        "s_cmp_gt_u32 s89, 0x4000\n\t"
        "s_cbranch_scc1 Lea_%=\n\t"
        "s_sleep 1\n\t"
        "s_branch Lda_%=\n\t"
        "Lea_%=:\n\t"
        "s_memrealtime s[64:65]\n\t"
        "s_nop 0\n\t"
        "s_memrealtime s[66:67]\n\t"
        "s_nop 0\n\t"
        "s_memrealtime s[68:69]\n\t"
        "s_nop 0\n\t"
        "s_memrealtime s[70:71]\n\t"
        "s_nop 0\n\t"
        "s_memrealtime s[72:73]\n\t"
        "s_nop 0\n\t"
        "s_memrealtime s[74:75]\n\t"
        "s_memtime s[90:91]\n\t"
        "s_memrealtime s[76:77]\n\t"
        "s_nop 0\n\t"
        "s_memrealtime s[78:79]\n\t"
        "s_nop 0\n\t"
        "s_memrealtime s[80:81]\n\t"
        "s_nop 0\n\t"
        "s_memrealtime s[82:83]\n\t"
        "s_nop 0\n\t"
        "s_memrealtime s[84:85]\n\t"
        "s_nop 0\n\t"
        "s_memrealtime s[86:87]\n\t"
        "s_nop 0\n\t"
        "s_nop 7\n\t"
        "s_waitcnt lgkmcnt(0)\n\t"
        "s_add_u32 s64, s64, s66\n\t"
        "s_add_u32 s64, s64, s68\n\t"
        "s_add_u32 s64, s64, s70\n\t"
        "s_add_u32 s64, s64, s72\n\t"
        "s_add_u32 s64, s64, s74\n\t"
        "s_add_u32 s64, s64, s76\n\t"
        "s_add_u32 s64, s64, s78\n\t"
        "s_add_u32 s64, s64, s80\n\t"
        "s_add_u32 s64, s64, s82\n\t"
        "s_add_u32 s64, s64, s84\n\t"
        "s_add_u32 s64, s64, s86\n\t"
        "s_lshl_b32 s64, s64, 1\n\t"
        "s_mov_b32 %[c0], s90\n\t"
        "s_mov_b32 %[r0], s64\n\t"
        "s_sub_u32 s88, s95, s64\n\t"
        "s_mul_i32 s88, s88, %[kr]\n\t"
        "s_ashr_i32 s88, s88, 12\n\t"
        "s_sub_u32 s88, s88, 244\n\t"
        "s_nop 1\n\t"
        "s_max_i32 s88, s88, 0\n\t"
        "s_min_i32 s88, s88, 384\n\t"
        "s_andn2_b32 s88, s88, 3\n\t"
        "s_getpc_b64 s[90:91]\n\t"
        "Lga_%=:\n\t"
        "s_sub_u32 s90, s90, s88\n\t"
        "s_subb_u32 s91, s91, 0\n\t"
        "s_add_u32 s90, s90, Lza_%=-Lga_%=\n\t"
        "s_addc_u32 s91, s91, 0\n\t"
        "s_setpc_b64 s[90:91]\n\t"
        "s_nop 0\n\ts_nop 0\n\ts_nop 0\n\ts_nop 0\n\t"
        "s_nop 0\n\ts_nop 0\n\ts_nop 0\n\ts_nop 0\n\t"
        "s_nop 0\n\ts_nop 0\n\ts_nop 0\n\ts_nop 0\n\t"
        "s_nop 0\n\ts_nop 0\n\ts_nop 0\n\ts_nop 0\n\t"
        "s_nop 0\n\ts_nop 0\n\ts_nop 0\n\ts_nop 0\n\t"
        "s_nop 0\n\ts_nop 0\n\ts_nop 0\n\ts_nop 0\n\t"
        "s_nop 0\n\ts_nop 0\n\ts_nop 0\n\ts_nop 0\n\t"
        "s_nop 0\n\ts_nop 0\n\ts_nop 0\n\ts_nop 0\n\t"
        "s_nop 0\n\ts_nop 0\n\ts_nop 0\n\ts_nop 0\n\t"
        "s_nop 0\n\ts_nop 0\n\ts_nop 0\n\ts_nop 0\n\t"
        "s_nop 0\n\ts_nop 0\n\ts_nop 0\n\ts_nop 0\n\t"
        "s_nop 0\n\ts_nop 0\n\ts_nop 0\n\ts_nop 0\n\t"
        "s_nop 0\n\ts_nop 0\n\ts_nop 0\n\ts_nop 0\n\t"
        "s_nop 0\n\ts_nop 0\n\ts_nop 0\n\ts_nop 0\n\t"
        "s_nop 0\n\ts_nop 0\n\ts_nop 0\n\ts_nop 0\n\t"
        "s_nop 0\n\ts_nop 0\n\ts_nop 0\n\ts_nop 0\n\t"
        "s_nop 0\n\ts_nop 0\n\ts_nop 0\n\ts_nop 0\n\t"
        "s_nop 0\n\ts_nop 0\n\ts_nop 0\n\ts_nop 0\n\t"
        "s_nop 0\n\ts_nop 0\n\ts_nop 0\n\ts_nop 0\n\t"
        "s_nop 0\n\ts_nop 0\n\ts_nop 0\n\ts_nop 0\n\t"
        "s_nop 0\n\ts_nop 0\n\ts_nop 0\n\ts_nop 0\n\t"
        "s_nop 0\n\ts_nop 0\n\ts_nop 0\n\ts_nop 0\n\t"
        "s_nop 0\n\ts_nop 0\n\ts_nop 0\n\ts_nop 0\n\t"
        "s_nop 0\n\ts_nop 0\n\ts_nop 0\n\ts_nop 0\n\t"
        "Lza_%=:\n\t"
        "buffer_load_dwordx4 %[vm], %[rs], %[so] offen offset:0 sc1 lds\n\t"
        "buffer_load_dwordx4 %[vm], %[rs], %[so] offen offset:1024 sc1 lds\n\t"
        "buffer_load_dwordx4 %[vm], %[rs], %[so] offen offset:2048 sc1 lds\n\t"
        "buffer_load_dwordx4 %[vm], %[rs], %[so] offen offset:3072 sc1 lds\n\t"
        "s_add_u32 m0, m0, 0x1000\n\t"
        "s_nop 0\n\t"
        "buffer_load_dwordx4 %[vm], %[rs], s92 offen offset:0 sc1 lds\n\t"
        "buffer_load_dwordx4 %[vm], %[rs], s92 offen offset:1024 sc1 lds\n\t"
        "buffer_load_dwordx4 %[vm], %[rs], s92 offen offset:2048 sc1 lds\n\t"
        "buffer_load_dwordx4 %[vm], %[rs], s92 offen offset:3072 sc1 lds\n\t"
        "s_add_u32 m0, m0, 0x1000\n\t"
        "s_nop 0\n\t"
        "buffer_load_dwordx4 %[vm], %[rs], s93 offen offset:0 sc1 lds\n\t"
        "buffer_load_dwordx4 %[vm], %[rs], s93 offen offset:2048 sc1 lds\n\t"
        "s_cmp_lg_u32 s96, 0\n\t"
        "s_cbranch_scc1 Lnn%=\n\t"
        "v_readlane_b32 %[gb], v200, 0\n\t"
        "s_and_b32 %[gb], %[gb], 15\n\t"
        "s_lshl_b32 %[gb], %[gb], 4\n\t"
        "v_subrev_u32 v200, %[tn], v200\n\t"
        "v_bfe_i32 v200, v200, 0, 30\n\t"
        "s_nop 1\n\t"
        "v_max_i32_dpp v200, v200, v200 quad_perm:[1,0,3,2] row_mask:0xf bank_mask:0xf\n\t"
        "s_nop 1\n\t"
        "v_max_i32_dpp v200, v200, v200 quad_perm:[2,3,0,1] row_mask:0xf bank_mask:0xf\n\t"
        "s_nop 1\n\t"
        "v_max_i32_dpp v200, v200, v200 row_ror:4 row_mask:0xf bank_mask:0xf\n\t"
        "s_nop 1\n\t"
        "v_max_i32_dpp v200, v200, v200 row_ror:8 row_mask:0xf bank_mask:0xf\n\t"
        "s_nop 0\n\t"
        "v_readlane_b32 s88, v200, 0\n\t"
        "v_readlane_b32 s89, v200, 16\n\t"
        "s_max_i32 s88, s88, s89\n\t"
        "s_add_u32 %[tn], %[tn], s88\n\t"
        "Lnn%=:\n\t"
        "s_add_u32 s88, %[tn], %[dw]\n\t"
        "s_add_u32 s88, s88, %[gb]\n\t"
        "s_add_u32 s89, %[tn], %[gp]\n\t"
        "s_sub_u32 s88, s88, %[lev]\n\t"
        "s_and_b32 s88, s88, 0x3ffffff0\n\t"
        "s_or_b32 s88, s88, %[lev]\n\t"
        "v_mov_b32 v193, %[sn]\n\t"
        "v_mov_b32 v194, s89\n\t"
        "v_mov_b32 v196, s88\n\t"
        "s_mov_b32 exec_hi, 0\n\t"
        "buffer_store_dwordx4 v[196:199], %[tsw], %[rs], %[tso] offen sc1\n\t"
        "buffer_store_dwordx4 v[192:195], %[tsw], %[rs], %[tsl] offen sc1\n\t"
        "s_mov_b32 exec_hi, -1\n\t"
        "s_mov_b32 %[te], %[tn]\n\t"
        "s_add_u32 %[tn], %[tn], %[df]\n\t"
        "s_add_u32 %[tn], %[tn], %[gb]\n\t"
        "s_mov_b32 m0, s94\n\t"
        "s_branch Lend%=\n\t"
        "Ls%=:\n\t"
        "s_mov_b32 s90, 0xbfffffff\n\t"
        "s_cmp_eq_u32 s97, 0\n\t"
        "s_cselect_b32 s90, 0xffbfffbf, s90\n\t"
        "s_mov_b32 s96, 0xc0000000\n\t"
        "s_waitcnt vmcnt(0) lgkmcnt(0)\n\t"
        "v_cmp_eq_u32 s[88:89], s90, v200\n\t"
        "v_cmp_gt_i32 vcc, s96, v200\n\t"
        "v_and_b32 v202, 0xc0000001, v200\n\t"
        "v_subrev_u32 v200, %[te], v200\n\t"
        "s_sub_u32 s99, s99, 1\n\t"
        "s_cselect_b32 s88, 0, s88\n\t"
        "s_or_b32 s89, s88, vcc_lo\n\t"
        "s_cbranch_scc0 Lu%=\n\t"
        "v_bfe_i32 v200, v200, 0, 30\n\t"
        "s_cmp_lg_u32 s88, 0\n\t"
        "v_cndmask_b32 v200, 0, v200, vcc\n\t"
        "s_cbranch_scc1 Lq%=\n\t"
        "v_cmp_eq_u32 vcc, 0x80000000, v202\n\t"
        "s_and_b32 s98, s97, 1\n\t"
        "v_max_i32_dpp v200, v200, v200 quad_perm:[1,0,3,2] row_mask:0xf bank_mask:0xf\n\t"
        "s_lshl_b32 s98, s98, 12\n\t"
        "s_lshr_b32 s88, s97, 1\n\t"
        "v_max_i32_dpp v200, v200, v200 quad_perm:[2,3,0,1] row_mask:0xf bank_mask:0xf\n\t"
        "s_lshl_b32 s88, s88, 14\n\t"
        "s_add_u32 s98, s98, s88\n\t"
        "v_max_i32_dpp v200, v200, v200 row_ror:4 row_mask:0xf bank_mask:0xf\n\t"
        "s_add_u32 s98, s98, %[rk]\n\t"
        "s_add_u32 s97, s97, 1\n\t"
        "v_max_i32_dpp v200, v200, v200 row_ror:8 row_mask:0xf bank_mask:0xf\n\t"
        "s_nop 0\n\t"
        "v_readlane_b32 s88, v200, 0\n\t"
        "v_readlane_b32 s89, v200, 16\n\t"
        "s_max_i32 s88, s88, s89\n\t"
        "s_max_i32 s88, s88, 0\n\t"
        "s_add_u32 %[te], %[te], s88\n\t"
        "s_add_u32 %[tn], %[tn], s88\n\t"
        "s_cbranch_vccz Lu%=\n\t"
        "s_cmp_gt_u32 s97, 4\n\t"
        "s_cbranch_scc1 Lu%=\n\t"
        "v_add_u32 v201, 0x100, v201\n\t"
        "s_cmp_le_u32 s97, %[own]\n\t"
        "ds_read_b32 v200, v201 offset:29696\n\t"
        "s_cbranch_scc1 Ls%=\n\t"
        "s_sub_u32 s90, %[te], %[lk]\n\t"
        "Lt%=:\n\t"
        "s_memrealtime s[88:89]\n\t"
        "s_waitcnt lgkmcnt(0)\n\t"
        "s_mul_i32 s88, s88, 24\n\t"
        "s_sub_u32 s88, s90, s88\n\t"
        "s_cmp_lt_u32 s88, 0x4000\n\t"
        "s_cbranch_scc1 Lt%=\n\t"
        "s_add_u32 s89, %[rk], 20480\n\t"
        "s_cmp_lt_i32 s88, -6500\n\t"
        "s_cselect_b32 s97, 4, s97\n\t"
        "s_cselect_b32 s98, s89, s98\n\t"
        "v_mov_b32 v200, 0\n\t"
        "buffer_load_dword v200, %[ro], %[rs], s98 offen sc1\n\t"
        "s_branch Ls%=\n\t"
        "Lq%=:\n\t"
        "s_sleep 6\n\t"
        "buffer_load_dword v200, %[ro], %[rs], s98 offen sc1\n\t"
        "s_branch Ls%=\n\t"
        "Lu%=:\n\t"
        "v_mov_b32 v200, 0\n\t"
        "s_branch Lg%=\n\t"
        "Lend%=:\n\t"
        : [so] "+&s"(so), [c0] "+&s"(c0), [r0] "+&s"(r0), [te] "+&s"(te), [tn] "+&s"(tn), [gb] "+&s"(gb)
        : [vm] "v"(vm), [lb] "s"(lb), [rs] "s"(rs), [own] "s"(own), [gp] "s"(gp), [kr] "s"(kr), [dw] "s"(dw), [df] "s"(df), [tso] "s"(tso), [tsl] "s"(tsl), [sn] "s"(sn), [tsw] "v"(tsw), [ro] "v"(ro), [rb] "s"(rb), [rk] "s"(rk), [lk] "s"(lk), [lev] "s"(lev), [gx] "s"(gx)
        : "s64", "s65", "s66", "s67", "s68", "s69", "s70", "s71", "s72", "s73", "s74", "s75", "s76", "s77", "s78", "s79", "s80", "s81", "s82", "s83", "s84", "s85", "s86", "s87", "s88", "s89", "s90", "s91", "s92", "s93", "s94", "s95", "s96", "s97", "s98", "s99", "v192", "v193", "v194", "v195", "v196", "v197", "v198", "v199", "v200", "v201", "v202", "vcc", "scc", "memory");
}

__device__ __forceinline__ void s_l(const u32x32 (&W)[5], u32 va, u32 lz, u32 sidx, u32 qw, u32 kw, u32 fv, u32& redo) {
    u32 dl;
    asm volatile(
        ".p2align 6\n\t"
        "v_mov_b32 v206, %[va]\n\t"
        "s_mov_b32 %[dl], 81920\n\t"
        "v_mov_b64 v[248:249], 0\n\t"
        "v_mov_b64 v[250:251], 0\n\t"
        "v_mov_b64 v[252:253], 0\n\t"
        "v_mov_b64 v[254:255], 0\n\t"
        "v_mov_b32 v205, %[fv]\n\t"
        "s_waitcnt vmcnt(0)\n\t"
        "s_barrier\n\t"
        "ds_read_b128 v[208:211], v206 offset:0\n\t"
        "ds_read_b128 v[212:215], v206 offset:1024\n\t"
        "ds_read_b128 v[216:219], v206 offset:2048\n\t"
        "ds_read_b128 v[220:223], v206 offset:3072\n\t"
        "ds_read_b128 v[224:227], v206 offset:4096\n\t"
        "ds_read_b128 v[228:231], v206 offset:5120\n\t"
        "ds_read_b128 v[232:235], v206 offset:6144\n\t"
        "ds_read_b128 v[236:239], v206 offset:7168\n\t"
        "ds_read_b128 v[240:243], v206 offset:8192\n\t"
        "ds_read_b128 v[244:247], v206 offset:10240\n\t"
        "s_waitcnt lgkmcnt(9)\n\t"
        "v_smfmac_f32_16x16x64_bf16 v[252:255], v[208:211], v[96:103], %[sidx]\n\t"
        "s_waitcnt lgkmcnt(8)\n\t"
        "v_smfmac_f32_16x16x64_bf16 v[252:255], v[212:215], v[104:111], %[sidx]\n\t"
        "s_waitcnt lgkmcnt(7)\n\t"
        "v_smfmac_f32_16x16x64_bf16 v[252:255], v[216:219], v[112:119], %[sidx]\n\t"
        "s_waitcnt lgkmcnt(6)\n\t"
        "v_smfmac_f32_16x16x64_bf16 v[252:255], v[220:223], v[120:127], %[sidx]\n\t"
        "s_waitcnt lgkmcnt(5)\n\t"
        "v_smfmac_f32_16x16x64_bf16 v[252:255], v[224:227], v[128:135], %[sidx]\n\t"
        "s_waitcnt lgkmcnt(4)\n\t"
        "v_smfmac_f32_16x16x64_bf16 v[252:255], v[228:231], v[136:143], %[sidx]\n\t"
        "s_waitcnt lgkmcnt(3)\n\t"
        "v_smfmac_f32_16x16x64_bf16 v[252:255], v[232:235], v[144:151], %[sidx]\n\t"
        "s_waitcnt lgkmcnt(2)\n\t"
        "v_smfmac_f32_16x16x64_bf16 v[252:255], v[236:239], v[152:159], %[sidx]\n\t"
        "s_waitcnt lgkmcnt(1)\n\t"
        "v_smfmac_f32_16x16x64_bf16 v[252:255], v[240:243], v[160:167], %[sidx]\n\t"
        "s_waitcnt lgkmcnt(0)\n\t"
        "v_smfmac_f32_16x16x64_bf16 v[252:255], v[244:247], v[168:175], %[sidx]\n\t"
        "s_nop 7\n\t"
        "v_add_f32 v252, v252, v254\n\t"
        "v_add_f32 v253, v253, v255\n\t"
        "v_cvt_pk_bf16_f32 v204, v252, v253\n\t"
        "ds_write_b64 %[kw], v[204:205]\n\t"
        "v_cmp_u_f32 vcc, v252, v253\n\t"
        "s_cbranch_vccnz Lnan%=\n\t"
        "Lok%=:\n\t"
        "s_waitcnt lgkmcnt(0)\n\t"
        "s_barrier\n\t"
        "s_branch Lend%=\n\t"
        "Lnan%=:\n\t"
        "v_max_u32 v207, v208, v209\n\t"
        "v_max3_u32 v207, v207, v210, v211\n\t"
        "v_max3_u32 v207, v207, v212, v213\n\t"
        "v_max3_u32 v207, v207, v214, v215\n\t"
        "v_max3_u32 v207, v207, v216, v217\n\t"
        "v_max3_u32 v207, v207, v218, v219\n\t"
        "v_max3_u32 v207, v207, v220, v221\n\t"
        "v_max3_u32 v207, v207, v222, v223\n\t"
        "v_max3_u32 v207, v207, v224, v225\n\t"
        "v_max3_u32 v207, v207, v226, v227\n\t"
        "v_max3_u32 v207, v207, v228, v229\n\t"
        "v_max3_u32 v207, v207, v230, v231\n\t"
        "v_max3_u32 v207, v207, v232, v233\n\t"
        "v_max3_u32 v207, v207, v234, v235\n\t"
        "v_max3_u32 v207, v207, v236, v237\n\t"
        "v_max3_u32 v207, v207, v238, v239\n\t"
        "v_max3_u32 v207, v207, v240, v241\n\t"
        "v_max3_u32 v207, v207, v242, v243\n\t"
        "v_max3_u32 v207, v207, v244, v245\n\t"
        "v_max3_u32 v207, v207, v246, v247\n\t"
        "v_cmp_eq_u32 vcc, 0xffbfffbf, v207\n\t"
        "s_cbranch_vccz Lok%=\n\t"
        "Lredo%=:\n\t"
        "s_waitcnt lgkmcnt(0)\n\t"
        "s_mov_b32 %[redo], 1\n\t"
        "s_barrier\n\t"
        "s_branch Lend%=\n\t"
        "Lend%=:\n\t"
        : [lz] "+&s"(lz), [dl] "=&s"(dl), [redo] "+&s"(redo)
        : [w0] "{v[16:47]}"(W[0]), [w1] "{v[48:79]}"(W[1]), [w2] "{v[80:111]}"(W[2]), [w3] "{v[112:143]}"(W[3]), [w4] "{v[144:175]}"(W[4]), [va] "v"(va), [sidx] "v"(sidx), [qw] "v"(qw), [kw] "v"(kw), [fv] "s"(fv)
        : "v200", "v201", "v202", "v203", "v204", "v205", "v206", "v207", "v208", "v209", "v210", "v211", "v212", "v213", "v214", "v215", "v216", "v217", "v218", "v219", "v220", "v221", "v222", "v223", "v224", "v225", "v226", "v227", "v228", "v229", "v230", "v231", "v232", "v233", "v234", "v235", "v236", "v237", "v238", "v239", "v240", "v241", "v242", "v243", "v244", "v245", "v246", "v247", "v248", "v249", "v250", "v251", "v252", "v253", "v254", "v255", "vcc", "scc", "memory");
}

__device__ __forceinline__ void gather(u32 tg, u32 kr, u32 vm, u32 so, u32 lb, u32x4 rs) {
    asm volatile(
        ".p2align 6\n\t"
        "s_mov_b32 s94, m0\n\t"
        "s_add_u32 s92, %[so], 0x8000\n\t"
        "s_add_u32 s93, %[so], 0x10000\n\t"
        "s_mov_b32 m0, %[lb]\n\t"
        "Lda_%=:\n\t"
        "s_memrealtime s[90:91]\n\t"
        "s_waitcnt lgkmcnt(0)\n\t"
        "s_mul_i32 s90, s90, 24\n\t"
        "s_sub_u32 s88, %[tg], s90\n\t"
        "s_sub_u32 s89, s88, 620\n\t"
        "s_cmp_gt_u32 s89, 0x4000\n\t"
        "s_cbranch_scc1 Lea_%=\n\t"
        "s_sleep 1\n\t"
        "s_branch Lda_%=\n\t"
        "Lea_%=:\n\t"
        "s_cmp_lt_i32 s88, 120\n\t"
        "s_cbranch_scc1 Lza_%=\n\t"
        "s_memrealtime s[64:65]\n\t"
        "s_nop 0\n\t"
        "s_memrealtime s[66:67]\n\t"
        "s_nop 0\n\t"
        "s_memrealtime s[68:69]\n\t"
        "s_nop 0\n\t"
        "s_memrealtime s[70:71]\n\t"
        "s_nop 0\n\t"
        "s_memrealtime s[72:73]\n\t"
        "s_nop 0\n\t"
        "s_memrealtime s[74:75]\n\t"
        "s_memtime s[90:91]\n\t"
        "s_memrealtime s[76:77]\n\t"
        "s_nop 0\n\t"
        "s_memrealtime s[78:79]\n\t"
        "s_nop 0\n\t"
        "s_memrealtime s[80:81]\n\t"
        "s_nop 0\n\t"
        "s_memrealtime s[82:83]\n\t"
        "s_nop 0\n\t"
        "s_memrealtime s[84:85]\n\t"
        "s_nop 0\n\t"
        "s_memrealtime s[86:87]\n\t"
        "s_nop 0\n\t"
        "s_nop 7\n\t"
        "s_waitcnt lgkmcnt(0)\n\t"
        "s_add_u32 s64, s64, s66\n\t"
        "s_add_u32 s64, s64, s68\n\t"
        "s_add_u32 s64, s64, s70\n\t"
        "s_add_u32 s64, s64, s72\n\t"
        "s_add_u32 s64, s64, s74\n\t"
        "s_add_u32 s64, s64, s76\n\t"
        "s_add_u32 s64, s64, s78\n\t"
        "s_add_u32 s64, s64, s80\n\t"
        "s_add_u32 s64, s64, s82\n\t"
        "s_add_u32 s64, s64, s84\n\t"
        "s_add_u32 s64, s64, s86\n\t"
        "s_lshl_b32 s64, s64, 1\n\t"
        "s_nop 1\n\t"
        "s_sub_u32 s88, %[tg], s64\n\t"
        "s_mul_i32 s88, s88, %[kr]\n\t"
        "s_ashr_i32 s88, s88, 12\n\t"
        "s_sub_u32 s88, s88, 244\n\t"
        "s_nop 1\n\t"
        "s_max_i32 s88, s88, 0\n\t"
        "s_min_i32 s88, s88, 384\n\t"
        "s_andn2_b32 s88, s88, 3\n\t"
        "s_getpc_b64 s[90:91]\n\t"
        "Lga_%=:\n\t"
        "s_sub_u32 s90, s90, s88\n\t"
        "s_subb_u32 s91, s91, 0\n\t"
        "s_add_u32 s90, s90, Lza_%=-Lga_%=\n\t"
        "s_addc_u32 s91, s91, 0\n\t"
        "s_setpc_b64 s[90:91]\n\t"
        "s_nop 0\n\ts_nop 0\n\ts_nop 0\n\ts_nop 0\n\t"
        "s_nop 0\n\ts_nop 0\n\ts_nop 0\n\ts_nop 0\n\t"
        "s_nop 0\n\ts_nop 0\n\ts_nop 0\n\ts_nop 0\n\t"
        "s_nop 0\n\ts_nop 0\n\ts_nop 0\n\ts_nop 0\n\t"
        "s_nop 0\n\ts_nop 0\n\ts_nop 0\n\ts_nop 0\n\t"
        "s_nop 0\n\ts_nop 0\n\ts_nop 0\n\ts_nop 0\n\t"
        "s_nop 0\n\ts_nop 0\n\ts_nop 0\n\ts_nop 0\n\t"
        "s_nop 0\n\ts_nop 0\n\ts_nop 0\n\ts_nop 0\n\t"
        "s_nop 0\n\ts_nop 0\n\ts_nop 0\n\ts_nop 0\n\t"
        "s_nop 0\n\ts_nop 0\n\ts_nop 0\n\ts_nop 0\n\t"
        "s_nop 0\n\ts_nop 0\n\ts_nop 0\n\ts_nop 0\n\t"
        "s_nop 0\n\ts_nop 0\n\ts_nop 0\n\ts_nop 0\n\t"
        "s_nop 0\n\ts_nop 0\n\ts_nop 0\n\ts_nop 0\n\t"
        "s_nop 0\n\ts_nop 0\n\ts_nop 0\n\ts_nop 0\n\t"
        "s_nop 0\n\ts_nop 0\n\ts_nop 0\n\ts_nop 0\n\t"
        "s_nop 0\n\ts_nop 0\n\ts_nop 0\n\ts_nop 0\n\t"
        "s_nop 0\n\ts_nop 0\n\ts_nop 0\n\ts_nop 0\n\t"
        "s_nop 0\n\ts_nop 0\n\ts_nop 0\n\ts_nop 0\n\t"
        "s_nop 0\n\ts_nop 0\n\ts_nop 0\n\ts_nop 0\n\t"
        "s_nop 0\n\ts_nop 0\n\ts_nop 0\n\ts_nop 0\n\t"
        "s_nop 0\n\ts_nop 0\n\ts_nop 0\n\ts_nop 0\n\t"
        "s_nop 0\n\ts_nop 0\n\ts_nop 0\n\ts_nop 0\n\t"
        "s_nop 0\n\ts_nop 0\n\ts_nop 0\n\ts_nop 0\n\t"
        "s_nop 0\n\ts_nop 0\n\ts_nop 0\n\ts_nop 0\n\t"
        "Lza_%=:\n\t"
        "buffer_load_dwordx4 %[vm], %[rs], %[so] offen offset:0 sc1 lds\n\t"
        "buffer_load_dwordx4 %[vm], %[rs], %[so] offen offset:1024 sc1 lds\n\t"
        "buffer_load_dwordx4 %[vm], %[rs], %[so] offen offset:2048 sc1 lds\n\t"
        "buffer_load_dwordx4 %[vm], %[rs], %[so] offen offset:3072 sc1 lds\n\t"
        "s_add_u32 m0, m0, 0x1000\n\t"
        "s_nop 0\n\t"
        "buffer_load_dwordx4 %[vm], %[rs], s92 offen offset:0 sc1 lds\n\t"
        "buffer_load_dwordx4 %[vm], %[rs], s92 offen offset:1024 sc1 lds\n\t"
        "buffer_load_dwordx4 %[vm], %[rs], s92 offen offset:2048 sc1 lds\n\t"
        "buffer_load_dwordx4 %[vm], %[rs], s92 offen offset:3072 sc1 lds\n\t"
        "s_add_u32 m0, m0, 0x1000\n\t"
        "s_nop 0\n\t"
        "buffer_load_dwordx4 %[vm], %[rs], s93 offen offset:0 sc1 lds\n\t"
        "buffer_load_dwordx4 %[vm], %[rs], s93 offen offset:2048 sc1 lds\n\t"
        "s_mov_b32 m0, s94\n\t"
        :
        : [rs] "s"(rs), [tg] "s"(tg), [kr] "s"(kr), [vm] "v"(vm), [lb] "s"(lb), [so] "s"(so)
        : "s64", "s65", "s66", "s67", "s68", "s69", "s70", "s71", "s72", "s73", "s74", "s75", "s76", "s77", "s78", "s79", "s80", "s81", "s82", "s83", "s84", "s85", "s86", "s87", "s88", "s89", "s90", "s91", "s92", "s93", "s94", "vcc", "scc", "memory");
}

__device__ __forceinline__ void calib(u32& c0, u32& r0) {
    asm volatile(
        "s_memrealtime s[64:65]\n\t"
        "s_nop 0\n\t"
        "s_memrealtime s[66:67]\n\t"
        "s_nop 0\n\t"
        "s_memrealtime s[68:69]\n\t"
        "s_nop 0\n\t"
        "s_memrealtime s[70:71]\n\t"
        "s_nop 0\n\t"
        "s_memrealtime s[72:73]\n\t"
        "s_nop 0\n\t"
        "s_memrealtime s[74:75]\n\t"
        "s_memtime s[90:91]\n\t"
        "s_memrealtime s[76:77]\n\t"
        "s_nop 0\n\t"
        "s_memrealtime s[78:79]\n\t"
        "s_nop 0\n\t"
        "s_memrealtime s[80:81]\n\t"
        "s_nop 0\n\t"
        "s_memrealtime s[82:83]\n\t"
        "s_nop 0\n\t"
        "s_memrealtime s[84:85]\n\t"
        "s_nop 0\n\t"
        "s_memrealtime s[86:87]\n\t"
        "s_nop 0\n\t"
        "s_nop 7\n\t"
        "s_waitcnt lgkmcnt(0)\n\t"
        "s_add_u32 s64, s64, s66\n\t"
        "s_add_u32 s64, s64, s68\n\t"
        "s_add_u32 s64, s64, s70\n\t"
        "s_add_u32 s64, s64, s72\n\t"
        "s_add_u32 s64, s64, s74\n\t"
        "s_add_u32 s64, s64, s76\n\t"
        "s_add_u32 s64, s64, s78\n\t"
        "s_add_u32 s64, s64, s80\n\t"
        "s_add_u32 s64, s64, s82\n\t"
        "s_add_u32 s64, s64, s84\n\t"
        "s_add_u32 s64, s64, s86\n\t"
        "s_lshl_b32 s64, s64, 1\n\t"
        "s_mov_b32 %[c0], s90\n\t"
        "s_mov_b32 %[r0], s64\n\t"
        : [c0] "=&s"(c0), [r0] "=&s"(r0)
        :
        : "s64", "s65", "s66", "s67", "s68", "s69", "s70", "s71", "s72", "s73", "s74", "s75", "s76", "s77", "s78", "s79", "s80", "s81", "s82", "s83", "s84", "s85", "s86", "s87", "s88", "s89", "s90", "s91", "vcc", "scc", "memory");
}

__device__ __forceinline__ void px_step(u32x4& p0, u32& p1, u32 ka, u32 qa, u32 ma, u32 ktr, float ce) {
    asm volatile(
        ".p2align 6\n\t"
        "ds_read_b128 v[240:243], %[ma] offset:0\n\t"
        "ds_read_b128 v[244:247], %[ma] offset:1024\n\t"
        "ds_read_b128 v[208:211], %[qa] offset:0\n\t"
        "ds_read_b128 v[224:227], %[ka] offset:0\n\t"
        "ds_read_b128 v[212:215], %[qa] offset:1024\n\t"
        "ds_read_b128 v[228:231], %[ka] offset:1024\n\t"
        "ds_read_b128 v[216:219], %[qa] offset:2048\n\t"
        "ds_read_b128 v[232:235], %[ka] offset:2048\n\t"
        "ds_read_b128 v[220:223], %[qa] offset:3072\n\t"
        "ds_read_b128 v[236:239], %[ka] offset:3072\n\t"
        "s_waitcnt lgkmcnt(6)\n\t"
        "v_mfma_f32_16x16x32_bf16 v[240:243], v[224:227], v[208:211], v[240:243]\n\t"
        "ds_read_b128 v[224:227], %[ka] offset:4096\n\t"
        "s_waitcnt lgkmcnt(5)\n\t"
        "v_mfma_f32_16x16x32_bf16 v[240:243], v[228:231], v[212:215], v[240:243]\n\t"
        "ds_read_b128 v[228:231], %[ka] offset:5120\n\t"
        "s_waitcnt lgkmcnt(4)\n\t"
        "v_mfma_f32_16x16x32_bf16 v[240:243], v[232:235], v[216:219], v[240:243]\n\t"
        "ds_read_b128 v[232:235], %[ka] offset:6144\n\t"
        "s_waitcnt lgkmcnt(3)\n\t"
        "v_mfma_f32_16x16x32_bf16 v[240:243], v[236:239], v[220:223], v[240:243]\n\t"
        "ds_read_b128 v[236:239], %[ka] offset:7168\n\t"
        "s_waitcnt lgkmcnt(3)\n\t"
        "v_mfma_f32_16x16x32_bf16 v[244:247], v[224:227], v[208:211], v[244:247]\n\t"
        "ds_read_b64_tr_b16 v[224:225], %[ktr] offset:0\n\t"
        "ds_read_b64_tr_b16 v[226:227], %[ktr] offset:768\n\t"
        "s_waitcnt lgkmcnt(4)\n\t"
        "v_mfma_f32_16x16x32_bf16 v[244:247], v[228:231], v[212:215], v[244:247]\n\t"
        "ds_read_b64_tr_b16 v[228:229], %[ktr] offset:1536\n\t"
        "ds_read_b64_tr_b16 v[230:231], %[ktr] offset:2304\n\t"
        "s_waitcnt lgkmcnt(5)\n\t"
        "v_mfma_f32_16x16x32_bf16 v[244:247], v[232:235], v[216:219], v[244:247]\n\t"
        "ds_read_b64_tr_b16 v[232:233], %[ktr] offset:3072\n\t"
        "ds_read_b64_tr_b16 v[234:235], %[ktr] offset:3840\n\t"
        "s_waitcnt lgkmcnt(6)\n\t"
        "v_mfma_f32_16x16x32_bf16 v[244:247], v[236:239], v[220:223], v[244:247]\n\t"
        "ds_read_b64_tr_b16 v[236:237], %[ktr] offset:4608\n\t"
        "ds_read_b64_tr_b16 v[238:239], %[ktr] offset:5376\n\t"
        "s_waitcnt lgkmcnt(6)\n\t"
        "v_mfma_f32_16x16x32_bf16 v[248:251], v[224:227], v[208:211], 0\n\t"
        "s_waitcnt lgkmcnt(4)\n\t"
        "v_mfma_f32_16x16x32_bf16 v[248:251], v[228:231], v[212:215], v[248:251]\n\t"
        "s_waitcnt lgkmcnt(2)\n\t"
        "v_mfma_f32_16x16x32_bf16 v[248:251], v[232:235], v[216:219], v[248:251]\n\t"
        "s_waitcnt lgkmcnt(0)\n\t"
        "v_mfma_f32_16x16x32_bf16 v[248:251], v[236:239], v[220:223], v[248:251]\n\t"
        "v_max3_f32 v253, v240, v241, v242\n\t"
        "v_max3_f32 v253, v253, v243, v244\n\t"
        "v_max3_f32 v253, v253, v245, v246\n\t"
        "v_max_f32 v253, v253, v247\n\t"
        "s_nop 3\n\t"
        "v_mul_f32 v248, %[ce], v248\n\t"
        "v_mul_f32 v249, %[ce], v249\n\t"
        "v_max3_f32 v253, v253, v248, v249\n\t"
        "v_mov_b32 v254, v253\n\t"
        "s_nop 1\n\t"
        "v_permlane16_swap_b32 v253, v254\n\t"
        "v_max_f32 v253, v253, v254\n\t"
        "v_mov_b32 v254, v253\n\t"
        "s_nop 1\n\t"
        "v_permlane32_swap_b32 v253, v254\n\t"
        "v_max_f32 v253, v253, v254\n\t"
        "v_sub_f32 v240, v240, v253\n\t"
        "v_sub_f32 v241, v241, v253\n\t"
        "v_sub_f32 v242, v242, v253\n\t"
        "v_sub_f32 v243, v243, v253\n\t"
        "v_sub_f32 v244, v244, v253\n\t"
        "v_sub_f32 v245, v245, v253\n\t"
        "v_sub_f32 v246, v246, v253\n\t"
        "v_sub_f32 v247, v247, v253\n\t"
        "v_sub_f32 v248, v248, v253\n\t"
        "v_sub_f32 v249, v249, v253\n\t"
        "v_exp_f32 v240, v240\n\t"
        "v_exp_f32 v241, v241\n\t"
        "v_exp_f32 v242, v242\n\t"
        "v_exp_f32 v243, v243\n\t"
        "v_exp_f32 v244, v244\n\t"
        "v_exp_f32 v245, v245\n\t"
        "v_exp_f32 v246, v246\n\t"
        "v_exp_f32 v247, v247\n\t"
        "v_exp_f32 v248, v248\n\t"
        "v_exp_f32 v249, v249\n\t"
        "v_pk_add_f32 v[252:253], v[240:241], v[242:243]\n\t"
        "v_pk_add_f32 v[254:255], v[244:245], v[246:247]\n\t"
        "v_pk_add_f32 v[252:253], v[252:253], v[248:249]\n\t"
        "v_pk_add_f32 v[252:253], v[252:253], v[254:255]\n\t"
        "v_add_f32 v252, v252, v253\n\t"
        "v_mov_b32 v253, v252\n\t"
        "s_nop 1\n\t"
        "v_permlane16_swap_b32 v252, v253\n\t"
        "v_add_f32 v252, v252, v253\n\t"
        "v_mov_b32 v253, v252\n\t"
        "s_nop 1\n\t"
        "v_permlane32_swap_b32 v252, v253\n\t"
        "v_add_f32 v252, v252, v253\n\t"
        "v_rcp_f32 v252, v252\n\t"
        "s_nop 0\n\t"
        "v_pk_mul_f32 v[240:241], v[240:241], v[252:253] op_sel_hi:[1,0]\n\t"
        "v_pk_mul_f32 v[242:243], v[242:243], v[252:253] op_sel_hi:[1,0]\n\t"
        "v_pk_mul_f32 v[244:245], v[244:245], v[252:253] op_sel_hi:[1,0]\n\t"
        "v_pk_mul_f32 v[246:247], v[246:247], v[252:253] op_sel_hi:[1,0]\n\t"
        "v_pk_mul_f32 v[248:249], v[248:249], v[252:253] op_sel_hi:[1,0]\n\t"
        "v_cvt_pk_bf16_f32 v192, v240, v241\n\t"
        "v_cvt_pk_bf16_f32 v193, v242, v243\n\t"
        "v_cvt_pk_bf16_f32 v194, v244, v245\n\t"
        "v_cvt_pk_bf16_f32 v195, v246, v247\n\t"
        "v_cvt_pk_bf16_f32 v196, v248, v249\n\t"
        : [p0] "=&{v[192:195]}"(p0), [p1] "=&{v196}"(p1)
        : [ka] "v"(ka), [qa] "v"(qa), [ma] "v"(ma), [ktr] "v"(ktr), [ce] "s"(ce)
        : "v200", "v201", "v202", "v203", "v204", "v205", "v206", "v207", "v208", "v209", "v210", "v211", "v212", "v213", "v214", "v215", "v216", "v217", "v218", "v219", "v220", "v221", "v222", "v223", "v224", "v225", "v226", "v227", "v228", "v229", "v230", "v231", "v232", "v233", "v234", "v235", "v236", "v237", "v238", "v239", "v240", "v241", "v242", "v243", "v244", "v245", "v246", "v247", "v248", "v249", "v250", "v251", "v252", "v253", "v254", "v255", "vcc", "scc", "memory");
}

__device__ __forceinline__ void v_a(const u32x32 (&W)[5], u32 va, u32 lz, u32 sidx, u32 vw STP) {
    u32 dl;
    asm volatile(
        ".p2align 6\n\t"
        "v_mov_b32 v206, %[va]\n\t"
        "s_mov_b32 %[dl], 81920\n\t"
        "v_mov_b64 v[248:249], 0\n\t"
        "v_mov_b64 v[250:251], 0\n\t"
        "v_mov_b64 v[252:253], 0\n\t"
        "v_mov_b64 v[254:255], 0\n\t"
        "s_setprio 3\n\t"
        "Lb1%=:\n\t"
        "s_barrier\n\t"
        "ds_read_b128 v[208:211], v206 offset:0\n\t"
        "ds_read_b128 v[212:215], v206 offset:1024\n\t"
        "ds_read_b128 v[216:219], v206 offset:2048\n\t"
        "ds_read_b128 v[220:223], v206 offset:3072\n\t"
        "ds_read_b128 v[224:227], v206 offset:4096\n\t"
        "ds_read_b128 v[228:231], v206 offset:5120\n\t"
        "s_waitcnt lgkmcnt(5)\n\t"
        "v_smfmac_f32_16x16x64_bf16 v[248:251], v[208:211], v[16:23], %[sidx]\n\t"
        "ds_read_b128 v[232:235], v206 offset:6144\n\t"
        "s_waitcnt lgkmcnt(5)\n\t"
        "v_smfmac_f32_16x16x64_bf16 v[248:251], v[212:215], v[24:31], %[sidx]\n\t"
        "ds_read_b128 v[236:239], v206 offset:7168\n\t"
        "s_waitcnt lgkmcnt(5)\n\t"
        "v_smfmac_f32_16x16x64_bf16 v[248:251], v[216:219], v[32:39], %[sidx]\n\t"
        "ds_read_b128 v[240:243], v206 offset:8192\n\t"
        "s_waitcnt lgkmcnt(5)\n\t"
        "v_smfmac_f32_16x16x64_bf16 v[248:251], v[220:223], v[40:47], %[sidx]\n\t"
        "ds_read_b128 v[244:247], v206 offset:10240\n\t"
        "s_waitcnt lgkmcnt(5)\n\t"
        "v_smfmac_f32_16x16x64_bf16 v[248:251], v[224:227], v[48:55], %[sidx]\n\t"
        "s_waitcnt lgkmcnt(4)\n\t"
        "v_smfmac_f32_16x16x64_bf16 v[248:251], v[228:231], v[56:63], %[sidx]\n\t"
        "s_waitcnt lgkmcnt(3)\n\t"
        "v_smfmac_f32_16x16x64_bf16 v[248:251], v[232:235], v[64:71], %[sidx]\n\t"
        "s_waitcnt lgkmcnt(2)\n\t"
        "v_smfmac_f32_16x16x64_bf16 v[248:251], v[236:239], v[72:79], %[sidx]\n\t"
        "s_waitcnt lgkmcnt(1)\n\t"
        "v_smfmac_f32_16x16x64_bf16 v[248:251], v[240:243], v[80:87], %[sidx]\n\t"
        "s_waitcnt lgkmcnt(0)\n\t"
        "v_smfmac_f32_16x16x64_bf16 v[248:251], v[244:247], v[88:95], %[sidx]\n\t"
        "v_smfmac_f32_16x16x64_bf16 v[252:255], v[208:211], v[96:103], %[sidx]\n\t"
        "v_smfmac_f32_16x16x64_bf16 v[252:255], v[212:215], v[104:111], %[sidx]\n\t"
        "v_add_f32 v248, v248, v250\n\t"
        "v_smfmac_f32_16x16x64_bf16 v[252:255], v[216:219], v[112:119], %[sidx]\n\t"
        "v_add_f32 v249, v249, v251\n\t"
        "v_smfmac_f32_16x16x64_bf16 v[252:255], v[220:223], v[120:127], %[sidx]\n\t"
        "v_cvt_pk_bf16_f32 v204, v248, v249\n\t"
        "ds_write_b32 %[vw], v204\n\t"
        "v_smfmac_f32_16x16x64_bf16 v[252:255], v[224:227], v[128:135], %[sidx]\n\t"
        "v_cmp_u_f32 vcc, v248, v249\n\t"
        "v_smfmac_f32_16x16x64_bf16 v[252:255], v[228:231], v[136:143], %[sidx]\n\t"
        "v_smfmac_f32_16x16x64_bf16 v[252:255], v[232:235], v[144:151], %[sidx]\n\t"
        "v_smfmac_f32_16x16x64_bf16 v[252:255], v[236:239], v[152:159], %[sidx]\n\t"
        "s_waitcnt lgkmcnt(0)\n\t"
        "s_barrier\n\t"
        "v_smfmac_f32_16x16x64_bf16 v[252:255], v[240:243], v[160:167], %[sidx]\n\t"
        "v_smfmac_f32_16x16x64_bf16 v[252:255], v[244:247], v[168:175], %[sidx]\n\t"
        "s_cbranch_vccnz Lnan%=\n\t"
        "Lok%=:\n\t"
        "s_nop 6\n\t"
        "v_add_f32 v252, v252, v254\n\t"
        "v_add_f32 v253, v253, v255\n\t"
        "v_cvt_pk_bf16_f32 v205, v252, v253\n\t"
        "ds_write_b32 %[vw], v205 offset:1024\n\t"
        "s_waitcnt lgkmcnt(0)\n\t"
        "s_barrier\n\t"
        "s_setprio 0\n\t"
        "s_sleep 6\n\t"
        "s_branch Lend%=\n\t"
        "Lnan%=:\n\t"
        "v_mbcnt_lo_u32_b32 v202, -1, 0\n\t"
        "v_mbcnt_hi_u32_b32 v202, -1, v202\n\t"
        "v_lshl_add_u32 v207, v202, 7, %[lz]\n\t"
        "v_and_b32 v202, 31, v202\n\t"
        "v_lshl_add_u32 v207, v202, 2, v207\n\t"
        "ds_read2st64_b32 v[202:203], v207 offset1:32\n\t"
        "s_waitcnt lgkmcnt(0)\n\t"
        "s_mov_b64 exec, 0x00ff00ff\n\t"
        "v_max_u32 v202, v202, v203\n\t"
        "s_mov_b64 exec, -1\n\t"
        "v_cmp_eq_u32 vcc, 0xffbfffbf, v202\n\t"
        "s_cbranch_vccnz Lredo%=\n\t"
        "v_max_u32 v207, v208, v209\n\t"
        "v_max3_u32 v207, v207, v210, v211\n\t"
        "v_max3_u32 v207, v207, v212, v213\n\t"
        "v_max3_u32 v207, v207, v214, v215\n\t"
        "v_max3_u32 v207, v207, v216, v217\n\t"
        "v_max3_u32 v207, v207, v218, v219\n\t"
        "v_max3_u32 v207, v207, v220, v221\n\t"
        "v_max3_u32 v207, v207, v222, v223\n\t"
        "v_max3_u32 v207, v207, v224, v225\n\t"
        "v_max3_u32 v207, v207, v226, v227\n\t"
        "v_max3_u32 v207, v207, v228, v229\n\t"
        "v_max3_u32 v207, v207, v230, v231\n\t"
        "v_max3_u32 v207, v207, v232, v233\n\t"
        "v_max3_u32 v207, v207, v234, v235\n\t"
        "v_max3_u32 v207, v207, v236, v237\n\t"
        "v_max3_u32 v207, v207, v238, v239\n\t"
        "v_max3_u32 v207, v207, v240, v241\n\t"
        "v_max3_u32 v207, v207, v242, v243\n\t"
        "v_max3_u32 v207, v207, v244, v245\n\t"
        "v_max3_u32 v207, v207, v246, v247\n\t"
        "v_cmp_eq_u32 vcc, 0xffbfffbf, v207\n\t"
        "s_cbranch_vccz Lok%=\n\t"
        "Lredo%=:\n\t"
        "s_waitcnt lgkmcnt(0)\n\t"
        "v_add_u32 v206, %[dl], v206\n\t"
        "v_mov_b64 v[248:249], 0\n\t"
        "v_mov_b64 v[250:251], 0\n\t"
        "v_mov_b64 v[252:253], 0\n\t"
        "v_mov_b64 v[254:255], 0\n\t"
        "s_add_u32 %[lz], %[lz], %[dl]\n\t"
        "s_sub_u32 %[dl], 0, %[dl]\n\t"
        "s_setprio 3\n\t"
        "s_barrier\n\t"
        "s_branch Lb1%=\n\t"
        "Lend%=:\n\t"
        : [lz] "+&s"(lz), [dl] "=&s"(dl)
        : [w0] "{v[16:47]}"(W[0]), [w1] "{v[48:79]}"(W[1]), [w2] "{v[80:111]}"(W[2]), [w3] "{v[112:143]}"(W[3]), [w4] "{v[144:175]}"(W[4]), [va] "v"(va), [sidx] "v"(sidx), [vw] "v"(vw)
        : "v202", "v203", "v204", "v205", "v206", "v207", "v208", "v209", "v210", "v211", "v212", "v213", "v214", "v215", "v216", "v217", "v218", "v219", "v220", "v221", "v222", "v223", "v224", "v225", "v226", "v227", "v228", "v229", "v230", "v231", "v232", "v233", "v234", "v235", "v236", "v237", "v238", "v239", "v240", "v241", "v242", "v243", "v244", "v245", "v246", "v247", "v248", "v249", "v250", "v251", "v252", "v253", "v254", "v255", "vcc", "scc", "memory");
}

__device__ __forceinline__ void v_b0(const u32x32 (&W)[5], u32 va, u32 lz, u32 si, u32 vr, u32 z1, u32 z2, u32 z3, u32x4 rs STP) {
    u32 dl;
    asm volatile(
        ".p2align 6\n\t"
        "s_setprio 2\n\t"
        "v_mov_b32 v206, %[va]\n\t"
        "s_mov_b32 %[dl], 81920\n\t"
        "v_mov_b64 v[176:177], 0\n\t"
        "v_mov_b64 v[178:179], 0\n\t"
        "v_mov_b64 v[180:181], 0\n\t"
        "v_mov_b64 v[182:183], 0\n\t"
        "v_mov_b64 v[184:185], 0\n\t"
        "v_mov_b64 v[186:187], 0\n\t"
        "v_mov_b64 v[188:189], 0\n\t"
        "v_mov_b64 v[190:191], 0\n\t"
        "v_mov_b64 v[192:193], 0\n\t"
        "v_mov_b64 v[194:195], 0\n\t"
        "Lb1%=:\n\t"
        "s_barrier\n\t"
        "s_barrier\n\t"
        "ds_read_b64_tr_b16 v[248:249], %[vr] offset:0\n\t"
        "ds_read_b64_tr_b16 v[250:251], %[vr] offset:256\n\t"
        "ds_read_b64_tr_b16 v[252:253], %[vr] offset:512\n\t"
        "ds_read_b64_tr_b16 v[254:255], %[vr] offset:768\n\t"
        "s_waitcnt lgkmcnt(0)\n\t"
        "v_lshlrev_b32 v204, 16, v248\n\t"
        "v_cmp_u_f32 vcc, v204, v204\n\t"
        "s_cbranch_vccnz Lnb%=\n\t"
        "Lok%=:\n\t"
        "v_smfmac_f32_16x16x64_bf16 v[176:179], v[248:251], v[16:23], %[si]\n\t"
        "v_smfmac_f32_16x16x64_bf16 v[180:183], v[248:251], v[48:55], %[si]\n\t"
        "v_smfmac_f32_16x16x64_bf16 v[184:187], v[248:251], v[80:87], %[si]\n\t"
        "v_smfmac_f32_16x16x64_bf16 v[188:191], v[248:251], v[112:119], %[si]\n\t"
        "v_smfmac_f32_16x16x64_bf16 v[192:195], v[248:251], v[144:151], %[si]\n\t"
        "v_smfmac_f32_16x16x64_bf16 v[176:179], v[252:255], v[24:31], %[si]\n\t"
        "v_smfmac_f32_16x16x64_bf16 v[180:183], v[252:255], v[56:63], %[si]\n\t"
        "v_smfmac_f32_16x16x64_bf16 v[184:187], v[252:255], v[88:95], %[si]\n\t"
        "s_barrier\n\t"
        "ds_read_b64_tr_b16 v[208:209], %[vr] offset:1024\n\t"
        "ds_read_b64_tr_b16 v[210:211], %[vr] offset:1280\n\t"
        "ds_read_b64_tr_b16 v[212:213], %[vr] offset:1536\n\t"
        "ds_read_b64_tr_b16 v[214:215], %[vr] offset:1792\n\t"
        "v_smfmac_f32_16x16x64_bf16 v[188:191], v[252:255], v[120:127], %[si]\n\t"
        "v_smfmac_f32_16x16x64_bf16 v[192:195], v[252:255], v[152:159], %[si]\n\t"
        "s_waitcnt lgkmcnt(2)\n\t"
        "v_smfmac_f32_16x16x64_bf16 v[176:179], v[208:211], v[32:39], %[si]\n\t"
        "s_waitcnt lgkmcnt(0)\n\t"
        "v_smfmac_f32_16x16x64_bf16 v[176:179], v[212:215], v[40:47], %[si]\n\t"
        "v_smfmac_f32_16x16x64_bf16 v[180:183], v[208:211], v[64:71], %[si]\n\t"
        "v_smfmac_f32_16x16x64_bf16 v[180:183], v[212:215], v[72:79], %[si]\n\t"
        "v_cvt_pk_bf16_f32 v216, v176, v177\n\t"
        "v_smfmac_f32_16x16x64_bf16 v[184:187], v[208:211], v[96:103], %[si]\n\t"
        "v_cvt_pk_bf16_f32 v217, v178, v179\n\t"
        "v_smfmac_f32_16x16x64_bf16 v[184:187], v[212:215], v[104:111], %[si]\n\t"
        "v_cvt_pk_bf16_f32 v218, v180, v181\n\t"
        "v_smfmac_f32_16x16x64_bf16 v[188:191], v[208:211], v[128:135], %[si]\n\t"
        "v_cvt_pk_bf16_f32 v219, v182, v183\n\t"
        "v_smfmac_f32_16x16x64_bf16 v[188:191], v[212:215], v[136:143], %[si]\n\t"
        "v_permlane16_swap_b32 v216, v218\n\t"
        "v_smfmac_f32_16x16x64_bf16 v[192:195], v[208:211], v[160:167], %[si]\n\t"
        "v_permlane16_swap_b32 v217, v219\n\t"
        "buffer_store_dwordx4 v[216:219], %[z1], %[rs], 0 offen sc1\n\t"
        "v_smfmac_f32_16x16x64_bf16 v[192:195], v[212:215], v[168:175], %[si]\n\t"
        "v_cvt_pk_bf16_f32 v220, v184, v185\n\t"
        "v_cvt_pk_bf16_f32 v222, v188, v189\n\t"
        "v_cvt_pk_bf16_f32 v221, v186, v187\n\t"
        "v_cvt_pk_bf16_f32 v223, v190, v191\n\t"
        "v_permlane16_swap_b32 v220, v222\n\t"
        "s_nop 0\n\t"
        "v_permlane16_swap_b32 v221, v223\n\t"
        "buffer_store_dwordx4 v[220:223], %[z2], %[rs], 0 offen sc1\n\t"
        "v_cvt_pk_bf16_f32 v224, v192, v193\n\t"
        "v_cvt_pk_bf16_f32 v225, v194, v195\n\t"
        "buffer_store_dwordx2 v[224:225], %[z3], %[rs], 0 offen sc1\n\t"
        "s_branch Lend%=\n\t"
        "Lnb%=:\n\t"
        "ds_read_b128 v[208:211], v206 offset:0\n\t"
        "ds_read_b128 v[212:215], v206 offset:1024\n\t"
        "ds_read_b128 v[216:219], v206 offset:2048\n\t"
        "ds_read_b128 v[220:223], v206 offset:3072\n\t"
        "ds_read_b128 v[224:227], v206 offset:4096\n\t"
        "ds_read_b128 v[228:231], v206 offset:5120\n\t"
        "ds_read_b128 v[232:235], v206 offset:6144\n\t"
        "ds_read_b128 v[236:239], v206 offset:7168\n\t"
        "ds_read_b128 v[240:243], v206 offset:8192\n\t"
        "ds_read_b128 v[244:247], v206 offset:10240\n\t"
        "s_waitcnt lgkmcnt(0)\n\t"
        "Lnan%=:\n\t"
        "v_mbcnt_lo_u32_b32 v202, -1, 0\n\t"
        "v_mbcnt_hi_u32_b32 v202, -1, v202\n\t"
        "v_lshl_add_u32 v207, v202, 7, %[lz]\n\t"
        "v_and_b32 v202, 31, v202\n\t"
        "v_lshl_add_u32 v207, v202, 2, v207\n\t"
        "ds_read2st64_b32 v[202:203], v207 offset1:32\n\t"
        "s_waitcnt lgkmcnt(0)\n\t"
        "s_mov_b64 exec, 0x00ff00ff\n\t"
        "v_max_u32 v202, v202, v203\n\t"
        "s_mov_b64 exec, -1\n\t"
        "v_cmp_eq_u32 vcc, 0xffbfffbf, v202\n\t"
        "s_cbranch_vccnz Lredo%=\n\t"
        "v_max_u32 v207, v208, v209\n\t"
        "v_max3_u32 v207, v207, v210, v211\n\t"
        "v_max3_u32 v207, v207, v212, v213\n\t"
        "v_max3_u32 v207, v207, v214, v215\n\t"
        "v_max3_u32 v207, v207, v216, v217\n\t"
        "v_max3_u32 v207, v207, v218, v219\n\t"
        "v_max3_u32 v207, v207, v220, v221\n\t"
        "v_max3_u32 v207, v207, v222, v223\n\t"
        "v_max3_u32 v207, v207, v224, v225\n\t"
        "v_max3_u32 v207, v207, v226, v227\n\t"
        "v_max3_u32 v207, v207, v228, v229\n\t"
        "v_max3_u32 v207, v207, v230, v231\n\t"
        "v_max3_u32 v207, v207, v232, v233\n\t"
        "v_max3_u32 v207, v207, v234, v235\n\t"
        "v_max3_u32 v207, v207, v236, v237\n\t"
        "v_max3_u32 v207, v207, v238, v239\n\t"
        "v_max3_u32 v207, v207, v240, v241\n\t"
        "v_max3_u32 v207, v207, v242, v243\n\t"
        "v_max3_u32 v207, v207, v244, v245\n\t"
        "v_max3_u32 v207, v207, v246, v247\n\t"
        "v_cmp_eq_u32 vcc, 0xffbfffbf, v207\n\t"
        "s_cbranch_vccz Lok%=\n\t"
        "Lredo%=:\n\t"
        "s_waitcnt lgkmcnt(0)\n\t"
        "v_add_u32 v206, %[dl], v206\n\t"
        "v_mov_b64 v[248:249], 0\n\t"
        "v_mov_b64 v[250:251], 0\n\t"
        "v_mov_b64 v[252:253], 0\n\t"
        "v_mov_b64 v[254:255], 0\n\t"
        "s_add_u32 %[lz], %[lz], %[dl]\n\t"
        "s_sub_u32 %[dl], 0, %[dl]\n\t"
        "s_setprio 2\n\t"
        "s_barrier\n\t"
        "s_branch Lb1%=\n\t"
        "Lend%=:\n\t"
        : [lz] "+&s"(lz), [dl] "=&s"(dl)
        : [w0] "{v[16:47]}"(W[0]), [w1] "{v[48:79]}"(W[1]), [w2] "{v[80:111]}"(W[2]), [w3] "{v[112:143]}"(W[3]), [w4] "{v[144:175]}"(W[4]), [va] "v"(va), [si] "v"(si), [vr] "v"(vr), [z1] "v"(z1), [z2] "v"(z2), [z3] "v"(z3), [rs] "s"(rs)
        : "v176", "v177", "v178", "v179", "v180", "v181", "v182", "v183", "v184", "v185", "v186", "v187", "v188", "v189", "v190", "v191", "v192", "v193", "v194", "v195", "v202", "v203", "v204", "v205", "v206", "v207", "v208", "v209", "v210", "v211", "v212", "v213", "v214", "v215", "v216", "v217", "v218", "v219", "v220", "v221", "v222", "v223", "v224", "v225", "v226", "v227", "v228", "v229", "v230", "v231", "v232", "v233", "v234", "v235", "v236", "v237", "v238", "v239", "v240", "v241", "v242", "v243", "v244", "v245", "v246", "v247", "v248", "v249", "v250", "v251", "v252", "v253", "v254", "v255", "vcc", "scc", "memory");
}

__device__ __forceinline__ void v_b1(const u32x32 (&W)[5], u32 va, u32 lz, u32 si, u32 vr, u32 z1, u32 z2, u32 z3, u32x4 rs, u32& left, u32& c0, u32& r0, u32& te, u32& tn, u32& gb, u32 gx, u32 poll, u32 gp, u32 lk, u32 kr, u32 df, u32 vm, u32 so, u32 so2, u32 lb, u32 ro, u32 rn, u32 rb, u32 rk STP) {
    u32 dl;
    asm volatile(
        ".p2align 6\n\t"
        "s_setprio 2\n\t"
        "s_mov_b32 s94, m0\n\t"
        "s_add_u32 s92, %[so], 0x8000\n\t"
        "s_add_u32 s93, %[so], 0x10000\n\t"
        "s_mov_b32 m0, %[lb]\n\t"
        "s_cmp_eq_u32 %[poll], 0\n\t"
        "s_cbranch_scc1 Lpb%=\n\t"
        "v_lshrrev_b32 v201, 2, %[vm]\n\t"
        "v_add_u32 v201, %[lb], v201\n\t"
        "s_waitcnt vmcnt(0)\n\t"
        "ds_read_b32 v200, v201 offset:29696\n\t"
        "s_mov_b64 s[96:97], 0\n\t"
        "s_mov_b32 s98, %[rb]\n\t"
        "s_mov_b32 s99, 0x2000\n\t"
        "s_waitcnt lgkmcnt(0)\n\t"
        "v_cmp_gt_i32 vcc, s96, v200\n\t"
        "s_cbranch_vccnz Ls%=\n\t"
        "Lg%=:\n\t"
        "s_cmp_lg_u32 s96, 0\n\t"
        "s_cselect_b32 s95, %[gx], 0\n\t"
        "s_add_u32 s95, s95, %[te]\n\t"
        "s_add_u32 s95, s95, %[gp]\n\t"
        "s_add_u32 s95, s95, %[gb]\n\t"
        "Lda_%=:\n\t"
        "s_memrealtime s[90:91]\n\t"
        "s_waitcnt lgkmcnt(0)\n\t"
        "s_mul_i32 s90, s90, 24\n\t"
        "s_sub_u32 s88, s95, s90\n\t"
        "s_sub_u32 s89, s88, 620\n\t"
        "s_cmp_gt_u32 s89, 0x4000\n\t"
        "s_cbranch_scc1 Lea_%=\n\t"
        "s_sleep 1\n\t"
        "s_branch Lda_%=\n\t"
        "Lea_%=:\n\t"
        "s_memrealtime s[64:65]\n\t"
        "s_nop 0\n\t"
        "s_memrealtime s[66:67]\n\t"
        "s_nop 0\n\t"
        "s_memrealtime s[68:69]\n\t"
        "s_nop 0\n\t"
        "s_memrealtime s[70:71]\n\t"
        "s_nop 0\n\t"
        "s_memrealtime s[72:73]\n\t"
        "s_nop 0\n\t"
        "s_memrealtime s[74:75]\n\t"
        "s_memtime s[90:91]\n\t"
        "s_memrealtime s[76:77]\n\t"
        "s_nop 0\n\t"
        "s_memrealtime s[78:79]\n\t"
        "s_nop 0\n\t"
        "s_memrealtime s[80:81]\n\t"
        "s_nop 0\n\t"
        "s_memrealtime s[82:83]\n\t"
        "s_nop 0\n\t"
        "s_memrealtime s[84:85]\n\t"
        "s_nop 0\n\t"
        "s_memrealtime s[86:87]\n\t"
        "s_nop 0\n\t"
        "s_nop 7\n\t"
        "s_waitcnt lgkmcnt(0)\n\t"
        "s_add_u32 s64, s64, s66\n\t"
        "s_add_u32 s64, s64, s68\n\t"
        "s_add_u32 s64, s64, s70\n\t"
        "s_add_u32 s64, s64, s72\n\t"
        "s_add_u32 s64, s64, s74\n\t"
        "s_add_u32 s64, s64, s76\n\t"
        "s_add_u32 s64, s64, s78\n\t"
        "s_add_u32 s64, s64, s80\n\t"
        "s_add_u32 s64, s64, s82\n\t"
        "s_add_u32 s64, s64, s84\n\t"
        "s_add_u32 s64, s64, s86\n\t"
        "s_lshl_b32 s64, s64, 1\n\t"
        "s_mov_b32 %[c0], s90\n\t"
        "s_mov_b32 %[r0], s64\n\t"
        "s_sub_u32 s88, s95, s64\n\t"
        "s_mul_i32 s88, s88, %[kr]\n\t"
        "s_ashr_i32 s88, s88, 12\n\t"
        "s_sub_u32 s88, s88, 244\n\t"
        "s_nop 1\n\t"
        "s_max_i32 s88, s88, 0\n\t"
        "s_min_i32 s88, s88, 384\n\t"
        "s_andn2_b32 s88, s88, 3\n\t"
        "s_getpc_b64 s[90:91]\n\t"
        "Lga_%=:\n\t"
        "s_sub_u32 s90, s90, s88\n\t"
        "s_subb_u32 s91, s91, 0\n\t"
        "s_add_u32 s90, s90, Lza_%=-Lga_%=\n\t"
        "s_addc_u32 s91, s91, 0\n\t"
        "s_setpc_b64 s[90:91]\n\t"
        "s_nop 0\n\ts_nop 0\n\ts_nop 0\n\ts_nop 0\n\t"
        "s_nop 0\n\ts_nop 0\n\ts_nop 0\n\ts_nop 0\n\t"
        "s_nop 0\n\ts_nop 0\n\ts_nop 0\n\ts_nop 0\n\t"
        "s_nop 0\n\ts_nop 0\n\ts_nop 0\n\ts_nop 0\n\t"
        "s_nop 0\n\ts_nop 0\n\ts_nop 0\n\ts_nop 0\n\t"
        "s_nop 0\n\ts_nop 0\n\ts_nop 0\n\ts_nop 0\n\t"
        "s_nop 0\n\ts_nop 0\n\ts_nop 0\n\ts_nop 0\n\t"
        "s_nop 0\n\ts_nop 0\n\ts_nop 0\n\ts_nop 0\n\t"
        "s_nop 0\n\ts_nop 0\n\ts_nop 0\n\ts_nop 0\n\t"
        "s_nop 0\n\ts_nop 0\n\ts_nop 0\n\ts_nop 0\n\t"
        "s_nop 0\n\ts_nop 0\n\ts_nop 0\n\ts_nop 0\n\t"
        "s_nop 0\n\ts_nop 0\n\ts_nop 0\n\ts_nop 0\n\t"
        "s_nop 0\n\ts_nop 0\n\ts_nop 0\n\ts_nop 0\n\t"
        "s_nop 0\n\ts_nop 0\n\ts_nop 0\n\ts_nop 0\n\t"
        "s_nop 0\n\ts_nop 0\n\ts_nop 0\n\ts_nop 0\n\t"
        "s_nop 0\n\ts_nop 0\n\ts_nop 0\n\ts_nop 0\n\t"
        "s_nop 0\n\ts_nop 0\n\ts_nop 0\n\ts_nop 0\n\t"
        "s_nop 0\n\ts_nop 0\n\ts_nop 0\n\ts_nop 0\n\t"
        "s_nop 0\n\ts_nop 0\n\ts_nop 0\n\ts_nop 0\n\t"
        "s_nop 0\n\ts_nop 0\n\ts_nop 0\n\ts_nop 0\n\t"
        "s_nop 0\n\ts_nop 0\n\ts_nop 0\n\ts_nop 0\n\t"
        "s_nop 0\n\ts_nop 0\n\ts_nop 0\n\ts_nop 0\n\t"
        "s_nop 0\n\ts_nop 0\n\ts_nop 0\n\ts_nop 0\n\t"
        "s_nop 0\n\ts_nop 0\n\ts_nop 0\n\ts_nop 0\n\t"
        "Lza_%=:\n\t"
        "buffer_load_dwordx4 %[vm], %[rs], %[so] offen offset:0 sc1 lds\n\t"
        "buffer_load_dwordx4 %[vm], %[rs], %[so] offen offset:1024 sc1 lds\n\t"
        "buffer_load_dwordx4 %[vm], %[rs], %[so] offen offset:2048 sc1 lds\n\t"
        "buffer_load_dwordx4 %[vm], %[rs], %[so] offen offset:3072 sc1 lds\n\t"
        "s_add_u32 m0, m0, 0x1000\n\t"
        "s_nop 0\n\t"
        "buffer_load_dwordx4 %[vm], %[rs], s92 offen offset:0 sc1 lds\n\t"
        "buffer_load_dwordx4 %[vm], %[rs], s92 offen offset:1024 sc1 lds\n\t"
        "buffer_load_dwordx4 %[vm], %[rs], s92 offen offset:2048 sc1 lds\n\t"
        "buffer_load_dwordx4 %[vm], %[rs], s92 offen offset:3072 sc1 lds\n\t"
        "s_add_u32 m0, m0, 0x1000\n\t"
        "s_nop 0\n\t"
        "buffer_load_dwordx4 %[vm], %[rs], s93 offen offset:0 sc1 lds\n\t"
        "buffer_load_dwordx4 %[vm], %[rs], s93 offen offset:2048 sc1 lds\n\t"
        "s_cmp_lg_u32 s96, 0\n\t"
        "s_cbranch_scc1 Lnn%=\n\t"
        "v_readlane_b32 %[gb], v200, 0\n\t"
        "s_and_b32 %[gb], %[gb], 15\n\t"
        "s_lshl_b32 %[gb], %[gb], 4\n\t"
        "v_subrev_u32 v200, %[tn], v200\n\t"
        "v_bfe_i32 v200, v200, 0, 30\n\t"
        "s_nop 1\n\t"
        "v_max_i32_dpp v200, v200, v200 quad_perm:[1,0,3,2] row_mask:0xf bank_mask:0xf\n\t"
        "s_nop 1\n\t"
        "v_max_i32_dpp v200, v200, v200 quad_perm:[2,3,0,1] row_mask:0xf bank_mask:0xf\n\t"
        "s_nop 1\n\t"
        "v_max_i32_dpp v200, v200, v200 row_ror:4 row_mask:0xf bank_mask:0xf\n\t"
        "s_nop 1\n\t"
        "v_max_i32_dpp v200, v200, v200 row_ror:8 row_mask:0xf bank_mask:0xf\n\t"
        "s_nop 0\n\t"
        "v_readlane_b32 s88, v200, 0\n\t"
        "v_readlane_b32 s89, v200, 16\n\t"
        "s_max_i32 s88, s88, s89\n\t"
        "s_add_u32 %[tn], %[tn], s88\n\t"
        "Lnn%=:\n\t"
        "s_mov_b32 %[te], %[tn]\n\t"
        "s_add_u32 %[tn], %[tn], %[df]\n\t"
        "s_add_u32 %[tn], %[tn], %[gb]\n\t"
        "Lpb%=:\n\t"
        "v_mbcnt_lo_u32_b32 v202, -1, 0\n\t"
        "v_mbcnt_hi_u32_b32 v202, -1, v202\n\t"
        "v_lshl_add_u32 v205, v202, 7, %[lz]\n\t"
        "v_and_b32 v202, 31, v202\n\t"
        "v_lshl_add_u32 v205, v202, 2, v205\n\t"
        "v_mov_b32 v206, %[va]\n\t"
        "s_mov_b32 %[dl], 81920\n\t"
        "v_mov_b64 v[176:177], 0\n\t"
        "v_mov_b64 v[178:179], 0\n\t"
        "v_mov_b64 v[180:181], 0\n\t"
        "v_mov_b64 v[182:183], 0\n\t"
        "v_mov_b64 v[184:185], 0\n\t"
        "v_mov_b64 v[186:187], 0\n\t"
        "v_mov_b64 v[188:189], 0\n\t"
        "v_mov_b64 v[190:191], 0\n\t"
        "v_mov_b64 v[192:193], 0\n\t"
        "v_mov_b64 v[194:195], 0\n\t"
        "s_mov_b32 m0, s94\n\t"
        "s_waitcnt vmcnt(0)\n\t"
        "Lb1%=:\n\t"
        "ds_read2st64_b32 v[202:203], v205 offset1:32\n\t"
        "s_barrier\n\t"
        "s_waitcnt lgkmcnt(0)\n\t"
        "s_mov_b64 exec, 0x00ff00ff\n\t"
        "v_max_u32 v202, v202, v203\n\t"
        "s_mov_b64 exec, -1\n\t"
        "v_cmp_eq_u32 vcc, 0xffbfffbf, v202\n\t"
        "s_cbranch_vccnz Lbad%=\n\t"
        "s_barrier\n\t"
        "ds_read_b64_tr_b16 v[248:249], %[vr] offset:0\n\t"
        "ds_read_b64_tr_b16 v[250:251], %[vr] offset:256\n\t"
        "ds_read_b64_tr_b16 v[252:253], %[vr] offset:512\n\t"
        "ds_read_b64_tr_b16 v[254:255], %[vr] offset:768\n\t"
        "s_waitcnt lgkmcnt(0)\n\t"
        "v_lshlrev_b32 v204, 16, v248\n\t"
        "v_cmp_u_f32 vcc, v204, v204\n\t"
        "s_cbranch_vccnz Lnb%=\n\t"
        "Lok%=:\n\t"
        "v_smfmac_f32_16x16x64_bf16 v[176:179], v[248:251], v[16:23], %[si]\n\t"
        "v_smfmac_f32_16x16x64_bf16 v[180:183], v[248:251], v[48:55], %[si]\n\t"
        "v_smfmac_f32_16x16x64_bf16 v[184:187], v[248:251], v[80:87], %[si]\n\t"
        "v_smfmac_f32_16x16x64_bf16 v[188:191], v[248:251], v[112:119], %[si]\n\t"
        "v_smfmac_f32_16x16x64_bf16 v[192:195], v[248:251], v[144:151], %[si]\n\t"
        "v_smfmac_f32_16x16x64_bf16 v[176:179], v[252:255], v[24:31], %[si]\n\t"
        "v_smfmac_f32_16x16x64_bf16 v[180:183], v[252:255], v[56:63], %[si]\n\t"
        "v_smfmac_f32_16x16x64_bf16 v[184:187], v[252:255], v[88:95], %[si]\n\t"
        "s_barrier\n\t"
        "ds_read_b64_tr_b16 v[208:209], %[vr] offset:1024\n\t"
        "ds_read_b64_tr_b16 v[210:211], %[vr] offset:1280\n\t"
        "ds_read_b64_tr_b16 v[212:213], %[vr] offset:1536\n\t"
        "ds_read_b64_tr_b16 v[214:215], %[vr] offset:1792\n\t"
        "v_smfmac_f32_16x16x64_bf16 v[188:191], v[252:255], v[120:127], %[si]\n\t"
        "v_smfmac_f32_16x16x64_bf16 v[192:195], v[252:255], v[152:159], %[si]\n\t"
        "s_waitcnt lgkmcnt(2)\n\t"
        "v_smfmac_f32_16x16x64_bf16 v[176:179], v[208:211], v[32:39], %[si]\n\t"
        "s_waitcnt lgkmcnt(0)\n\t"
        "v_smfmac_f32_16x16x64_bf16 v[176:179], v[212:215], v[40:47], %[si]\n\t"
        "v_smfmac_f32_16x16x64_bf16 v[180:183], v[208:211], v[64:71], %[si]\n\t"
        "v_smfmac_f32_16x16x64_bf16 v[180:183], v[212:215], v[72:79], %[si]\n\t"
        "v_cvt_pk_bf16_f32 v216, v176, v177\n\t"
        "v_smfmac_f32_16x16x64_bf16 v[184:187], v[208:211], v[96:103], %[si]\n\t"
        "v_cvt_pk_bf16_f32 v217, v178, v179\n\t"
        "v_smfmac_f32_16x16x64_bf16 v[184:187], v[212:215], v[104:111], %[si]\n\t"
        "v_cvt_pk_bf16_f32 v218, v180, v181\n\t"
        "v_smfmac_f32_16x16x64_bf16 v[188:191], v[208:211], v[128:135], %[si]\n\t"
        "v_cvt_pk_bf16_f32 v219, v182, v183\n\t"
        "v_smfmac_f32_16x16x64_bf16 v[188:191], v[212:215], v[136:143], %[si]\n\t"
        "v_permlane16_swap_b32 v216, v218\n\t"
        "v_smfmac_f32_16x16x64_bf16 v[192:195], v[208:211], v[160:167], %[si]\n\t"
        "v_permlane16_swap_b32 v217, v219\n\t"
        "buffer_store_dwordx4 v[216:219], %[z1], %[rs], 0 offen sc1\n\t"
        "v_smfmac_f32_16x16x64_bf16 v[192:195], v[212:215], v[168:175], %[si]\n\t"
        "v_cvt_pk_bf16_f32 v220, v184, v185\n\t"
        "v_cvt_pk_bf16_f32 v222, v188, v189\n\t"
        "v_cvt_pk_bf16_f32 v221, v186, v187\n\t"
        "v_cvt_pk_bf16_f32 v223, v190, v191\n\t"
        "v_permlane16_swap_b32 v220, v222\n\t"
        "s_nop 0\n\t"
        "v_permlane16_swap_b32 v221, v223\n\t"
        "buffer_store_dwordx4 v[220:223], %[z2], %[rs], 0 offen sc1\n\t"
        "v_cvt_pk_bf16_f32 v224, v192, v193\n\t"
        "v_cvt_pk_bf16_f32 v225, v194, v195\n\t"
        "buffer_store_dwordx2 v[224:225], %[z3], %[rs], 0 offen sc1\n\t"
        "s_add_u32 m0, %[lb], 29696\n\t"
        "s_nop 0\n\t"
        "buffer_load_dword %[ro], %[rs], %[rn] offen sc1 lds\n\t"
        "s_mov_b32 m0, s94\n\t"
        "s_branch Lend%=\n\t"
        "Lnb%=:\n\t"
        "ds_read_b128 v[208:211], v206 offset:0\n\t"
        "ds_read_b128 v[212:215], v206 offset:1024\n\t"
        "ds_read_b128 v[216:219], v206 offset:2048\n\t"
        "ds_read_b128 v[220:223], v206 offset:3072\n\t"
        "ds_read_b128 v[224:227], v206 offset:4096\n\t"
        "ds_read_b128 v[228:231], v206 offset:5120\n\t"
        "ds_read_b128 v[232:235], v206 offset:6144\n\t"
        "ds_read_b128 v[236:239], v206 offset:7168\n\t"
        "ds_read_b128 v[240:243], v206 offset:8192\n\t"
        "ds_read_b128 v[244:247], v206 offset:10240\n\t"
        "s_waitcnt lgkmcnt(0)\n\t"
        "Lnan%=:\n\t"
        "v_max_u32 v207, v208, v209\n\t"
        "v_max3_u32 v207, v207, v210, v211\n\t"
        "v_max3_u32 v207, v207, v212, v213\n\t"
        "v_max3_u32 v207, v207, v214, v215\n\t"
        "v_max3_u32 v207, v207, v216, v217\n\t"
        "v_max3_u32 v207, v207, v218, v219\n\t"
        "v_max3_u32 v207, v207, v220, v221\n\t"
        "v_max3_u32 v207, v207, v222, v223\n\t"
        "v_max3_u32 v207, v207, v224, v225\n\t"
        "v_max3_u32 v207, v207, v226, v227\n\t"
        "v_max3_u32 v207, v207, v228, v229\n\t"
        "v_max3_u32 v207, v207, v230, v231\n\t"
        "v_max3_u32 v207, v207, v232, v233\n\t"
        "v_max3_u32 v207, v207, v234, v235\n\t"
        "v_max3_u32 v207, v207, v236, v237\n\t"
        "v_max3_u32 v207, v207, v238, v239\n\t"
        "v_max3_u32 v207, v207, v240, v241\n\t"
        "v_max3_u32 v207, v207, v242, v243\n\t"
        "v_max3_u32 v207, v207, v244, v245\n\t"
        "v_max3_u32 v207, v207, v246, v247\n\t"
        "v_cmp_eq_u32 vcc, 0xffbfffbf, v207\n\t"
        "s_cbranch_vccz Lok%=\n\t"
        "s_branch Lby%=\n\t"
        "Lredo%=:\n\t"
        "s_waitcnt lgkmcnt(0)\n\t"
        "v_add_u32 v206, %[dl], v206\n\t"
        "v_add_u32 v205, %[dl], v205\n\t"
        "v_mov_b64 v[248:249], 0\n\t"
        "v_mov_b64 v[250:251], 0\n\t"
        "v_mov_b64 v[252:253], 0\n\t"
        "v_mov_b64 v[254:255], 0\n\t"
        "s_add_u32 %[lz], %[lz], %[dl]\n\t"
        "s_sub_u32 %[dl], 0, %[dl]\n\t"
        "s_setprio 2\n\t"
        "s_barrier\n\t"
        "s_waitcnt vmcnt(0)\n\t"
        "s_branch Lb1%=\n\t"
        "Lbad%=:\n\t"
        "s_mov_b32 s95, 0\n\t"
        "Lbz%=:\n\t"
        "s_mov_b32 s92, %[so]\n\t"
        "s_mov_b32 %[so], %[so2]\n\t"
        "s_mov_b32 %[so2], s92\n\t"
        "s_add_u32 s92, %[so], 0x8000\n\t"
        "s_add_u32 s93, %[so], 0x10000\n\t"
        "s_add_u32 m0, %[lz], %[dl]\n\t"
        "s_sub_i32 %[left], %[left], 1\n\t"
        "s_cmp_lt_i32 %[left], 0\n\t"
        "s_cbranch_scc1 Lgu%=\n\t"
        "buffer_load_dwordx4 %[vm], %[rs], %[so] offen offset:0 sc1 lds\n\t"
        "buffer_load_dwordx4 %[vm], %[rs], %[so] offen offset:1024 sc1 lds\n\t"
        "buffer_load_dwordx4 %[vm], %[rs], %[so] offen offset:2048 sc1 lds\n\t"
        "buffer_load_dwordx4 %[vm], %[rs], %[so] offen offset:3072 sc1 lds\n\t"
        "s_add_u32 m0, m0, 0x1000\n\t"
        "s_nop 0\n\t"
        "buffer_load_dwordx4 %[vm], %[rs], s92 offen offset:0 sc1 lds\n\t"
        "buffer_load_dwordx4 %[vm], %[rs], s92 offen offset:1024 sc1 lds\n\t"
        "buffer_load_dwordx4 %[vm], %[rs], s92 offen offset:2048 sc1 lds\n\t"
        "buffer_load_dwordx4 %[vm], %[rs], s92 offen offset:3072 sc1 lds\n\t"
        "s_add_u32 m0, m0, 0x1000\n\t"
        "s_nop 0\n\t"
        "buffer_load_dwordx4 %[vm], %[rs], s93 offen offset:0 sc1 lds\n\t"
        "buffer_load_dwordx4 %[vm], %[rs], s93 offen offset:2048 sc1 lds\n\t"
        "Lgv%=:\n\t"
        "s_mov_b32 m0, s94\n\t"
        "s_cmp_lg_u32 s95, 0\n\t"
        "s_cbranch_scc1 Lredo%=\n\t"
        "s_barrier\n\t"
        "s_branch Lredo%=\n\t"
        "Lby%=:\n\t"
        "s_mov_b32 s95, 1\n\t"
        "s_branch Lbz%=\n\t"
        "Lgu%=:\n\t"
        "v_add_u32 v207, m0, %[vm]\n\t"
        "v_mov_b64 v[196:197], 0\n\t"
        "v_mov_b64 v[198:199], 0\n\t"
        "ds_write_b128 v207, v[196:199] offset:0\n\t"
        "ds_write_b128 v207, v[196:199] offset:1024\n\t"
        "ds_write_b128 v207, v[196:199] offset:2048\n\t"
        "ds_write_b128 v207, v[196:199] offset:3072\n\t"
        "ds_write_b128 v207, v[196:199] offset:4096\n\t"
        "ds_write_b128 v207, v[196:199] offset:5120\n\t"
        "ds_write_b128 v207, v[196:199] offset:6144\n\t"
        "ds_write_b128 v207, v[196:199] offset:7168\n\t"
        "ds_write_b128 v207, v[196:199] offset:8192\n\t"
        "ds_write_b128 v207, v[196:199] offset:9216\n\t"
        "ds_write_b128 v207, v[196:199] offset:10240\n\t"
        "s_branch Lgv%=\n\t"
        "Ls%=:\n\t"
        "s_mov_b32 s90, 0xbfffffff\n\t"
        "s_cmp_eq_u32 s97, 0\n\t"
        "s_cselect_b32 s90, 0xffbfffbf, s90\n\t"
        "s_mov_b32 s96, 0xc0000000\n\t"
        "s_waitcnt vmcnt(0) lgkmcnt(0)\n\t"
        "v_cmp_eq_u32 s[88:89], s90, v200\n\t"
        "v_cmp_gt_i32 vcc, s96, v200\n\t"
        "v_and_b32 v202, 0xc0000001, v200\n\t"
        "v_subrev_u32 v200, %[te], v200\n\t"
        "s_sub_u32 s99, s99, 1\n\t"
        "s_cselect_b32 s88, 0, s88\n\t"
        "s_or_b32 s89, s88, vcc_lo\n\t"
        "s_cbranch_scc0 Lu%=\n\t"
        "v_bfe_i32 v200, v200, 0, 30\n\t"
        "s_cmp_lg_u32 s88, 0\n\t"
        "v_cndmask_b32 v200, 0, v200, vcc\n\t"
        "s_cbranch_scc1 Lq%=\n\t"
        "v_cmp_eq_u32 vcc, 0x80000000, v202\n\t"
        "s_and_b32 s98, s97, 1\n\t"
        "v_max_i32_dpp v200, v200, v200 quad_perm:[1,0,3,2] row_mask:0xf bank_mask:0xf\n\t"
        "s_lshl_b32 s98, s98, 12\n\t"
        "s_lshr_b32 s88, s97, 1\n\t"
        "v_max_i32_dpp v200, v200, v200 quad_perm:[2,3,0,1] row_mask:0xf bank_mask:0xf\n\t"
        "s_lshl_b32 s88, s88, 14\n\t"
        "s_add_u32 s98, s98, s88\n\t"
        "v_max_i32_dpp v200, v200, v200 row_ror:4 row_mask:0xf bank_mask:0xf\n\t"
        "s_add_u32 s98, s98, %[rk]\n\t"
        "s_add_u32 s97, s97, 1\n\t"
        "v_max_i32_dpp v200, v200, v200 row_ror:8 row_mask:0xf bank_mask:0xf\n\t"
        "s_nop 0\n\t"
        "v_readlane_b32 s88, v200, 0\n\t"
        "v_readlane_b32 s89, v200, 16\n\t"
        "s_max_i32 s88, s88, s89\n\t"
        "s_max_i32 s88, s88, 0\n\t"
        "s_add_u32 %[te], %[te], s88\n\t"
        "s_add_u32 %[tn], %[tn], s88\n\t"
        "s_cbranch_vccz Lu%=\n\t"
        "s_cmp_gt_u32 s97, 4\n\t"
        "s_cbranch_scc1 Lu%=\n\t"
        "s_sub_u32 s90, %[te], %[lk]\n\t"
        "Lt%=:\n\t"
        "s_memrealtime s[88:89]\n\t"
        "s_waitcnt lgkmcnt(0)\n\t"
        "s_mul_i32 s88, s88, 24\n\t"
        "s_sub_u32 s88, s90, s88\n\t"
        "s_cmp_lt_u32 s88, 0x4000\n\t"
        "s_cbranch_scc1 Lt%=\n\t"
        "s_add_u32 s89, %[rk], 20480\n\t"
        "s_cmp_lt_i32 s88, -6500\n\t"
        "s_cselect_b32 s97, 4, s97\n\t"
        "s_cselect_b32 s98, s89, s98\n\t"
        "v_mov_b32 v200, 0\n\t"
        "buffer_load_dword v200, %[ro], %[rs], s98 offen sc1\n\t"
        "s_branch Ls%=\n\t"
        "Lq%=:\n\t"
        "s_sleep 6\n\t"
        "buffer_load_dword v200, %[ro], %[rs], s98 offen sc1\n\t"
        "s_branch Ls%=\n\t"
        "Lu%=:\n\t"
        "v_mov_b32 v200, 0\n\t"
        "s_branch Lg%=\n\t"
        "Lend%=:\n\t"
        : [c0] "+&s"(c0), [r0] "+&s"(r0), [so] "+&s"(so), [te] "+&s"(te), [tn] "+&s"(tn), [gb] "+&s"(gb), [lz] "+&s"(lz), [dl] "=&s"(dl), [so2] "+&s"(so2), [left] "+&s"(left)
        : [w0] "{v[16:47]}"(W[0]), [w1] "{v[48:79]}"(W[1]), [w2] "{v[80:111]}"(W[2]), [w3] "{v[112:143]}"(W[3]), [w4] "{v[144:175]}"(W[4]), [va] "v"(va), [si] "v"(si), [vr] "v"(vr), [z1] "v"(z1), [z2] "v"(z2), [z3] "v"(z3), [rs] "s"(rs), [poll] "s"(poll), [gp] "s"(gp), [kr] "s"(kr), [df] "s"(df), [rn] "s"(rn), [vm] "v"(vm), [lb] "s"(lb), [ro] "v"(ro), [rb] "s"(rb), [rk] "s"(rk), [lk] "s"(lk), [gx] "s"(gx)
        : "s64", "s65", "s66", "s67", "s68", "s69", "s70", "s71", "s72", "s73", "s74", "s75", "s76", "s77", "s78", "s79", "s80", "s81", "s82", "s83", "s84", "s85", "s86", "s87", "s88", "s89", "s90", "s91", "s92", "s93", "s94", "s95", "s96", "s97", "s98", "s99", "v176", "v177", "v178", "v179", "v180", "v181", "v182", "v183", "v184", "v185", "v186", "v187", "v188", "v189", "v190", "v191", "v192", "v193", "v194", "v195", "v196", "v197", "v198", "v199", "v200", "v201", "v202", "v203", "v204", "v205", "v206", "v207", "v208", "v209", "v210", "v211", "v212", "v213", "v214", "v215", "v216", "v217", "v218", "v219", "v220", "v221", "v222", "v223", "v224", "v225", "v226", "v227", "v228", "v229", "v230", "v231", "v232", "v233", "v234", "v235", "v236", "v237", "v238", "v239", "v240", "v241", "v242", "v243", "v244", "v245", "v246", "v247", "v248", "v249", "v250", "v251", "v252", "v253", "v254", "v255", "vcc", "scc", "memory");
}

__device__ __forceinline__ void r_u(u32 bo, u32 eo, u32 si, u32 tw, u32x4 rs, u32 so, u32 bs, u32 fv, u32 uw, u32 lag STP) {
    asm volatile(
        ".p2align 6\n\t"
        "s_add_u32 s93, %[bs], 0x1000\n\t"
        "s_mov_b32 s94, 0x1000\n\t"
        "s_barrier\n\t"
        "buffer_load_dwordx4 v[16:19], %[bo], %[rs], %[bs] offen offset:0 sc1\n\t"
        "buffer_load_dwordx4 v[20:23], %[bo], %[rs], %[bs] offen offset:1024 sc1\n\t"
        "buffer_load_dwordx4 v[24:27], %[bo], %[rs], %[bs] offen offset:2048 sc1\n\t"
        "buffer_load_dwordx4 v[28:31], %[bo], %[rs], %[bs] offen offset:3072 sc1\n\t"
        "buffer_load_dwordx4 v[32:35], %[bo], %[rs], s93 offen offset:0 sc1\n\t"
        "buffer_load_dwordx4 v[36:39], %[bo], %[rs], s93 offen offset:1024 sc1\n\t"
        "buffer_load_dwordx4 v[40:43], %[bo], %[rs], s93 offen offset:2048 sc1\n\t"
        "buffer_load_dwordx4 v[44:47], %[bo], %[rs], s93 offen offset:3072 sc1\n\t"
        "v_mov_b64 v[64:65], 0\n\t"
        "v_mov_b64 v[66:67], 0\n\t"
        "v_mov_b64 v[68:69], 0\n\t"
        "v_mov_b64 v[70:71], 0\n\t"
        "v_mov_b64 v[72:73], 0\n\t"
        "v_mov_b64 v[74:75], 0\n\t"
        "v_mov_b64 v[76:77], 0\n\t"
        "v_mov_b64 v[78:79], 0\n\t"
        "s_mov_b32 s88, %[lag]\n\t"
        "s_setprio 3\n\t"
        "s_barrier\n\t"
        "Ll%=:\n\t"
        "s_sub_u32 s88, s88, 1\n\t"
        "s_cbranch_scc1 Lm%=\n\t"
        "s_nop 7\n\t"
        "s_branch Ll%=\n\t"
        "Lm%=:\n\t"
        "buffer_load_dwordx4 v[48:51], %[eo], %[rs], %[so] offen offset:0 sc1\n\t"
        "buffer_load_dwordx4 v[52:55], %[eo], %[rs], %[so] offen offset:1024 sc1\n\t"
        "buffer_load_dwordx4 v[56:59], %[eo], %[rs], %[so] offen offset:2048 sc1\n\t"
        "buffer_load_dwordx4 v[60:63], %[eo], %[rs], %[so] offen offset:3072 sc1\n\t"
        "s_waitcnt vmcnt(4)\n\t"
        "v_swap_b32 v17, v20\n\t"
        "v_swap_b32 v19, v22\n\t"
        "v_swap_b32 v25, v28\n\t"
        "v_swap_b32 v27, v30\n\t"
        "v_swap_b32 v33, v36\n\t"
        "v_swap_b32 v35, v38\n\t"
        "v_swap_b32 v41, v44\n\t"
        "v_swap_b32 v43, v46\n\t"
        "s_nop 0\n\t"
        "Le%=:\n\t"
        "s_waitcnt vmcnt(3)\n\t"
        "v_smfmac_f32_16x16x64_bf16 v[64:67], v[48:51], v[16:23], %[si]\n\t"
        "s_waitcnt vmcnt(2)\n\t"
        "v_smfmac_f32_16x16x64_bf16 v[68:71], v[52:55], v[24:31], %[si]\n\t"
        "s_waitcnt vmcnt(1)\n\t"
        "v_smfmac_f32_16x16x64_bf16 v[72:75], v[56:59], v[32:39], %[si]\n\t"
        "v_max3_u32 v244, v48, v52, v56\n\t"
        "s_waitcnt vmcnt(0)\n\t"
        "v_smfmac_f32_16x16x64_bf16 v[76:79], v[60:63], v[40:47], %[si]\n\t"
        "v_cvt_pk_bf16_f32 v80, v64, v65\n\t"
        "v_cvt_pk_bf16_f32 v81, v66, v67\n\t"
        "ds_write_b64 %[uw], v[80:81]\n\t"
        "v_cvt_pk_bf16_f32 v82, v68, v69\n\t"
        "v_cvt_pk_bf16_f32 v83, v70, v71\n\t"
        "ds_write_b64 %[uw], v[82:83] offset:256\n\t"
        "v_max_u32 v244, v244, v60\n\t"
        "v_cmp_eq_u32 vcc, 0xffbfffbf, v244\n\t"
        "v_cvt_pk_bf16_f32 v84, v72, v73\n\t"
        "v_cvt_pk_bf16_f32 v85, v74, v75\n\t"
        "v_cvt_pk_bf16_f32 v86, v76, v77\n\t"
        "v_cvt_pk_bf16_f32 v87, v78, v79\n\t"
        "ds_write_b64 %[uw], v[84:85] offset:1024\n\t"
        "ds_write_b64 %[uw], v[86:87] offset:1280\n\t"
        "s_cbranch_vccnz Lf%=\n\t"
        "Lh%=:\n\t"
        "s_waitcnt lgkmcnt(0)\n\t"
        "s_barrier\n\t"
        "s_setprio 0\n\t"
        "s_sleep 4\n\t"
        "s_branch Lend%=\n\t"
        "Lf%=:\n\t"
        "s_sub_u32 s94, s94, 1\n\t"
        "s_cbranch_scc1 Lh%=\n\t"
        "Lwa%=:\n\t"
        "ds_read_b64 v[244:245], %[tw] offset:0\n\t"
        "s_memrealtime s[90:91]\n\t"
        "s_waitcnt lgkmcnt(0)\n\t"
        "v_readfirstlane_b32 s88, v244\n\t"
        "v_readfirstlane_b32 s89, v245\n\t"
        "s_mul_i32 s90, s90, 24\n\t"
        "s_cmp_lg_u32 s89, %[fv]\n\t"
        "s_cbranch_scc1 Lwa%=\n\t"
        "s_add_u32 s88, s88, -96\n\t"
        "s_sub_u32 s88, s88, s90\n\t"
        "s_cmp_lt_u32 s88, 0x4000\n\t"
        "s_cbranch_scc1 Lwa%=\n\t"
        "buffer_load_dwordx4 v[48:51], %[eo], %[rs], %[so] offen offset:0 sc1\n\t"
        "buffer_load_dwordx4 v[52:55], %[eo], %[rs], %[so] offen offset:1024 sc1\n\t"
        "buffer_load_dwordx4 v[56:59], %[eo], %[rs], %[so] offen offset:2048 sc1\n\t"
        "buffer_load_dwordx4 v[60:63], %[eo], %[rs], %[so] offen offset:3072 sc1\n\t"
        "v_mov_b64 v[64:65], 0\n\t"
        "v_mov_b64 v[66:67], 0\n\t"
        "v_mov_b64 v[68:69], 0\n\t"
        "v_mov_b64 v[70:71], 0\n\t"
        "v_mov_b64 v[72:73], 0\n\t"
        "v_mov_b64 v[74:75], 0\n\t"
        "v_mov_b64 v[76:77], 0\n\t"
        "v_mov_b64 v[78:79], 0\n\t"
        "s_branch Le%=\n\t"
        "Lend%=:\n\t"
        :
        : [bo] "v"(bo), [eo] "v"(eo), [si] "v"(si), [tw] "v"(tw), [rs] "s"(rs), [so] "s"(so), [bs] "s"(bs), [fv] "s"(fv), [uw] "v"(uw), [lag] "s"(lag)
        : "s88", "s89", "s90", "s91", "s92", "s93", "s94", "v16", "v17", "v18", "v19", "v20", "v21", "v22", "v23", "v24", "v25", "v26", "v27", "v28", "v29", "v30", "v31", "v32", "v33", "v34", "v35", "v36", "v37", "v38", "v39", "v40", "v41", "v42", "v43", "v44", "v45", "v46", "v47", "v48", "v49", "v50", "v51", "v52", "v53", "v54", "v55", "v56", "v57", "v58", "v59", "v60", "v61", "v62", "v63", "v64", "v65", "v66", "v67", "v68", "v69", "v70", "v71", "v72", "v73", "v74", "v75", "v76", "v77", "v78", "v79", "v80", "v81", "v82", "v83", "v84", "v85", "v86", "v87", "v244", "v245", "v246", "vcc", "scc", "memory");
}

__device__ __forceinline__ void r_f(u32 bo, u32 eo, u32 si, u32 tw, u32x4 rs, u32 so, u32 bs, u32 fv, u32& bad, u32x8 za, u32 po, u32 mo, u32 sf, u32 ur, u32 zs, u32 ps, u32 sm, u32 d0, u32 d1 STP) {
    asm volatile(
        ".p2align 6\n\t"
        "s_add_u32 s93, %[bs], 0x1000\n\t"
        "s_mov_b32 s94, 0x1000\n\t"
        "s_barrier\n\t"
        "buffer_load_dwordx4 v[16:19], %[bo], %[rs], %[bs] offen offset:0 sc1\n\t"
        "buffer_load_dwordx4 v[20:23], %[bo], %[rs], %[bs] offen offset:1024 sc1\n\t"
        "buffer_load_dwordx4 v[24:27], %[bo], %[rs], %[bs] offen offset:2048 sc1\n\t"
        "buffer_load_dwordx4 v[28:31], %[bo], %[rs], %[bs] offen offset:3072 sc1\n\t"
        "buffer_load_dwordx4 v[32:35], %[bo], %[rs], s93 offen offset:0 sc1\n\t"
        "buffer_load_dwordx4 v[36:39], %[bo], %[rs], s93 offen offset:1024 sc1\n\t"
        "buffer_load_dwordx4 v[40:43], %[bo], %[rs], s93 offen offset:2048 sc1\n\t"
        "buffer_load_dwordx4 v[44:47], %[bo], %[rs], s93 offen offset:3072 sc1\n\t"
        "v_mov_b64 v[64:65], 0\n\t"
        "v_mov_b64 v[66:67], 0\n\t"
        "v_mov_b64 v[68:69], 0\n\t"
        "v_mov_b64 v[70:71], 0\n\t"
        "v_mov_b64 v[72:73], 0\n\t"
        "v_mov_b64 v[74:75], 0\n\t"
        "v_mov_b64 v[76:77], 0\n\t"
        "v_mov_b64 v[78:79], 0\n\t"
        "s_mov_b32 %[bad], 0\n\t"
        "s_add_u32 s65, %[ps], 0x8000\n\t"
        "s_add_u32 s64, %[sm], 4096\n\t"
        "s_add_u32 s66, %[sm], 16384\n\t"
        "s_add_u32 s67, %[sm], 20480\n\t"
        "s_add_u32 s68, %[sm], 131072\n\t"
        "s_add_u32 s69, %[sm], 135168\n\t"
        "s_add_u32 s70, %[sm], 147456\n\t"
        "s_add_u32 s71, %[sm], 151552\n\t"
        "s_mov_b32 s72, 0\n\t"
        "v_mov_b64 v[180:181], 0\n\t"
        "v_mov_b64 v[182:183], 0\n\t"
        "s_setprio 3\n\t"
        "s_barrier\n\t"
        "s_getpc_b64 s[90:91]\n\t"
        "Lga_%=:\n\t"
        "s_sub_u32 s90, s90, %[d0]\n\t"
        "s_subb_u32 s91, s91, 0\n\t"
        "s_add_u32 s90, s90, Lza_%=-Lga_%=\n\t"
        "s_addc_u32 s91, s91, 0\n\t"
        "s_setpc_b64 s[90:91]\n\t"
        "s_nop 0\n\ts_nop 0\n\ts_nop 0\n\ts_nop 0\n\t"
        "s_nop 0\n\ts_nop 0\n\ts_nop 0\n\ts_nop 0\n\t"
        "s_nop 0\n\ts_nop 0\n\ts_nop 0\n\ts_nop 0\n\t"
        "s_nop 0\n\ts_nop 0\n\ts_nop 0\n\ts_nop 0\n\t"
        "s_nop 0\n\ts_nop 0\n\ts_nop 0\n\ts_nop 0\n\t"
        "s_nop 0\n\ts_nop 0\n\ts_nop 0\n\ts_nop 0\n\t"
        "s_nop 0\n\ts_nop 0\n\ts_nop 0\n\ts_nop 0\n\t"
        "s_nop 0\n\ts_nop 0\n\ts_nop 0\n\ts_nop 0\n\t"
        "s_nop 0\n\ts_nop 0\n\ts_nop 0\n\ts_nop 0\n\t"
        "s_nop 0\n\ts_nop 0\n\ts_nop 0\n\ts_nop 0\n\t"
        "s_nop 0\n\ts_nop 0\n\ts_nop 0\n\ts_nop 0\n\t"
        "s_nop 0\n\ts_nop 0\n\ts_nop 0\n\ts_nop 0\n\t"
        "s_nop 0\n\ts_nop 0\n\ts_nop 0\n\ts_nop 0\n\t"
        "s_nop 0\n\ts_nop 0\n\ts_nop 0\n\ts_nop 0\n\t"
        "s_nop 0\n\ts_nop 0\n\ts_nop 0\n\ts_nop 0\n\t"
        "s_nop 0\n\ts_nop 0\n\ts_nop 0\n\ts_nop 0\n\t"
        "Lza_%=:\n\t"
        "Lst%=:\n\t"
        "buffer_load_dwordx4 v[48:51], %[eo], %[rs], %[so] offen offset:0 sc1\n\t"
        "buffer_load_dwordx4 v[52:55], %[eo], %[rs], %[so] offen offset:1024 sc1\n\t"
        "buffer_load_dwordx4 v[56:59], %[eo], %[rs], %[so] offen offset:2048 sc1\n\t"
        "buffer_load_dwordx4 v[60:63], %[eo], %[rs], %[so] offen offset:3072 sc1\n\t"
        "buffer_load_dwordx4 v[88:91], v198, %[rs], %[zs] offen sc1\n\t"
        "buffer_load_dwordx4 v[92:95], v199, %[rs], %[zs] offen sc1\n\t"
        "buffer_load_dwordx4 v[96:99], v192, %[rs], %[zs] offen sc1\n\t"
        "buffer_load_dwordx4 v[100:103], v193, %[rs], %[zs] offen sc1\n\t"
        "buffer_load_dwordx4 v[104:107], v194, %[rs], %[zs] offen sc1\n\t"
        "buffer_load_dwordx4 v[108:111], v195, %[rs], %[zs] offen sc1\n\t"
        "buffer_load_dwordx4 v[112:115], v196, %[rs], %[zs] offen sc1\n\t"
        "buffer_load_dwordx4 v[116:119], v197, %[rs], %[zs] offen sc1\n\t"
        "s_getpc_b64 s[90:91]\n\t"
        "Lgb_%=:\n\t"
        "s_sub_u32 s90, s90, %[d1]\n\t"
        "s_subb_u32 s91, s91, 0\n\t"
        "s_add_u32 s90, s90, Lzb_%=-Lgb_%=\n\t"
        "s_addc_u32 s91, s91, 0\n\t"
        "s_setpc_b64 s[90:91]\n\t"
        "s_nop 0\n\ts_nop 0\n\ts_nop 0\n\ts_nop 0\n\t"
        "s_nop 0\n\ts_nop 0\n\ts_nop 0\n\ts_nop 0\n\t"
        "s_nop 0\n\ts_nop 0\n\ts_nop 0\n\ts_nop 0\n\t"
        "s_nop 0\n\ts_nop 0\n\ts_nop 0\n\ts_nop 0\n\t"
        "s_nop 0\n\ts_nop 0\n\ts_nop 0\n\ts_nop 0\n\t"
        "s_nop 0\n\ts_nop 0\n\ts_nop 0\n\ts_nop 0\n\t"
        "s_nop 0\n\ts_nop 0\n\ts_nop 0\n\ts_nop 0\n\t"
        "s_nop 0\n\ts_nop 0\n\ts_nop 0\n\ts_nop 0\n\t"
        "s_nop 0\n\ts_nop 0\n\ts_nop 0\n\ts_nop 0\n\t"
        "s_nop 0\n\ts_nop 0\n\ts_nop 0\n\ts_nop 0\n\t"
        "s_nop 0\n\ts_nop 0\n\ts_nop 0\n\ts_nop 0\n\t"
        "s_nop 0\n\ts_nop 0\n\ts_nop 0\n\ts_nop 0\n\t"
        "s_nop 0\n\ts_nop 0\n\ts_nop 0\n\ts_nop 0\n\t"
        "s_nop 0\n\ts_nop 0\n\ts_nop 0\n\ts_nop 0\n\t"
        "s_nop 0\n\ts_nop 0\n\ts_nop 0\n\ts_nop 0\n\t"
        "s_nop 0\n\ts_nop 0\n\ts_nop 0\n\ts_nop 0\n\t"
        "Lzb_%=:\n\t"
        "buffer_load_dwordx4 v[120:123], %[po], %[rs], s65 offen offset:1024 sc1\n\t"
        "buffer_load_dwordx4 v[124:127], %[po], %[rs], s65 offen offset:1536 sc1\n\t"
        "buffer_load_dwordx4 v[128:131], %[po], %[rs], %[ps] offen offset:0 sc1\n\t"
        "buffer_load_dwordx4 v[132:135], %[po], %[rs], %[ps] offen offset:512 sc1\n\t"
        "buffer_load_dwordx4 v[136:139], %[po], %[rs], %[ps] offen offset:1024 sc1\n\t"
        "buffer_load_dwordx4 v[140:143], %[po], %[rs], %[ps] offen offset:1536 sc1\n\t"
        "buffer_load_dwordx4 v[144:147], %[po], %[rs], s65 offen offset:0 sc1\n\t"
        "buffer_load_dwordx4 v[148:151], %[po], %[rs], s65 offen offset:512 sc1\n\t"
        "s_cmp_lg_u32 s94, 0x1000\n\t"
        "s_cbranch_scc1 Le%=\n\t"
        "ds_read_b32 v244, %[tw] offset:8\n\t"
        "s_waitcnt vmcnt(20)\n\t"
        "v_swap_b32 v17, v20\n\t"
        "v_swap_b32 v19, v22\n\t"
        "v_swap_b32 v25, v28\n\t"
        "v_swap_b32 v27, v30\n\t"
        "v_swap_b32 v33, v36\n\t"
        "v_swap_b32 v35, v38\n\t"
        "v_swap_b32 v41, v44\n\t"
        "v_swap_b32 v43, v46\n\t"
        "s_nop 0\n\t"
        "s_waitcnt lgkmcnt(0)\n\t"
        "v_cmp_ne_u32 vcc, %[fv], v244\n\t"
        "s_cbranch_vccnz Lmv%=\n\t"
        "Le%=:\n\t"
        "s_waitcnt vmcnt(19)\n\t"
        "v_smfmac_f32_16x16x64_bf16 v[64:67], v[48:51], v[16:23], %[si]\n\t"
        "s_waitcnt vmcnt(18)\n\t"
        "v_smfmac_f32_16x16x64_bf16 v[68:71], v[52:55], v[24:31], %[si]\n\t"
        "s_waitcnt vmcnt(17)\n\t"
        "v_smfmac_f32_16x16x64_bf16 v[72:75], v[56:59], v[32:39], %[si]\n\t"
        "v_max3_u32 v244, v48, v52, v56\n\t"
        "s_waitcnt vmcnt(16)\n\t"
        "v_smfmac_f32_16x16x64_bf16 v[76:79], v[60:63], v[40:47], %[si]\n\t"
        "v_cvt_pk_bf16_f32 v80, v64, v65\n\t"
        "v_cvt_pk_bf16_f32 v81, v66, v67\n\t"
        "v_cvt_pk_bf16_f32 v82, v68, v69\n\t"
        "v_cvt_pk_bf16_f32 v83, v70, v71\n\t"
        "v_max_u32 v244, v244, v60\n\t"
        "v_cmp_eq_u32 vcc, 0xffbfffbf, v244\n\t"
        "v_permlane16_swap_b32 v80, v82\n\t"
        "v_permlane16_swap_b32 v81, v83\n\t"
        "v_cvt_pk_bf16_f32 v84, v72, v73\n\t"
        "v_cvt_pk_bf16_f32 v85, v74, v75\n\t"
        "v_cvt_pk_bf16_f32 v86, v76, v77\n\t"
        "v_cvt_pk_bf16_f32 v87, v78, v79\n\t"
        "s_cbranch_vccnz Lf%=\n\t"
        "Lh%=:\n\t"
        "s_barrier\n\t"
        "ds_read_b128 v[156:159], %[ur] offset:0\n\t"
        "ds_read_b128 v[160:163], %[ur] offset:1024\n\t"
        "ds_read_b128 v[164:167], %[ur] offset:2048\n\t"
        "ds_read_b128 v[168:171], %[ur] offset:3072\n\t"
        "ds_read_b128 v[172:175], %[ur] offset:4096\n\t"
        "ds_read_b128 v[176:179], %[ur] offset:5120\n\t"
        "v_permlane16_swap_b32 v84, v86\n\t"
        "v_permlane16_swap_b32 v85, v87\n\t"
        "Lgo%=:\n\t"
        "s_waitcnt vmcnt(7)\n\t"
        "v_mov_b64 v[152:153], v[120:121]\n\t"
        "s_waitcnt vmcnt(6)\n\t"
        "v_smfmac_f32_16x16x64_bf16 v[180:183], v[122:125], v[80:87], %[sf]\n\t"
        "s_waitcnt vmcnt(5)\n\t"
        "v_smfmac_f32_16x16x64_bf16 v[180:183], v[126:129], v[88:95], %[sf]\n\t"
        "s_waitcnt vmcnt(4) lgkmcnt(4)\n\t"
        "v_smfmac_f32_16x16x64_bf16 v[180:183], v[130:133], v[156:163], %[sf]\n\t"
        "s_waitcnt vmcnt(3)\n\t"
        "v_smfmac_f32_16x16x64_bf16 v[180:183], v[134:137], v[96:103], %[sf]\n\t"
        "s_waitcnt vmcnt(2) lgkmcnt(2)\n\t"
        "v_smfmac_f32_16x16x64_bf16 v[180:183], v[138:141], v[164:171], %[sf]\n\t"
        "s_waitcnt vmcnt(1)\n\t"
        "v_smfmac_f32_16x16x64_bf16 v[180:183], v[142:145], v[104:111], %[sf]\n\t"
        "s_waitcnt vmcnt(0) lgkmcnt(0)\n\t"
        "v_smfmac_f32_16x16x64_bf16 v[180:183], v[146:149], v[172:179], %[sf]\n\t"
        "s_waitcnt vmcnt(0)\n\t"
        "v_smfmac_f32_16x16x64_bf16 v[180:183], v[150:153], v[112:119], %[sf]\n\t"
        "s_nop 7\n\t"
        "v_add_f32 v184, v180, v182\n\t"
        "v_add_f32 v185, v181, v183\n\t"
        "v_add_f32 v186, v180, v182\n\t"
        "s_nop 0\n\t"
        "v_mov_b32_dpp v186, v185 row_ror:8 row_mask:0xf bank_mask:0xc\n\t"
        "v_mov_b32_dpp v185, v184 row_ror:8 row_mask:0xf bank_mask:0x3\n\t"
        "v_cvt_pk_bf16_f32 v187, v186, v185\n\t"
        "v_cmpx_o_f32 vcc, v186, v185\n\t"
        "buffer_store_dword v187, %[mo], %[rs], %[sm] offen sc1\n\t"
        "buffer_store_dword v187, %[mo], %[rs], s64 offen sc1\n\t"
        "buffer_store_dword v187, %[mo], %[rs], s66 offen sc1\n\t"
        "buffer_store_dword v187, %[mo], %[rs], s67 offen sc1\n\t"
        "buffer_store_dword v187, %[mo], %[rs], s68 offen sc1\n\t"
        "buffer_store_dword v187, %[mo], %[rs], s69 offen sc1\n\t"
        "buffer_store_dword v187, %[mo], %[rs], s70 offen sc1\n\t"
        "buffer_store_dword v187, %[mo], %[rs], s71 offen sc1\n\t"
        "s_cmp_eq_u64 exec, -1\n\t"
        "s_mov_b64 exec, -1\n\t"
        "s_cbranch_scc1 Lend%=\n\t"
        "v_max_u32 v244, v88, v120\n\t"
        "v_max3_u32 v244, v244, v92, v124\n\t"
        "v_max3_u32 v244, v244, v96, v128\n\t"
        "v_max3_u32 v244, v244, v100, v132\n\t"
        "v_max3_u32 v244, v244, v104, v136\n\t"
        "v_max3_u32 v244, v244, v108, v140\n\t"
        "v_max3_u32 v244, v244, v112, v144\n\t"
        "v_max3_u32 v244, v244, v116, v148\n\t"
        "v_cmp_eq_u32 vcc, 0xffbfffbf, v244\n\t"
        "s_mov_b32 %[bad], 3\n\t"
        "s_sub_i32 s94, s94, 1\n\t"
        "s_cmp_lt_i32 s94, 0\n\t"
        "s_cbranch_scc1 Lend%=\n\t"
        "s_cbranch_vccz Lend%=\n\t"
        "s_mov_b32 %[bad], 1\n\t"
        "buffer_load_dwordx4 v[88:91], v198, %[rs], %[zs] offen sc1\n\t"
        "buffer_load_dwordx4 v[92:95], v199, %[rs], %[zs] offen sc1\n\t"
        "buffer_load_dwordx4 v[96:99], v192, %[rs], %[zs] offen sc1\n\t"
        "buffer_load_dwordx4 v[100:103], v193, %[rs], %[zs] offen sc1\n\t"
        "buffer_load_dwordx4 v[104:107], v194, %[rs], %[zs] offen sc1\n\t"
        "buffer_load_dwordx4 v[108:111], v195, %[rs], %[zs] offen sc1\n\t"
        "buffer_load_dwordx4 v[112:115], v196, %[rs], %[zs] offen sc1\n\t"
        "buffer_load_dwordx4 v[116:119], v197, %[rs], %[zs] offen sc1\n\t"
        "buffer_load_dwordx4 v[120:123], %[po], %[rs], s65 offen offset:1024 sc1\n\t"
        "buffer_load_dwordx4 v[124:127], %[po], %[rs], s65 offen offset:1536 sc1\n\t"
        "buffer_load_dwordx4 v[128:131], %[po], %[rs], %[ps] offen offset:0 sc1\n\t"
        "buffer_load_dwordx4 v[132:135], %[po], %[rs], %[ps] offen offset:512 sc1\n\t"
        "buffer_load_dwordx4 v[136:139], %[po], %[rs], %[ps] offen offset:1024 sc1\n\t"
        "buffer_load_dwordx4 v[140:143], %[po], %[rs], %[ps] offen offset:1536 sc1\n\t"
        "buffer_load_dwordx4 v[144:147], %[po], %[rs], s65 offen offset:0 sc1\n\t"
        "buffer_load_dwordx4 v[148:151], %[po], %[rs], s65 offen offset:512 sc1\n\t"
        "v_mov_b64 v[180:181], 0\n\t"
        "v_mov_b64 v[182:183], 0\n\t"
        "s_branch Lgo%=\n\t"
        "Lmv%=:\n\t"
        "ds_read_b96 v[244:246], %[tw]\n\t"
        "s_waitcnt lgkmcnt(0)\n\t"
        "v_readfirstlane_b32 s88, v245\n\t"
        "v_readfirstlane_b32 s89, v246\n\t"
        "s_cmp_lg_u32 s88, %[fv]\n\t"
        "s_cbranch_scc1 Lmv%=\n\t"
        "s_cmp_eq_u32 s89, %[fv]\n\t"
        "s_cbranch_scc1 Le%=\n\t"
        "Lf%=:\n\t"
        "s_sub_u32 s94, s94, 1\n\t"
        "s_cbranch_scc1 Lh%=\n\t"
        "Lwa%=:\n\t"
        "ds_read_b64 v[244:245], %[tw] offset:0\n\t"
        "s_memrealtime s[90:91]\n\t"
        "s_waitcnt lgkmcnt(0)\n\t"
        "v_readfirstlane_b32 s88, v244\n\t"
        "v_readfirstlane_b32 s89, v245\n\t"
        "s_mul_i32 s90, s90, 24\n\t"
        "s_cmp_lg_u32 s89, %[fv]\n\t"
        "s_cbranch_scc1 Lwa%=\n\t"
        "s_add_u32 s88, s88, 32\n\t"
        "s_sub_u32 s88, s88, s90\n\t"
        "s_cmp_lt_u32 s88, 0x4000\n\t"
        "s_cbranch_scc1 Lwa%=\n\t"
        "v_mov_b64 v[64:65], 0\n\t"
        "v_mov_b64 v[66:67], 0\n\t"
        "v_mov_b64 v[68:69], 0\n\t"
        "v_mov_b64 v[70:71], 0\n\t"
        "v_mov_b64 v[72:73], 0\n\t"
        "v_mov_b64 v[74:75], 0\n\t"
        "v_mov_b64 v[76:77], 0\n\t"
        "v_mov_b64 v[78:79], 0\n\t"
        "s_branch Lst%=\n\t"
        "Lend%=:\n\t"
        : [bad] "=&s"(bad)
        : [bo] "v"(bo), [eo] "v"(eo), [si] "v"(si), [tw] "v"(tw), [rs] "s"(rs), [so] "s"(so), [bs] "s"(bs), [fv] "s"(fv), [po] "v"(po), [mo] "v"(mo), [sf] "v"(sf), [ur] "v"(ur), [za] "{v[192:199]}"(za), [zs] "s"(zs), [ps] "s"(ps), [sm] "s"(sm), [d0] "s"(d0), [d1] "s"(d1)
        : "s64", "s65", "s66", "s67", "s68", "s69", "s70", "s71", "s72", "s88", "s89", "s90", "s91", "s92", "s93", "s94", "v16", "v17", "v18", "v19", "v20", "v21", "v22", "v23", "v24", "v25", "v26", "v27", "v28", "v29", "v30", "v31", "v32", "v33", "v34", "v35", "v36", "v37", "v38", "v39", "v40", "v41", "v42", "v43", "v44", "v45", "v46", "v47", "v48", "v49", "v50", "v51", "v52", "v53", "v54", "v55", "v56", "v57", "v58", "v59", "v60", "v61", "v62", "v63", "v64", "v65", "v66", "v67", "v68", "v69", "v70", "v71", "v72", "v73", "v74", "v75", "v76", "v77", "v78", "v79", "v80", "v81", "v82", "v83", "v84", "v85", "v86", "v87", "v88", "v89", "v90", "v91", "v92", "v93", "v94", "v95", "v96", "v97", "v98", "v99", "v100", "v101", "v102", "v103", "v104", "v105", "v106", "v107", "v108", "v109", "v110", "v111", "v112", "v113", "v114", "v115", "v116", "v117", "v118", "v119", "v120", "v121", "v122", "v123", "v124", "v125", "v126", "v127", "v128", "v129", "v130", "v131", "v132", "v133", "v134", "v135", "v136", "v137", "v138", "v139", "v140", "v141", "v142", "v143", "v144", "v145", "v146", "v147", "v148", "v149", "v150", "v151", "v152", "v153", "v154", "v155", "v156", "v157", "v158", "v159", "v160", "v161", "v162", "v163", "v164", "v165", "v166", "v167", "v168", "v169", "v170", "v171", "v172", "v173", "v174", "v175", "v176", "v177", "v178", "v179", "v180", "v181", "v182", "v183", "v184", "v185", "v186", "v187", "v188", "v189", "v190", "v191", "v244", "v245", "v246", "vcc", "scc", "memory");
}

__device__ __forceinline__ void r_t(u32& c0, u32& r0, u32& te, u32& tn, u32& gb, u32& nl, u32 tw, u32x4 rs, u32 fv, u32 kr, u32 tb, u32 lk0, u32 lk, u32 ro, u32 rb, u32 rk STP) {
    asm volatile(
        ".p2align 6\n\t"
        "s_mov_b64 s[96:97], 0\n\t"
        "s_mov_b32 s98, %[rb]\n\t"
        "s_mov_b32 s99, 0x2000\n\t"
        "v_mov_b32 v200, 0\n\t"
        "s_mov_b32 s92, 0\n\t"
        "s_mov_b32 s94, %[fv]\n\t"
        "s_sub_u32 s95, %[te], %[lk0]\n\t"
        "s_setprio 3\n\t"
        "Lua_%=:\n\t"
        "s_memrealtime s[90:91]\n\t"
        "s_waitcnt lgkmcnt(0)\n\t"
        "s_mul_i32 s90, s90, 24\n\t"
        "s_sub_u32 s88, %[tb], s90\n\t"
        "s_cmp_lt_u32 s88, 0x4000\n\t"
        "s_cbranch_scc1 Lua_%=\n\t"
        "s_barrier\n\t"
        "s_sub_u32 s93, s95, 520\n\t"
        "Lub_%=:\n\t"
        "s_memrealtime s[90:91]\n\t"
        "s_waitcnt lgkmcnt(0)\n\t"
        "s_mul_i32 s90, s90, 24\n\t"
        "s_sub_u32 s88, s93, s90\n\t"
        "s_cmp_lt_u32 s88, 0x4000\n\t"
        "s_cbranch_scc1 Lub_%=\n\t"
        "s_mov_b32 s63, s88\n\t"
        "s_memrealtime s[64:65]\n\t"
        "s_nop 0\n\t"
        "s_memrealtime s[66:67]\n\t"
        "s_nop 0\n\t"
        "s_memrealtime s[68:69]\n\t"
        "s_nop 0\n\t"
        "s_memrealtime s[70:71]\n\t"
        "s_nop 0\n\t"
        "s_memrealtime s[72:73]\n\t"
        "s_nop 0\n\t"
        "s_memrealtime s[74:75]\n\t"
        "s_memtime s[90:91]\n\t"
        "s_memrealtime s[76:77]\n\t"
        "s_nop 0\n\t"
        "s_memrealtime s[78:79]\n\t"
        "s_nop 0\n\t"
        "s_memrealtime s[80:81]\n\t"
        "s_nop 0\n\t"
        "s_memrealtime s[82:83]\n\t"
        "s_nop 0\n\t"
        "s_memrealtime s[84:85]\n\t"
        "s_nop 0\n\t"
        "s_memrealtime s[86:87]\n\t"
        "s_nop 0\n\t"
        "s_nop 7\n\t"
        "s_waitcnt lgkmcnt(0)\n\t"
        "s_add_u32 s64, s64, s66\n\t"
        "s_add_u32 s64, s64, s68\n\t"
        "s_add_u32 s64, s64, s70\n\t"
        "s_add_u32 s64, s64, s72\n\t"
        "s_add_u32 s64, s64, s74\n\t"
        "s_add_u32 s64, s64, s76\n\t"
        "s_add_u32 s64, s64, s78\n\t"
        "s_add_u32 s64, s64, s80\n\t"
        "s_add_u32 s64, s64, s82\n\t"
        "s_add_u32 s64, s64, s84\n\t"
        "s_add_u32 s64, s64, s86\n\t"
        "s_lshl_b32 s64, s64, 1\n\t"
        "s_mov_b32 %[c0], s90\n\t"
        "s_mov_b32 %[r0], s64\n\t"
        "s_sub_u32 s93, s95, %[r0]\n\t"
        "s_mul_i32 s93, s93, %[kr]\n\t"
        "s_ashr_i32 s93, s93, 12\n\t"
        "s_add_u32 s93, s93, %[c0]\n\t"
        "Ldl_%=:\n\t"
        "s_memtime s[90:91]\n\t"
        "s_waitcnt lgkmcnt(0)\n\t"
        "s_sub_u32 s88, s93, s90\n\t"
        "s_sub_u32 s89, s88, 336\n\t"
        "s_cmp_gt_u32 s89, 0x4000\n\t"
        "s_cbranch_scc1 Lel_%=\n\t"
        "s_sleep 1\n\t"
        "s_branch Ldl_%=\n\t"
        "Lel_%=:\n\t"
        "s_sub_u32 s88, s88, 112\n\t"
        "s_max_i32 s88, s88, 0\n\t"
        "s_min_i32 s88, s88, 224\n\t"
        "s_andn2_b32 s88, s88, 3\n\t"
        "s_getpc_b64 s[90:91]\n\t"
        "Lgl_%=:\n\t"
        "s_sub_u32 s90, s90, s88\n\t"
        "s_subb_u32 s91, s91, 0\n\t"
        "s_add_u32 s90, s90, Lzl_%=-Lgl_%=\n\t"
        "s_addc_u32 s91, s91, 0\n\t"
        "s_setpc_b64 s[90:91]\n\t"
        "s_nop 0\n\ts_nop 0\n\ts_nop 0\n\ts_nop 0\n\t"
        "s_nop 0\n\ts_nop 0\n\ts_nop 0\n\ts_nop 0\n\t"
        "s_nop 0\n\ts_nop 0\n\ts_nop 0\n\ts_nop 0\n\t"
        "s_nop 0\n\ts_nop 0\n\ts_nop 0\n\ts_nop 0\n\t"
        "s_nop 0\n\ts_nop 0\n\ts_nop 0\n\ts_nop 0\n\t"
        "s_nop 0\n\ts_nop 0\n\ts_nop 0\n\ts_nop 0\n\t"
        "s_nop 0\n\ts_nop 0\n\ts_nop 0\n\ts_nop 0\n\t"
        "s_nop 0\n\ts_nop 0\n\ts_nop 0\n\ts_nop 0\n\t"
        "s_nop 0\n\ts_nop 0\n\ts_nop 0\n\ts_nop 0\n\t"
        "s_nop 0\n\ts_nop 0\n\ts_nop 0\n\ts_nop 0\n\t"
        "s_nop 0\n\ts_nop 0\n\ts_nop 0\n\ts_nop 0\n\t"
        "s_nop 0\n\ts_nop 0\n\ts_nop 0\n\ts_nop 0\n\t"
        "s_nop 0\n\ts_nop 0\n\ts_nop 0\n\ts_nop 0\n\t"
        "s_nop 0\n\ts_nop 0\n\ts_nop 0\n\ts_nop 0\n\t"
        "Lzl_%=:\n\t"
        "buffer_load_dword v200, %[ro], %[rs], %[rb] offen sc1\n\t"
        "s_cmp_lt_i32 s63, -700\n\t"
        "s_cbranch_scc0 Ly%=\n\t"
        "s_waitcnt vmcnt(0)\n\t"
        "Ly%=:\n\t"
        "s_sub_u32 s93, %[te], %[r0]\n\t"
        "s_mul_i32 s93, s93, %[kr]\n\t"
        "s_ashr_i32 s93, s93, 12\n\t"
        "s_add_u32 s93, s93, %[c0]\n\t"
        "Lde_%=:\n\t"
        "s_memtime s[90:91]\n\t"
        "v_cmp_gt_i32 vcc, s96, v200\n\t"
        "s_waitcnt lgkmcnt(0)\n\t"
        "s_cbranch_vccnz Ls%=\n\t"
        "s_sub_u32 s88, s93, s90\n\t"
        "s_sub_u32 s89, s88, 396\n\t"
        "s_cmp_gt_u32 s89, 0x4000\n\t"
        "s_cbranch_scc1 Lee_%=\n\t"
        "s_sleep 1\n\t"
        "s_branch Lde_%=\n\t"
        "Lee_%=:\n\t"
        "s_sub_u32 s88, s88, 172\n\t"
        "s_max_i32 s88, s88, 0\n\t"
        "s_min_i32 s88, s88, 224\n\t"
        "s_andn2_b32 s88, s88, 3\n\t"
        "s_getpc_b64 s[90:91]\n\t"
        "Lge_%=:\n\t"
        "s_sub_u32 s90, s90, s88\n\t"
        "s_subb_u32 s91, s91, 0\n\t"
        "s_add_u32 s90, s90, Lze_%=-Lge_%=\n\t"
        "s_addc_u32 s91, s91, 0\n\t"
        "s_setpc_b64 s[90:91]\n\t"
        "s_nop 0\n\ts_nop 0\n\ts_nop 0\n\ts_nop 0\n\t"
        "s_nop 0\n\ts_nop 0\n\ts_nop 0\n\ts_nop 0\n\t"
        "s_nop 0\n\ts_nop 0\n\ts_nop 0\n\ts_nop 0\n\t"
        "s_nop 0\n\ts_nop 0\n\ts_nop 0\n\ts_nop 0\n\t"
        "s_nop 0\n\ts_nop 0\n\ts_nop 0\n\ts_nop 0\n\t"
        "s_nop 0\n\ts_nop 0\n\ts_nop 0\n\ts_nop 0\n\t"
        "s_nop 0\n\ts_nop 0\n\ts_nop 0\n\ts_nop 0\n\t"
        "s_nop 0\n\ts_nop 0\n\ts_nop 0\n\ts_nop 0\n\t"
        "s_nop 0\n\ts_nop 0\n\ts_nop 0\n\ts_nop 0\n\t"
        "s_nop 0\n\ts_nop 0\n\ts_nop 0\n\ts_nop 0\n\t"
        "s_nop 0\n\ts_nop 0\n\ts_nop 0\n\ts_nop 0\n\t"
        "s_nop 0\n\ts_nop 0\n\ts_nop 0\n\ts_nop 0\n\t"
        "s_nop 0\n\ts_nop 0\n\ts_nop 0\n\ts_nop 0\n\t"
        "s_nop 0\n\ts_nop 0\n\ts_nop 0\n\ts_nop 0\n\t"
        "Lze_%=:\n\t"
        "v_cmp_gt_i32 vcc, s96, v200\n\t"
        "s_cbranch_vccnz Ls%=\n\t"
        "s_barrier\n\t"
        "s_mov_b32 s92, 1\n\t"
        "s_waitcnt vmcnt(0)\n\t"
        "v_cmp_gt_i32 vcc, s96, v200\n\t"
        "v_mov_b32 v197, %[fv]\n\t"
        "s_cbranch_vccnz Ls%=\n\t"
        "Lw%=:\n\t"
        "s_sub_u32 %[nl], 0x2000, s99\n\t"
        "v_mov_b32 v196, %[te]\n\t"
        "v_mov_b32 v198, s94\n\t"
        "ds_write_b96 %[tw], v[196:198]\n\t"
        "s_cmp_lg_u32 s96, 0\n\t"
        "s_cbranch_scc1 Lnn%=\n\t"
        "v_readlane_b32 %[gb], v200, 0\n\t"
        "s_and_b32 %[gb], %[gb], 15\n\t"
        "s_lshl_b32 %[gb], %[gb], 4\n\t"
        "v_subrev_u32 v200, %[tn], v200\n\t"
        "v_bfe_i32 v200, v200, 0, 30\n\t"
        "s_nop 1\n\t"
        "v_max_i32_dpp v200, v200, v200 quad_perm:[1,0,3,2] row_mask:0xf bank_mask:0xf\n\t"
        "s_nop 1\n\t"
        "v_max_i32_dpp v200, v200, v200 quad_perm:[2,3,0,1] row_mask:0xf bank_mask:0xf\n\t"
        "s_nop 1\n\t"
        "v_max_i32_dpp v200, v200, v200 row_ror:4 row_mask:0xf bank_mask:0xf\n\t"
        "s_nop 1\n\t"
        "v_max_i32_dpp v200, v200, v200 row_ror:8 row_mask:0xf bank_mask:0xf\n\t"
        "s_nop 0\n\t"
        "v_readlane_b32 s88, v200, 0\n\t"
        "v_readlane_b32 s89, v200, 16\n\t"
        "s_max_i32 s88, s88, s89\n\t"
        "s_add_u32 %[tn], %[tn], s88\n\t"
        "Lnn%=:\n\t"
        "s_waitcnt lgkmcnt(0)\n\t"
        "s_barrier\n\t"
        "s_branch Lend%=\n\t"
        "Ls%=:\n\t"
        "s_mov_b32 s90, 0xbfffffff\n\t"
        "s_cmp_eq_u32 s97, 0\n\t"
        "s_cselect_b32 s90, 0xffbfffbf, s90\n\t"
        "s_mov_b32 s96, 0xc0000000\n\t"
        "s_waitcnt vmcnt(0) lgkmcnt(0)\n\t"
        "v_cmp_eq_u32 s[88:89], s90, v200\n\t"
        "v_cmp_gt_i32 vcc, s96, v200\n\t"
        "v_and_b32 v202, 0xc0000001, v200\n\t"
        "v_subrev_u32 v200, %[te], v200\n\t"
        "s_sub_u32 s99, s99, 1\n\t"
        "s_cselect_b32 s88, 0, s88\n\t"
        "s_or_b32 s89, s88, vcc_lo\n\t"
        "s_cbranch_scc0 Lu%=\n\t"
        "v_bfe_i32 v200, v200, 0, 30\n\t"
        "s_cmp_lg_u32 s88, 0\n\t"
        "v_cndmask_b32 v200, 0, v200, vcc\n\t"
        "s_cbranch_scc1 Lq%=\n\t"
        "v_cmp_eq_u32 vcc, 0x80000000, v202\n\t"
        "s_and_b32 s98, s97, 1\n\t"
        "v_max_i32_dpp v200, v200, v200 quad_perm:[1,0,3,2] row_mask:0xf bank_mask:0xf\n\t"
        "s_lshl_b32 s98, s98, 12\n\t"
        "s_lshr_b32 s88, s97, 1\n\t"
        "v_max_i32_dpp v200, v200, v200 quad_perm:[2,3,0,1] row_mask:0xf bank_mask:0xf\n\t"
        "s_lshl_b32 s88, s88, 14\n\t"
        "s_add_u32 s98, s98, s88\n\t"
        "v_max_i32_dpp v200, v200, v200 row_ror:4 row_mask:0xf bank_mask:0xf\n\t"
        "s_add_u32 s98, s98, %[rk]\n\t"
        "s_add_u32 s97, s97, 1\n\t"
        "v_max_i32_dpp v200, v200, v200 row_ror:8 row_mask:0xf bank_mask:0xf\n\t"
        "s_nop 0\n\t"
        "v_readlane_b32 s88, v200, 0\n\t"
        "v_readlane_b32 s89, v200, 16\n\t"
        "s_max_i32 s88, s88, s89\n\t"
        "s_max_i32 s88, s88, 0\n\t"
        "s_add_u32 %[te], %[te], s88\n\t"
        "s_add_u32 %[tn], %[tn], s88\n\t"
        "s_cbranch_vccz Lu%=\n\t"
        "s_cmp_gt_u32 s97, 4\n\t"
        "s_cbranch_scc1 Lu%=\n\t"
        "s_sub_u32 s90, %[te], %[lk]\n\t"
        "Lt%=:\n\t"
        "s_memrealtime s[88:89]\n\t"
        "s_waitcnt lgkmcnt(0)\n\t"
        "s_mul_i32 s88, s88, 24\n\t"
        "s_sub_u32 s88, s90, s88\n\t"
        "s_cmp_lt_u32 s88, 0x4000\n\t"
        "s_cbranch_scc1 Lt%=\n\t"
        "s_add_u32 s89, %[rk], 20480\n\t"
        "s_cmp_lt_i32 s88, -6500\n\t"
        "s_cselect_b32 s97, 4, s97\n\t"
        "s_cselect_b32 s98, s89, s98\n\t"
        "v_mov_b32 v200, 0\n\t"
        "buffer_load_dword v200, %[ro], %[rs], s98 offen sc1\n\t"
        "s_add_u32 s88, s88, %[lk]\n\t"
        "s_cmp_lt_i32 s88, 0\n\t"
        "s_cbranch_scc1 Ls%=\n\t"
        "s_cmp_eq_u32 s92, 0\n\t"
        "s_cbranch_scc1 La%=\n\t"
        "s_branch Ls%=\n\t"
        "Lq%=:\n\t"
        "s_sleep 6\n\t"
        "buffer_load_dword v200, %[ro], %[rs], s98 offen sc1\n\t"
        "s_branch Ls%=\n\t"
        "Lu%=:\n\t"
        "v_mov_b32 v200, 0\n\t"
        "s_cmp_lg_u32 s92, 0\n\t"
        "s_cselect_b32 s94, 0, s94\n\t"
        "s_cbranch_scc1 Lw%=\n\t"
        "La%=:\n\t"
        "s_sub_u32 s88, %[te], %[r0]\n\t"
        "s_cmp_lt_i32 s88, 4000\n\t"
        "s_cbranch_scc1 Ly%=\n\t"
        "s_memrealtime s[64:65]\n\t"
        "s_nop 0\n\t"
        "s_memrealtime s[66:67]\n\t"
        "s_nop 0\n\t"
        "s_memrealtime s[68:69]\n\t"
        "s_nop 0\n\t"
        "s_memrealtime s[70:71]\n\t"
        "s_nop 0\n\t"
        "s_memrealtime s[72:73]\n\t"
        "s_nop 0\n\t"
        "s_memrealtime s[74:75]\n\t"
        "s_memtime s[90:91]\n\t"
        "s_memrealtime s[76:77]\n\t"
        "s_nop 0\n\t"
        "s_memrealtime s[78:79]\n\t"
        "s_nop 0\n\t"
        "s_memrealtime s[80:81]\n\t"
        "s_nop 0\n\t"
        "s_memrealtime s[82:83]\n\t"
        "s_nop 0\n\t"
        "s_memrealtime s[84:85]\n\t"
        "s_nop 0\n\t"
        "s_memrealtime s[86:87]\n\t"
        "s_nop 0\n\t"
        "s_nop 7\n\t"
        "s_waitcnt lgkmcnt(0)\n\t"
        "s_add_u32 s64, s64, s66\n\t"
        "s_add_u32 s64, s64, s68\n\t"
        "s_add_u32 s64, s64, s70\n\t"
        "s_add_u32 s64, s64, s72\n\t"
        "s_add_u32 s64, s64, s74\n\t"
        "s_add_u32 s64, s64, s76\n\t"
        "s_add_u32 s64, s64, s78\n\t"
        "s_add_u32 s64, s64, s80\n\t"
        "s_add_u32 s64, s64, s82\n\t"
        "s_add_u32 s64, s64, s84\n\t"
        "s_add_u32 s64, s64, s86\n\t"
        "s_lshl_b32 s64, s64, 1\n\t"
        "s_mov_b32 %[c0], s90\n\t"
        "s_mov_b32 %[r0], s64\n\t"
        "s_branch Ly%=\n\t"
        "Lend%=:\n\t"
        : [c0] "=&s"(c0), [r0] "=&s"(r0), [nl] "=&s"(nl), [te] "+&s"(te), [tn] "+&s"(tn), [gb] "+&s"(gb)
        : [tw] "v"(tw), [rs] "s"(rs), [fv] "s"(fv), [kr] "s"(kr), [tb] "s"(tb), [lk0] "s"(lk0), [ro] "v"(ro), [rb] "s"(rb), [rk] "s"(rk), [lk] "s"(lk)
        : "s63", "s64", "s65", "s66", "s67", "s68", "s69", "s70", "s71", "s72", "s73", "s74", "s75", "s76", "s77", "s78", "s79", "s80", "s81", "s82", "s83", "s84", "s85", "s86", "s87", "s88", "s89", "s90", "s91", "s92", "s93", "s94", "s95", "s96", "s97", "s98", "s99", "v196", "v197", "v198", "v199", "v200", "v201", "v202", "vcc", "scc", "memory");
}

__device__ __forceinline__ void w_q(const u32x32 (&W)[5], u32x4 rs, u32 now, u32 xa, u32 vm, u32 xs, u32 xd, u32 o1, u32 ob) {
    asm volatile(
        ".p2align 6\n\t"
        "s_setprio 2\n\t"
        "s_barrier\n\t"
        "ds_read_b128 v[176:179], %[xa] offset:0\n\t"
        "ds_read_b128 v[180:183], %[xa] offset:20480\n\t"
        "ds_read_b128 v[184:187], %[xa] offset:1024\n\t"
        "ds_read_b128 v[188:191], %[xa] offset:21504\n\t"
        "ds_read_b128 v[192:195], %[xa] offset:2048\n\t"
        "ds_read_b128 v[196:199], %[xa] offset:22528\n\t"
        "ds_read_b128 v[200:203], %[xa] offset:3072\n\t"
        "ds_read_b128 v[204:207], %[xa] offset:23552\n\t"
        "s_waitcnt lgkmcnt(7)\n\t"
        "v_mfma_f32_16x16x32_bf16 v[208:211], v[16:19], v[176:179], 0\n\t"
        "v_mfma_f32_16x16x32_bf16 v[212:215], v[96:99], v[176:179], 0\n\t"
        "ds_read_b128 v[176:179], %[xa] offset:4096\n\t"
        "s_waitcnt lgkmcnt(7)\n\t"
        "v_mfma_f32_16x16x32_bf16 v[216:219], v[16:19], v[180:183], 0\n\t"
        "v_mfma_f32_16x16x32_bf16 v[220:223], v[96:99], v[180:183], 0\n\t"
        "ds_read_b128 v[180:183], %[xa] offset:24576\n\t"
        "s_waitcnt lgkmcnt(7)\n\t"
        "v_mfma_f32_16x16x32_bf16 v[208:211], v[20:23], v[184:187], v[208:211]\n\t"
        "v_mfma_f32_16x16x32_bf16 v[212:215], v[100:103], v[184:187], v[212:215]\n\t"
        "ds_read_b128 v[184:187], %[xa] offset:5120\n\t"
        "s_waitcnt lgkmcnt(7)\n\t"
        "v_mfma_f32_16x16x32_bf16 v[216:219], v[20:23], v[188:191], v[216:219]\n\t"
        "v_mfma_f32_16x16x32_bf16 v[220:223], v[100:103], v[188:191], v[220:223]\n\t"
        "ds_read_b128 v[188:191], %[xa] offset:25600\n\t"
        "s_waitcnt lgkmcnt(7)\n\t"
        "v_mfma_f32_16x16x32_bf16 v[208:211], v[24:27], v[192:195], v[208:211]\n\t"
        "v_mfma_f32_16x16x32_bf16 v[212:215], v[104:107], v[192:195], v[212:215]\n\t"
        "ds_read_b128 v[192:195], %[xa] offset:6144\n\t"
        "s_waitcnt lgkmcnt(7)\n\t"
        "v_mfma_f32_16x16x32_bf16 v[216:219], v[24:27], v[196:199], v[216:219]\n\t"
        "v_mfma_f32_16x16x32_bf16 v[220:223], v[104:107], v[196:199], v[220:223]\n\t"
        "ds_read_b128 v[196:199], %[xa] offset:26624\n\t"
        "s_waitcnt lgkmcnt(7)\n\t"
        "v_mfma_f32_16x16x32_bf16 v[208:211], v[28:31], v[200:203], v[208:211]\n\t"
        "v_mfma_f32_16x16x32_bf16 v[212:215], v[108:111], v[200:203], v[212:215]\n\t"
        "ds_read_b128 v[200:203], %[xa] offset:7168\n\t"
        "s_waitcnt lgkmcnt(7)\n\t"
        "v_mfma_f32_16x16x32_bf16 v[216:219], v[28:31], v[204:207], v[216:219]\n\t"
        "v_mfma_f32_16x16x32_bf16 v[220:223], v[108:111], v[204:207], v[220:223]\n\t"
        "ds_read_b128 v[204:207], %[xa] offset:27648\n\t"
        "s_waitcnt lgkmcnt(7)\n\t"
        "v_mfma_f32_16x16x32_bf16 v[208:211], v[32:35], v[176:179], v[208:211]\n\t"
        "v_mfma_f32_16x16x32_bf16 v[212:215], v[112:115], v[176:179], v[212:215]\n\t"
        "ds_read_b128 v[176:179], %[xa] offset:8192\n\t"
        "s_waitcnt lgkmcnt(7)\n\t"
        "v_mfma_f32_16x16x32_bf16 v[216:219], v[32:35], v[180:183], v[216:219]\n\t"
        "v_mfma_f32_16x16x32_bf16 v[220:223], v[112:115], v[180:183], v[220:223]\n\t"
        "ds_read_b128 v[180:183], %[xa] offset:28672\n\t"
        "s_waitcnt lgkmcnt(7)\n\t"
        "v_mfma_f32_16x16x32_bf16 v[208:211], v[36:39], v[184:187], v[208:211]\n\t"
        "v_mfma_f32_16x16x32_bf16 v[212:215], v[116:119], v[184:187], v[212:215]\n\t"
        "ds_read_b128 v[184:187], %[xa] offset:9216\n\t"
        "s_waitcnt lgkmcnt(7)\n\t"
        "v_mfma_f32_16x16x32_bf16 v[216:219], v[36:39], v[188:191], v[216:219]\n\t"
        "v_mfma_f32_16x16x32_bf16 v[220:223], v[116:119], v[188:191], v[220:223]\n\t"
        "ds_read_b128 v[188:191], %[xa] offset:29696\n\t"
        "s_waitcnt lgkmcnt(7)\n\t"
        "v_mfma_f32_16x16x32_bf16 v[208:211], v[40:43], v[192:195], v[208:211]\n\t"
        "v_mfma_f32_16x16x32_bf16 v[212:215], v[120:123], v[192:195], v[212:215]\n\t"
        "ds_read_b128 v[192:195], %[xa] offset:10240\n\t"
        "s_waitcnt lgkmcnt(7)\n\t"
        "v_mfma_f32_16x16x32_bf16 v[216:219], v[40:43], v[196:199], v[216:219]\n\t"
        "v_mfma_f32_16x16x32_bf16 v[220:223], v[120:123], v[196:199], v[220:223]\n\t"
        "ds_read_b128 v[196:199], %[xa] offset:30720\n\t"
        "s_waitcnt lgkmcnt(7)\n\t"
        "v_mfma_f32_16x16x32_bf16 v[208:211], v[44:47], v[200:203], v[208:211]\n\t"
        "v_mfma_f32_16x16x32_bf16 v[212:215], v[124:127], v[200:203], v[212:215]\n\t"
        "ds_read_b128 v[200:203], %[xa] offset:11264\n\t"
        "s_waitcnt lgkmcnt(7)\n\t"
        "v_mfma_f32_16x16x32_bf16 v[216:219], v[44:47], v[204:207], v[216:219]\n\t"
        "v_mfma_f32_16x16x32_bf16 v[220:223], v[124:127], v[204:207], v[220:223]\n\t"
        "ds_read_b128 v[204:207], %[xa] offset:31744\n\t"
        "s_waitcnt lgkmcnt(7)\n\t"
        "v_mfma_f32_16x16x32_bf16 v[208:211], v[48:51], v[176:179], v[208:211]\n\t"
        "v_mfma_f32_16x16x32_bf16 v[212:215], v[128:131], v[176:179], v[212:215]\n\t"
        "ds_read_b128 v[176:179], %[xa] offset:12288\n\t"
        "s_waitcnt lgkmcnt(7)\n\t"
        "v_mfma_f32_16x16x32_bf16 v[216:219], v[48:51], v[180:183], v[216:219]\n\t"
        "v_mfma_f32_16x16x32_bf16 v[220:223], v[128:131], v[180:183], v[220:223]\n\t"
        "ds_read_b128 v[180:183], %[xa] offset:32768\n\t"
        "s_waitcnt lgkmcnt(7)\n\t"
        "v_mfma_f32_16x16x32_bf16 v[208:211], v[52:55], v[184:187], v[208:211]\n\t"
        "v_mfma_f32_16x16x32_bf16 v[212:215], v[132:135], v[184:187], v[212:215]\n\t"
        "ds_read_b128 v[184:187], %[xa] offset:13312\n\t"
        "s_waitcnt lgkmcnt(7)\n\t"
        "v_mfma_f32_16x16x32_bf16 v[216:219], v[52:55], v[188:191], v[216:219]\n\t"
        "v_mfma_f32_16x16x32_bf16 v[220:223], v[132:135], v[188:191], v[220:223]\n\t"
        "ds_read_b128 v[188:191], %[xa] offset:33792\n\t"
        "s_waitcnt lgkmcnt(7)\n\t"
        "v_mfma_f32_16x16x32_bf16 v[208:211], v[56:59], v[192:195], v[208:211]\n\t"
        "v_mfma_f32_16x16x32_bf16 v[212:215], v[136:139], v[192:195], v[212:215]\n\t"
        "ds_read_b128 v[192:195], %[xa] offset:14336\n\t"
        "s_waitcnt lgkmcnt(7)\n\t"
        "v_mfma_f32_16x16x32_bf16 v[216:219], v[56:59], v[196:199], v[216:219]\n\t"
        "v_mfma_f32_16x16x32_bf16 v[220:223], v[136:139], v[196:199], v[220:223]\n\t"
        "ds_read_b128 v[196:199], %[xa] offset:34816\n\t"
        "s_waitcnt lgkmcnt(7)\n\t"
        "v_mfma_f32_16x16x32_bf16 v[208:211], v[60:63], v[200:203], v[208:211]\n\t"
        "v_mfma_f32_16x16x32_bf16 v[212:215], v[140:143], v[200:203], v[212:215]\n\t"
        "ds_read_b128 v[200:203], %[xa] offset:15360\n\t"
        "s_waitcnt lgkmcnt(7)\n\t"
        "v_mfma_f32_16x16x32_bf16 v[216:219], v[60:63], v[204:207], v[216:219]\n\t"
        "v_mfma_f32_16x16x32_bf16 v[220:223], v[140:143], v[204:207], v[220:223]\n\t"
        "ds_read_b128 v[204:207], %[xa] offset:35840\n\t"
        "s_waitcnt lgkmcnt(7)\n\t"
        "v_mfma_f32_16x16x32_bf16 v[208:211], v[64:67], v[176:179], v[208:211]\n\t"
        "v_mfma_f32_16x16x32_bf16 v[212:215], v[144:147], v[176:179], v[212:215]\n\t"
        "ds_read_b128 v[176:179], %[xa] offset:16384\n\t"
        "s_waitcnt lgkmcnt(7)\n\t"
        "v_mfma_f32_16x16x32_bf16 v[216:219], v[64:67], v[180:183], v[216:219]\n\t"
        "v_mfma_f32_16x16x32_bf16 v[220:223], v[144:147], v[180:183], v[220:223]\n\t"
        "ds_read_b128 v[180:183], %[xa] offset:36864\n\t"
        "s_waitcnt lgkmcnt(7)\n\t"
        "v_mfma_f32_16x16x32_bf16 v[208:211], v[68:71], v[184:187], v[208:211]\n\t"
        "v_mfma_f32_16x16x32_bf16 v[212:215], v[148:151], v[184:187], v[212:215]\n\t"
        "ds_read_b128 v[184:187], %[xa] offset:17408\n\t"
        "s_waitcnt lgkmcnt(7)\n\t"
        "v_mfma_f32_16x16x32_bf16 v[216:219], v[68:71], v[188:191], v[216:219]\n\t"
        "v_mfma_f32_16x16x32_bf16 v[220:223], v[148:151], v[188:191], v[220:223]\n\t"
        "ds_read_b128 v[188:191], %[xa] offset:37888\n\t"
        "s_waitcnt lgkmcnt(7)\n\t"
        "v_mfma_f32_16x16x32_bf16 v[208:211], v[72:75], v[192:195], v[208:211]\n\t"
        "v_mfma_f32_16x16x32_bf16 v[212:215], v[152:155], v[192:195], v[212:215]\n\t"
        "ds_read_b128 v[192:195], %[xa] offset:18432\n\t"
        "s_waitcnt lgkmcnt(7)\n\t"
        "v_mfma_f32_16x16x32_bf16 v[216:219], v[72:75], v[196:199], v[216:219]\n\t"
        "v_mfma_f32_16x16x32_bf16 v[220:223], v[152:155], v[196:199], v[220:223]\n\t"
        "ds_read_b128 v[196:199], %[xa] offset:38912\n\t"
        "s_waitcnt lgkmcnt(7)\n\t"
        "v_mfma_f32_16x16x32_bf16 v[208:211], v[76:79], v[200:203], v[208:211]\n\t"
        "v_mfma_f32_16x16x32_bf16 v[212:215], v[156:159], v[200:203], v[212:215]\n\t"
        "ds_read_b128 v[200:203], %[xa] offset:19456\n\t"
        "s_waitcnt lgkmcnt(7)\n\t"
        "v_mfma_f32_16x16x32_bf16 v[216:219], v[76:79], v[204:207], v[216:219]\n\t"
        "v_mfma_f32_16x16x32_bf16 v[220:223], v[156:159], v[204:207], v[220:223]\n\t"
        "ds_read_b128 v[204:207], %[xa] offset:39936\n\t"
        "s_waitcnt lgkmcnt(7)\n\t"
        "v_mfma_f32_16x16x32_bf16 v[208:211], v[80:83], v[176:179], v[208:211]\n\t"
        "v_mfma_f32_16x16x32_bf16 v[212:215], v[160:163], v[176:179], v[212:215]\n\t"
        "s_waitcnt lgkmcnt(6)\n\t"
        "v_mfma_f32_16x16x32_bf16 v[216:219], v[80:83], v[180:183], v[216:219]\n\t"
        "v_mfma_f32_16x16x32_bf16 v[220:223], v[160:163], v[180:183], v[220:223]\n\t"
        "s_waitcnt lgkmcnt(5)\n\t"
        "v_mfma_f32_16x16x32_bf16 v[208:211], v[84:87], v[184:187], v[208:211]\n\t"
        "v_mfma_f32_16x16x32_bf16 v[212:215], v[164:167], v[184:187], v[212:215]\n\t"
        "s_waitcnt lgkmcnt(4)\n\t"
        "v_mfma_f32_16x16x32_bf16 v[216:219], v[84:87], v[188:191], v[216:219]\n\t"
        "v_mfma_f32_16x16x32_bf16 v[220:223], v[164:167], v[188:191], v[220:223]\n\t"
        "s_waitcnt lgkmcnt(3)\n\t"
        "v_mfma_f32_16x16x32_bf16 v[208:211], v[88:91], v[192:195], v[208:211]\n\t"
        "v_mfma_f32_16x16x32_bf16 v[212:215], v[168:171], v[192:195], v[212:215]\n\t"
        "s_waitcnt lgkmcnt(2)\n\t"
        "v_mfma_f32_16x16x32_bf16 v[216:219], v[88:91], v[196:199], v[216:219]\n\t"
        "v_mfma_f32_16x16x32_bf16 v[220:223], v[168:171], v[196:199], v[220:223]\n\t"
        "s_waitcnt lgkmcnt(1)\n\t"
        "v_mfma_f32_16x16x32_bf16 v[208:211], v[92:95], v[200:203], v[208:211]\n\t"
        "v_mfma_f32_16x16x32_bf16 v[212:215], v[172:175], v[200:203], v[212:215]\n\t"
        "s_waitcnt lgkmcnt(0)\n\t"
        "v_mfma_f32_16x16x32_bf16 v[216:219], v[92:95], v[204:207], v[216:219]\n\t"
        "v_mfma_f32_16x16x32_bf16 v[220:223], v[172:175], v[204:207], v[220:223]\n\t"
        "s_setprio 0\n\t"
        "s_nop 6\n\t"
        "v_cvt_pk_bf16_f32 v224, v208, v209\n\t"
        "v_cvt_pk_bf16_f32 v225, v210, v211\n\t"
        "v_cvt_pk_bf16_f32 v226, v212, v213\n\t"
        "v_cvt_pk_bf16_f32 v227, v214, v215\n\t"
        "v_cvt_pk_bf16_f32 v228, v216, v217\n\t"
        "v_cvt_pk_bf16_f32 v229, v218, v219\n\t"
        "v_cvt_pk_bf16_f32 v230, v220, v221\n\t"
        "v_cvt_pk_bf16_f32 v231, v222, v223\n\t"
        "s_add_u32 s93, %[ob], 0x1000\n\t"
        "buffer_store_dwordx4 v[224:227], %[o1], %[rs], %[ob] offen sc1\n\t"
        "buffer_store_dwordx4 v[228:231], %[o1], %[rs], s93 offen sc1\n\t"
        "s_mov_b32 s92, m0\n\t"
        "s_add_u32 m0, %[xd], 0\n\t"
        "s_add_u32 s93, %[xs], 0\n\t"
        "buffer_load_dwordx4 %[vm], %[rs], s93 offen offset:0 sc1 lds\n\t"
        "buffer_load_dwordx4 %[vm], %[rs], s93 offen offset:1024 sc1 lds\n\t"
        "buffer_load_dwordx4 %[vm], %[rs], s93 offen offset:2048 sc1 lds\n\t"
        "buffer_load_dwordx4 %[vm], %[rs], s93 offen offset:3072 sc1 lds\n\t"
        "s_add_u32 m0, %[xd], 4096\n\t"
        "s_add_u32 s93, %[xs], 4096\n\t"
        "buffer_load_dwordx4 %[vm], %[rs], s93 offen offset:0 sc1 lds\n\t"
        "s_mov_b32 m0, s92\n\t"
        "s_cmp_eq_u32 %[now], 0\n\t"
        "s_cbranch_scc1 Ln%=\n\t"
        "s_waitcnt vmcnt(0)\n\t"
        "Ln%=:\n\t"
        "s_waitcnt vmcnt(7)\n\t"
        :
        : [w0] "{v[16:47]}"(W[0]), [w1] "{v[48:79]}"(W[1]), [w2] "{v[80:111]}"(W[2]), [w3] "{v[112:143]}"(W[3]), [w4] "{v[144:175]}"(W[4]), [rs] "s"(rs), [xa] "v"(xa), [o1] "v"(o1), [ob] "s"(ob), [vm] "v"(vm), [xs] "s"(xs), [xd] "s"(xd), [now] "s"(now)
        : "s92", "s93", "v176", "v177", "v178", "v179", "v180", "v181", "v182", "v183", "v184", "v185", "v186", "v187", "v188", "v189", "v190", "v191", "v192", "v193", "v194", "v195", "v196", "v197", "v198", "v199", "v200", "v201", "v202", "v203", "v204", "v205", "v206", "v207", "v208", "v209", "v210", "v211", "v212", "v213", "v214", "v215", "v216", "v217", "v218", "v219", "v220", "v221", "v222", "v223", "v224", "v225", "v226", "v227", "v228", "v229", "v230", "v231", "vcc", "scc", "memory");
}

__device__ __forceinline__ void w_k(const u32x32 (&W)[5], u32x4 rs, u32 now, u32 xa, u32 vm, u32 xs, u32 xd, u32 on, u32 o1, u32 ob, float ks, f32x16& acc) {
    asm volatile(
        ".p2align 6\n\t"
        "s_nop 7\n\t"
        "v_mul_f32 v208, %[ks], v208\n\t"
        "v_mul_f32 v209, %[ks], v209\n\t"
        "v_mul_f32 v210, %[ks], v210\n\t"
        "v_mul_f32 v211, %[ks], v211\n\t"
        "v_mul_f32 v212, %[ks], v212\n\t"
        "v_mul_f32 v213, %[ks], v213\n\t"
        "v_mul_f32 v214, %[ks], v214\n\t"
        "v_mul_f32 v215, %[ks], v215\n\t"
        "v_mul_f32 v216, %[ks], v216\n\t"
        "v_mul_f32 v217, %[ks], v217\n\t"
        "v_mul_f32 v218, %[ks], v218\n\t"
        "v_mul_f32 v219, %[ks], v219\n\t"
        "v_mul_f32 v220, %[ks], v220\n\t"
        "v_mul_f32 v221, %[ks], v221\n\t"
        "v_mul_f32 v222, %[ks], v222\n\t"
        "v_mul_f32 v223, %[ks], v223\n\t"
        "v_cvt_pk_bf16_f32 v224, v208, v209\n\t"
        "v_cvt_pk_bf16_f32 v225, v210, v211\n\t"
        "v_cvt_pk_bf16_f32 v226, v212, v213\n\t"
        "v_cvt_pk_bf16_f32 v227, v214, v215\n\t"
        "v_cvt_pk_bf16_f32 v228, v216, v217\n\t"
        "v_cvt_pk_bf16_f32 v229, v218, v219\n\t"
        "v_cvt_pk_bf16_f32 v230, v220, v221\n\t"
        "v_cvt_pk_bf16_f32 v231, v222, v223\n\t"
        "s_add_u32 s93, %[ob], 0x1000\n\t"
        "buffer_store_dwordx4 v[224:227], %[o1], %[rs], %[ob] offen sc1\n\t"
        "buffer_store_dwordx4 v[228:231], %[o1], %[rs], s93 offen sc1\n\t"
        "s_mov_b32 s92, m0\n\t"
        "s_add_u32 m0, %[xd], 0\n\t"
        "s_add_u32 s93, %[xs], 0\n\t"
        "buffer_load_dwordx4 %[vm], %[rs], s93 offen offset:0 sc1 lds\n\t"
        "buffer_load_dwordx4 %[vm], %[rs], s93 offen offset:1024 sc1 lds\n\t"
        "buffer_load_dwordx4 %[vm], %[rs], s93 offen offset:2048 sc1 lds\n\t"
        "buffer_load_dwordx4 %[vm], %[rs], s93 offen offset:3072 sc1 lds\n\t"
        "s_add_u32 m0, %[xd], 4096\n\t"
        "s_add_u32 s93, %[xs], 4096\n\t"
        "buffer_load_dwordx4 %[vm], %[rs], s93 offen offset:0 sc1 lds\n\t"
        "s_mov_b32 m0, s92\n\t"
        "ds_read_b128 v[176:179], %[xa] offset:0\n\t"
        "ds_read_b128 v[180:183], %[xa] offset:20480\n\t"
        "ds_read_b128 v[184:187], %[xa] offset:1024\n\t"
        "ds_read_b128 v[188:191], %[xa] offset:21504\n\t"
        "ds_read_b128 v[192:195], %[xa] offset:2048\n\t"
        "ds_read_b128 v[196:199], %[xa] offset:22528\n\t"
        "ds_read_b128 v[200:203], %[xa] offset:3072\n\t"
        "ds_read_b128 v[204:207], %[xa] offset:23552\n\t"
        "s_waitcnt lgkmcnt(7)\n\t"
        "v_mfma_f32_16x16x32_bf16 v[208:211], v[16:19], v[176:179], 0\n\t"
        "v_mfma_f32_16x16x32_bf16 v[212:215], v[96:99], v[176:179], 0\n\t"
        "ds_read_b128 v[176:179], %[xa] offset:4096\n\t"
        "s_waitcnt lgkmcnt(7)\n\t"
        "v_mfma_f32_16x16x32_bf16 v[216:219], v[16:19], v[180:183], 0\n\t"
        "v_mfma_f32_16x16x32_bf16 v[220:223], v[96:99], v[180:183], 0\n\t"
        "ds_read_b128 v[180:183], %[xa] offset:24576\n\t"
        "s_waitcnt lgkmcnt(7)\n\t"
        "v_mfma_f32_16x16x32_bf16 v[208:211], v[20:23], v[184:187], v[208:211]\n\t"
        "v_mfma_f32_16x16x32_bf16 v[212:215], v[100:103], v[184:187], v[212:215]\n\t"
        "ds_read_b128 v[184:187], %[xa] offset:5120\n\t"
        "s_waitcnt lgkmcnt(7)\n\t"
        "v_mfma_f32_16x16x32_bf16 v[216:219], v[20:23], v[188:191], v[216:219]\n\t"
        "v_mfma_f32_16x16x32_bf16 v[220:223], v[100:103], v[188:191], v[220:223]\n\t"
        "ds_read_b128 v[188:191], %[xa] offset:25600\n\t"
        "s_waitcnt lgkmcnt(7)\n\t"
        "v_mfma_f32_16x16x32_bf16 v[208:211], v[24:27], v[192:195], v[208:211]\n\t"
        "v_mfma_f32_16x16x32_bf16 v[212:215], v[104:107], v[192:195], v[212:215]\n\t"
        "ds_read_b128 v[192:195], %[xa] offset:6144\n\t"
        "s_waitcnt lgkmcnt(7)\n\t"
        "v_mfma_f32_16x16x32_bf16 v[216:219], v[24:27], v[196:199], v[216:219]\n\t"
        "v_mfma_f32_16x16x32_bf16 v[220:223], v[104:107], v[196:199], v[220:223]\n\t"
        "ds_read_b128 v[196:199], %[xa] offset:26624\n\t"
        "s_waitcnt lgkmcnt(7)\n\t"
        "v_mfma_f32_16x16x32_bf16 v[208:211], v[28:31], v[200:203], v[208:211]\n\t"
        "v_mfma_f32_16x16x32_bf16 v[212:215], v[108:111], v[200:203], v[212:215]\n\t"
        "ds_read_b128 v[200:203], %[xa] offset:7168\n\t"
        "s_waitcnt lgkmcnt(7)\n\t"
        "v_mfma_f32_16x16x32_bf16 v[216:219], v[28:31], v[204:207], v[216:219]\n\t"
        "v_mfma_f32_16x16x32_bf16 v[220:223], v[108:111], v[204:207], v[220:223]\n\t"
        "ds_read_b128 v[204:207], %[xa] offset:27648\n\t"
        "s_waitcnt lgkmcnt(7)\n\t"
        "v_mfma_f32_16x16x32_bf16 v[208:211], v[32:35], v[176:179], v[208:211]\n\t"
        "v_mfma_f32_16x16x32_bf16 v[212:215], v[112:115], v[176:179], v[212:215]\n\t"
        "ds_read_b128 v[176:179], %[xa] offset:8192\n\t"
        "s_waitcnt lgkmcnt(7)\n\t"
        "v_mfma_f32_16x16x32_bf16 v[216:219], v[32:35], v[180:183], v[216:219]\n\t"
        "v_mfma_f32_16x16x32_bf16 v[220:223], v[112:115], v[180:183], v[220:223]\n\t"
        "ds_read_b128 v[180:183], %[xa] offset:28672\n\t"
        "s_waitcnt lgkmcnt(7)\n\t"
        "v_mfma_f32_16x16x32_bf16 v[208:211], v[36:39], v[184:187], v[208:211]\n\t"
        "v_mfma_f32_16x16x32_bf16 v[212:215], v[116:119], v[184:187], v[212:215]\n\t"
        "ds_read_b128 v[184:187], %[xa] offset:9216\n\t"
        "s_waitcnt lgkmcnt(7)\n\t"
        "v_mfma_f32_16x16x32_bf16 v[216:219], v[36:39], v[188:191], v[216:219]\n\t"
        "v_mfma_f32_16x16x32_bf16 v[220:223], v[116:119], v[188:191], v[220:223]\n\t"
        "ds_read_b128 v[188:191], %[xa] offset:29696\n\t"
        "s_waitcnt lgkmcnt(7)\n\t"
        "v_mfma_f32_16x16x32_bf16 v[208:211], v[40:43], v[192:195], v[208:211]\n\t"
        "v_mfma_f32_16x16x32_bf16 v[212:215], v[120:123], v[192:195], v[212:215]\n\t"
        "ds_read_b128 v[192:195], %[xa] offset:10240\n\t"
        "s_waitcnt lgkmcnt(7)\n\t"
        "v_mfma_f32_16x16x32_bf16 v[216:219], v[40:43], v[196:199], v[216:219]\n\t"
        "v_mfma_f32_16x16x32_bf16 v[220:223], v[120:123], v[196:199], v[220:223]\n\t"
        "ds_read_b128 v[196:199], %[xa] offset:30720\n\t"
        "s_waitcnt lgkmcnt(7)\n\t"
        "v_mfma_f32_16x16x32_bf16 v[208:211], v[44:47], v[200:203], v[208:211]\n\t"
        "v_mfma_f32_16x16x32_bf16 v[212:215], v[124:127], v[200:203], v[212:215]\n\t"
        "ds_read_b128 v[200:203], %[xa] offset:11264\n\t"
        "s_waitcnt lgkmcnt(7)\n\t"
        "v_mfma_f32_16x16x32_bf16 v[216:219], v[44:47], v[204:207], v[216:219]\n\t"
        "v_mfma_f32_16x16x32_bf16 v[220:223], v[124:127], v[204:207], v[220:223]\n\t"
        "ds_read_b128 v[204:207], %[xa] offset:31744\n\t"
        "s_waitcnt lgkmcnt(7)\n\t"
        "v_mfma_f32_16x16x32_bf16 v[208:211], v[48:51], v[176:179], v[208:211]\n\t"
        "v_mfma_f32_16x16x32_bf16 v[212:215], v[128:131], v[176:179], v[212:215]\n\t"
        "ds_read_b128 v[176:179], %[xa] offset:12288\n\t"
        "s_waitcnt lgkmcnt(7)\n\t"
        "v_mfma_f32_16x16x32_bf16 v[216:219], v[48:51], v[180:183], v[216:219]\n\t"
        "v_mfma_f32_16x16x32_bf16 v[220:223], v[128:131], v[180:183], v[220:223]\n\t"
        "ds_read_b128 v[180:183], %[xa] offset:32768\n\t"
        "s_waitcnt lgkmcnt(7)\n\t"
        "v_mfma_f32_16x16x32_bf16 v[208:211], v[52:55], v[184:187], v[208:211]\n\t"
        "v_mfma_f32_16x16x32_bf16 v[212:215], v[132:135], v[184:187], v[212:215]\n\t"
        "ds_read_b128 v[184:187], %[xa] offset:13312\n\t"
        "s_waitcnt lgkmcnt(7)\n\t"
        "v_mfma_f32_16x16x32_bf16 v[216:219], v[52:55], v[188:191], v[216:219]\n\t"
        "v_mfma_f32_16x16x32_bf16 v[220:223], v[132:135], v[188:191], v[220:223]\n\t"
        "ds_read_b128 v[188:191], %[xa] offset:33792\n\t"
        "s_waitcnt lgkmcnt(7)\n\t"
        "v_mfma_f32_16x16x32_bf16 v[208:211], v[56:59], v[192:195], v[208:211]\n\t"
        "v_mfma_f32_16x16x32_bf16 v[212:215], v[136:139], v[192:195], v[212:215]\n\t"
        "ds_read_b128 v[192:195], %[xa] offset:14336\n\t"
        "s_waitcnt lgkmcnt(7)\n\t"
        "v_mfma_f32_16x16x32_bf16 v[216:219], v[56:59], v[196:199], v[216:219]\n\t"
        "v_mfma_f32_16x16x32_bf16 v[220:223], v[136:139], v[196:199], v[220:223]\n\t"
        "ds_read_b128 v[196:199], %[xa] offset:34816\n\t"
        "s_waitcnt lgkmcnt(7)\n\t"
        "v_mfma_f32_16x16x32_bf16 v[208:211], v[60:63], v[200:203], v[208:211]\n\t"
        "v_mfma_f32_16x16x32_bf16 v[212:215], v[140:143], v[200:203], v[212:215]\n\t"
        "ds_read_b128 v[200:203], %[xa] offset:15360\n\t"
        "s_waitcnt lgkmcnt(7)\n\t"
        "v_mfma_f32_16x16x32_bf16 v[216:219], v[60:63], v[204:207], v[216:219]\n\t"
        "v_mfma_f32_16x16x32_bf16 v[220:223], v[140:143], v[204:207], v[220:223]\n\t"
        "ds_read_b128 v[204:207], %[xa] offset:35840\n\t"
        "s_waitcnt lgkmcnt(7)\n\t"
        "v_mfma_f32_16x16x32_bf16 v[208:211], v[64:67], v[176:179], v[208:211]\n\t"
        "v_mfma_f32_16x16x32_bf16 v[212:215], v[144:147], v[176:179], v[212:215]\n\t"
        "ds_read_b128 v[176:179], %[xa] offset:16384\n\t"
        "s_waitcnt lgkmcnt(7)\n\t"
        "v_mfma_f32_16x16x32_bf16 v[216:219], v[64:67], v[180:183], v[216:219]\n\t"
        "v_mfma_f32_16x16x32_bf16 v[220:223], v[144:147], v[180:183], v[220:223]\n\t"
        "ds_read_b128 v[180:183], %[xa] offset:36864\n\t"
        "s_waitcnt lgkmcnt(7)\n\t"
        "v_mfma_f32_16x16x32_bf16 v[208:211], v[68:71], v[184:187], v[208:211]\n\t"
        "v_mfma_f32_16x16x32_bf16 v[212:215], v[148:151], v[184:187], v[212:215]\n\t"
        "ds_read_b128 v[184:187], %[xa] offset:17408\n\t"
        "s_waitcnt lgkmcnt(7)\n\t"
        "v_mfma_f32_16x16x32_bf16 v[216:219], v[68:71], v[188:191], v[216:219]\n\t"
        "v_mfma_f32_16x16x32_bf16 v[220:223], v[148:151], v[188:191], v[220:223]\n\t"
        "ds_read_b128 v[188:191], %[xa] offset:37888\n\t"
        "s_waitcnt lgkmcnt(7)\n\t"
        "v_mfma_f32_16x16x32_bf16 v[208:211], v[72:75], v[192:195], v[208:211]\n\t"
        "v_mfma_f32_16x16x32_bf16 v[212:215], v[152:155], v[192:195], v[212:215]\n\t"
        "ds_read_b128 v[192:195], %[xa] offset:18432\n\t"
        "s_waitcnt lgkmcnt(7)\n\t"
        "v_mfma_f32_16x16x32_bf16 v[216:219], v[72:75], v[196:199], v[216:219]\n\t"
        "v_mfma_f32_16x16x32_bf16 v[220:223], v[152:155], v[196:199], v[220:223]\n\t"
        "ds_read_b128 v[196:199], %[xa] offset:38912\n\t"
        "s_waitcnt lgkmcnt(7)\n\t"
        "v_mfma_f32_16x16x32_bf16 v[208:211], v[76:79], v[200:203], v[208:211]\n\t"
        "v_mfma_f32_16x16x32_bf16 v[212:215], v[156:159], v[200:203], v[212:215]\n\t"
        "ds_read_b128 v[200:203], %[xa] offset:19456\n\t"
        "s_waitcnt lgkmcnt(7)\n\t"
        "v_mfma_f32_16x16x32_bf16 v[216:219], v[76:79], v[204:207], v[216:219]\n\t"
        "v_mfma_f32_16x16x32_bf16 v[220:223], v[156:159], v[204:207], v[220:223]\n\t"
        "ds_read_b128 v[204:207], %[xa] offset:39936\n\t"
        "s_waitcnt lgkmcnt(7)\n\t"
        "v_mfma_f32_16x16x32_bf16 v[208:211], v[80:83], v[176:179], v[208:211]\n\t"
        "v_mfma_f32_16x16x32_bf16 v[212:215], v[160:163], v[176:179], v[212:215]\n\t"
        "s_waitcnt lgkmcnt(6)\n\t"
        "v_mfma_f32_16x16x32_bf16 v[216:219], v[80:83], v[180:183], v[216:219]\n\t"
        "v_mfma_f32_16x16x32_bf16 v[220:223], v[160:163], v[180:183], v[220:223]\n\t"
        "s_waitcnt lgkmcnt(5)\n\t"
        "v_mfma_f32_16x16x32_bf16 v[208:211], v[84:87], v[184:187], v[208:211]\n\t"
        "v_mfma_f32_16x16x32_bf16 v[212:215], v[164:167], v[184:187], v[212:215]\n\t"
        "s_waitcnt lgkmcnt(4)\n\t"
        "v_mfma_f32_16x16x32_bf16 v[216:219], v[84:87], v[188:191], v[216:219]\n\t"
        "v_mfma_f32_16x16x32_bf16 v[220:223], v[164:167], v[188:191], v[220:223]\n\t"
        "s_waitcnt lgkmcnt(3)\n\t"
        "v_mfma_f32_16x16x32_bf16 v[208:211], v[88:91], v[192:195], v[208:211]\n\t"
        "v_mfma_f32_16x16x32_bf16 v[212:215], v[168:171], v[192:195], v[212:215]\n\t"
        "s_waitcnt lgkmcnt(2)\n\t"
        "v_mfma_f32_16x16x32_bf16 v[216:219], v[88:91], v[196:199], v[216:219]\n\t"
        "v_mfma_f32_16x16x32_bf16 v[220:223], v[168:171], v[196:199], v[220:223]\n\t"
        "s_waitcnt lgkmcnt(1)\n\t"
        "v_mfma_f32_16x16x32_bf16 v[208:211], v[92:95], v[200:203], v[208:211]\n\t"
        "v_mfma_f32_16x16x32_bf16 v[212:215], v[172:175], v[200:203], v[212:215]\n\t"
        "s_waitcnt lgkmcnt(0)\n\t"
        "v_mfma_f32_16x16x32_bf16 v[216:219], v[92:95], v[204:207], v[216:219]\n\t"
        "v_mfma_f32_16x16x32_bf16 v[220:223], v[172:175], v[204:207], v[220:223]\n\t"
        "s_cmp_eq_u32 %[now], 0\n\t"
        "s_cbranch_scc1 Ln%=\n\t"
        "s_nop 5\n\t"
        "v_mul_f32 v208, %[ks], v208\n\t"
        "v_mul_f32 v209, %[ks], v209\n\t"
        "v_mul_f32 v210, %[ks], v210\n\t"
        "v_mul_f32 v211, %[ks], v211\n\t"
        "v_mul_f32 v212, %[ks], v212\n\t"
        "v_mul_f32 v213, %[ks], v213\n\t"
        "v_mul_f32 v214, %[ks], v214\n\t"
        "v_mul_f32 v215, %[ks], v215\n\t"
        "v_mul_f32 v216, %[ks], v216\n\t"
        "v_mul_f32 v217, %[ks], v217\n\t"
        "v_mul_f32 v218, %[ks], v218\n\t"
        "v_mul_f32 v219, %[ks], v219\n\t"
        "v_mul_f32 v220, %[ks], v220\n\t"
        "v_mul_f32 v221, %[ks], v221\n\t"
        "v_mul_f32 v222, %[ks], v222\n\t"
        "v_mul_f32 v223, %[ks], v223\n\t"
        "v_cvt_pk_bf16_f32 v224, v208, v209\n\t"
        "v_cvt_pk_bf16_f32 v225, v210, v211\n\t"
        "v_cvt_pk_bf16_f32 v226, v212, v213\n\t"
        "v_cvt_pk_bf16_f32 v227, v214, v215\n\t"
        "v_cvt_pk_bf16_f32 v228, v216, v217\n\t"
        "v_cvt_pk_bf16_f32 v229, v218, v219\n\t"
        "v_cvt_pk_bf16_f32 v230, v220, v221\n\t"
        "v_cvt_pk_bf16_f32 v231, v222, v223\n\t"
        "s_add_u32 s93, %[on], 0x1000\n\t"
        "buffer_store_dwordx4 v[224:227], %[o1], %[rs], %[on] offen sc1\n\t"
        "buffer_store_dwordx4 v[228:231], %[o1], %[rs], s93 offen sc1\n\t"
        "s_waitcnt vmcnt(0)\n\t"
        "Ln%=:\n\t"
        "s_waitcnt vmcnt(5)\n\t"
        "s_barrier\n\t"
        : [acc] "+{v[208:223]}"(acc)
        : [rs] "s"(rs), [ks] "s"(ks), [o1] "v"(o1), [ob] "s"(ob), [w0] "{v[16:47]}"(W[0]), [w1] "{v[48:79]}"(W[1]), [w2] "{v[80:111]}"(W[2]), [w3] "{v[112:143]}"(W[3]), [w4] "{v[144:175]}"(W[4]), [vm] "v"(vm), [xs] "s"(xs), [xd] "s"(xd), [xa] "v"(xa), [now] "s"(now), [on] "s"(on)
        : "s92", "s93", "v176", "v177", "v178", "v179", "v180", "v181", "v182", "v183", "v184", "v185", "v186", "v187", "v188", "v189", "v190", "v191", "v192", "v193", "v194", "v195", "v196", "v197", "v198", "v199", "v200", "v201", "v202", "v203", "v204", "v205", "v206", "v207", "v224", "v225", "v226", "v227", "v228", "v229", "v230", "v231", "vcc", "scc", "memory");
}

__device__ __forceinline__ void w_kl(u32x4 rs, u32 o1, u32 ob, float ks, f32x16& acc) {
    asm volatile(
        "s_nop 7\n\t"
        "v_mul_f32 v208, %[ks], v208\n\t"
        "v_mul_f32 v209, %[ks], v209\n\t"
        "v_mul_f32 v210, %[ks], v210\n\t"
        "v_mul_f32 v211, %[ks], v211\n\t"
        "v_mul_f32 v212, %[ks], v212\n\t"
        "v_mul_f32 v213, %[ks], v213\n\t"
        "v_mul_f32 v214, %[ks], v214\n\t"
        "v_mul_f32 v215, %[ks], v215\n\t"
        "v_mul_f32 v216, %[ks], v216\n\t"
        "v_mul_f32 v217, %[ks], v217\n\t"
        "v_mul_f32 v218, %[ks], v218\n\t"
        "v_mul_f32 v219, %[ks], v219\n\t"
        "v_mul_f32 v220, %[ks], v220\n\t"
        "v_mul_f32 v221, %[ks], v221\n\t"
        "v_mul_f32 v222, %[ks], v222\n\t"
        "v_mul_f32 v223, %[ks], v223\n\t"
        "v_cvt_pk_bf16_f32 v224, v208, v209\n\t"
        "v_cvt_pk_bf16_f32 v225, v210, v211\n\t"
        "v_cvt_pk_bf16_f32 v226, v212, v213\n\t"
        "v_cvt_pk_bf16_f32 v227, v214, v215\n\t"
        "v_cvt_pk_bf16_f32 v228, v216, v217\n\t"
        "v_cvt_pk_bf16_f32 v229, v218, v219\n\t"
        "v_cvt_pk_bf16_f32 v230, v220, v221\n\t"
        "v_cvt_pk_bf16_f32 v231, v222, v223\n\t"
        "s_add_u32 s93, %[ob], 0x1000\n\t"
        "buffer_store_dwordx4 v[224:227], %[o1], %[rs], %[ob] offen sc1\n\t"
        "buffer_store_dwordx4 v[228:231], %[o1], %[rs], s93 offen sc1\n\t"
        "s_nop 1\n\t"
        : [acc] "+{v[208:223]}"(acc)
        : [rs] "s"(rs), [ks] "s"(ks), [o1] "v"(o1), [ob] "s"(ob)
        : "s93", "v224", "v225", "v226", "v227", "v228", "v229", "v230", "v231", "vcc", "scc", "memory");
}

__device__ __forceinline__ void w_v(const u32x32 (&W)[5], u32x4 rs, u32 xa, u32 vm, u32 xs, u32 xd, u32 vw, u32 ao, u32 ad STP) {
    asm volatile(
        ".p2align 6\n\t"
        "s_mov_b32 s92, m0\n\t"
        "s_setprio 2\n\t"
        "ds_read_b128 v[176:179], %[xa] offset:0\n\t"
        "ds_read_b128 v[180:183], %[xa] offset:20480\n\t"
        "ds_read_b128 v[184:187], %[xa] offset:1024\n\t"
        "ds_read_b128 v[188:191], %[xa] offset:21504\n\t"
        "ds_read_b128 v[192:195], %[xa] offset:2048\n\t"
        "ds_read_b128 v[196:199], %[xa] offset:22528\n\t"
        "ds_read_b128 v[200:203], %[xa] offset:3072\n\t"
        "ds_read_b128 v[204:207], %[xa] offset:23552\n\t"
        "s_waitcnt lgkmcnt(7)\n\t"
        "v_mfma_f32_16x16x32_bf16 v[208:211], v[16:19], v[176:179], 0\n\t"
        "v_mfma_f32_16x16x32_bf16 v[212:215], v[96:99], v[176:179], 0\n\t"
        "ds_read_b128 v[176:179], %[xa] offset:4096\n\t"
        "s_mov_b32 m0, %[ad]\n\t"
        "s_nop 0\n\t"
        "buffer_load_dword off, %[rs], %[ao] sc1 lds\n\t"
        "s_waitcnt lgkmcnt(7)\n\t"
        "v_mfma_f32_16x16x32_bf16 v[216:219], v[16:19], v[180:183], 0\n\t"
        "v_mfma_f32_16x16x32_bf16 v[220:223], v[96:99], v[180:183], 0\n\t"
        "ds_read_b128 v[180:183], %[xa] offset:24576\n\t"
        "s_add_u32 m0, %[xd], 0\n\t"
        "s_add_u32 s93, %[xs], 0\n\t"
        "buffer_load_dwordx4 %[vm], %[rs], s93 offen offset:0 sc1 lds\n\t"
        "s_waitcnt lgkmcnt(7)\n\t"
        "v_mfma_f32_16x16x32_bf16 v[208:211], v[20:23], v[184:187], v[208:211]\n\t"
        "v_mfma_f32_16x16x32_bf16 v[212:215], v[100:103], v[184:187], v[212:215]\n\t"
        "ds_read_b128 v[184:187], %[xa] offset:5120\n\t"
        "s_waitcnt lgkmcnt(7)\n\t"
        "v_mfma_f32_16x16x32_bf16 v[216:219], v[20:23], v[188:191], v[216:219]\n\t"
        "v_mfma_f32_16x16x32_bf16 v[220:223], v[100:103], v[188:191], v[220:223]\n\t"
        "ds_read_b128 v[188:191], %[xa] offset:25600\n\t"
        "s_waitcnt lgkmcnt(7)\n\t"
        "v_mfma_f32_16x16x32_bf16 v[208:211], v[24:27], v[192:195], v[208:211]\n\t"
        "v_mfma_f32_16x16x32_bf16 v[212:215], v[104:107], v[192:195], v[212:215]\n\t"
        "ds_read_b128 v[192:195], %[xa] offset:6144\n\t"
        "buffer_load_dwordx4 %[vm], %[rs], s93 offen offset:1024 sc1 lds\n\t"
        "s_waitcnt lgkmcnt(7)\n\t"
        "v_mfma_f32_16x16x32_bf16 v[216:219], v[24:27], v[196:199], v[216:219]\n\t"
        "v_mfma_f32_16x16x32_bf16 v[220:223], v[104:107], v[196:199], v[220:223]\n\t"
        "ds_read_b128 v[196:199], %[xa] offset:26624\n\t"
        "s_waitcnt lgkmcnt(7)\n\t"
        "v_mfma_f32_16x16x32_bf16 v[208:211], v[28:31], v[200:203], v[208:211]\n\t"
        "v_mfma_f32_16x16x32_bf16 v[212:215], v[108:111], v[200:203], v[212:215]\n\t"
        "ds_read_b128 v[200:203], %[xa] offset:7168\n\t"
        "s_waitcnt lgkmcnt(7)\n\t"
        "v_mfma_f32_16x16x32_bf16 v[216:219], v[28:31], v[204:207], v[216:219]\n\t"
        "v_mfma_f32_16x16x32_bf16 v[220:223], v[108:111], v[204:207], v[220:223]\n\t"
        "ds_read_b128 v[204:207], %[xa] offset:27648\n\t"
        "buffer_load_dwordx4 %[vm], %[rs], s93 offen offset:2048 sc1 lds\n\t"
        "s_waitcnt lgkmcnt(7)\n\t"
        "v_mfma_f32_16x16x32_bf16 v[208:211], v[32:35], v[176:179], v[208:211]\n\t"
        "v_mfma_f32_16x16x32_bf16 v[212:215], v[112:115], v[176:179], v[212:215]\n\t"
        "ds_read_b128 v[176:179], %[xa] offset:8192\n\t"
        "s_waitcnt lgkmcnt(7)\n\t"
        "v_mfma_f32_16x16x32_bf16 v[216:219], v[32:35], v[180:183], v[216:219]\n\t"
        "v_mfma_f32_16x16x32_bf16 v[220:223], v[112:115], v[180:183], v[220:223]\n\t"
        "ds_read_b128 v[180:183], %[xa] offset:28672\n\t"
        "s_waitcnt lgkmcnt(7)\n\t"
        "v_mfma_f32_16x16x32_bf16 v[208:211], v[36:39], v[184:187], v[208:211]\n\t"
        "v_mfma_f32_16x16x32_bf16 v[212:215], v[116:119], v[184:187], v[212:215]\n\t"
        "ds_read_b128 v[184:187], %[xa] offset:9216\n\t"
        "buffer_load_dwordx4 %[vm], %[rs], s93 offen offset:3072 sc1 lds\n\t"
        "s_waitcnt lgkmcnt(7)\n\t"
        "v_mfma_f32_16x16x32_bf16 v[216:219], v[36:39], v[188:191], v[216:219]\n\t"
        "v_mfma_f32_16x16x32_bf16 v[220:223], v[116:119], v[188:191], v[220:223]\n\t"
        "ds_read_b128 v[188:191], %[xa] offset:29696\n\t"
        "s_waitcnt lgkmcnt(7)\n\t"
        "v_mfma_f32_16x16x32_bf16 v[208:211], v[40:43], v[192:195], v[208:211]\n\t"
        "v_mfma_f32_16x16x32_bf16 v[212:215], v[120:123], v[192:195], v[212:215]\n\t"
        "ds_read_b128 v[192:195], %[xa] offset:10240\n\t"
        "s_waitcnt lgkmcnt(7)\n\t"
        "v_mfma_f32_16x16x32_bf16 v[216:219], v[40:43], v[196:199], v[216:219]\n\t"
        "v_mfma_f32_16x16x32_bf16 v[220:223], v[120:123], v[196:199], v[220:223]\n\t"
        "ds_read_b128 v[196:199], %[xa] offset:30720\n\t"
        "s_add_u32 m0, %[xd], 4096\n\t"
        "s_add_u32 s93, %[xs], 4096\n\t"
        "buffer_load_dwordx4 %[vm], %[rs], s93 offen offset:0 sc1 lds\n\t"
        "s_waitcnt lgkmcnt(7)\n\t"
        "v_mfma_f32_16x16x32_bf16 v[208:211], v[44:47], v[200:203], v[208:211]\n\t"
        "v_mfma_f32_16x16x32_bf16 v[212:215], v[124:127], v[200:203], v[212:215]\n\t"
        "ds_read_b128 v[200:203], %[xa] offset:11264\n\t"
        "s_waitcnt lgkmcnt(7)\n\t"
        "v_mfma_f32_16x16x32_bf16 v[216:219], v[44:47], v[204:207], v[216:219]\n\t"
        "v_mfma_f32_16x16x32_bf16 v[220:223], v[124:127], v[204:207], v[220:223]\n\t"
        "ds_read_b128 v[204:207], %[xa] offset:31744\n\t"
        "s_waitcnt lgkmcnt(7)\n\t"
        "v_mfma_f32_16x16x32_bf16 v[208:211], v[48:51], v[176:179], v[208:211]\n\t"
        "v_mfma_f32_16x16x32_bf16 v[212:215], v[128:131], v[176:179], v[212:215]\n\t"
        "ds_read_b128 v[176:179], %[xa] offset:12288\n\t"
        "buffer_load_dwordx4 %[vm], %[rs], s93 offen offset:1024 sc1 lds\n\t"
        "s_waitcnt lgkmcnt(7)\n\t"
        "v_mfma_f32_16x16x32_bf16 v[216:219], v[48:51], v[180:183], v[216:219]\n\t"
        "v_mfma_f32_16x16x32_bf16 v[220:223], v[128:131], v[180:183], v[220:223]\n\t"
        "ds_read_b128 v[180:183], %[xa] offset:32768\n\t"
        "s_waitcnt lgkmcnt(7)\n\t"
        "v_mfma_f32_16x16x32_bf16 v[208:211], v[52:55], v[184:187], v[208:211]\n\t"
        "v_mfma_f32_16x16x32_bf16 v[212:215], v[132:135], v[184:187], v[212:215]\n\t"
        "ds_read_b128 v[184:187], %[xa] offset:13312\n\t"
        "s_waitcnt lgkmcnt(7)\n\t"
        "v_mfma_f32_16x16x32_bf16 v[216:219], v[52:55], v[188:191], v[216:219]\n\t"
        "v_mfma_f32_16x16x32_bf16 v[220:223], v[132:135], v[188:191], v[220:223]\n\t"
        "ds_read_b128 v[188:191], %[xa] offset:33792\n\t"
        "buffer_load_dwordx4 %[vm], %[rs], s93 offen offset:2048 sc1 lds\n\t"
        "s_waitcnt lgkmcnt(7)\n\t"
        "v_mfma_f32_16x16x32_bf16 v[208:211], v[56:59], v[192:195], v[208:211]\n\t"
        "v_mfma_f32_16x16x32_bf16 v[212:215], v[136:139], v[192:195], v[212:215]\n\t"
        "ds_read_b128 v[192:195], %[xa] offset:14336\n\t"
        "s_waitcnt lgkmcnt(7)\n\t"
        "v_mfma_f32_16x16x32_bf16 v[216:219], v[56:59], v[196:199], v[216:219]\n\t"
        "v_mfma_f32_16x16x32_bf16 v[220:223], v[136:139], v[196:199], v[220:223]\n\t"
        "ds_read_b128 v[196:199], %[xa] offset:34816\n\t"
        "s_waitcnt lgkmcnt(7)\n\t"
        "v_mfma_f32_16x16x32_bf16 v[208:211], v[60:63], v[200:203], v[208:211]\n\t"
        "v_mfma_f32_16x16x32_bf16 v[212:215], v[140:143], v[200:203], v[212:215]\n\t"
        "ds_read_b128 v[200:203], %[xa] offset:15360\n\t"
        "buffer_load_dwordx4 %[vm], %[rs], s93 offen offset:3072 sc1 lds\n\t"
        "s_waitcnt lgkmcnt(7)\n\t"
        "v_mfma_f32_16x16x32_bf16 v[216:219], v[60:63], v[204:207], v[216:219]\n\t"
        "v_mfma_f32_16x16x32_bf16 v[220:223], v[140:143], v[204:207], v[220:223]\n\t"
        "ds_read_b128 v[204:207], %[xa] offset:35840\n\t"
        "s_waitcnt lgkmcnt(7)\n\t"
        "v_mfma_f32_16x16x32_bf16 v[208:211], v[64:67], v[176:179], v[208:211]\n\t"
        "v_mfma_f32_16x16x32_bf16 v[212:215], v[144:147], v[176:179], v[212:215]\n\t"
        "ds_read_b128 v[176:179], %[xa] offset:16384\n\t"
        "s_waitcnt lgkmcnt(7)\n\t"
        "v_mfma_f32_16x16x32_bf16 v[216:219], v[64:67], v[180:183], v[216:219]\n\t"
        "v_mfma_f32_16x16x32_bf16 v[220:223], v[144:147], v[180:183], v[220:223]\n\t"
        "ds_read_b128 v[180:183], %[xa] offset:36864\n\t"
        "s_add_u32 m0, %[xd], 8192\n\t"
        "s_add_u32 s93, %[xs], 8192\n\t"
        "buffer_load_dwordx4 %[vm], %[rs], s93 offen offset:0 sc1 lds\n\t"
        "s_waitcnt lgkmcnt(7)\n\t"
        "v_mfma_f32_16x16x32_bf16 v[208:211], v[68:71], v[184:187], v[208:211]\n\t"
        "v_mfma_f32_16x16x32_bf16 v[212:215], v[148:151], v[184:187], v[212:215]\n\t"
        "ds_read_b128 v[184:187], %[xa] offset:17408\n\t"
        "s_waitcnt lgkmcnt(7)\n\t"
        "v_mfma_f32_16x16x32_bf16 v[216:219], v[68:71], v[188:191], v[216:219]\n\t"
        "v_mfma_f32_16x16x32_bf16 v[220:223], v[148:151], v[188:191], v[220:223]\n\t"
        "ds_read_b128 v[188:191], %[xa] offset:37888\n\t"
        "s_waitcnt lgkmcnt(7)\n\t"
        "v_mfma_f32_16x16x32_bf16 v[208:211], v[72:75], v[192:195], v[208:211]\n\t"
        "v_mfma_f32_16x16x32_bf16 v[212:215], v[152:155], v[192:195], v[212:215]\n\t"
        "ds_read_b128 v[192:195], %[xa] offset:18432\n\t"
        "buffer_load_dwordx4 %[vm], %[rs], s93 offen offset:1024 sc1 lds\n\t"
        "s_waitcnt lgkmcnt(7)\n\t"
        "v_mfma_f32_16x16x32_bf16 v[216:219], v[72:75], v[196:199], v[216:219]\n\t"
        "v_mfma_f32_16x16x32_bf16 v[220:223], v[152:155], v[196:199], v[220:223]\n\t"
        "ds_read_b128 v[196:199], %[xa] offset:38912\n\t"
        "s_waitcnt lgkmcnt(7)\n\t"
        "v_mfma_f32_16x16x32_bf16 v[208:211], v[76:79], v[200:203], v[208:211]\n\t"
        "v_mfma_f32_16x16x32_bf16 v[212:215], v[156:159], v[200:203], v[212:215]\n\t"
        "ds_read_b128 v[200:203], %[xa] offset:19456\n\t"
        "s_waitcnt lgkmcnt(7)\n\t"
        "v_mfma_f32_16x16x32_bf16 v[216:219], v[76:79], v[204:207], v[216:219]\n\t"
        "v_mfma_f32_16x16x32_bf16 v[220:223], v[156:159], v[204:207], v[220:223]\n\t"
        "ds_read_b128 v[204:207], %[xa] offset:39936\n\t"
        "s_waitcnt lgkmcnt(7)\n\t"
        "v_mfma_f32_16x16x32_bf16 v[208:211], v[80:83], v[176:179], v[208:211]\n\t"
        "v_mfma_f32_16x16x32_bf16 v[212:215], v[160:163], v[176:179], v[212:215]\n\t"
        "s_waitcnt lgkmcnt(6)\n\t"
        "v_mfma_f32_16x16x32_bf16 v[216:219], v[80:83], v[180:183], v[216:219]\n\t"
        "v_mfma_f32_16x16x32_bf16 v[220:223], v[160:163], v[180:183], v[220:223]\n\t"
        "s_waitcnt lgkmcnt(5)\n\t"
        "v_mfma_f32_16x16x32_bf16 v[208:211], v[84:87], v[184:187], v[208:211]\n\t"
        "v_mfma_f32_16x16x32_bf16 v[212:215], v[164:167], v[184:187], v[212:215]\n\t"
        "s_waitcnt lgkmcnt(4)\n\t"
        "v_mfma_f32_16x16x32_bf16 v[216:219], v[84:87], v[188:191], v[216:219]\n\t"
        "v_mfma_f32_16x16x32_bf16 v[220:223], v[164:167], v[188:191], v[220:223]\n\t"
        "s_waitcnt lgkmcnt(3)\n\t"
        "v_mfma_f32_16x16x32_bf16 v[208:211], v[88:91], v[192:195], v[208:211]\n\t"
        "v_mfma_f32_16x16x32_bf16 v[212:215], v[168:171], v[192:195], v[212:215]\n\t"
        "s_waitcnt lgkmcnt(2)\n\t"
        "v_mfma_f32_16x16x32_bf16 v[216:219], v[88:91], v[196:199], v[216:219]\n\t"
        "v_mfma_f32_16x16x32_bf16 v[220:223], v[168:171], v[196:199], v[220:223]\n\t"
        "s_waitcnt lgkmcnt(1)\n\t"
        "v_mfma_f32_16x16x32_bf16 v[208:211], v[92:95], v[200:203], v[208:211]\n\t"
        "v_mfma_f32_16x16x32_bf16 v[212:215], v[172:175], v[200:203], v[212:215]\n\t"
        "s_waitcnt lgkmcnt(0)\n\t"
        "v_mfma_f32_16x16x32_bf16 v[216:219], v[92:95], v[204:207], v[216:219]\n\t"
        "v_mfma_f32_16x16x32_bf16 v[220:223], v[172:175], v[204:207], v[220:223]\n\t"
        "s_mov_b32 m0, s92\n\t"
        "s_nop 6\n\t"
        "v_cvt_pk_bf16_f32 v224, v208, v209\n\t"
        "v_cvt_pk_bf16_f32 v225, v210, v211\n\t"
        "v_cvt_pk_bf16_f32 v226, v212, v213\n\t"
        "v_cvt_pk_bf16_f32 v227, v214, v215\n\t"
        "v_cvt_pk_bf16_f32 v228, v216, v217\n\t"
        "v_cvt_pk_bf16_f32 v229, v218, v219\n\t"
        "v_cvt_pk_bf16_f32 v230, v220, v221\n\t"
        "v_cvt_pk_bf16_f32 v231, v222, v223\n\t"
        "ds_write_b128 %[vw], v[224:227]\n\t"
        "ds_write_b128 %[vw], v[228:231] offset:4096\n\t"
        "s_waitcnt vmcnt(10) lgkmcnt(0)\n\t"
        "s_barrier\n\t"
        :
        : [w0] "{v[16:47]}"(W[0]), [w1] "{v[48:79]}"(W[1]), [w2] "{v[80:111]}"(W[2]), [w3] "{v[112:143]}"(W[3]), [w4] "{v[144:175]}"(W[4]), [rs] "s"(rs), [vw] "v"(vw), [ao] "s"(ao), [ad] "s"(ad), [xs] "s"(xs), [xd] "s"(xd), [vm] "v"(vm), [xa] "v"(xa)
        : "s92", "s93", "v176", "v177", "v178", "v179", "v180", "v181", "v182", "v183", "v184", "v185", "v186", "v187", "v188", "v189", "v190", "v191", "v192", "v193", "v194", "v195", "v196", "v197", "v198", "v199", "v200", "v201", "v202", "v203", "v204", "v205", "v206", "v207", "v208", "v209", "v210", "v211", "v212", "v213", "v214", "v215", "v216", "v217", "v218", "v219", "v220", "v221", "v222", "v223", "v224", "v225", "v226", "v227", "v228", "v229", "v230", "v231", "vcc", "scc", "memory");
}

__device__ __forceinline__ void w_b(const u32x32 (&W)[5], u32x4 rs, u32 now, u32 ca, u32 cn, u32 fa, u32 fv, u32 vr, u32 n1, u32 n2, u32 f1, u32 f2, u32 o1, u32 n0, u32 f0, f32x16& acc STP) {
    asm volatile(
        ".p2align 6\n\t"
        "s_setprio 2\n\t"
        "s_barrier\n\t"
        "ds_read_b128 v[176:179], %[vr] offset:0\n\t"
        "ds_read_b128 v[192:195], %[vr] offset:4096\n\t"
        "ds_read_b128 v[180:183], %[vr] offset:1024\n\t"
        "ds_read_b128 v[196:199], %[vr] offset:5120\n\t"
        "ds_read_b128 v[184:187], %[vr] offset:2048\n\t"
        "ds_read_b128 v[200:203], %[vr] offset:6144\n\t"
        "ds_read_b128 v[188:191], %[vr] offset:3072\n\t"
        "ds_read_b128 v[204:207], %[vr] offset:7168\n\t"
        "s_nop 3\n\t"
        "v_cvt_pk_bf16_f32 v228, v216, v217\n\t"
        "v_cvt_pk_bf16_f32 v229, v218, v219\n\t"
        "v_cvt_pk_bf16_f32 v230, v220, v221\n\t"
        "v_cvt_pk_bf16_f32 v231, v222, v223\n\t"
        "s_waitcnt lgkmcnt(7)\n\t"
        "v_mfma_f32_16x16x32_bf16 v[208:211], v[176:179], v[16:19], 0\n\t"
        "s_waitcnt lgkmcnt(6)\n\t"
        "v_mfma_f32_16x16x32_bf16 v[212:215], v[192:195], v[16:19], 0\n\t"
        "s_waitcnt lgkmcnt(5)\n\t"
        "v_mfma_f32_16x16x32_bf16 v[208:211], v[180:183], v[20:23], v[208:211]\n\t"
        "s_waitcnt lgkmcnt(4)\n\t"
        "v_mfma_f32_16x16x32_bf16 v[212:215], v[196:199], v[20:23], v[212:215]\n\t"
        "s_waitcnt lgkmcnt(3)\n\t"
        "v_mfma_f32_16x16x32_bf16 v[208:211], v[184:187], v[24:27], v[208:211]\n\t"
        "s_waitcnt lgkmcnt(2)\n\t"
        "v_mfma_f32_16x16x32_bf16 v[212:215], v[200:203], v[24:27], v[212:215]\n\t"
        "s_waitcnt lgkmcnt(1)\n\t"
        "v_mfma_f32_16x16x32_bf16 v[208:211], v[188:191], v[28:31], v[208:211]\n\t"
        "s_waitcnt lgkmcnt(0)\n\t"
        "v_mfma_f32_16x16x32_bf16 v[212:215], v[204:207], v[28:31], v[212:215]\n\t"
        "s_add_u32 s92, %[f0], 65536\n\t"
        "buffer_store_dwordx4 v[228:231], %[o1], %[rs], s92 offen sc1\n\t"
        "v_mfma_f32_16x16x32_bf16 v[216:219], v[176:179], v[96:99], 0\n\t"
        "v_mfma_f32_16x16x32_bf16 v[220:223], v[192:195], v[96:99], 0\n\t"
        "v_mfma_f32_16x16x32_bf16 v[216:219], v[180:183], v[100:103], v[216:219]\n\t"
        "v_cvt_pk_bf16_f32 v224, v208, v209\n\t"
        "v_cvt_pk_bf16_f32 v225, v210, v211\n\t"
        "v_mfma_f32_16x16x32_bf16 v[220:223], v[196:199], v[100:103], v[220:223]\n\t"
        "v_cvt_pk_bf16_f32 v226, v212, v213\n\t"
        "v_cvt_pk_bf16_f32 v227, v214, v215\n\t"
        "v_mfma_f32_16x16x32_bf16 v[216:219], v[184:187], v[104:107], v[216:219]\n\t"
        "s_add_u32 s92, %[n1], 0\n\t"
        "buffer_store_dwordx4 v[224:227], %[o1], %[rs], s92 offen sc1\n\t"
        "v_mfma_f32_16x16x32_bf16 v[220:223], v[200:203], v[104:107], v[220:223]\n\t"
        "v_mfma_f32_16x16x32_bf16 v[216:219], v[188:191], v[108:111], v[216:219]\n\t"
        "v_mfma_f32_16x16x32_bf16 v[220:223], v[204:207], v[108:111], v[220:223]\n\t"
        "s_add_u32 s92, %[f1], 0\n\t"
        "buffer_store_dwordx4 v[224:227], %[o1], %[rs], s92 offen sc1\n\t"
        "v_mfma_f32_16x16x32_bf16 v[208:211], v[176:179], v[32:35], 0\n\t"
        "v_mfma_f32_16x16x32_bf16 v[212:215], v[192:195], v[32:35], 0\n\t"
        "v_mfma_f32_16x16x32_bf16 v[208:211], v[180:183], v[36:39], v[208:211]\n\t"
        "v_cvt_pk_bf16_f32 v228, v216, v217\n\t"
        "v_cvt_pk_bf16_f32 v229, v218, v219\n\t"
        "v_mfma_f32_16x16x32_bf16 v[212:215], v[196:199], v[36:39], v[212:215]\n\t"
        "v_cvt_pk_bf16_f32 v230, v220, v221\n\t"
        "v_cvt_pk_bf16_f32 v231, v222, v223\n\t"
        "v_mfma_f32_16x16x32_bf16 v[208:211], v[184:187], v[40:43], v[208:211]\n\t"
        "s_add_u32 s92, %[n1], 65536\n\t"
        "buffer_store_dwordx4 v[228:231], %[o1], %[rs], s92 offen sc1\n\t"
        "v_mfma_f32_16x16x32_bf16 v[212:215], v[200:203], v[40:43], v[212:215]\n\t"
        "v_mfma_f32_16x16x32_bf16 v[208:211], v[188:191], v[44:47], v[208:211]\n\t"
        "v_mfma_f32_16x16x32_bf16 v[212:215], v[204:207], v[44:47], v[212:215]\n\t"
        "s_add_u32 s92, %[f1], 65536\n\t"
        "buffer_store_dwordx4 v[228:231], %[o1], %[rs], s92 offen sc1\n\t"
        "v_mfma_f32_16x16x32_bf16 v[216:219], v[176:179], v[112:115], 0\n\t"
        "v_mfma_f32_16x16x32_bf16 v[220:223], v[192:195], v[112:115], 0\n\t"
        "v_mfma_f32_16x16x32_bf16 v[216:219], v[180:183], v[116:119], v[216:219]\n\t"
        "v_cvt_pk_bf16_f32 v232, v208, v209\n\t"
        "v_cvt_pk_bf16_f32 v233, v210, v211\n\t"
        "v_mfma_f32_16x16x32_bf16 v[220:223], v[196:199], v[116:119], v[220:223]\n\t"
        "v_cvt_pk_bf16_f32 v234, v212, v213\n\t"
        "v_cvt_pk_bf16_f32 v235, v214, v215\n\t"
        "v_mfma_f32_16x16x32_bf16 v[216:219], v[184:187], v[120:123], v[216:219]\n\t"
        "s_add_u32 s92, %[n1], 524288\n\t"
        "buffer_store_dwordx4 v[232:235], %[o1], %[rs], s92 offen sc1\n\t"
        "v_mfma_f32_16x16x32_bf16 v[220:223], v[200:203], v[120:123], v[220:223]\n\t"
        "v_mfma_f32_16x16x32_bf16 v[216:219], v[188:191], v[124:127], v[216:219]\n\t"
        "v_mfma_f32_16x16x32_bf16 v[220:223], v[204:207], v[124:127], v[220:223]\n\t"
        "s_add_u32 s92, %[f1], 524288\n\t"
        "buffer_store_dwordx4 v[232:235], %[o1], %[rs], s92 offen sc1\n\t"
        "v_mfma_f32_16x16x32_bf16 v[208:211], v[176:179], v[48:51], 0\n\t"
        "v_mov_b32 v241, 1\n\t"
        "s_mov_b64 exec, 1\n\t"
        "s_waitcnt vmcnt(6)\n\t"
        "ds_add_rtn_u32 v240, %[ca], v241\n\t"
        "s_mov_b64 exec, -1\n\t"
        "v_mfma_f32_16x16x32_bf16 v[212:215], v[192:195], v[48:51], 0\n\t"
        "v_mfma_f32_16x16x32_bf16 v[208:211], v[180:183], v[52:55], v[208:211]\n\t"
        "v_cvt_pk_bf16_f32 v236, v216, v217\n\t"
        "v_cvt_pk_bf16_f32 v237, v218, v219\n\t"
        "v_mfma_f32_16x16x32_bf16 v[212:215], v[196:199], v[52:55], v[212:215]\n\t"
        "v_cvt_pk_bf16_f32 v238, v220, v221\n\t"
        "v_cvt_pk_bf16_f32 v239, v222, v223\n\t"
        "v_mfma_f32_16x16x32_bf16 v[208:211], v[184:187], v[56:59], v[208:211]\n\t"
        "s_add_u32 s92, %[n1], 589824\n\t"
        "buffer_store_dwordx4 v[236:239], %[o1], %[rs], s92 offen sc1\n\t"
        "v_mfma_f32_16x16x32_bf16 v[212:215], v[200:203], v[56:59], v[212:215]\n\t"
        "v_mfma_f32_16x16x32_bf16 v[208:211], v[188:191], v[60:63], v[208:211]\n\t"
        "v_mfma_f32_16x16x32_bf16 v[212:215], v[204:207], v[60:63], v[212:215]\n\t"
        "s_add_u32 s92, %[f1], 589824\n\t"
        "buffer_store_dwordx4 v[236:239], %[o1], %[rs], s92 offen sc1\n\t"
        "v_mfma_f32_16x16x32_bf16 v[216:219], v[176:179], v[128:131], 0\n\t"
        "s_waitcnt lgkmcnt(0)\n\t"
        "v_readfirstlane_b32 s92, v240\n\t"
        "s_cmp_lg_u32 s92, %[cn]\n\t"
        "s_cbranch_scc1 Lf%=\n\t"
        "buffer_store_dword %[fv], %[fa], %[rs], 0 offen sc1\n\t"
        "Lf%=:\n\t"
        "v_mfma_f32_16x16x32_bf16 v[220:223], v[192:195], v[128:131], 0\n\t"
        "v_mfma_f32_16x16x32_bf16 v[216:219], v[180:183], v[132:135], v[216:219]\n\t"
        "v_cvt_pk_bf16_f32 v224, v208, v209\n\t"
        "v_cvt_pk_bf16_f32 v225, v210, v211\n\t"
        "v_mfma_f32_16x16x32_bf16 v[220:223], v[196:199], v[132:135], v[220:223]\n\t"
        "v_cvt_pk_bf16_f32 v226, v212, v213\n\t"
        "v_cvt_pk_bf16_f32 v227, v214, v215\n\t"
        "v_mfma_f32_16x16x32_bf16 v[216:219], v[184:187], v[136:139], v[216:219]\n\t"
        "v_mfma_f32_16x16x32_bf16 v[220:223], v[200:203], v[136:139], v[220:223]\n\t"
        "v_mfma_f32_16x16x32_bf16 v[216:219], v[188:191], v[140:143], v[216:219]\n\t"
        "v_mfma_f32_16x16x32_bf16 v[220:223], v[204:207], v[140:143], v[220:223]\n\t"
        "s_add_u32 s92, %[f1], 1048576\n\t"
        "buffer_store_dwordx4 v[224:227], %[o1], %[rs], s92 offen sc1\n\t"
        "v_mfma_f32_16x16x32_bf16 v[208:211], v[176:179], v[64:67], 0\n\t"
        "v_mfma_f32_16x16x32_bf16 v[212:215], v[192:195], v[64:67], 0\n\t"
        "v_mfma_f32_16x16x32_bf16 v[208:211], v[180:183], v[68:71], v[208:211]\n\t"
        "v_cvt_pk_bf16_f32 v228, v216, v217\n\t"
        "v_cvt_pk_bf16_f32 v229, v218, v219\n\t"
        "v_mfma_f32_16x16x32_bf16 v[212:215], v[196:199], v[68:71], v[212:215]\n\t"
        "v_cvt_pk_bf16_f32 v230, v220, v221\n\t"
        "v_cvt_pk_bf16_f32 v231, v222, v223\n\t"
        "v_mfma_f32_16x16x32_bf16 v[208:211], v[184:187], v[72:75], v[208:211]\n\t"
        "v_mfma_f32_16x16x32_bf16 v[212:215], v[200:203], v[72:75], v[212:215]\n\t"
        "v_mfma_f32_16x16x32_bf16 v[208:211], v[188:191], v[76:79], v[208:211]\n\t"
        "v_mfma_f32_16x16x32_bf16 v[212:215], v[204:207], v[76:79], v[212:215]\n\t"
        "s_add_u32 s92, %[f1], 1114112\n\t"
        "buffer_store_dwordx4 v[228:231], %[o1], %[rs], s92 offen sc1\n\t"
        "v_mfma_f32_16x16x32_bf16 v[216:219], v[176:179], v[144:147], 0\n\t"
        "v_mfma_f32_16x16x32_bf16 v[220:223], v[192:195], v[144:147], 0\n\t"
        "v_mfma_f32_16x16x32_bf16 v[216:219], v[180:183], v[148:151], v[216:219]\n\t"
        "v_cvt_pk_bf16_f32 v232, v208, v209\n\t"
        "v_cvt_pk_bf16_f32 v233, v210, v211\n\t"
        "v_mfma_f32_16x16x32_bf16 v[220:223], v[196:199], v[148:151], v[220:223]\n\t"
        "v_cvt_pk_bf16_f32 v234, v212, v213\n\t"
        "v_cvt_pk_bf16_f32 v235, v214, v215\n\t"
        "v_mfma_f32_16x16x32_bf16 v[216:219], v[184:187], v[152:155], v[216:219]\n\t"
        "v_mfma_f32_16x16x32_bf16 v[220:223], v[200:203], v[152:155], v[220:223]\n\t"
        "v_mfma_f32_16x16x32_bf16 v[216:219], v[188:191], v[156:159], v[216:219]\n\t"
        "v_mfma_f32_16x16x32_bf16 v[220:223], v[204:207], v[156:159], v[220:223]\n\t"
        "s_add_u32 s92, %[f1], 1572864\n\t"
        "buffer_store_dwordx4 v[232:235], %[o1], %[rs], s92 offen sc1\n\t"
        "v_mfma_f32_16x16x32_bf16 v[208:211], v[176:179], v[80:83], 0\n\t"
        "v_mfma_f32_16x16x32_bf16 v[212:215], v[192:195], v[80:83], 0\n\t"
        "v_mfma_f32_16x16x32_bf16 v[208:211], v[180:183], v[84:87], v[208:211]\n\t"
        "v_cvt_pk_bf16_f32 v236, v216, v217\n\t"
        "v_cvt_pk_bf16_f32 v237, v218, v219\n\t"
        "v_mfma_f32_16x16x32_bf16 v[212:215], v[196:199], v[84:87], v[212:215]\n\t"
        "v_cvt_pk_bf16_f32 v238, v220, v221\n\t"
        "v_cvt_pk_bf16_f32 v239, v222, v223\n\t"
        "v_mfma_f32_16x16x32_bf16 v[208:211], v[184:187], v[88:91], v[208:211]\n\t"
        "v_mfma_f32_16x16x32_bf16 v[212:215], v[200:203], v[88:91], v[212:215]\n\t"
        "v_mfma_f32_16x16x32_bf16 v[208:211], v[188:191], v[92:95], v[208:211]\n\t"
        "v_mfma_f32_16x16x32_bf16 v[212:215], v[204:207], v[92:95], v[212:215]\n\t"
        "s_add_u32 s92, %[f1], 1638400\n\t"
        "buffer_store_dwordx4 v[236:239], %[o1], %[rs], s92 offen sc1\n\t"
        "v_mfma_f32_16x16x32_bf16 v[216:219], v[176:179], v[160:163], 0\n\t"
        "v_mfma_f32_16x16x32_bf16 v[220:223], v[192:195], v[160:163], 0\n\t"
        "v_mfma_f32_16x16x32_bf16 v[216:219], v[180:183], v[164:167], v[216:219]\n\t"
        "v_cvt_pk_bf16_f32 v224, v208, v209\n\t"
        "v_cvt_pk_bf16_f32 v225, v210, v211\n\t"
        "v_mfma_f32_16x16x32_bf16 v[220:223], v[196:199], v[164:167], v[220:223]\n\t"
        "v_cvt_pk_bf16_f32 v226, v212, v213\n\t"
        "v_cvt_pk_bf16_f32 v227, v214, v215\n\t"
        "v_mfma_f32_16x16x32_bf16 v[216:219], v[184:187], v[168:171], v[216:219]\n\t"
        "v_mfma_f32_16x16x32_bf16 v[220:223], v[200:203], v[168:171], v[220:223]\n\t"
        "v_mfma_f32_16x16x32_bf16 v[216:219], v[188:191], v[172:175], v[216:219]\n\t"
        "v_mfma_f32_16x16x32_bf16 v[220:223], v[204:207], v[172:175], v[220:223]\n\t"
        "s_add_u32 s92, %[f2], 0\n\t"
        "buffer_store_dwordx4 v[224:227], %[o1], %[rs], s92 offen sc1\n\t"
        "s_setprio 0\n\t"
        "s_cmp_eq_u32 %[now], 0\n\t"
        "s_cbranch_scc1 Ln%=\n\t"
        "s_nop 4\n\t"
        "v_cvt_pk_bf16_f32 v228, v216, v217\n\t"
        "v_cvt_pk_bf16_f32 v229, v218, v219\n\t"
        "v_cvt_pk_bf16_f32 v230, v220, v221\n\t"
        "v_cvt_pk_bf16_f32 v231, v222, v223\n\t"
        "s_add_u32 s92, %[f2], 65536\n\t"
        "buffer_store_dwordx4 v[228:231], %[o1], %[rs], s92 offen sc1\n\t"
        "s_waitcnt vmcnt(0)\n\t"
        "Ln%=:\n\t"
        : [acc] "+{v[208:223]}"(acc)
        : [rs] "s"(rs), [n0] "s"(n0), [f0] "s"(f0), [o1] "v"(o1), [w0] "{v[16:47]}"(W[0]), [w1] "{v[48:79]}"(W[1]), [w2] "{v[80:111]}"(W[2]), [w3] "{v[112:143]}"(W[3]), [w4] "{v[144:175]}"(W[4]), [vr] "v"(vr), [n1] "s"(n1), [n2] "s"(n2), [f1] "s"(f1), [f2] "s"(f2), [ca] "v"(ca), [fa] "v"(fa), [fv] "v"(fv), [cn] "s"(cn), [now] "s"(now)
        : "s92", "v176", "v177", "v178", "v179", "v180", "v181", "v182", "v183", "v184", "v185", "v186", "v187", "v188", "v189", "v190", "v191", "v192", "v193", "v194", "v195", "v196", "v197", "v198", "v199", "v200", "v201", "v202", "v203", "v204", "v205", "v206", "v207", "v224", "v225", "v226", "v227", "v228", "v229", "v230", "v231", "v232", "v233", "v234", "v235", "v236", "v237", "v238", "v239", "v240", "v241", "vcc", "scc", "memory");
}

__device__ __forceinline__ void w_bl(u32x4 rs, u32 o1, u32 n0, u32 f0, f32x16& acc) {
    asm volatile(
        "s_setprio 2\n\t"
        "s_barrier\n\t"
        "s_nop 3\n\t"
        "v_cvt_pk_bf16_f32 v228, v216, v217\n\t"
        "v_cvt_pk_bf16_f32 v229, v218, v219\n\t"
        "v_cvt_pk_bf16_f32 v230, v220, v221\n\t"
        "v_cvt_pk_bf16_f32 v231, v222, v223\n\t"
        "s_add_u32 s92, %[f0], 65536\n\t"
        "buffer_store_dwordx4 v[228:231], %[o1], %[rs], s92 offen sc1\n\t"
        "s_nop 1\n\t"
        : [acc] "+{v[208:223]}"(acc)
        : [rs] "s"(rs), [n0] "s"(n0), [f0] "s"(f0), [o1] "v"(o1)
        : "s92", "v224", "v225", "v226", "v227", "v228", "v229", "v230", "v231", "v232", "v233", "v234", "v235", "v236", "v237", "v238", "v239", "vcc", "scc", "memory");
}

__device__ __forceinline__ void o_wide(f32x4& a0, f32x4& a1, u32 vm, u32 la, u32x4 rs, u32 ps, u32 pm, u32 ld, u32 bb, u32 zb) {
    asm volatile(
        ".p2align 6\n\t"
        "s_mov_b32 s92, m0\n\t"
        "v_add_u32 v152, 0x10000, %[la]\n\t"
        "s_add_u32 m0, %[ld], 0\n\t"
        "s_add_u32 s93, %[ps], 0\n\t"
        "buffer_load_dwordx4 %[vm], %[rs], s93 offen sc1 lds\n\t"
        "s_add_u32 m0, %[ld], 8192\n\t"
        "s_add_u32 s93, %[ps], 8192\n\t"
        "buffer_load_dwordx4 %[vm], %[rs], s93 offen sc1 lds\n\t"
        "s_add_u32 m0, %[ld], 16384\n\t"
        "s_add_u32 s93, %[ps], 16384\n\t"
        "buffer_load_dwordx4 %[vm], %[rs], s93 offen sc1 lds\n\t"
        "s_add_u32 m0, %[ld], 24576\n\t"
        "s_add_u32 s93, %[ps], 24576\n\t"
        "buffer_load_dwordx4 %[vm], %[rs], s93 offen sc1 lds\n\t"
        "s_add_u32 m0, %[ld], 32768\n\t"
        "s_add_u32 s93, %[ps], 32768\n\t"
        "buffer_load_dwordx4 %[vm], %[rs], s93 offen sc1 lds\n\t"
        "s_add_u32 m0, %[ld], 40960\n\t"
        "s_add_u32 s93, %[ps], 40960\n\t"
        "buffer_load_dwordx4 %[vm], %[rs], s93 offen sc1 lds\n\t"
        "s_add_u32 m0, %[ld], 49152\n\t"
        "s_add_u32 s93, %[ps], 49152\n\t"
        "buffer_load_dwordx4 %[vm], %[rs], s93 offen sc1 lds\n\t"
        "s_add_u32 m0, %[ld], 57344\n\t"
        "s_add_u32 s93, %[ps], 57344\n\t"
        "buffer_load_dwordx4 %[vm], %[rs], s93 offen sc1 lds\n\t"
        "s_add_u32 m0, %[ld], 65536\n\t"
        "s_add_u32 s93, %[pm], 0\n\t"
        "buffer_load_dwordx4 %[vm], %[rs], s93 offen sc1 lds\n\t"
        "s_add_u32 m0, %[ld], 73728\n\t"
        "s_add_u32 s93, %[pm], 8192\n\t"
        "buffer_load_dwordx4 %[vm], %[rs], s93 offen sc1 lds\n\t"
        "s_mov_b32 m0, s92\n\t"
        "s_add_u32 s93, %[bb], 0\n\t"
        "buffer_load_dwordx4 v[72:75], %[vm], %[rs], s93 offen offset:0 sc1\n\t"
        "buffer_load_dwordx4 v[76:79], %[vm], %[rs], s93 offen offset:1024 sc1\n\t"
        "buffer_load_dwordx4 v[80:83], %[vm], %[rs], s93 offen offset:2048 sc1\n\t"
        "buffer_load_dwordx4 v[84:87], %[vm], %[rs], s93 offen offset:3072 sc1\n\t"
        "s_add_u32 s93, %[bb], 4096\n\t"
        "buffer_load_dwordx4 v[88:91], %[vm], %[rs], s93 offen offset:0 sc1\n\t"
        "buffer_load_dwordx4 v[92:95], %[vm], %[rs], s93 offen offset:1024 sc1\n\t"
        "buffer_load_dwordx4 v[96:99], %[vm], %[rs], s93 offen offset:2048 sc1\n\t"
        "buffer_load_dwordx4 v[100:103], %[vm], %[rs], s93 offen offset:3072 sc1\n\t"
        "s_add_u32 s93, %[bb], 16384\n\t"
        "buffer_load_dwordx4 v[104:107], %[vm], %[rs], s93 offen offset:0 sc1\n\t"
        "buffer_load_dwordx4 v[108:111], %[vm], %[rs], s93 offen offset:1024 sc1\n\t"
        "buffer_load_dwordx4 v[112:115], %[vm], %[rs], s93 offen offset:2048 sc1\n\t"
        "buffer_load_dwordx4 v[116:119], %[vm], %[rs], s93 offen offset:3072 sc1\n\t"
        "s_waitcnt vmcnt(12)\n\t"
        "s_barrier\n\t"
        "ds_read_b128 v[120:123], %[la] offset:0\n\t"
        "ds_read_b128 v[124:127], %[la] offset:1024\n\t"
        "ds_read_b128 v[128:131], %[la] offset:2048\n\t"
        "ds_read_b128 v[132:135], %[la] offset:3072\n\t"
        "ds_read_b128 v[136:139], %[la] offset:4096\n\t"
        "ds_read_b128 v[140:143], %[la] offset:5120\n\t"
        "ds_read_b128 v[144:147], %[la] offset:6144\n\t"
        "ds_read_b128 v[148:151], %[la] offset:7168\n\t"
        "s_waitcnt vmcnt(11) lgkmcnt(6)\n\t"
        "v_mfma_f32_16x16x32_bf16 v[64:67], v[72:75], v[120:123], 0\n\t"
        "v_mfma_f32_16x16x32_bf16 v[68:71], v[72:75], v[124:127], 0\n\t"
        "s_add_u32 s93, %[bb], 20480\n\t"
        "buffer_load_dwordx4 v[72:75], %[vm], %[rs], s93 offen offset:0 sc1\n\t"
        "ds_read_b128 v[120:123], %[la] offset:8192\n\t"
        "ds_read_b128 v[124:127], %[la] offset:9216\n\t"
        "s_waitcnt vmcnt(11) lgkmcnt(6)\n\t"
        "v_mfma_f32_16x16x32_bf16 v[64:67], v[76:79], v[128:131], v[64:67]\n\t"
        "v_mfma_f32_16x16x32_bf16 v[68:71], v[76:79], v[132:135], v[68:71]\n\t"
        "buffer_load_dwordx4 v[76:79], %[vm], %[rs], s93 offen offset:1024 sc1\n\t"
        "ds_read_b128 v[128:131], %[la] offset:10240\n\t"
        "ds_read_b128 v[132:135], %[la] offset:11264\n\t"
        "s_waitcnt vmcnt(11) lgkmcnt(6)\n\t"
        "v_mfma_f32_16x16x32_bf16 v[64:67], v[80:83], v[136:139], v[64:67]\n\t"
        "v_mfma_f32_16x16x32_bf16 v[68:71], v[80:83], v[140:143], v[68:71]\n\t"
        "buffer_load_dwordx4 v[80:83], %[vm], %[rs], s93 offen offset:2048 sc1\n\t"
        "ds_read_b128 v[136:139], %[la] offset:12288\n\t"
        "ds_read_b128 v[140:143], %[la] offset:13312\n\t"
        "s_waitcnt vmcnt(11) lgkmcnt(6)\n\t"
        "v_mfma_f32_16x16x32_bf16 v[64:67], v[84:87], v[144:147], v[64:67]\n\t"
        "v_mfma_f32_16x16x32_bf16 v[68:71], v[84:87], v[148:151], v[68:71]\n\t"
        "buffer_load_dwordx4 v[84:87], %[vm], %[rs], s93 offen offset:3072 sc1\n\t"
        "ds_read_b128 v[144:147], %[la] offset:14336\n\t"
        "ds_read_b128 v[148:151], %[la] offset:15360\n\t"
        "s_waitcnt vmcnt(11) lgkmcnt(6)\n\t"
        "v_mfma_f32_16x16x32_bf16 v[64:67], v[88:91], v[120:123], v[64:67]\n\t"
        "v_mfma_f32_16x16x32_bf16 v[68:71], v[88:91], v[124:127], v[68:71]\n\t"
        "s_add_u32 s93, %[bb], 32768\n\t"
        "buffer_load_dwordx4 v[88:91], %[vm], %[rs], s93 offen offset:0 sc1\n\t"
        "ds_read_b128 v[120:123], %[la] offset:16384\n\t"
        "ds_read_b128 v[124:127], %[la] offset:17408\n\t"
        "s_waitcnt vmcnt(11) lgkmcnt(6)\n\t"
        "v_mfma_f32_16x16x32_bf16 v[64:67], v[92:95], v[128:131], v[64:67]\n\t"
        "v_mfma_f32_16x16x32_bf16 v[68:71], v[92:95], v[132:135], v[68:71]\n\t"
        "buffer_load_dwordx4 v[92:95], %[vm], %[rs], s93 offen offset:1024 sc1\n\t"
        "ds_read_b128 v[128:131], %[la] offset:18432\n\t"
        "ds_read_b128 v[132:135], %[la] offset:19456\n\t"
        "s_waitcnt vmcnt(11) lgkmcnt(6)\n\t"
        "v_mfma_f32_16x16x32_bf16 v[64:67], v[96:99], v[136:139], v[64:67]\n\t"
        "v_mfma_f32_16x16x32_bf16 v[68:71], v[96:99], v[140:143], v[68:71]\n\t"
        "buffer_load_dwordx4 v[96:99], %[vm], %[rs], s93 offen offset:2048 sc1\n\t"
        "ds_read_b128 v[136:139], %[la] offset:20480\n\t"
        "ds_read_b128 v[140:143], %[la] offset:21504\n\t"
        "s_waitcnt vmcnt(11) lgkmcnt(6)\n\t"
        "v_mfma_f32_16x16x32_bf16 v[64:67], v[100:103], v[144:147], v[64:67]\n\t"
        "v_mfma_f32_16x16x32_bf16 v[68:71], v[100:103], v[148:151], v[68:71]\n\t"
        "buffer_load_dwordx4 v[100:103], %[vm], %[rs], s93 offen offset:3072 sc1\n\t"
        "ds_read_b128 v[144:147], %[la] offset:22528\n\t"
        "ds_read_b128 v[148:151], %[la] offset:23552\n\t"
        "s_waitcnt vmcnt(11) lgkmcnt(6)\n\t"
        "v_mfma_f32_16x16x32_bf16 v[64:67], v[104:107], v[120:123], v[64:67]\n\t"
        "v_mfma_f32_16x16x32_bf16 v[68:71], v[104:107], v[124:127], v[68:71]\n\t"
        "s_add_u32 s93, %[bb], 36864\n\t"
        "buffer_load_dwordx4 v[104:107], %[vm], %[rs], s93 offen offset:0 sc1\n\t"
        "ds_read_b128 v[120:123], %[la] offset:24576\n\t"
        "ds_read_b128 v[124:127], %[la] offset:25600\n\t"
        "s_waitcnt vmcnt(11) lgkmcnt(6)\n\t"
        "v_mfma_f32_16x16x32_bf16 v[64:67], v[108:111], v[128:131], v[64:67]\n\t"
        "v_mfma_f32_16x16x32_bf16 v[68:71], v[108:111], v[132:135], v[68:71]\n\t"
        "buffer_load_dwordx4 v[108:111], %[vm], %[rs], s93 offen offset:1024 sc1\n\t"
        "ds_read_b128 v[128:131], %[la] offset:26624\n\t"
        "ds_read_b128 v[132:135], %[la] offset:27648\n\t"
        "s_waitcnt vmcnt(11) lgkmcnt(6)\n\t"
        "v_mfma_f32_16x16x32_bf16 v[64:67], v[112:115], v[136:139], v[64:67]\n\t"
        "v_mfma_f32_16x16x32_bf16 v[68:71], v[112:115], v[140:143], v[68:71]\n\t"
        "buffer_load_dwordx4 v[112:115], %[vm], %[rs], s93 offen offset:2048 sc1\n\t"
        "ds_read_b128 v[136:139], %[la] offset:28672\n\t"
        "ds_read_b128 v[140:143], %[la] offset:29696\n\t"
        "s_waitcnt vmcnt(11) lgkmcnt(6)\n\t"
        "v_mfma_f32_16x16x32_bf16 v[64:67], v[116:119], v[144:147], v[64:67]\n\t"
        "v_mfma_f32_16x16x32_bf16 v[68:71], v[116:119], v[148:151], v[68:71]\n\t"
        "buffer_load_dwordx4 v[116:119], %[vm], %[rs], s93 offen offset:3072 sc1\n\t"
        "ds_read_b128 v[144:147], %[la] offset:30720\n\t"
        "ds_read_b128 v[148:151], %[la] offset:31744\n\t"
        "s_waitcnt vmcnt(11) lgkmcnt(6)\n\t"
        "v_mfma_f32_16x16x32_bf16 v[64:67], v[72:75], v[120:123], v[64:67]\n\t"
        "v_mfma_f32_16x16x32_bf16 v[68:71], v[72:75], v[124:127], v[68:71]\n\t"
        "s_add_u32 s93, %[bb], 49152\n\t"
        "buffer_load_dwordx4 v[72:75], %[vm], %[rs], s93 offen offset:0 sc1\n\t"
        "ds_read_b128 v[120:123], %[la] offset:32768\n\t"
        "ds_read_b128 v[124:127], %[la] offset:33792\n\t"
        "s_waitcnt vmcnt(11) lgkmcnt(6)\n\t"
        "v_mfma_f32_16x16x32_bf16 v[64:67], v[76:79], v[128:131], v[64:67]\n\t"
        "v_mfma_f32_16x16x32_bf16 v[68:71], v[76:79], v[132:135], v[68:71]\n\t"
        "buffer_load_dwordx4 v[76:79], %[vm], %[rs], s93 offen offset:1024 sc1\n\t"
        "ds_read_b128 v[128:131], %[la] offset:34816\n\t"
        "ds_read_b128 v[132:135], %[la] offset:35840\n\t"
        "s_waitcnt vmcnt(11) lgkmcnt(6)\n\t"
        "v_mfma_f32_16x16x32_bf16 v[64:67], v[80:83], v[136:139], v[64:67]\n\t"
        "v_mfma_f32_16x16x32_bf16 v[68:71], v[80:83], v[140:143], v[68:71]\n\t"
        "buffer_load_dwordx4 v[80:83], %[vm], %[rs], s93 offen offset:2048 sc1\n\t"
        "ds_read_b128 v[136:139], %[la] offset:36864\n\t"
        "ds_read_b128 v[140:143], %[la] offset:37888\n\t"
        "s_waitcnt vmcnt(11) lgkmcnt(6)\n\t"
        "v_mfma_f32_16x16x32_bf16 v[64:67], v[84:87], v[144:147], v[64:67]\n\t"
        "v_mfma_f32_16x16x32_bf16 v[68:71], v[84:87], v[148:151], v[68:71]\n\t"
        "buffer_load_dwordx4 v[84:87], %[vm], %[rs], s93 offen offset:3072 sc1\n\t"
        "ds_read_b128 v[144:147], %[la] offset:38912\n\t"
        "ds_read_b128 v[148:151], %[la] offset:39936\n\t"
        "s_waitcnt vmcnt(11) lgkmcnt(6)\n\t"
        "v_mfma_f32_16x16x32_bf16 v[64:67], v[88:91], v[120:123], v[64:67]\n\t"
        "v_mfma_f32_16x16x32_bf16 v[68:71], v[88:91], v[124:127], v[68:71]\n\t"
        "s_add_u32 s93, %[bb], 53248\n\t"
        "buffer_load_dwordx4 v[88:91], %[vm], %[rs], s93 offen offset:0 sc1\n\t"
        "ds_read_b128 v[120:123], %[la] offset:40960\n\t"
        "ds_read_b128 v[124:127], %[la] offset:41984\n\t"
        "s_waitcnt vmcnt(11) lgkmcnt(6)\n\t"
        "v_mfma_f32_16x16x32_bf16 v[64:67], v[92:95], v[128:131], v[64:67]\n\t"
        "v_mfma_f32_16x16x32_bf16 v[68:71], v[92:95], v[132:135], v[68:71]\n\t"
        "buffer_load_dwordx4 v[92:95], %[vm], %[rs], s93 offen offset:1024 sc1\n\t"
        "ds_read_b128 v[128:131], %[la] offset:43008\n\t"
        "ds_read_b128 v[132:135], %[la] offset:44032\n\t"
        "s_waitcnt vmcnt(11) lgkmcnt(6)\n\t"
        "v_mfma_f32_16x16x32_bf16 v[64:67], v[96:99], v[136:139], v[64:67]\n\t"
        "v_mfma_f32_16x16x32_bf16 v[68:71], v[96:99], v[140:143], v[68:71]\n\t"
        "buffer_load_dwordx4 v[96:99], %[vm], %[rs], s93 offen offset:2048 sc1\n\t"
        "ds_read_b128 v[136:139], %[la] offset:45056\n\t"
        "ds_read_b128 v[140:143], %[la] offset:46080\n\t"
        "s_waitcnt vmcnt(11) lgkmcnt(6)\n\t"
        "v_mfma_f32_16x16x32_bf16 v[64:67], v[100:103], v[144:147], v[64:67]\n\t"
        "v_mfma_f32_16x16x32_bf16 v[68:71], v[100:103], v[148:151], v[68:71]\n\t"
        "buffer_load_dwordx4 v[100:103], %[vm], %[rs], s93 offen offset:3072 sc1\n\t"
        "ds_read_b128 v[144:147], %[la] offset:47104\n\t"
        "ds_read_b128 v[148:151], %[la] offset:48128\n\t"
        "s_waitcnt vmcnt(11) lgkmcnt(6)\n\t"
        "v_mfma_f32_16x16x32_bf16 v[64:67], v[104:107], v[120:123], v[64:67]\n\t"
        "v_mfma_f32_16x16x32_bf16 v[68:71], v[104:107], v[124:127], v[68:71]\n\t"
        "s_add_u32 s93, %[zb], 0\n\t"
        "buffer_load_dwordx4 v[104:107], %[vm], %[rs], s93 offen offset:0 sc1\n\t"
        "ds_read_b128 v[120:123], %[la] offset:49152\n\t"
        "ds_read_b128 v[124:127], %[la] offset:50176\n\t"
        "s_waitcnt vmcnt(11) lgkmcnt(6)\n\t"
        "v_mfma_f32_16x16x32_bf16 v[64:67], v[108:111], v[128:131], v[64:67]\n\t"
        "v_mfma_f32_16x16x32_bf16 v[68:71], v[108:111], v[132:135], v[68:71]\n\t"
        "buffer_load_dwordx4 v[108:111], %[vm], %[rs], s93 offen offset:1024 sc1\n\t"
        "ds_read_b128 v[128:131], %[la] offset:51200\n\t"
        "ds_read_b128 v[132:135], %[la] offset:52224\n\t"
        "s_waitcnt vmcnt(11) lgkmcnt(6)\n\t"
        "v_mfma_f32_16x16x32_bf16 v[64:67], v[112:115], v[136:139], v[64:67]\n\t"
        "v_mfma_f32_16x16x32_bf16 v[68:71], v[112:115], v[140:143], v[68:71]\n\t"
        "buffer_load_dwordx4 v[112:115], %[vm], %[rs], s93 offen offset:2048 sc1\n\t"
        "ds_read_b128 v[136:139], %[la] offset:53248\n\t"
        "ds_read_b128 v[140:143], %[la] offset:54272\n\t"
        "s_waitcnt vmcnt(11) lgkmcnt(6)\n\t"
        "v_mfma_f32_16x16x32_bf16 v[64:67], v[116:119], v[144:147], v[64:67]\n\t"
        "v_mfma_f32_16x16x32_bf16 v[68:71], v[116:119], v[148:151], v[68:71]\n\t"
        "buffer_load_dwordx4 v[116:119], %[vm], %[rs], s93 offen offset:3072 sc1\n\t"
        "ds_read_b128 v[144:147], %[la] offset:55296\n\t"
        "ds_read_b128 v[148:151], %[la] offset:56320\n\t"
        "s_waitcnt vmcnt(11) lgkmcnt(6)\n\t"
        "v_mfma_f32_16x16x32_bf16 v[64:67], v[72:75], v[120:123], v[64:67]\n\t"
        "v_mfma_f32_16x16x32_bf16 v[68:71], v[72:75], v[124:127], v[68:71]\n\t"
        "s_add_u32 s93, %[zb], 32768\n\t"
        "buffer_load_dwordx4 v[72:75], %[vm], %[rs], s93 offen offset:0 sc1\n\t"
        "ds_read_b128 v[120:123], %[la] offset:57344\n\t"
        "ds_read_b128 v[124:127], %[la] offset:58368\n\t"
        "s_waitcnt vmcnt(11) lgkmcnt(6)\n\t"
        "v_mfma_f32_16x16x32_bf16 v[64:67], v[76:79], v[128:131], v[64:67]\n\t"
        "v_mfma_f32_16x16x32_bf16 v[68:71], v[76:79], v[132:135], v[68:71]\n\t"
        "buffer_load_dwordx4 v[76:79], %[vm], %[rs], s93 offen offset:1024 sc1\n\t"
        "ds_read_b128 v[128:131], %[la] offset:59392\n\t"
        "ds_read_b128 v[132:135], %[la] offset:60416\n\t"
        "s_waitcnt vmcnt(11) lgkmcnt(6)\n\t"
        "v_mfma_f32_16x16x32_bf16 v[64:67], v[80:83], v[136:139], v[64:67]\n\t"
        "v_mfma_f32_16x16x32_bf16 v[68:71], v[80:83], v[140:143], v[68:71]\n\t"
        "buffer_load_dwordx4 v[80:83], %[vm], %[rs], s93 offen offset:2048 sc1\n\t"
        "ds_read_b128 v[136:139], %[la] offset:61440\n\t"
        "ds_read_b128 v[140:143], %[la] offset:62464\n\t"
        "s_waitcnt vmcnt(11) lgkmcnt(6)\n\t"
        "v_mfma_f32_16x16x32_bf16 v[64:67], v[84:87], v[144:147], v[64:67]\n\t"
        "v_mfma_f32_16x16x32_bf16 v[68:71], v[84:87], v[148:151], v[68:71]\n\t"
        "buffer_load_dwordx4 v[84:87], %[vm], %[rs], s93 offen offset:3072 sc1\n\t"
        "ds_read_b128 v[144:147], %[la] offset:63488\n\t"
        "ds_read_b128 v[148:151], %[la] offset:64512\n\t"
        "s_waitcnt vmcnt(11) lgkmcnt(6)\n\t"
        "v_mfma_f32_16x16x32_bf16 v[64:67], v[88:91], v[120:123], v[64:67]\n\t"
        "v_mfma_f32_16x16x32_bf16 v[68:71], v[88:91], v[124:127], v[68:71]\n\t"
        "ds_read_b128 v[120:123], v152 offset:0\n\t"
        "ds_read_b128 v[124:127], v152 offset:1024\n\t"
        "s_waitcnt vmcnt(10) lgkmcnt(6)\n\t"
        "v_mfma_f32_16x16x32_bf16 v[64:67], v[92:95], v[128:131], v[64:67]\n\t"
        "v_mfma_f32_16x16x32_bf16 v[68:71], v[92:95], v[132:135], v[68:71]\n\t"
        "ds_read_b128 v[128:131], v152 offset:2048\n\t"
        "ds_read_b128 v[132:135], v152 offset:3072\n\t"
        "s_waitcnt vmcnt(9) lgkmcnt(6)\n\t"
        "v_mfma_f32_16x16x32_bf16 v[64:67], v[96:99], v[136:139], v[64:67]\n\t"
        "v_mfma_f32_16x16x32_bf16 v[68:71], v[96:99], v[140:143], v[68:71]\n\t"
        "ds_read_b128 v[136:139], v152 offset:4096\n\t"
        "ds_read_b128 v[140:143], v152 offset:5120\n\t"
        "s_waitcnt vmcnt(8) lgkmcnt(6)\n\t"
        "v_mfma_f32_16x16x32_bf16 v[64:67], v[100:103], v[144:147], v[64:67]\n\t"
        "v_mfma_f32_16x16x32_bf16 v[68:71], v[100:103], v[148:151], v[68:71]\n\t"
        "ds_read_b128 v[144:147], v152 offset:6144\n\t"
        "ds_read_b128 v[148:151], v152 offset:7168\n\t"
        "s_waitcnt vmcnt(7) lgkmcnt(6)\n\t"
        "v_mfma_f32_16x16x32_bf16 v[64:67], v[104:107], v[120:123], v[64:67]\n\t"
        "v_mfma_f32_16x16x32_bf16 v[68:71], v[104:107], v[124:127], v[68:71]\n\t"
        "ds_read_b128 v[120:123], v152 offset:8192\n\t"
        "ds_read_b128 v[124:127], v152 offset:9216\n\t"
        "s_waitcnt vmcnt(6) lgkmcnt(6)\n\t"
        "v_mfma_f32_16x16x32_bf16 v[64:67], v[108:111], v[128:131], v[64:67]\n\t"
        "v_mfma_f32_16x16x32_bf16 v[68:71], v[108:111], v[132:135], v[68:71]\n\t"
        "ds_read_b128 v[128:131], v152 offset:10240\n\t"
        "ds_read_b128 v[132:135], v152 offset:11264\n\t"
        "s_waitcnt vmcnt(5) lgkmcnt(6)\n\t"
        "v_mfma_f32_16x16x32_bf16 v[64:67], v[112:115], v[136:139], v[64:67]\n\t"
        "v_mfma_f32_16x16x32_bf16 v[68:71], v[112:115], v[140:143], v[68:71]\n\t"
        "ds_read_b128 v[136:139], v152 offset:12288\n\t"
        "ds_read_b128 v[140:143], v152 offset:13312\n\t"
        "s_waitcnt vmcnt(4) lgkmcnt(6)\n\t"
        "v_mfma_f32_16x16x32_bf16 v[64:67], v[116:119], v[144:147], v[64:67]\n\t"
        "v_mfma_f32_16x16x32_bf16 v[68:71], v[116:119], v[148:151], v[68:71]\n\t"
        "ds_read_b128 v[144:147], v152 offset:14336\n\t"
        "ds_read_b128 v[148:151], v152 offset:15360\n\t"
        "s_waitcnt vmcnt(3) lgkmcnt(6)\n\t"
        "v_mfma_f32_16x16x32_bf16 v[64:67], v[72:75], v[120:123], v[64:67]\n\t"
        "v_mfma_f32_16x16x32_bf16 v[68:71], v[72:75], v[124:127], v[68:71]\n\t"
        "s_waitcnt vmcnt(2) lgkmcnt(4)\n\t"
        "v_mfma_f32_16x16x32_bf16 v[64:67], v[76:79], v[128:131], v[64:67]\n\t"
        "v_mfma_f32_16x16x32_bf16 v[68:71], v[76:79], v[132:135], v[68:71]\n\t"
        "s_waitcnt vmcnt(1) lgkmcnt(2)\n\t"
        "v_mfma_f32_16x16x32_bf16 v[64:67], v[80:83], v[136:139], v[64:67]\n\t"
        "v_mfma_f32_16x16x32_bf16 v[68:71], v[80:83], v[140:143], v[68:71]\n\t"
        "s_waitcnt vmcnt(0) lgkmcnt(0)\n\t"
        "v_mfma_f32_16x16x32_bf16 v[64:67], v[84:87], v[144:147], v[64:67]\n\t"
        "v_mfma_f32_16x16x32_bf16 v[68:71], v[84:87], v[148:151], v[68:71]\n\t"
        "s_nop 7\n\t"
        : [a0] "=&{v[64:67]}"(a0), [a1] "=&{v[68:71]}"(a1)
        : [vm] "v"(vm), [la] "v"(la), [rs] "s"(rs), [ps] "s"(ps), [pm] "s"(pm), [ld] "s"(ld), [bb] "s"(bb), [zb] "s"(zb)
        : "s92", "s93", "v72", "v73", "v74", "v75", "v76", "v77", "v78", "v79", "v80", "v81", "v82", "v83", "v84", "v85", "v86", "v87", "v88", "v89", "v90", "v91", "v92", "v93", "v94", "v95", "v96", "v97", "v98", "v99", "v100", "v101", "v102", "v103", "v104", "v105", "v106", "v107", "v108", "v109", "v110", "v111", "v112", "v113", "v114", "v115", "v116", "v117", "v118", "v119", "v120", "v121", "v122", "v123", "v124", "v125", "v126", "v127", "v128", "v129", "v130", "v131", "v132", "v133", "v134", "v135", "v136", "v137", "v138", "v139", "v140", "v141", "v142", "v143", "v144", "v145", "v146", "v147", "v148", "v149", "v150", "v151", "v152", "vcc", "scc", "memory");
}

#define TP0 128
#define TP1 8
#define TP2 175
#define TP3 4
#define TP4 40
#define TP5 16
#define TP6 19
#define TP7 6
#define TP8 16
#define TP9 106
#define TP10 6
#define TP11 78
#define TP12 70
#define TP13 150
#define TP14 40
#define TP15 0
#define INJ 0u
#define PER_DFLT (TV(2) + TV(11) + (TV(0) * 4400u >> 12) + TV(10) + TV(14))
#define DBG 0
#define EARLY_N 50
#define BUMP_N 2
#define BUMP_WIN 0x7fcu
#define BUMP_D 8
#define CLK_G 90
#define BUMP_S0 4
#define BUMP_MAX 8
#define GB0 96u
#define TV(idx) ((u32)(TP##idx) * 8u)
#define TLOAD() do { } while (0)

__device__ __forceinline__ void nap(u32 n) {
    u32 hi = n >> 6, lo = (n >> 4) & 3u;
    asm volatile("s_cmp_eq_u32 %0, 0\n\ts_cbranch_scc1 Ln%=\nLm%=:\n\ts_sleep 1\n\ts_sub_u32 %0, %0, 1\n\ts_cmp_lg_u32 %0, 0\n\ts_cbranch_scc1 Lm%=\nLn%=:\n\t"
                 "s_cmp_eq_u32 %1, 0\n\ts_cbranch_scc1 Lp%=\nLo%=:\n\ts_sub_u32 %1, %1, 1\n\ts_cmp_lg_u32 %1, 0\n\ts_cbranch_scc1 Lo%=\nLp%=:"
                 : "+s"(hi), "+s"(lo) :: "scc", "memory");
}
typedef __attribute__((address_space(3))) u32* lds_u32p;
typedef __attribute__((address_space(3))) volatile u32* lds_vu32p;
typedef __attribute__((address_space(3))) u16* lds_u16p;
typedef __attribute__((address_space(3))) u32x2* lds_u32x2p;
typedef __attribute__((address_space(3))) u32x4* lds_u32x4p;
__device__ __forceinline__ u32 rfl(u32 x) { return __builtin_amdgcn_readfirstlane(x); }
__device__ __forceinline__ bool has_sent(u32x4 v) { return (v[0] == SENT) | (v[1] == SENT) | (v[2] == SENT) | (v[3] == SENT); }
__device__ __forceinline__ u32 now32() { return (u32)__builtin_amdgcn_s_memtime(); }
__device__ __forceinline__ u32 step_tag(int s) { return (u32)(s + 1) * 0x10001u; }

__device__ __forceinline__ void mem0_image(const u16* __restrict__ mem0, char* smem, const int tid) {
    for (int c = tid; c < 640; c += 512) {
        u32x4 v = *(const u32x4*)(mem0 + (c & 7) * D_ + (c >> 3) * 8);
#pragma unroll
        for (int i = 0; i < 4; ++i) v[i] = v[i] == SENT ? ~0u : v[i];
        ((u32x4*)smem)[c + (c >= 576 ? 64 : 0)] = v;
    }
}
__device__ __forceinline__ u32 once_more(u32& left, u32 lbase) {
    if (__builtin_expect((int)--left >= 0, 1)) return 2u;
    const int lane = lane_id();
    for (int k = 0; k < 11; ++k) *(lds_u32x4p)(lbase + (u32)(k * 1024 + lane * 16)) = (u32x4){0u, 0u, 0u, 0u};
    asm volatile("s_waitcnt lgkmcnt(0)" ::: "memory");
    return 0u;
}
#define COUNT(i) do { } while (0)
#define COUNT2() do { } while (0)
#define COUNT3() do { } while (0)
#define COUNT4() do { } while (0)
#define COUNT0() do { } while (0)
#define COUNT1(i) do { } while (0)
__device__ __forceinline__ void track_rate(u32 c0, u32 r0, u32& pc, u32& pr, u32& kr) {
    const u32 dr = r0 - pr;
    const int er = (int)(c0 - pc - (dr * kr >> 12)) >> 1;
    u32 k = (u32)max((int)kr + er, 2560);
    asm("" : "+s"(k));
    if (dr < 8192u) kr = min(k, 4352u);
    pc = c0; pr = r0;
}

__device__ __forceinline__ u32 opaque(u32 x) { asm volatile("" : "+s"(x)); return x; }
#define LANES() const int lane = lane_id(), n = lane & 15, g = lane >> 4; (void)n; (void)g
constexpr u32 L_Q = 12288, L_K = 18432, L_E = 24576, L_DR = 25088, L_MSK = 25600, L_KQ = 32768;
constexpr float LIM_X = 32.f, LIM_D = 1.0995116e12f;
__device__ __forceinline__ u32 kq_buf(int sg) { return L_KQ + (u32)(sg % 3) * 16384u; }

__device__ __forceinline__ u32 p_at(int h, int row, int d, int g) {
    return SL_P + (u32)((h >> 4) * 32768 + (h & 4) * 512 + (h & 8) * 128 + (h & 3) * 256 + ((row >> 1) * 4 + (row & 1) + (g & 1) * 2) * 16 + ((d ^ (h >> 1)) & 1) * 8 + (g >> 1) * 4);
}
__device__ __forceinline__ void s_slow(const int h, const int s, rsrc_t rs, u32 so, u32 lbase) {
    const int lane = lane_id(), row = lane & 7, j = lane >> 3;
    const float ce = 0.08838834764831845f * 1.4426950408889634f;
    float sc[5] = {0.f, 0.f, 0.f, 0.f, 0.f};
    const u32 kx = lbase + kq_buf(s) + (u32)((j & 1) * 4096 + (j >> 1) * 64);
#pragma unroll 1
    for (int e = 0; e < DH_; ++e) {
        const u32 qa = lbase + L_Q + (u32)((e >> 4) * 768 + (row >> 2) * 192 + (e & 15) * 8 + (row & 3) * 2);
        const float q = bf2f(*(lds_u16p)qa) + bf2f(*(lds_u16p)(qa + 384));
        const u32 ka = kx + (u32)((e >> 5) * 1024 + ((e >> 2) & 3) * 256 + ((e >> 4) & 1) * 8 + (e & 3) * 2);
#pragma unroll
        for (int i = 0; i < 4; ++i) sc[i] += q * bf2f(*(lds_u16p)(ka + i * 16));
        sc[4] += q * bf2f(*(lds_u16p)(lbase + L_K + (u32)((e >> 4) * 768 + (j >> 1) * 192 + (e & 15) * 8 + (j & 1) * 2)));
    }
    sc[4] *= ce;
    const u32x2 ev = *(lds_u32x2p)(lbase + L_E + (u32)((j >> 1) * 128 + row * 16 + (j & 1) * 8));
    float se = (bf2f((u16)ev[0]) + bf2f((u16)(ev[0] >> 16))) + (bf2f((u16)ev[1]) + bf2f((u16)(ev[1] >> 16)));
    float mx = fmaxf(fmaxf(fmaxf(sc[0], sc[1]), fmaxf(sc[2], sc[3])), sc[4]);
#pragma unroll
    for (int o = 8; o < 64; o <<= 1) { se += __shfl_xor(se, o); mx = fmaxf(mx, __shfl_xor(mx, o)); }
    const bool rew = ev[0] == SENT || ev[1] == SENT;
    u64 rw = __ballot(rew);
    rw |= rw >> 32; rw |= rw >> 16; rw |= rw >> 8;
    const bool again = (rw >> row) & 1u;
    float al = exp2f(-mx);
    if (again) {
        const u32x2 en = {pack2(exp2f(sc[0] - mx), exp2f(sc[1] - mx)), pack2(exp2f(sc[2] - mx), exp2f(sc[3] - mx))};
        se = (bf2f((u16)en[0]) + bf2f((u16)(en[0] >> 16))) + (bf2f((u16)en[1]) + bf2f((u16)(en[1] >> 16)));
        al = 1.f;
    }
    float sn = se;
#pragma unroll
    for (int o = 8; o < 64; o <<= 1) sn += __shfl_xor(sn, o);
    if (again) se = sn;
    const float em = exp2f(sc[4] - mx);
    float sm = em;
#pragma unroll
    for (int o = 8; o < 64; o <<= 1) sm += __shfl_xor(sm, o);
    const float rd = 1.f / (sm + al * se);
    if (rw & 255u) {
#pragma unroll 1
        for (u32 spins = 0; spins < SPIN_CAP && !__all(*(lds_vu32p)(lbase + L_DR + (u32)(lane & 1) * 4) == step_tag(s)); ++spins) __builtin_amdgcn_s_sleep(1);
        const u32x2 en = {pack2(exp2f(sc[0] - mx), exp2f(sc[1] - mx)), pack2(exp2f(sc[2] - mx), exp2f(sc[3] - mx))};
        const u32 eo = again ? (u32)((h >> 3) * 32768 + ((h >> 1) & 3) * 1024 + (j >> 2) * 256 + (h & 1) * 128 + row * 16 + (j & 3) * 4) : OOB;
#pragma unroll
        for (int c = 0; c < 8; ++c) stx1(rs, eo, en[c >> 2], so + (c & 1) * 4096 + (c & 2) * 8192 + (c >> 2) * 512);
    }
    DRAIN();
    const u32 pm = pack2(em * rd, __shfl_xor(em * rd, 8));
    const int g = j >> 1;
    u32 dg = pack2(row == 2 * g ? al * rd : 0.f, row == 2 * g + 1 ? al * rd : 0.f);
    if (again && (row >> 1) == (g ^ 1)) dg = MARK;
    const u32 po = p_at(h, row, j & 1, g);
    const bool done = ldx1(rs, po, so) != SENT;
#pragma unroll
    for (int c = 0; c < 4; ++c) stx1(rs, done ? OOB : po, (j & 1) ? dg : pm, so + (c & 1) * 4096 + (c >> 1) * 16384);
}

__device__ __forceinline__ void s_role(const int wave, const int h, const int xg, const u64 tag, const u16* __restrict__ mem0, const u32x32 (&W)[5],
                                       char* __restrict__ ws, char* smem) {
    const int lane = lane_id(), tid = wave * 64 + lane, n = lane & 15, g = lane >> 4;
    u32* errw = (u32*)(ws + OFF_ERR);
    const rsrc_t rs = mk_rsrc(ws);
    const u32x4 srd = mk_srd(ws);
    const u32 lbase = (u32)(uintptr_t)(__attribute__((address_space(3))) char*)smem;
    TLOAD();
    int wslot = 0; const int wid = 128 + h;
    WSTAMP(2);
    const u32 wp = wait_init(rs, ws, tag, lane);
    mem0_image(mem0, smem, tid);
    if (tid < 448) *(u32x4*)(smem + L_K + tid * 16) = (u32x4){0u, 0u, 0u, 0u};
    const u32 sidx = (n & 2) ? 0xEEEE : 0x4444;
    const u32 va = lbase + (u32)(g * 256 + (n & 2) * 64 + (n >> 2) * 32 + (n & 1) * 16);
    const u32 qw = lbase + L_Q + (u32)(wave * 768 + (g >> 1) * 192 + n * 8 + (g & 1) * 4);
    const u32 kw = lbase + L_K + (u32)(wave * 768 + g * 192 + n * 8);
    const u32 qtr = lbase + L_Q + (u32)((lane & 3) * 192 + (lane >> 2) * 8);
    const float ce = 0.08838834764831845f * 1.4426950408889634f;
    STAMPS();
    COUNT0();
#define S_LAST() do { u32 redo; do { redo = 0; s_l(W, va, lbase, sidx, qw, kw, step_tag(NSEG - 1), redo); } while (__builtin_expect(redo, 0)); } while (0)

    if (wave < 2) {
        LANES();
        const int c = 2 * wave + (n >> 3);
        const u32 kxa = lbase + (u32)(((n >> 2) & 1) * 4096 + g * 256 + (4 * c + (n & 3)) * 16);
        const u32 ep = (u32)((n & 7) * 16 + (g & 1) * 8 + (n >> 3) * 4);
        const u32 eo = (u32)((h >> 3) * 32768 + ((h >> 1) & 3) * 1024 + (wave + 2 * (n >> 3)) * 256 + (h & 1) * 128 + (n & 7) * 16 + (g >> 1) * 8 + (g & 1) * 4096);
        const u32 ew = lbase + L_E + (u32)((2 * wave + (g >> 1)) * 128) + ep;
        const u32 ts = slot_v(wp, lane), tk = kq_buf(lane);
        LBAR();
#pragma unroll 1
        for (int s = 0; s + 1 < NSEG; ++s) {
            s_e(W, va, lbase, sidx, qw, kw, step_tag(s), kxa + tab(tk, s), qtr, eo, ew, LIM_X, srd, tab(ts, s) STA);
            nap(256);
            DRAIN();
            *(lds_u32p)(lbase + L_DR + (u32)wave * 4) = step_tag(s);
            FLUSH();
        }
        S_LAST();
    } else if (wave == 2) {
        LANES();
        const u32 po = p_at(h, n & 7, n >> 3, g & 1) + (u32)(g >> 1) * 4096;
        const u32 er = lbase + L_E + (u32)(g * 128 + (n & 7) * 16);
        const u32x4 ones = {0x3f803f80u, 0x3f803f80u, 0x3f803f80u, 0x3f803f80u};
        const u32 ts = slot_v(wp, lane);
        LBAR();
#pragma unroll 1
        for (int s = 0; s + 1 < NSEG; ++s) {
            const u32 so = tab(ts, s);
            u32 bad;
            s_f(W, va, lbase, sidx, qw, kw, step_tag(s), bad, ones, qtr, er, po, n < 8 ? 1.f : 0.f, n == 8 + 2 * g ? 1.f : 0.f, n == 9 + 2 * g ? 1.f : 0.f, srd, so, ce, LIM_D,
                    0xccccccccccccccccull STA);
            if (__builtin_expect(bad, 0)) s_slow(h, s, rs, so, lbase);
            FLUSH();
        }
        S_LAST();
    } else if (wave == 3) {
        LANES();
        const u32 vm = (u32)lane * 16, cp = (u32)xg ^ (wp >> 1);
        const bool lead = h == 0;
        const u32 tsw = (u32)((lane & 7) * 16 + (lane & 8) * 512 + (lane & 16) * 1024 + (lane >> 5) * SL_2ND);
        const u32 ro = (u32)(lane & 31) * 128, rl = SL_REC + (u32)h * 128;
        LBAR();
        u32 c0, r0, kr = 3950;
        calib(c0, r0);
        u32 pc = c0, pr = r0, te = r0 - 65536u, tn = te + PER_DFLT + GB0, left = SPIN_CAP, own = 0, sp = 0, lim = c0 - 65536u, tg = 0, gb = GB0, irr = 0, hist = ~0u, bump = 0;
#pragma unroll 1
        for (int s = 0; s + 1 < NSEG; ++s) {
            const u32 sm = slot_off(wp ^ 8u, s), so = sp + copy_off(cp, s - 1);
            const u32 mg = s < EARLY_N ? TV(10) : 0u, er = 4096u - kr, ki = 4096u + er + (u32)((int)(er * er) >> 12);
            const u32 off = (TV(0) * ki >> 12) + mg + bump * BUMP_D;
            const u32 lev = min((u32)max((int)(ki - 4200u) * CLK_G >> 12, 0) + (mg >> 4) + bump, 15u);
            u32 poll = ((u32)s + 63u) >> 6, lv;
            s_m(W, va, lbase, sidx, qw, kw, step_tag(s), left, poll, lv, lim, tg, irr, ki, off + 192u, TV(11), 2000u, TV(7), TV(13), TV(13) * 4400u >> 12, TV(15), sm + copy_off(cp, s) + SL_REC,
                sm + SL_REC + SL_2ND, so + SL_2ND, c0, r0, te, tn, gb, TV(14), lev, own, TV(2), TV(9), kr, TV(2) + TV(11) + off + 15u, PER_DFLT, vm, so, lbase, srd, sm + rl,
                lead ? sp + SL_TS : OOB, (u32)s, tsw, ro, so + SL_REC, sp + SL_REC + SL_2ND STA);
            if (lv) COUNT(0);
            if (lv > 1) COUNT2();
            own = lv; sp = sm;
            hist = hist * 2 + (irr >> 31);
            if (s > BUMP_S0 && (hist & 3u) == 1u && (hist & BUMP_WIN) && bump < BUMP_MAX) bump += BUMP_N;
            track_rate(c0, r0, pc, pr, kr);
            FLUSH();
        }
        const u32 so = sp + copy_off(cp, NSEG - 2);
        s_g(c0, r0, te, tn, gb, TV(14), 0u, own, TV(2), TV(9), kr, 0u, 0u, vm, so, lbase, srd, OOB, lead ? sp + SL_TS : OOB, NSEG - 1u, tsw, ro, so + SL_REC, sp + SL_REC + SL_2ND STA);
        for (;;) {
            u32 redo = 0;
            s_l(W, va, lbase, sidx, qw, kw, step_tag(NSEG - 1), redo);
            if (__builtin_expect(!redo, 1)) break;
            if (once_more(left, lbase)) gather(r0, kr, vm, so + SL_2ND, lbase, srd);
        }
        if ((int)left < 0) atomicOr(errw, 1u);
    } else if (wave < 6) {
        LANES();
        const int nt = wave & 1;
        f32x4* msk = (f32x4*)(smem + L_MSK) + nt * 128 + lane;
#pragma unroll
        for (int q = 0; q < 2; ++q) {
            f32x4 m;
#pragma unroll
            for (int i = 0; i < 4; ++i) m[i] = q * 16 + 4 * g + i <= nt * 16 + n ? 0.f : -__builtin_huge_valf();
            msk[q * 64] = m;
        }
        const u32 vl = lbase + (u32)lane * 16;
        const u32 o0 = (u32)OFF_PX + (u32)((h * 2 + nt) * 1024 + lane * 16);
        const u32 o1 = (u32)OFF_PXM + (u32)(((h >> 2) * 2 + nt) * 1024 + ((h & 3) * 16 + n) * 16 + g * 4);
        u32x4 p0 = {0u, 0u, 0u, 0u};
        u32 p1 = 0;
        LBAR();
#pragma unroll 1
        for (int s = 0; s < NSEG; ++s) {
            if (s + 1 < NSEG) {
                const u32 off = s == 0 || (DBG & 8) ? OOB : 0u;
                s_p1(W, va, lbase, sidx, qw, kw, step_tag(s), p0, p1, (o0 + (u32)s * 65536 - 65536) | off, (o1 + (u32)s * 16384 - 16384) | off, srd STA);
                nap(TV(8));
            } else {
                stx4(rs, o0 + (NSEG - 2) * 65536, p0);
                stx1(rs, o1 + (NSEG - 2) * 16384, p1);
                S_LAST();
                stx1(rs, lane == 0 ? (u32)(OFF_FP + (h >> 2) * 128 + ((h & 3) * 2 + nt) * 4) : OOB, 1u);
            }
            px_step(p0, p1, vl + kq_buf(s), vl + kq_buf(s) + 8192 + (u32)nt * 4096, vl + L_MSK + (u32)nt * 2048, qtr + (L_K - L_Q), ce);
            STAMP(3);
            FLUSH();
        }
        stx4(rs, o0 + (NSEG - 1) * 65536, p0);
        stx1(rs, o1 + (NSEG - 1) * 16384, p1);
        DRAIN();
        stx1(rs, lane == 0 ? (u32)(OFF_FZ + ((h >> 2) * 2 + nt) * 128 + (h & 3) * 4) : OOB, 1u);
    } else if (wave == 6) {
        LANES();
        const u32 fq = done_at(wp, 0, h), fb = done_at(wp, 0, (lane & 1) && h < NFILL ? 64 + h : 32 + h), dv = done_tag(tag);
        u32 cap = SPIN_CAP;
        wait_done(rs, fq, dv, cap, errw);
#pragma unroll
        for (int i = 0; i < 16; ++i) dma16_sc1(ws + far_off(wp ^ (i < 8 ? KFL : QFL), (i < 8 ? NEAR_KX : NEAR_QX) + (u32)h * 8192) + lane * 16 + (i & 7) * 1024, lbase + L_KQ + i * 1024);
        wait_done(rs, ((lane & 2) ? fq : fb) + (u32)(lane & 4) * 8192, dv, cap, errw);
        DRAIN();
        LBAR();
#pragma unroll 1
        for (int s = 0; s + 1 < NSEG; ++s) {
            const u32 pfd = lbase + kq_buf(s + 1), fs = fq + (u32)min(s + 2, NSEG - 1) * 32768, c0 = cap;
            s_p0(W, va, lbase, sidx, qw, kw, step_tag(s), (u32)lane * 16, srd, (DBG & 4) ? OOB : far_off(wp ^ KFL, NEAR_KX + (u32)((s + 1) * H_ + h) * 8192), pfd,
                 lane == 0 ? fs : OOB, lbase + 11264u, fb + (u32)(s + 1) * 32768, dv, cap STA);
            nap(TV(4));
            if (__builtin_expect(cap != c0, 0)) { COUNT4(); if ((int)cap < 0) { atomicOr(errw, 8u); cap = 1; } }
            DRAIN();
            if (rfl(*(lds_vu32p)(lbase + 11264u)) != dv) { COUNT4(); wait_done(rs, fs, dv, cap, errw); }
            FLUSH();
        }
        S_LAST();
    } else {
        LANES();
        u32 cap = SPIN_CAP;
        wait_done(rs, done_at(wp, 0, 64 + min(lane, NFILL - 1)), done_tag(tag), cap, errw);
        LBAR();
#pragma unroll 1
        for (int s = 0; s + 1 < NSEG; ++s) {
            const u32 pfd = lbase + kq_buf(s + 1) + 8192;
            s_p2(W, va, lbase, sidx, qw, kw, step_tag(s), (u32)lane * 16, srd, (DBG & 4) ? OOB : far_off(wp ^ QFL, NEAR_QX + (u32)((s + 1) * H_ + h) * 8192), pfd STA);
            FLUSH();
        }
        S_LAST();
    }
    COUNT1(0);
}

__device__ __forceinline__ void v_role(const int wave, const int h, const int xg, const u64 tag, const u16* __restrict__ mem0, const u32x32 (&W)[5],
                                       char* __restrict__ ws, char* smem) {
    const int lane = lane_id(), tid = wave * 64 + lane, n = lane & 15, g = lane >> 4;
    u32* errw = (u32*)(ws + OFF_ERR);
    const rsrc_t rs = mk_rsrc(ws);
    const u32x4 srd = mk_srd(ws);
    const u32 lbase = (u32)(uintptr_t)(__attribute__((address_space(3))) char*)smem;
    TLOAD();
    const u32 wp = wait_init(rs, ws, tag, lane);
    mem0_image(mem0, smem, tid);
    const u32 va = lbase + (u32)(g * 256 + (n & 2) * 64 + (n >> 2) * 32 + (n & 1) * 16);
    const u32 si = n >= 8 ? 0xEEEE : 0x4444, vr = lbase + 12288 + (u32)((lane >> 2) * 16 + (lane & 1) * 8);
    auto zat = [&](int s, int i) {
        const int l = lane_id(), g = l >> 4;
        return z_at(wp, s, (u32)(2 * wave + (g >> 1) + i * 16 + (i < 2 ? (g & 1) * 8 : 0))) + (u32)((h >> 4) * 32768 + (h & 15) * 256 + (l & 15) * 16 + (i < 2 ? 0 : (g & 1) * 8));
    };
    STAMPS();
    COUNT0();
    if (wave == 3) {
        const int lane = lane_id();
        const u32 cp = (u32)xg ^ (wp >> 1), ro = (u32)(lane & 31) * 128;
        u32 left = SPIN_CAP;
        wait_done(rs, done_at(wp, 0, 64 + min(lane, NFILL - 1)), done_tag(tag), left, errw);
        LBAR();
        u32 c0, r0, kr = 3950;
        calib(c0, r0);
        u32 pc = c0, pr = r0, te = r0 - 65536u, tn = te + PER_DFLT + GB0, so = 0, sp = 0, gb = GB0;
#pragma unroll 1
        for (int s = 0; s < NSEG; ++s) {
            const u32 sm = slot_off(wp ^ 8u, s), sn = sm + copy_off(cp, s), l0 = left;
            v_b1(W, va, lbase, si, vr, zat(s, 0), zat(s, 1), zat(s, 2), srd, left, c0, r0, te, tn, gb, TV(14), (u32)s, TV(2) + TV(3), TV(9), kr, PER_DFLT,
                 (u32)lane * 16, so, so + SL_2ND, lbase, ro, sn + SL_REC, so + SL_REC, sp + SL_REC + SL_2ND STA);
            so = sn; sp = sm;
            if (l0 != left) COUNT(3);
            track_rate(c0, r0, pc, pr, kr);
            FLUSH();
        }
        if ((int)left < 0) atomicOr(errw, 1u);
    } else if (wave < 4) {
        LBAR();
#pragma unroll 1
        for (int s = 0; s < NSEG; ++s) { v_b0(W, va, lbase, si, vr, zat(s, 0), zat(s, 1), zat(s, 2), srd STA); FLUSH(); }
    } else {
        LANES();
        const u32 vw = lbase + 12288 + (u32)((wave & 2) * 256 + (n & 4) * 64 + ((wave & 1) * 2 + (n >> 3)) * 64 + (n & 3) * 16 + g * 4);
        LBAR();
#pragma unroll 1
        for (int s = 0; s < NSEG; ++s) { v_a(W, va, lbase, (n & 2) ? 0xEEEE : 0x4444, vw STA); FLUSH(); }
    }
    DRAIN();
    LBAR();
    stx4(rs, wave == 0 && lane_id() == 0 ? (u32)(OFF_FR + h * 128) : OOB, (u32x4){1u, 1u, 1u, 1u});
    COUNT1(3);
}

__device__ __forceinline__ void r_slow(rsrc_t rs, u32 so, u32 bs, u32 zs, u32 sm, u32 bo, u32x8 za, u32 po, u32 mo, u32 lbase, u32& cap, u32* errw) {
    const int lane = lane_id(), n = lane & 15, g = lane >> 4;
    u32x4 p[8], z[8];
    u32 spins = 0;
#pragma unroll 1
    for (; spins < cap; ++spins) {
        CFENCE();
        bool miss = false;
#pragma unroll
        for (int j = 0; j < 8; ++j) {
            p[j] = ldx4(rs, po + (u32)(j & 3) * 512, so + SL_P + (u32)(j >> 2) * 32768u);
            z[j] = ldx4(rs, za[j], zs);
        }
#pragma unroll
        for (int j = 0; j < 8; ++j) miss |= has_sent(p[j]) | has_sent(z[j]);
        if (!__any(miss)) break;
    }
#pragma unroll
    for (int j = 0; j < 8; ++j)
#pragma unroll
        for (int i = 0; i < 4; ++i) p[j][i] = p[j][i] == MARK ? 0u : p[j][i];
    const u32 uw = lbase + 8192 + (u32)((g >> 1) * 512 + n * 16 + (g & 1) * 8);
#pragma unroll 1
    for (int t = 0; t < 16; ++t) {
        u32x4 e;
#pragma unroll 1
        for (; spins < cap; ++spins) {
            CFENCE();
            e = ldx4(rs, (u32)(lane * 16 + (t & 3) * 1024), so + (u32)(t >> 2) * 32768u);
            if (!__any(has_sent(e))) break;
        }
        const u32x4 x = ldx4(rs, bo + (u32)(t & 3) * 2048, bs + (u32)(t >> 2) * 16384u), y = ldx4(rs, bo + (u32)(t & 3) * 2048 + 1024, bs + (u32)(t >> 2) * 16384u);
        const f32x4 u = smf(e, (u32x4){x[0], y[0], x[2], y[2]}, (u32x4){x[1], y[1], x[3], y[3]}, (f32x4){0.f, 0.f, 0.f, 0.f}, n >= 8 ? 0xEEEE : 0x4444);
        *(lds_u32x2p)(uw + (u32)((t >> 1) * 1024 + (t & 1) * 256)) = (u32x2){pack2(u[0], u[1]), pack2(u[2], u[3])};
    }
    if (spins >= cap) { atomicOr(errw, 2u); cap = 1; }
    f32x4 acc = {0.f, 0.f, 0.f, 0.f};
#pragma unroll
    for (int j = 0; j < 8; ++j) {
        const int k = (WORD >> (4 * j)) & 7, q = k >> 1;
        const u32x4 a = {p[k][2], p[k][3], p[(k + 1) & 7][0], p[(k + 1) & 7][1]};
        const u32 ur = lbase + 8192 + (u32)(q * 2048 + lane * 16);
        acc = smf(a, (k & 1) ? z[2 * q] : *(lds_u32x4p)ur, (k & 1) ? z[2 * q + 1] : *(lds_u32x4p)(ur + 1024), acc, (n & 2) ? 0xEEEE : 0x4444);
    }
    const float a0 = acc[0] + acc[2], a1 = acc[1] + acc[3], o = __shfl_xor(n < 8 ? a1 : a0, 8);
    const u32 r = n < 8 ? pack2(a0, o) : pack2(o, a1);
#pragma unroll
    for (int c = 0; c < 8; ++c) stx1(rs, mo, r, sm + (c & 1) * 4096 + (c & 2) * 8192 + (c >> 2) * SL_2ND);
}

__device__ __forceinline__ void r_role(const int wave, const int ct, const int xg, const u32 wp, const u32 dv, char* __restrict__ ws, char* smem) {
    if (wave == 4 || wave > 5) return;
    const int lane = lane_id(), n = lane & 15, g = lane >> 4;
    u32* errw = (u32*)(ws + OFF_ERR);
    const rsrc_t rs = mk_rsrc(ws);
    const u32x4 srd = mk_srd(ws);
    const u32 lbase = (u32)(uintptr_t)(__attribute__((address_space(3))) char*)smem;
    TLOAD();
    const u32 cp = (u32)xg ^ (wp >> 1), tw = lbase + 6144;
    const u32 te = slot_v(wp, lane) + copy_off(cp, lane);
    u32 cap = SPIN_CAP;
    STAMPS();
    COUNT0();
    if (wave == 5) {
        const u32 ro = (u32)(lane & 31) * 128, rd = SL_REC + ((wp & 8u) ? -0x40000 : 0x40000);
        u32 c0, r0, kr = 4000, nl;
        *(lds_u32x4p)tw = (u32x4){0u, 0u, 0u, 0u};
        wait_done(rs, done_at(wp, 0, 32 + min(lane, 31 + NFILL)), dv, cap, errw);
        calib(c0, r0);
        u32 pc = c0, pr = r0, tx = r0 - 65536u, tb = tx, gb = GB0;
#pragma unroll 1
        for (int s = 0; s + 1 < NSEG; ++s) {
            const u32 so = tab(te, s);
            const u32 g0 = gb;
            u32 tn = tx + PER_DFLT + gb;
            r_t(c0, r0, tx, tn, gb, nl, tw, srd, step_tag(s), kr, tb, TV(12), TV(9), ro, so + rd, so + rd + SL_2ND - copy_off(cp, s) STA);
            tb = tx + TV(2) + g0 + TV(5) + (nl ? TV(14) : 0u); tx = tn;
            track_rate(c0, r0, pc, pr, kr);
            FLUSH();
        }
    } else {
        const int q = (wave + 3) & 3;
        const u32 cn = (u32)((n & 7) * 32 + (n >> 3) * 16);
        const u32 bo = (u32)g * 256 + cn, eo = (u32)(q * 32768 + lane * 16), si = n >= 8 ? 0xEEEE : 0x4444;
        const u32 tb = ct < 8 * TWO ? far_v(wp ^ 1u, b_at(NEAR_B, RING, lane & (RING - 1), ct)) : far_v(wp, b_at(FAR_BX, NSEG, lane, ct));
        if (wave) {
            const u32 uw = lbase + (u32)(q * 2048 + (g >> 1) * 512 + n * 16 + (g & 1) * 8);
#pragma unroll 1
            for (int s = 0; s + 1 < NSEG; ++s) {
                r_u(bo, eo, si, tw, srd, tab(te, s), (DBG & 1) ? OOB : tab(tb, s) + (u32)q * 16384, step_tag(s), uw, (u32)q STA);
                FLUSH();
            }
        } else {
            const int kp = ct >= 36 ? 10 : ct >> 2;
            const u32 mo = (u32)((kp >> 2) * 32768 + (kp & 3) * 1024 + ((ct & 3) * 2 + ((n >> 2) & 1)) * 128 + (2 * g + (n >> 3)) * 16 + (n & 3) * 4);
            const u32 po = (u32)((lane >> 5) * 2048 + (lane & 31) * 16);
            u32x8 za;
#pragma unroll
            for (int j = 0; j < 8; ++j) { const int hh = 4 * j + ((g & 1) ? 8 : 2) + (g >> 1); za[j] = (u32)((hh & 16) * 2048 + (hh & 15) * 256) + cn; }
            const u32 tm = slot_v(wp ^ 8u, lane), tz = z_at(wp, lane, ct);
#pragma unroll 1
            for (int s = 0; s + 1 < NSEG; ++s) {
                const u32 so = tab(te, s), sm = tab(tm, s), bs = tab(tb, s), zs = tab(tz, s);
                u32 bad;
                r_f(bo, eo, si, tw, srd, so, (DBG & 1) ? OOB : bs + 3 * 16384, step_tag(s), bad, za, po, mo, (n & 2) ? 0xEEEE : 0x4444, lbase + (u32)lane * 16, zs, so + SL_P, sm,
                    min(TV(6), 256u), min(TV(1), 256u) STA);
                if (bad) COUNT(2);
                if (__builtin_expect(bad == 3, 0)) { COUNT3(); r_slow(rs, so, bs, zs, sm, bo, za, po, mo, lbase, cap, errw); }
                FLUSH();
            }
        }
    }
    COUNT1(2);
}

#define FILL_0 8
#define FILL_T 2
template <int PAUSE>
__device__ __forceinline__ void fill_share(rsrc_t rs, const u32 wp, const int s, const int w) {
    const int lane = lane_id(), u = w + lane * 4 * NFILL, pg = u >> 4, hf = pg >= 6;
    const u32 t = u < 224 ? slot_v(wp & ~8u, s) + (u32)((pg - 6 * hf) * 32768 + ((hf ^ (wp >> 3)) & 1) * 262144 + (u & 4) * 1024 + (u & 8) * 2048 + (u & 3) * 1024)
                          : (u < 544 ? far_v(wp ^ 1u, NEAR_Z + (u32)(s * NRED + ((u - 224) >> 3)) * 8192) + (u32)(u & 7) * 1024 : OOB);
#pragma unroll 1
    for (int i = 0; i < 10; ++i) {
        stx4(rs, (u32)lane * 16, (u32x4){SENT, SENT, SENT, SENT}, tab(t, i));
        if (PAUSE) __builtin_amdgcn_s_sleep(PAUSE);
    }
}
__device__ __forceinline__ void fill_first(const int wave, const int f, const u32 wp, const u32 dv, char* __restrict__ ws) {
    const rsrc_t rs = mk_rsrc(ws);
#pragma unroll 1
    for (int s = wave >> 2; s < FILL_0; s += 2) fill_share<0>(rs, wp, s, f * 4 + (wave & 3));
    DRAIN();
    LBAR();
    const int lane = lane_id();
    stx1(rs, wave + 1 < FILL_0 && lane < 32 ? done_at(wp, wave, 64 + f) + lane * 4 : OOB, dv);
}
__device__ __forceinline__ void fill_role(const int wave, const int f, const u32 wp, const u32 dv, char* __restrict__ ws, u32* cnt) {
    const int lane = lane_id();
    const rsrc_t rs = mk_rsrc(ws);
#pragma unroll 1
    for (int s = FILL_0; s <= NSEG; s += 2) {
        if (s < NSEG) { fill_share<FILL_T>(rs, wp, s, f * 4 + wave); fill_share<FILL_T>(rs, wp, s + 1, f * 4 + wave); }
        DRAIN();
        u32 a = 0;
        if (lane == 0) a = __hip_atomic_fetch_add(cnt + (s & 63), 1u, __ATOMIC_RELAXED, __HIP_MEMORY_SCOPE_WORKGROUP);
        stx1(rs, rfl(a) == 3u ? done_at(wp, min(s - 1 + (lane >> 5), NSEG - 1), 64 + f) + (lane & 31) * 4 : OOB, dv);
    }
}

__device__ __forceinline__ void relay_role(const u32 wp, const int xg, char* __restrict__ ws) {
    const int lane = lane_id();
    const rsrc_t rs = mk_rsrc(ws);
#pragma unroll 1
    for (int k = 0; k + 1 < NSEG; ++k) {
        const u32 o = slot_off(wp ^ 8u, k) + copy_off((u32)xg ^ (wp >> 1), k) + SL_TS;
        u32 t = 0;
#pragma unroll 1
        for (u32 spins = 0; spins < SPIN_CAP; ++spins) {
            CFENCE();
            const u32x4 f = ldx4(rs, 0u, o);
            t = rfl(f[2]);
            if (rfl(f[1]) == (u32)k + 1u) break;
            __builtin_amdgcn_s_sleep(8);
            if ((spins & 63) == 63 && ldx1(rs, (u32)OFF_ERR) != 0u) return;
        }
        if (k >= 3) stx1(rs, lane < 8 ? (u32)(OFF_RDY + lane * 128) : OOB, (u32)k - 2u);
        for (int q = 0; q < 64 && (int)((u32)__builtin_amdgcn_s_memrealtime() * 24u - t) < 700; ++q) __builtin_amdgcn_s_sleep(2);
    }
}

#define AHEAD 3
#define PULL_T 300
#define PULL_0 150
__device__ __forceinline__ u32 now24() { return (u32)__builtin_amdgcn_s_memrealtime() * 24u; }
__device__ __forceinline__ void until(u32 x) { for (int q = 0; q < 256 && x - now24() < 0x4000u; ++q) __builtin_amdgcn_s_sleep(1); }
__device__ __forceinline__ void watch_role(const u32 wp, const u32 dv, const int xg, char* __restrict__ ws, lds_vu32p lim) {
    const rsrc_t rs = mk_rsrc(ws);
    const u32 fo = done_at(wp, 0, 32 + min(lane_id(), 31 + NFILL));
    u32 have = 0, t = now24();
#pragma unroll 1
    for (int k = -1, spins = 0; k + AHEAD + 1 < NSEG && spins < 0x4000; ++spins) {
        CFENCE();
        u32x4 f = {0u, 0u, 0u, 0u};
        until(t + 700u);
        const u32 d = ldx1(rs, have <= (u32)(k + 1 + AHEAD) ? fo + have * 32768 : OOB), e = ldx1(rs, have <= (u32)(k + AHEAD) ? fo + have * 32768 + 32768 : OOB);
        if (have) f = ldx4(rs, 0u, slot_off(wp ^ 8u, k + 1) + copy_off((u32)xg ^ (wp >> 1), k + 1) + SL_TS);
        if (__all(d == dv)) have += __all(e == dv) ? 2 : 1;
        if (rfl(f[1]) == (u32)k + 2u) { ++k; t = rfl(f[2]); } else t = now24() + 300u;
        lim[1] = t;
        lim[0] = have;
    }
    lim[0] = NSEG;
}
__device__ __forceinline__ void pull_role(const u32 wp, const int xg, const int wi, char* __restrict__ ws, char* smem, lds_vu32p lim) {
    const u32x4 srd = mk_srd(ws);
    const u32 lbase = (u32)(uintptr_t)(__attribute__((address_space(3))) char*)smem, vo = (u32)lane_id() * 128;
#pragma unroll 1
    for (int s = 0; s + 1 < NSEG; ++s) {
        while (rfl(lim[0]) <= (u32)s) __builtin_amdgcn_s_sleep(2);
        u32 t = rfl(lim[0]) > (u32)s + 1u ? now24() : rfl(lim[1]) + PULL_0;
#pragma unroll 1
        for (int q = 8 * TWO + wi; q < 40; q += 6) {
            until(t);
            u32 sv;
            asm volatile("s_mov_b32 %0, m0\n\ts_mov_b32 m0, %4\n\ts_nop 3\n\tbuffer_load_dword %1, %2, %3 offen sc1 lds\n\ts_mov_b32 m0, %0"
                         : "=&s"(sv) : "v"(vo), "s"(srd), "s"(far_off(wp, b_at(FAR_BX, NSEG, s, (q & ~3) + xg) + (u32)(q & 3) * 8192)), "s"(lbase + 4096u) : "memory");
            t += PULL_T;
        }
    }
    DRAIN();
}

__device__ __forceinline__ u16* karg(int i) {
    __attribute__((address_space(1))) u16* p;
    asm volatile("s_load_dwordx2 %0, %1, %2\n\ts_waitcnt lgkmcnt(0)" : "=s"(p) : "s"(__builtin_amdgcn_kernarg_segment_ptr()), "s"(i * 8));
    return (u16*)p;
}
#define ARG(i) karg(i)

__device__ __forceinline__ u32 topk_role(const u64 tag, char* __restrict__ ws) {
    const int lane = lane_id();
    const rsrc_t rs = mk_rsrc(ws);
    wait_rows(rs, tag, lane & 31);
    u32 v[32];
#pragma unroll
    for (int i = 0; i < 32; ++i) v[i] = ldx1(rs, (u32)lane * 4, (u32)(OFF_R + i * 256));
#pragma unroll
    for (int i = 0; i < 32; ++i) v[i] = ((v[i] ^ ((u32)((int)v[i] >> 31) | 0x80000000u)) & 0xffff0000u) | (u32)(2047 - i * 64 - lane);
    u32 top = ~0u, res = 0;
#pragma unroll 1
    for (int c = 0; c < TOPC; ++c) {
        u32 d = ~0u;
#pragma unroll
        for (int i = 0; i < 32; ++i) d = min(d, top - v[i]);
        d = min(d, (u32)__builtin_amdgcn_update_dpp(0u, d, 0xb1, 0xf, 0xf, false));
        d = min(d, (u32)__builtin_amdgcn_update_dpp(0u, d, 0x4e, 0xf, 0xf, false));
        d = min(d, (u32)__builtin_amdgcn_update_dpp(0u, d, 0x124, 0xf, 0xf, false));
        d = min(d, (u32)__builtin_amdgcn_update_dpp(0u, d, 0x128, 0xf, 0xf, false));
        const u32 dm = min(min((u32)__builtin_amdgcn_readlane(d, 0), (u32)__builtin_amdgcn_readlane(d, 16)), min((u32)__builtin_amdgcn_readlane(d, 32), (u32)__builtin_amdgcn_readlane(d, 48)));
        top -= dm;
        if (lane == c) res = 2048u - (top & 2047u);
        top -= 1u;
    }
    return res;
}

template <int K0, int K1>
__device__ __forceinline__ void moe_ask(const u16* __restrict__ wp, const int ldw, const int nk, const u32 la, const int lane) {
#pragma unroll
    for (int i = 4 * K0; i < 4 * K1; ++i)
        if (i < 4 * nk) dma16<true>(wp + (size_t)(i * 8 + (lane >> 3)) * ldw + (lane & 7) * 8, la + (i - 4 * K0) * 1024);
}
template <int K0, int K1>
__device__ __forceinline__ void moe_get(const u32 la, const int lane, u32x4 (&r)[9][4]) {
    const u32 a = la + (u32)((lane >> 4) * 1024 + ((lane >> 2) & 3) * 128 + (lane & 3) * 8);
    DRAIN();
#pragma unroll
    for (int k = K0; k < K1; ++k)
#pragma unroll
        for (int t = 0; t < 4; ++t) { const u32x2 u = trd(a + (k - K0) * 4096 + t * 32), v = trd(a + (k - K0) * 4096 + t * 32 + 512); r[k][t] = (u32x4){u[0], u[1], v[0], v[1]}; }
    asm volatile("s_waitcnt lgkmcnt(0)" ::: "memory");
}
constexpr int NGU = FF_ / 32, NDN = D_ / 64;
template <bool down, typename F>
__device__ __forceinline__ void moe_part(F&& meanwhile, const int w, const u64 tag, const int wave, char* __restrict__ ws, char* smem) {
    const int lane = lane_id(), n = lane & 15, g = lane >> 4;
    const rsrc_t rs = mk_rsrc(ws);
    u32* sel = (u32*)(smem + 512);
    const u32 la = (u32)(uintptr_t)(__attribute__((address_space(3))) char*)smem + 8192 + (u32)wave * (down ? 20480 : 12288);
    auto meet = [&](u32 k) {
        if (!down) { LBAR(); return; }
        asm volatile("s_waitcnt lgkmcnt(0)" ::: "memory");
        if (lane == 0) __hip_atomic_fetch_add((u32*)smem + 72, 1u, __ATOMIC_RELAXED, __HIP_MEMORY_SCOPE_WORKGROUP);
        while (rfl(*(lds_vu32p)(smem + 288)) < 6 * k) __builtin_amdgcn_s_sleep(2);
        CFENCE();
    };
    const int k0 = down ? wave * 9 : (wave * 5 + 1) >> 1, nk = down ? 9 : 3 - (wave & 1), ldw = down ? D_ : FF_, c0 = (down ? w : w >> 1) * 64;
    const u16* src = ARG(down ? 9 : 7 + (w & 1)) + (size_t)k0 * 32 * ldw + c0;
    u32x4 r[9][4], af[9];
    moe_ask<0, down ? 5 : 3>(src, ldw, nk, la, lane);
    meanwhile();
    moe_get<0, down ? 5 : 3>(la, lane, r);
    if (down) {
        moe_ask<5, 9>(src, ldw, nk, la, lane);
        bool dead = false;
        if (wave == 0) {
            wait_word(rs, (lane & 8) ? (u32)OFF_CNT : (u32)(OFF_IDX + n * 4), true, (lane & 8) ? (u32)NGU * 2 : 1u, dead, (u32*)(ws + OFF_ERR), 16u, lane);
            sel[lane & 7] = ldx1(rs, (u32)(OFF_IDX + (lane & 7) * 4));
        }
        moe_get<5, 9>(la, lane, r);
    } else if (wave == 1) {
        const u32 res = topk_role(tag, ws);
        if (lane < TOPC) sel[lane] = res;
        stx1(rs, w == 0 && lane < TOPC ? (u32)(OFF_IDX + lane * 4) : OOB, res);
    }
    meet(1);
    const u32 row = (sel[n & 7] - 1u) & (S_ - 1);
    const float rv = __uint_as_float(ldx1(rs, (u32)OFF_R + row * 4));
    const u32 ao = (down ? (u32)OFF_HB + (u32)(n & 7) * (FF_ * 2) : (u32)OFF_XN + row * (D_ * 2)) + (u32)(k0 * 32 + g * 8) * 2;
#pragma unroll
    for (int k = 0; k < (down ? 9 : 3); ++k) af[k] = ldx4(rs, k < nk ? ao + k * 64 : OOB);
    if (down) {
#pragma unroll
        for (int k = 0; k < (down ? 9 : 3); ++k) {
            const u32x4 u = ldx4(rs, k < nk ? ao + k * 64 + 16 * FF_ : OOB);
#pragma unroll
            for (int i = 0; i < 4; ++i)
                af[k][i] = pack2(__uint_as_float(af[k][i] << 16) * __uint_as_float(u[i] << 16), __uint_as_float(af[k][i] & 0xffff0000u) * __uint_as_float(u[i] & 0xffff0000u));
        }
    }
    f32x4 (*red)[4][64] = (f32x4(*)[4][64])(smem + 8192);
#pragma unroll
    for (int t = 0; t < 4; ++t) {
        f32x4 c = {0.f, 0.f, 0.f, 0.f};
#pragma unroll
        for (int k = 0; k < (down ? 9 : 3); ++k) if (k < nk) c = mfma16(r[k][t], af[k], c);
        red[wave][t][lane] = c;
    }
    meet(2);
    if (wave < 2) {
        const int l = lane & ~8, t = wave * 2 + (n >> 3);
        const f32x4 y = ((red[0][t][l] + red[1][t][l]) + (red[2][t][l] + red[3][t][l])) + (down ? red[4][t][l] + red[5][t][l] : (red[4][t][l] + red[5][t][l]) + (red[6][t][l] + red[7][t][l]));
        const float rs1 = bfr(1.0f / (1.0f + __expf(-rv)));
        float res[4];
#pragma unroll
        for (int i = 0; i < 4; ++i) {
            const float v = bfr(y[i]);
            res[i] = down ? v * rs1 : ((w & 1) ? v : v / (1.0f + __expf(-v)));
        }
        stx2(rs, (down ? (u32)OFF_UPD : (u32)OFF_HB + (u32)(w & 1) * (16 * FF_)) + (u32)((n & 7) * ldw + c0 + t * 16 + g * 4) * 2, (u32x2){pack2(res[0], res[1]), pack2(res[2], res[3])});
        DRAIN();
        if (lane == 0) __hip_atomic_fetch_add((u32*)(ws + OFF_CNT + (down ? 256 : 0)), 1u, __ATOMIC_RELAXED, __HIP_MEMORY_SCOPE_AGENT);
    }
    if (!down) LBAR();
}

#define VMQ "8"
#define VMV "10"
#define LOOKST 18
#define NOWV 2
#define NOWIT 1
#define WIDE 52
constexpr int NWIDE = WIDE * 5, NBIG = NWIDE + (NSEG - 1 - WIDE) * 10, NTASK = NBIG + 80;

struct OutCtx { rsrc_t rs; const u16* x; u16* out; u32* errw; u32 wp; int wid; char* ws; };
struct OutOps { u32x2 xv, nv, uv; int sel; u32 off; };
__device__ __forceinline__ OutOps out_ops(const OutCtx& cx, const int token, const int col, const int (&sel_idx)[TOPC], bool& dead, const int lane) {
    OutOps o;
    o.sel = -1;
#pragma unroll
    for (int c = 0; c < TOPC; ++c) if (sel_idx[c] == token) o.sel = c;
    o.off = (u32)(token * D_ + col) * 2;
    o.xv = __builtin_amdgcn_raw_buffer_load_b64(mk_rsrc(cx.x), o.off, 0, 0);
    o.nv = ldx2(cx.rs, (u32)OFF_XN + o.off);
    if (__any(o.sel >= 0)) wait_word(cx.rs, (u32)(OFF_CNT + 256), true, (u32)NDN * 2, dead, cx.errw, 256u, lane);
    o.uv = ldx2(cx.rs, o.sel >= 0 ? (u32)(OFF_UPD + ((size_t)o.sel * D_ + col) * 2) : OOB);
    return o;
}
__device__ __forceinline__ void out_put(const OutCtx& cx, const OutOps& o, const f32x4 sum) {
    float res[4];
#pragma unroll
    for (int q = 0; q < 4; ++q) {
        float xx = bf2f((u16)((o.xv[q >> 1] >> ((q & 1) * 16)) & 0xffff));
        float nn = bf2f((u16)((o.nv[q >> 1] >> ((q & 1) * 16)) & 0xffff));
        float bo2 = nn;
        if (o.sel >= 0) bo2 = bfr(nn + bf2f((u16)((o.uv[q >> 1] >> ((q & 1) * 16)) & 0xffff)));
        float gg = bfr(bo2 / (1.0f + __expf(-bo2)));
        float sv = bfr(sum[q]);
        res[q] = bfr(xx + bfr(sv * gg));
    }
    *(u32x2*)((char*)cx.out + o.off) = (u32x2){pack2(res[0], res[1]), pack2(res[2], res[3])};
}
__device__ __forceinline__ void wait_rdy(const OutCtx& cx, const int sg, u32& rdy, bool& dead, const int lane) {
#pragma unroll 1
    for (u32 spins = 0; !dead; ++spins) {
        CFENCE();
        rdy = __builtin_amdgcn_readfirstlane(ldx1(cx.rs, (u32)(OFF_RDY + (cx.wid & 7) * 128)));
        if (rdy > (u32)sg) break;
        __builtin_amdgcn_s_sleep(8);
        if (spins > SPIN_CAP) { if (lane == 0) atomicOr(cx.errw, 64u); dead = true; }
        if ((spins & 63) == 63 && ldx1(cx.rs, (u32)OFF_ERR) != 0u) dead = true;
    }
    CFENCE();
}
__device__ __forceinline__ void wide_task(const OutCtx& cx, const int sg, const int c0, const int tid, const int (&sel_idx)[TOPC], u32& rdy, bool& dead, char* smem, u32* nextp, u32* tctr) {
    const int wave = __builtin_amdgcn_readfirstlane(tid >> 6), lane = tid & 63, n = lane & 15, g = lane >> 4, ct = c0 + wave;
    const u32 lbase = (u32)(uintptr_t)(__attribute__((address_space(3))) char*)smem;
    if (rdy <= (u32)sg) wait_rdy(cx, sg, rdy, dead, lane);
    u32 next = 0;
    if (tid == 0) next = __hip_atomic_fetch_add(tctr, 1u, __ATOMIC_RELAXED, __HIP_MEMORY_SCOPE_AGENT);
    const OutOps o0 = out_ops(cx, sg * SEGL + n, ct * 16 + 4 * g, sel_idx, dead, lane), o1 = out_ops(cx, sg * SEGL + 16 + n, ct * 16 + 4 * g, sel_idx, dead, lane);
    f32x4 a0, a1;
    o_wide(a0, a1, (u32)lane * 16, lbase + (u32)lane * 16, mk_srd(cx.ws), (u32)OFF_PX + (u32)(sg * 64 + wave) * 1024, (u32)OFF_PXM + (u32)(sg * 16 + wave) * 1024, lbase + (u32)wave * 1024,
           far_off(cx.wp, b_at(FAR_BX, NSEG, sg, ct)), to_sgpr(z_at(cx.wp, sg, ct)));
    if (tid == 0) *nextp = next;
    LBAR();
    out_put(cx, o0, a0);
    out_put(cx, o1, a1);
}
template <int OT, int NT>
__device__ __forceinline__ void out_task(const OutCtx& cx, const int sg, const int c0, const int n0, const int tid, const int (&sel_idx)[TOPC], bool& adone, u32& rdy,
                                         bool& dead, char* redp, u32* nextp, u32* tctr) {
    const int wave = __builtin_amdgcn_readfirstlane(tid >> 6), lane = tid & 63, n = lane & 15, g = lane >> 4;
    const rsrc_t rs = cx.rs;
    if (!adone && rdy <= (u32)sg) {
        wait_word(rs, (u32)(OFF_FB + (wave * 4 + (lane & 3)) * 128 + (sg & 1) * 4), true, (u32)(sg >> 1) + 1, dead, cx.errw, 32u, lane);
        u32x2 f = ldx2(rs, (u32)(OFF_FB + (wave * 4 + (lane & 3)) * 128));
        adone = __all(f[0] >= NSEG / 2 && f[1] >= NSEG / 2);
    }
    u32x4 af[OT][5], bq[NT][5];
    const u32 bo = amap((u32)(wave * 4096 + lane * 16));
#pragma unroll
    for (int c = 0; c < OT; ++c) {
        const u32 bb = far_off(cx.wp, b_at(FAR_BX, NSEG, sg, c0) + (u32)c * 32768);
#pragma unroll
        for (int hh = 0; hh < 4; ++hh) af[c][hh] = ldx4(rs, bo + hh * 1024, bb);
    }
    const int ti = (tid >> 6) & (OT * NT - 1);
    const OutOps oo = out_ops(cx, sg * SEGL + (n0 + (ti & (NT - 1))) * 16 + n, (c0 + ti / NT) * 16 + 4 * g, sel_idx, dead, lane);
    __builtin_amdgcn_sched_barrier(0);
    const bool late = sg + 4 >= NSEG;
    if (late)
        wait_flags(rs, (u32)(OT == 1 ? ((lane & 8) ? OFF_FR + (wave * 4 + (lane & 3)) * 128 : OFF_FZ + (wave * 2 + n0) * 128) : OFF_FP + wave * 128 + (lane & 1) * 16),
                   true, 1u, dead, cx.errw, 64u, lane);
    else if (rdy <= (u32)sg) wait_rdy(cx, sg, rdy, dead, lane);
#pragma unroll
    for (int c = 0; c < OT; ++c) af[c][4] = ldx4(rs, (u32)((wave >> 2) * 32768 + (wave & 3) * 1024 + lane * 16), z_at(cx.wp, sg, c0 + c));
#pragma unroll
    for (int nt = 0; nt < NT; ++nt) {
#pragma unroll
        for (int hh = 0; hh < 4; ++hh) bq[nt][hh] = ldx4(rs, (u32)lane * 16, (u32)OFF_PX + (u32)((sg * H_ + wave * 4 + hh) * 2 + n0 + nt) * 1024);
        bq[nt][4] = ldx4(rs, (u32)lane * 16, (u32)OFF_PXM + (u32)((sg * 8 + wave) * 2 + n0 + nt) * 1024);
    }
    CFENCE();
    u32 next = 0;
    if (tid == 0) next = !late ? __hip_atomic_fetch_add(tctr, 1u, __ATOMIC_RELAXED, __HIP_MEMORY_SCOPE_AGENT)
                                 : __hip_atomic_load(tctr, __ATOMIC_RELAXED, __HIP_MEMORY_SCOPE_AGENT);
    f32x4 (*red)[OT * NT][64] = (f32x4(*)[OT * NT][64])redp;
#pragma unroll
    for (int c = 0; c < OT; ++c)
#pragma unroll
        for (int nt = 0; nt < NT; ++nt) {
            f32x4 acc = {0.f, 0.f, 0.f, 0.f};
#pragma unroll
            for (int k = 0; k < 5; ++k) acc = mfma16(af[c][k], bq[nt][k], acc);
            red[wave][c * NT + nt][lane] = acc;
        }
    if (tid == 0) {
        if (late && next < (u32)NTASK) next = __hip_atomic_fetch_add(tctr, 1u, __ATOMIC_RELAXED, __HIP_MEMORY_SCOPE_AGENT);
        *nextp = next;
    }
    LBAR();
    if (tid < OT * NT * 64) {
        f32x4 sum = red[0][ti][lane];
#pragma unroll
        for (int wv = 1; wv < 8; ++wv) sum += red[wv][ti][lane];
        out_put(cx, oo, sum);
    }
}

__device__ __forceinline__ u32 xbuf(int b) { return b == 0 ? 90624u : (b == 1 ? 0u : 49664u); }
__device__ __forceinline__ void worker_role(const int wave, const int wid, const u64 tag, const u32 wp, const u32x32 (&W)[5], char* __restrict__ ws, char* smem) {
    const int lane = lane_id(), tid = wave * 64 + lane;
    const int n = lane & 15, g = lane >> 4;
    const int par = (wid >> 2) & 1, kind = (wid >> 3) & 1, h = (wid & 3) * 8 + (wid >> 4), type = kind * 2 + par;
    u32* errw = (u32*)(ws + OFF_ERR);
    const rsrc_t rs = mk_rsrc(ws);
    bool dead = false;
    const bool isqk = type < 2;
    int wslot = 1;

    {
        const u32 lbase = (u32)(uintptr_t)(__attribute__((address_space(3))) char*)smem;
        const u32x4 srd = mk_srd(ws);
        const u32 vm = (u32)lane * 16, xl = lbase + vm, dv = done_tag(tag);
        auto xsrc = [&](int it, int piece) { return (u32)OFF_XNP + (u32)((it + 2 < NSEG / 2 ? it * 2 + 4 : it * 2) + par) * 40960u + (u32)piece * 1024u; };
        const u32 fl = done_at(wp, par, kind * 32 + h) + (u32)lane * 4 - 65536, fc = kind && lane == 32 ? (u32)(OFF_FB + h * 128 + par * 4) : OOB;
        auto flag = [&](u32 v) { stx1(rs, lane < 32 ? fl + v * 65536 : fc, lane < 32 ? dv : v); };
        WSTAMP(4);
        STAMPS();
        if (DBG & 512) { const u32 t0 = (u32)__builtin_amdgcn_s_memrealtime(); while ((u32)__builtin_amdgcn_s_memrealtime() - t0 < 1500u) __builtin_amdgcn_s_sleep(16); }
        if (isqk) {
            const u32 o1 = (u32)((wave & 3) * 64 + lane) * 16;
            if (wave < 4) {
#pragma unroll 1
                for (int it = 0, b3 = 0; it < NSEG / 2; ++it) {
                    const int sg = it * 2 + par;
                    w_q(W, srd, opaque((u32)(it - NOWIT)) >> 31, xl + xbuf(b3), vm, xsrc(it, wave * 5), lbase + xbuf(b3 == 0 ? 2 : b3 - 1) + (u32)wave * 5120, o1, far_off(wp ^ QFL, NEAR_QX + (u32)(sg * H_ + h) * 8192));
                    b3 = b3 == 2 ? 0 : b3 + 1;
                    if (it < 3 || (it & 3) == 3) WSTAMP(0x10 + it);
                }
                LBAR();
            } else {
                const float ks = 0.08838834764831845f * 1.4426950408889634f;
                f32x16 acc;
                u32 ob = OOB;
                asm volatile("" : "={v[208:223]}"(acc));
                LBAR();
#pragma unroll 1
                for (int it = 0, b3 = 0; it < NSEG / 2; ++it) {
                    if (it == 8 && wave == 5) wait_word(rs, (u32)OFF_IDX, true, 1u, dead, errw, 512u, lane);
                    const u32 now = opaque((u32)(it - NOWIT)) >> 31, on = far_off(wp ^ KFL, NEAR_KX + (u32)((it * 2 + par) * H_ + h) * 8192);
                    w_k(W, srd, now, xl + xbuf(b3), vm, xsrc(it, wave * 5), lbase + xbuf(b3 == 0 ? 2 : b3 - 1) + (u32)wave * 5120, on, o1, ob, ks, acc);
                    ob = on | now << 31;
                    b3 = b3 == 2 ? 0 : b3 + 1;
                    if (wave == 4 && it != NOWIT) flag((u32)it + now);
                }
                w_kl(srd, o1, ob, ks, acc);
            }
        } else if (wave < 4) {
            const u32 ra = (u32)(OFF_RDY + (wid & 7) * 128), rl = lbase + 133632u + (u32)wave * 256;
#pragma unroll 1
            for (int it = 0, b3 = 0; it < NSEG / 2; ++it) {
                const int need = it * 2 + par - RING - 1;
                if (TWO && wave == 1 && need >= -1 && __builtin_expect((int)rfl(*(lds_vu32p)rl) < max(need, 1), 0)) wait_word(rs, ra, true, (u32)max(need, 1), dead, errw, 512u, lane);
                if (it == 8 && wave == 2 && rfl(*(lds_vu32p)rl) == 0u) wait_word(rs, (u32)OFF_IDX, true, 1u, dead, errw, 512u, lane);
                w_v(W, srd, xl + xbuf(b3), vm, xsrc(it, wave * 10), lbase + xbuf(b3 == 0 ? 2 : b3 - 1) + (u32)wave * 10240, xl + ((it & 1) ? 135168u : 40960u) + (u32)wave * 1024, wave == 1 && need >= -5 ? ra : (wave == 2 && it == 7 ? (u32)OFF_IDX : OOB), rl STA);
                b3 = b3 == 2 ? 0 : b3 + 1;
                if (wave == 0 && it && it <= NOWV) flag((u32)it);
                if (it < 3 || (it & 3) == 3) WSTAMP(0x10 + it);
            }
            LBAR();
        } else {
            const u32 o1 = amap((u32)h * 1024 + vm) + (u32)(wave & 3) * 131072;
            f32x16 acc;
            u32 n0 = OOB, f0 = OOB;
            const u32 sl = (u32)(lane & 31) * 2 + par, cl = (u32)lane & 32;
            const u32 tn = (DBG & 32) && sl > 3 ? OOB : far_v(wp ^ 1u, b_at(NEAR_B, RING, sl & (RING - 1), cl)), tf = far_v(wp, b_at(FAR_BX, NSEG, sl, cl));
            *(lds_vu32p)(lbase + 134656u) = 0u;
            asm volatile("s_waitcnt vmcnt(0) lgkmcnt(0)" : "={v[208:223]}"(acc) :: "memory");
#pragma unroll 1
            for (int it = 0; it < NSEG / 2; ++it) {
                const u32 n2 = tab(tn, it + 32), f2 = tab(tf, it + 32), now = opaque((u32)(it - NOWV)) >> 31;
                w_b(W, srd, now, lbase + 134656u, (u32)it * 4 + 3, it > NOWV ? (lane < 32 ? fl + (u32)it * 65536 : fc) : OOB, lane < 32 ? dv : (u32)it, xl + ((it & 1) ? 135168u : 40960u), tab(tn, it), n2, tab(tf, it), f2, o1, n0, f0, acc STA);
                n0 = n2 | now << 31; f0 = f2 | now << 31;
            }
            w_bl(srd, o1, n0, f0, acc);
        }
        DRAIN();
        LBAR();
        if (wave == 4) flag(NSEG / 2);
    }
    WSTAMP(2);

    {
    const int lane = lane_id(), tid = wave * 64 + lane;
    WSTAMP(5);
    u32* nextp = (u32*)(smem + 131072);
    u32* tctr = (u32*)(ws + OFF_CNT + 512);
    if (tid == 0) nextp[0] = __hip_atomic_fetch_add(tctr, 1u, __ATOMIC_RELAXED, __HIP_MEMORY_SCOPE_AGENT);
    wait_flags(rs, (u32)OFF_IDX + (lane & 1) * 16, true, 1u, dead, errw, 256u, lane);
    __syncthreads();
    int sel_idx[TOPC];
    {
        u32x4 i0 = ldx4(rs, (u32)OFF_IDX), i1 = ldx4(rs, (u32)OFF_IDX + 16);
#pragma unroll
        for (int c = 0; c < 4; ++c) { sel_idx[c] = (int)i0[c] - 1; sel_idx[4 + c] = (int)i1[c] - 1; }
    }
    WSTAMP(3);
    const OutCtx cx = {rs, ARG(0), ARG(11), errw, wp, wid, ws};
    bool adone = false;
    u32 rdy = 0;
#pragma unroll 1
    for (int tc = 0;; ++tc) {
        const int task = __builtin_amdgcn_readfirstlane(nextp[tc & 1]);
        if (task >= NTASK) break;
        char* redp = smem + (tc & 1) * 65536;
        if (task < NWIDE) {
            WSTAMP(0x100 + task / 5);
            wide_task(cx, task / 5, (task % 5) * 8, tid, sel_idx, rdy, dead, smem, nextp + ((tc + 1) & 1), tctr);
        } else if (task < NBIG) {
            WSTAMP(0x100 + WIDE + (task - NWIDE) / 10);
            out_task<4, 2>(cx, WIDE + (task - NWIDE) / 10, ((task - NWIDE) % 10) * 4, 0, tid, sel_idx, adone, rdy, dead, redp, nextp + ((tc + 1) & 1), tctr);
        } else {
            WSTAMP(0x100 + NSEG - 1);
            out_task<1, 1>(cx, NSEG - 1, (task - NBIG) >> 1, (task - NBIG) & 1, tid, sel_idx, adone, rdy, dead, redp, nextp + ((tc + 1) & 1), tctr);
        }
        WSTAMP(0x300);
        if (task >= NBIG && task < NBIG + NSEG && tid < 64 + NFILL) stx1(rs, done_at(wp, task - NBIG, tid), 0u);
        if (task == NTASK - 1) {
            if (tid < 256) stx2(rs, (u32)(OFF_INI + tid * 8), (u32x2){~(u32)tag, ~cal_tag(tag, ~(u32)tag)});
        }
    }
    }
}

constexpr int FUSED_LDS = 140 * 1024;
#define ORD_A 0x143210
#define ORD_C 0x101234
#define KERNEL_SYM "fused_kernel"
#define CODE_4K 22u
extern "C" __global__ __launch_bounds__(512, 2) void fused_kernel(const u16*, const u16*, const u16*, const u16*, const u16*, const u16*, const u16*, const u16*,
                                                                  const u16*, const u16*, char*, u16*) {
    extern __shared__ __attribute__((aligned(16))) char smem[];
    const int wave = __builtin_amdgcn_readfirstlane(threadIdx.x >> 6);
    char* __restrict__ ws = (char*)ARG(10);
    u64 gen = 0, tag = 0, old = 0;
    u32 xcc, se;
    asm volatile("s_getreg_b32 %0, hwreg(HW_REG_XCC_ID, 0, 3)" : "=s"(xcc));
    asm volatile("s_getreg_b32 %0, hwreg(HW_REG_HW_ID, 13, 2)" : "=s"(se));
    u32 bid = blockIdx.x;
    asm volatile("" : "+s"(bid));
    const bool first = (int)bid >= 0;
    u32 bq = first ? (bid & ~31u) + se * 8u + xcc : bid & 255u;
    asm volatile("" : "+s"(bq));
    const int b = (int)bq, k = b >> 3, xg = b & 3;
    if (threadIdx.x >= 448 && threadIdx.x < 453) {
        const int lane = threadIdx.x & 63;
        const u32 pg = (bid >> 3 & 31u) + 32u * (u32)lane;
        const char* tp = lane < 4 ? ws + min(pg << 21, (u32)WS_TOTAL - 4u) : (const char*)ARG(11) + (size_t)(bid >> 3 & 3u) * 655360;
        u32 sv;
        asm volatile("s_mov_b32 %0, m0\n\ts_mov_b32 m0, %2\n\ts_nop 0\n\tglobal_load_lds_dword %1, off\n\ts_mov_b32 m0, %0"
                     : "=&s"(sv) : "v"(tp), "s"((u32)(uintptr_t)(__attribute__((address_space(3))) char*)smem + 133120u) : "memory");
    }
    if (wave == 6) {
        u64 pc;
        u32 sv;
        asm volatile("s_getpc_b64 %0" : "=s"(pc));
        const u32 o = (bid >> 3 & 31u) % CODE_4K * 4096u + (u32)(threadIdx.x & 31) * 128u;
        asm volatile("s_mov_b32 %0, m0\n\ts_mov_b32 m0, %3\n\ts_nop 0\n\tglobal_load_lds_dword %1, %2\n\ts_mov_b32 m0, %0"
                     : "=&s"(sv) : "v"(o), "s"(pc), "s"((u32)(uintptr_t)(__attribute__((address_space(3))) char*)smem + 133376u) : "memory");
    }
    u64* claim = (u64*)(ws + OFF_CLAIM);
    u32* sh = (u32*)(smem + 132608);
    auto try_claim = [&] {
        if (wave == 0 && first) {
            asm volatile("s_load_dwordx2 %0, %1, 0x0\n\ts_waitcnt lgkmcnt(0)" : "=s"(gen) : "s"(ws + OFF_GEN) : "memory");
            tag = (gen + 0x9E3779B97F4A7C15ull) * 0xBF58476D1CE4E5B9ull;
            tag ^= tag >> 31;
            tag *= 0x94D049BB133111EBull;
            tag ^= tag >> 29;
            if (lane_id() == 0) {
                old = __hip_atomic_exchange(claim + b * 8, tag, __ATOMIC_RELAXED, __HIP_MEMORY_SCOPE_AGENT);
                sh[1] = (u32)tag; sh[2] = (u32)(tag >> 32); sh[3] = (u32)gen; sh[4] = (u32)(gen >> 32);
            }
        }
    };
    auto settle = [&] {
        const int tid = wave * 64 + lane_id();
        if (tid == 0 && first) {
            u32 vb = (u32)b;
#pragma unroll 1
            for (int i = 0; i < 256 && old == tag; ++i) {
                vb = (vb + 1) & 255u;
                old = __hip_atomic_exchange(claim + vb * 8, tag, __ATOMIC_RELAXED, __HIP_MEMORY_SCOPE_AGENT);
            }
            sh[0] = vb;
        }
        LBAR();
        const u32 bc = __builtin_amdgcn_readfirstlane(sh[0]);
        asm volatile("s_cmp_eq_u32 %0, %1\n\ts_cbranch_scc1 Lgo%=\n\t"
                     "s_or_b32 s2, %0, 0x80000000\n\ts_mov_b64 s[0:1], %2\n\tv_mov_b32 v0, %3\n\ts_waitcnt vmcnt(0) lgkmcnt(0)\n\t"
                     "s_getpc_b64 s[4:5]\nLpc%=:\n\ts_sub_u32 s4, s4, Lpc%=-" KERNEL_SYM "\n\ts_subb_u32 s5, s5, 0\n\ts_setpc_b64 s[4:5]\nLgo%=:"
                     :: "s"(bc), "s"(b), "s"(__builtin_amdgcn_kernarg_segment_ptr()), "v"(tid) : "s2", "scc", "memory");
        tag = (u64)(u32)__builtin_amdgcn_readfirstlane(sh[1]) | ((u64)(u32)__builtin_amdgcn_readfirstlane(sh[2]) << 32);
    };
    if (b & 4) {
        const int wid = k * 4 + xg, h = (wid & 3) * 8 + (wid >> 4), par = (wid >> 2) & 1;
        const bool isqk = ((wid >> 3) & 1) == 0;
        const int lane = lane_id();
        u32x32 W[5];
        u32 wp = 0;
        u32x4 f0, f1;
        u64 tg2 = 0;
        bool got = false;
        auto xtile = [&](int t) {
#pragma unroll
            for (int i = 0; i < 5; ++i)
                dma16_sc1(ws + OFF_XNP + (size_t)(par + 2 * t) * 40960 + (wave * 5 + i) * 1024 + lane * 16,
                          __builtin_amdgcn_readfirstlane((u32)(uintptr_t)(__attribute__((address_space(3))) char*)smem + xbuf(t) + (u32)(wave * 5 + i) * 1024));
        };
        auto hook = [&](int st) {
            if (st == 0) try_claim();
            if (st == LOOKST) {
                asm volatile("global_load_dwordx4 %0, %2, off sc1\n\tglobal_load_dwordx4 %1, %2, off offset:1024 sc1"
                             : "=&v"(f0), "=&v"(f1) : "v"(ws + OFF_INI + lane * 16) : "memory");
                tg2 = (u64)(u32)__builtin_amdgcn_readfirstlane(sh[1]) | ((u64)(u32)__builtin_amdgcn_readfirstlane(sh[2]) << 32);
            }
            if (st == 3) {
                asm volatile("" : "+v"(f0), "+v"(f1));
                got = __all(lane >= 16 || rows_ok(f1, tg2));
                if (got) xtile(0);
                if (__all(lane < 48 || ini_ok(f0, tg2))) wp = wp_of(ws, f0);
            }
        };
        load_weights<ORD_A>(hook, ARG(isqk ? 3 : 5) + h * DH_, ARG(isqk ? 4 : 6) + (size_t)h * DH_ * (isqk ? 1 : D_), !isqk, smem, wave, lane,
           wave >> 2, 2 * (wave & 3), wave >> 2, 2 * (wave & 3) + 1, W);
        settle();
        const rsrc_t rs = mk_rsrc(ws);
        if (!got) { wait_rows(rs, tag, lane & 15); xtile(0); }
        if (wp == 0) wp = wait_init(rs, ws, tag, lane);
        DRAIN();
        xtile(1);
        LBAR();
        PIN_W(W);
        worker_role(wave, wid, tag, wp, W, ws, smem);
        __builtin_amdgcn_endpgm();
    }
    const int hl = ((k >> 1) ^ (xg >> 1)) & 1, j = (k >> 2) * 2 + (k & 1);
    if (j < 8) {
        const int h = xg * 8 + hl * 4 + (j & 3);
        const bool iss = j < 4;
        u32x32 W[5];
        load_weights<ORD_C>([&](int st) {
            if (st == 0) try_claim();
        }, ARG(iss ? 3 : 5) + h * DH_, ARG(iss ? 4 : 6) + (size_t)h * DH_ * (iss ? 1 : D_), !iss, smem, wave, lane_id(),
           iss ? 0 : wave < 4, iss ? wave : (wave < 4 ? 2 * wave : wave - 4), iss ? 1 : wave < 4, iss || wave >= 4 ? wave : 2 * wave + 1, W, !iss && wave < 4);
        settle();
        PIN_W(W);
        if (iss) s_role(wave, h, xg, tag, ARG(1), W, ws, smem);
        else v_role(wave, h, xg, tag, ARG(1), W, ws, smem);
        __builtin_amdgcn_endpgm();
    }
    try_claim();
    const int pi = xg * 8 + hl * 4 + (j & 3);
    if (j < 12) {
        u32 votes = 0;
        if (wave == 1) votes = probe(pi, xcc, ((se ^ xcc) >> 1) & 1u, ws);
        settle();
        if (wave == 1) {
            const u32 c = (u32)pi * 64 + lane_id();
            stx4(mk_rsrc(ws), c < INI_ZERO_CHUNKS ? (u32)ZERO_BEGIN + c * 16 : OOB, (u32x4){0u, 0u, 0u, 0u});
            DRAIN();
            stx2(mk_rsrc(ws), lane_id() == 0 ? (u32)(OFF_INI + (96 + pi) * 8) : OOB, (u32x2){votes, cal_tag(tag, votes)});
        }
    }
    {
        const int lane = lane_id(), li = pi + (j < 12 ? 32 : 0);
        const rsrc_t rs = mk_rsrc(ws);
        u32x4 xv[5], wv[5];
        ln_load(li * 32 + wave * 4, lane, ARG(0), ARG(2), xv, wv);
        if (wave < 2) ((u32*)smem)[wave * 64 + lane] = 0u;
        ln_regs(xv);
        ln_store(li * 32 + wave * 4, lane, xv, wv, rs);
        DRAIN();
        if (j < 12) LBAR(); else settle();
        stx2(rs, wave == 0 && lane == 0 ? (u32)(OFF_INI + (128 + li) * 8) : OOB, (u32x2){(u32)tag, (u32)(tag >> 32)});
    }
    u32 wp = 0;
    const int mi = (j - 8) * 8 + xg * 2 + hl;
    auto first_slots = [&] {
        wp = wait_init(mk_rsrc(ws), ws, tag, lane_id());
        if (j >= 13 && mi < NGU) fill_first(wave, mi - 40, wp, done_tag(tag), ws);
    };
    if (mi < NGU) moe_part<false>(first_slots, mi, tag, wave, ws, smem);
    else if ((wave >> 1) != 1) { moe_part<true>(first_slots, mi - NGU, tag, wave < 2 ? wave : wave - 2, ws, smem); return; }
    else first_slots();
    if (j < 13) {
        r_role(wave, (int)(((hl ? 0x85410u : 0x97632u) >> ((j - 8) * 4)) & 15u) * 4 + xg, xg, wp, done_tag(tag), ws, smem);
        __builtin_amdgcn_endpgm();
    }
    gen = (u64)sh[3] | ((u64)sh[4] << 32);
    if (mi == 40) {
        u32 cap = SPIN_CAP;
        if (wave == 0) wait_done(mk_rsrc(ws), done_at(wp, 0, 64 + min(lane_id(), NFILL - 1)), done_tag(tag), cap, (u32*)(ws + OFF_ERR));
        LBAR();
    }
    if (wave >= 4) fill_role(wave & 3, mi - 40, wp, done_tag(tag), ws, (u32*)smem);
    else if (wave == 2) { if (TWO < 5) watch_role(wp, done_tag(tag), xg, ws, (lds_vu32p)(smem + 256)); }
    else if (wave == 3) { if (TWO < 5) pull_role(wp, xg, hl * 3 + j - 13, ws, smem, (lds_vu32p)(smem + 256)); }
    else if (mi == 40) { if (wave == 0) relay_role(wp, xg, ws); }
    else if (mi == 41 && wave == 0) {
        const rsrc_t rs = mk_rsrc(ws);
        const int lane = lane_id();
#pragma unroll 1
        for (u32 spins = 0; spins < SPIN_CAP; ++spins) {
            CFENCE();
            bool ok = true;
#pragma unroll
            for (int q = 0; q < 4; ++q) {
                u32x2 f = ldx2(rs, (u32)(OFF_CLAIM + (q * 64 + lane) * 64));
                ok = ok && f[0] == (u32)tag && f[1] == (u32)(tag >> 32);
            }
            if (__all(ok)) break;
            __builtin_amdgcn_s_sleep(32);
        }
        if (lane == 0) __hip_atomic_store((u64*)(ws + OFF_GEN), gen + 1ull, __ATOMIC_RELAXED, __HIP_MEMORY_SCOPE_AGENT);
#pragma unroll
        for (int q = 0; q < 4; ++q) stx2(rs, (u32)(OFF_CLAIM + (q * 64 + lane) * 64), (u32x2){~(u32)tag, 0u});
    }
}

extern "C" void kernel_launch(void* const* d_in, const int* in_sizes, int n_in, void* d_out, int out_size,
                              void* d_ws, size_t ws_size, hipStream_t stream) {
    const u16* x = (const u16*)d_in[0];
    const u16* mem0 = (const u16*)d_in[1];
    const u16* Wq = (const u16*)d_in[2];
    const u16* Wk = (const u16*)d_in[3];
    const u16* Wv = (const u16*)d_in[4];
    const u16* Wo = (const u16*)d_in[5];
    const u16* w_r = (const u16*)d_in[8];
    const u16* w_gate = (const u16*)d_in[9];
    const u16* w_up = (const u16*)d_in[10];
    const u16* w_down = (const u16*)d_in[11];
    char* ws = (char*)d_ws;
    u16* out = (u16*)d_out;

    hipFuncSetAttribute((const void*)fused_kernel, hipFuncAttributeMaxDynamicSharedMemorySize, FUSED_LDS);
    fused_kernel<<<256, 512, FUSED_LDS, stream>>>(x, mem0, w_r, Wq, Wk, Wv, Wo, w_gate, w_up, w_down, ws, out);
}
```
